```python
import torch

def _softmax(x, axis=-1):
    return torch.softmax(x, dim=axis)
def _gelu(x):
    return torch.nn.functional.gelu(x, approximate="tanh")
def _silu(x):
    return torch.nn.functional.silu(x)
def _sigmoid(x):
    return torch.sigmoid(x)
def _layernorm(x, axis=-1, eps=1e-5):
    m = x.mean(dim=axis, keepdim=True)
    v = x.var(dim=axis, keepdim=True, unbiased=False)
    return (x - m) / torch.sqrt(v + eps)
def _scatter_add(tgt, idx, src, axis=0):
    return tgt.clone().index_add_(axis, idx, src)
def _segment_sum(src, seg, n):
    out = torch.zeros((n,) + src.shape[1:], dtype=src.dtype)
    return out.index_add_(0, seg, src)
def _pad(x, pw):
    flat = tuple(v for pair in reversed(pw) for v in pair)
    return torch.nn.functional.pad(x, flat)

kernel_name = 'parallel_sum__ssm_conv_s4__swiglu_ffn__prompt'

def setup_inputs():

    _w = (lambda a: (lambda t: t.to(torch.bfloat16) if t.is_floating_point() else t)(torch.as_tensor(a)))
    def _A_setup():
        B, L, D, N = 1, 2048, 640, 192
        A_re = -(lambda _x: torch.exp(torch.as_tensor(_x)))((lambda *s: torch.randn(*s, dtype=torch.bfloat16) * 3.2)(D, N))
        A_im = torch.pi * (lambda *s: torch.randn(*s, dtype=torch.bfloat16) * 0.2)(D, N)
        B_re = (lambda *s: torch.randn(*s, dtype=torch.bfloat16) * 0.4)(D, N)
        B_im = (lambda *s: torch.randn(*s, dtype=torch.bfloat16) * 0.4)(D, N)
        C_re = (lambda *s: torch.randn(*s, dtype=torch.bfloat16) * 0.4)(D, N)
        C_im = (lambda *s: torch.randn(*s, dtype=torch.bfloat16) * 0.4)(D, N)
        log_dt = (lambda *s: torch.randn(*s, dtype=torch.bfloat16) * 0.2)(D) * 0.5 - 4.0
        Dskip = (lambda *s: torch.randn(*s, dtype=torch.bfloat16) * 0.4)(D)
        return {
            "u": _w((lambda *s: torch.randn(*s, dtype=torch.bfloat16) * 0.05)(B, L, D)),
            "A_re": _w(A_re),
            "A_im": _w(A_im),
            "B_re": _w(B_re),
            "B_im": _w(B_im),
            "C_re": _w(C_re),
            "C_im": _w(C_im),
            "log_dt": _w(log_dt),
            "Dskip": _w(Dskip),
        }
    _A_in = _A_setup()

    def _B_setup():
        B, S, D, F = 1, 2048, 640, 1536
        return {
            "x": _w((lambda *s: torch.randn(*s, dtype=torch.bfloat16) * 0.05)(B, S, D)),
            "w_gate": _w((lambda *s: torch.randn(*s, dtype=torch.bfloat16) * 0.05)(D, F)),
            "w_up": _w((lambda *s: torch.randn(*s, dtype=torch.bfloat16) * 0.05)(D, F)),
            "w_down": _w((lambda *s: torch.randn(*s, dtype=torch.bfloat16) * 0.05)(F, D)),
        }
    _B_in = _B_setup()

    return {
        "A_u": _A_in["u"],
        "A_A_re": _A_in["A_re"],
        "A_A_im": _A_in["A_im"],
        "A_B_re": _A_in["B_re"],
        "A_B_im": _A_in["B_im"],
        "A_C_re": _A_in["C_re"],
        "A_C_im": _A_in["C_im"],
        "A_log_dt": _A_in["log_dt"],
        "A_Dskip": _A_in["Dskip"],
        "B_x": _B_in["x"],
        "B_w_gate": _B_in["w_gate"],
        "B_w_up": _B_in["w_up"],
        "B_w_down": _B_in["w_down"],
    }

def reference(A_u, A_A_re, A_A_im, A_B_re, A_B_im, A_C_re, A_C_im, A_log_dt, A_Dskip, B_x, B_w_gate, B_w_up, B_w_down):
    def _A_ref(u, A_re, A_im, B_re, B_im, C_re, C_im, log_dt, Dskip):
        Bsz, L, D = u.shape
        dt = torch.exp(log_dt)[:, None]
        A = A_re + 1j * A_im
        Bc = B_re + 1j * B_im
        Cc = C_re + 1j * C_im
        logdA = dt * A
        dB = dt * Bc
        l = (lambda x, dt: x.to(dt))(torch.arange(L), A_re.dtype)
        P = torch.exp(logdA[:, :, None] * l[None, None, :])
        K = torch.einsum("dn,dnl,dn->dl", Cc, P, dB).real
        L2 = 2 * L
        Kp = _pad(K, ((0, 0), (0, L)))
        up = (lambda x, *d: x.permute(*d))(u, 0, 2, 1)
        up = _pad(up, ((0, 0), (0, 0), (0, L)))
        Kf = (lambda x, axes=(-2,-1): torch.fft.rfft2(x.float(), dim=axes))(Kp[:, None, :], axes=(-2, -1))
        Uf = (lambda x, axes=(-2,-1): torch.fft.rfft2(x.float(), dim=axes))(up[:, :, None, :], axes=(-2, -1))
        Yf = Uf * Kf[None, :, :, :]
        y = (lambda x, s, axes=(-2,-1): torch.fft.irfft2(x, s=s, dim=axes).to(torch.bfloat16))(Yf, (1, L2), axes=(-2, -1))[:, :, 0, :L]
        y = (lambda x, *d: x.permute(*d))(y, 0, 2, 1) + u * Dskip[None, None, :]
        return y

    def _B_ref(x, w_gate, w_up, w_down):
        g = _silu(x @ w_gate)
        u = x @ w_up
        return (g * u) @ w_down

    x = A_u
    xn = _layernorm(x, axis=-1)
    return x + _A_ref(xn, A_A_re, A_A_im, A_B_re, A_B_im, A_C_re, A_C_im, A_log_dt, A_Dskip) + _B_ref(xn, B_w_gate, B_w_up, B_w_down)
```

```cpp
#include <hip/hip_runtime.h>
#include <hip/hip_bf16.h>
#include <stdint.h>

constexpr int L = 2048, D = 640, NS = 192, F = 1536;

__device__ __forceinline__ float bf2f(uint16_t v) { return __uint_as_float(((uint32_t)v) << 16); }
__device__ __forceinline__ uint16_t f2bf(float f) {
  uint32_t u = __float_as_uint(f);
  u += 0x7fffu + ((u >> 16) & 1u);
  return (uint16_t)(u >> 16);
}
__device__ __forceinline__ float rbf(float f) { return bf2f(f2bf(f)); }

__global__ void k_ln(const uint16_t* x, uint16_t* xn) {
  int row = blockIdx.x;
  int lane = threadIdx.x;
  float v[10];
  float s = 0;
  for (int i = 0; i < 10; ++i) { v[i] = bf2f(x[row * D + lane + 64 * i]); s += v[i]; }
  for (int o = 32; o; o >>= 1) s += __shfl_xor(s, o);
  float mean = s / D;
  float q = 0;
  for (int i = 0; i < 10; ++i) { float d = v[i] - mean; q += d * d; }
  for (int o = 32; o; o >>= 1) q += __shfl_xor(q, o);
  float var = rbf(q / D);
  float m = rbf(mean);
  float den = rbf(sqrtf(rbf(var + 1e-5f)));
  for (int i = 0; i < 10; ++i) xn[row * D + lane + 64 * i] = f2bf(rbf(v[i] - m) / den);
}

__global__ void k_kgen(const uint16_t* Are, const uint16_t* Aim, const uint16_t* Bre, const uint16_t* Bim,
                       const uint16_t* Cre, const uint16_t* Cim, const uint16_t* logdt, float* K) {
  int d = blockIdx.y;
  int l = blockIdx.x * 256 + threadIdx.x;
  float dt = rbf(expf(bf2f(logdt[d])));
  float lf = rbf((float)l);
  float acc = 0;
  for (int n = 0; n < NS; ++n) {
    int i = d * NS + n;
    float ar = dt * bf2f(Are[i]), ai = dt * bf2f(Aim[i]);
    float br = dt * bf2f(Bre[i]), bi = dt * bf2f(Bim[i]);
    float cr = bf2f(Cre[i]), ci = bf2f(Cim[i]);
    float wr = cr * br - ci * bi, wi = cr * bi + ci * br;
    float mag = expf(ar * lf);
    float s, c;
    sincosf(ai * lf, &s, &c);
    acc += mag * (wr * c - wi * s);
  }
  K[d * L + l] = acc;
}

__global__ void k_conv(const uint16_t* xn, const float* K, float* y) {
  int d = blockIdx.x * 64 + threadIdx.x % 64;
  int t = blockIdx.y * 4 + threadIdx.x / 64;
  float acc = 0;
  for (int s = 0; s <= t; ++s) acc += K[d * L + s] * bf2f(xn[(t - s) * D + d]);
  y[t * D + d] = acc;
}

__global__ void k_g1(const uint16_t* xn, const uint16_t* wg, const uint16_t* wu, uint16_t* h) {
  int f = blockIdx.x * 256 + threadIdx.x;
  int t = blockIdx.y;
  float g = 0, u = 0;
  for (int k = 0; k < D; ++k) {
    float a = bf2f(xn[t * D + k]);
    g += a * bf2f(wg[k * F + f]);
    u += a * bf2f(wu[k * F + f]);
  }
  g = rbf(g); u = rbf(u);
  float sg = rbf(g / (1.f + expf(-g)));
  h[t * F + f] = f2bf(sg * u);
}

__global__ void k_g2(const uint16_t* x, const uint16_t* xn, const uint16_t* h, const uint16_t* wd, const float* y,
                     const uint16_t* dskip, uint16_t* out) {
  int d = blockIdx.x * 128 + threadIdx.x;
  int t = blockIdx.y;
  float acc = 0;
  for (int k = 0; k < F; ++k) acc += bf2f(h[t * F + k]) * bf2f(wd[k * D + d]);
  float b = rbf(acc);
  float a = rbf(rbf(y[t * D + d]) + rbf(bf2f(xn[t * D + d]) * bf2f(dskip[d])));
  out[t * D + d] = f2bf(rbf(bf2f(x[t * D + d]) + a) + b);
}

extern "C" void kernel_launch(void* const* d_in, const int* in_sizes, int n_in, void* d_out, int out_size,
                              void* d_ws, size_t ws_size, hipStream_t stream) {
  const uint16_t* x = (const uint16_t*)d_in[0];
  char* ws = (char*)d_ws;
  uint16_t* xn = (uint16_t*)ws;
  float* K = (float*)(ws + (4 << 20));
  float* y = (float*)(ws + (12 << 20));
  uint16_t* h = (uint16_t*)(ws + (20 << 20));
  k_ln<<<L, 64, 0, stream>>>(x, xn);
  k_kgen<<<dim3(L / 256, D), 256, 0, stream>>>((const uint16_t*)d_in[1], (const uint16_t*)d_in[2], (const uint16_t*)d_in[3],
                                               (const uint16_t*)d_in[4], (const uint16_t*)d_in[5], (const uint16_t*)d_in[6],
                                               (const uint16_t*)d_in[7], K);
  k_conv<<<dim3(D / 64, L / 4), 256, 0, stream>>>(xn, K, y);
  k_g1<<<dim3(F / 256, L), 256, 0, stream>>>(xn, (const uint16_t*)d_in[10], (const uint16_t*)d_in[11], h);
  k_g2<<<dim3(D / 128, L), 128, 0, stream>>>(x, xn, h, (const uint16_t*)d_in[12], y, (const uint16_t*)d_in[8], (uint16_t*)d_out);
}
```

```cpp
#include <hip/hip_runtime.h>
#include <hip/hip_bf16.h>
#include <stdint.h>

constexpr int L = 2048, D = 640, NS = 192, F = 1536;

__device__ __forceinline__ float bf2f(uint16_t v) { return __uint_as_float(((uint32_t)v) << 16); }
__device__ __forceinline__ uint16_t f2bf(float f) {
  uint32_t u = __float_as_uint(f);
  u += 0x7fffu + ((u >> 16) & 1u);
  return (uint16_t)(u >> 16);
}
__device__ __forceinline__ float rbf(float f) { return bf2f(f2bf(f)); }

__global__ void k_ln(const uint16_t* x, uint16_t* xn) {
  int row = blockIdx.x;
  int lane = threadIdx.x;
  float v[10];
  float s = 0;
  for (int i = 0; i < 10; ++i) { v[i] = bf2f(x[row * D + lane + 64 * i]); s += v[i]; }
  for (int o = 32; o; o >>= 1) s += __shfl_xor(s, o);
  float mean = s / D;
  float q = 0;
  for (int i = 0; i < 10; ++i) { float d = v[i] - mean; q += d * d; }
  for (int o = 32; o; o >>= 1) q += __shfl_xor(q, o);
  float var = rbf(q / D);
  float m = rbf(mean);
  float den = rbf(sqrtf(rbf(var + 1e-5f)));
  for (int i = 0; i < 10; ++i) xn[row * D + lane + 64 * i] = f2bf(rbf(v[i] - m) / den);
}

__global__ void k_kgen(const uint16_t* Are, const uint16_t* Aim, const uint16_t* Bre, const uint16_t* Bim,
                       const uint16_t* Cre, const uint16_t* Cim, const uint16_t* logdt, float* K) {
  int d = blockIdx.y;
  int l = blockIdx.x * 256 + threadIdx.x;
  float dt = rbf(expf(bf2f(logdt[d])));
  float lf = rbf((float)l);
  float acc = 0;
  for (int n = 0; n < NS; ++n) {
    int i = d * NS + n;
    float ar = dt * bf2f(Are[i]), ai = dt * bf2f(Aim[i]);
    float br = dt * bf2f(Bre[i]), bi = dt * bf2f(Bim[i]);
    float cr = bf2f(Cre[i]), ci = bf2f(Cim[i]);
    float wr = cr * br - ci * bi, wi = cr * bi + ci * br;
    float mag = expf(ar * lf);
    float s, c;
    sincosf(ai * lf, &s, &c);
    acc += mag * (wr * c - wi * s);
  }
  K[d * L + l] = acc;
}

__global__ void k_conv(const uint16_t* xn, const float* K, float* y) {
  int d = blockIdx.x * 64 + threadIdx.x % 64;
  int t = blockIdx.y * 4 + threadIdx.x / 64;
  float acc = 0;
  for (int s = 0; s <= t; ++s) acc += K[d * L + s] * bf2f(xn[(t - s) * D + d]);
  y[t * D + d] = acc;
}

__global__ void k_g1(const uint16_t* xn, const uint16_t* wg, const uint16_t* wu, uint16_t* h) {
  int f = blockIdx.x * 256 + threadIdx.x;
  int t = blockIdx.y;
  float g = 0, u = 0;
  for (int k = 0; k < D; ++k) {
    float a = bf2f(xn[t * D + k]);
    g += a * bf2f(wg[k * F + f]);
    u += a * bf2f(wu[k * F + f]);
  }
  g = rbf(g); u = rbf(u);
  float sg = rbf(g / (1.f + expf(-g)));
  h[t * F + f] = f2bf(sg * u);
}

__global__ void k_g2(const uint16_t* x, const uint16_t* xn, const uint16_t* h, const uint16_t* wd, const float* y,
                     const uint16_t* dskip, uint16_t* out) {
  int d = blockIdx.x * 128 + threadIdx.x;
  int t = blockIdx.y;
  float acc = 0;
  for (int k = 0; k < F; ++k) acc += bf2f(h[t * F + k]) * bf2f(wd[k * D + d]);
  float b = rbf(acc);
  float a = rbf(rbf(y[t * D + d]) + rbf(bf2f(xn[t * D + d]) * bf2f(dskip[d])));
  out[t * D + d] = f2bf(rbf(bf2f(x[t * D + d]) + a) + b);
}

#define VAR 0

typedef unsigned v4u __attribute__((ext_vector_type(4)));
__global__ __launch_bounds__(256) void probeA(const char* x, const char* wg, const char* wu, const char* wd, unsigned long long* tm, unsigned* sink) {
#if __HIP_DEVICE_COMPILE__
  unsigned long long T0 = __builtin_amdgcn_s_memrealtime();
  unsigned long long c0 = __builtin_amdgcn_s_memtime();
  int b = blockIdx.x;
  const char* base = b < 80 ? x + b * 32768 : b < 140 ? wg + (b - 80) * 32768 : b < 200 ? wu + (b - 140) * 32768 : wd + (b - 200) * 32768;
  const char* p = base + (threadIdx.x >> 6) * 8192 + (threadIdx.x & 63) * 16;
  v4u r[8];
#pragma unroll
  for (int i = 0; i < 8; ++i) r[i] = *(const v4u*)(p + i * 1024);
  asm volatile("s_waitcnt vmcnt(7)" ::: "memory");
  unsigned long long c1 = __builtin_amdgcn_s_memtime();
  asm volatile("s_waitcnt vmcnt(0)" ::: "memory");
  v4u a = r[0];
#pragma unroll
  for (int i = 1; i < 8; ++i) a ^= r[i];
  __syncthreads();
  unsigned long long c2 = __builtin_amdgcn_s_memtime();
  unsigned long long T1 = __builtin_amdgcn_s_memrealtime();
  sink[blockIdx.x * 256 + threadIdx.x] = a.x ^ a.y ^ a.z ^ a.w;
  if (threadIdx.x == 0) { unsigned long long* o = tm + blockIdx.x * 4; o[0] = T0; o[1] = T1; o[2] = c1 - c0; o[3] = c2 - c0; }
#endif
}
__global__ __launch_bounds__(256) void probeB(unsigned long long* tm) {
#if __HIP_DEVICE_COMPILE__
  unsigned long long T0 = __builtin_amdgcn_s_memrealtime();
  if (threadIdx.x == 0) tm[2048 + blockIdx.x] = T0;
#endif
}
__global__ __launch_bounds__(64) void probeSpin(unsigned long long* tm) {
#if __HIP_DEVICE_COMPILE__
  unsigned long long lo = ~0ull, hiS = 0, hiE = 0, s1 = 0, s2 = 0, loB = ~0ull;
  for (int i = 0; i < 256; ++i) {
    unsigned long long a = tm[i * 4], e = tm[i * 4 + 1];
    lo = a < lo ? a : lo; hiS = a > hiS ? a : hiS; hiE = e > hiE ? e : hiE; s1 += tm[i * 4 + 2]; s2 += tm[i * 4 + 3];
    unsigned long long bb = tm[2048 + i]; loB = bb < loB ? bb : loB;
  }
  unsigned long long ticks;
  if (VAR == 0) ticks = 0;
  else if (VAR == 1) ticks = s2 / 256 / 24;
  else if (VAR == 2) ticks = s1 / 256 / 24;
  else if (VAR == 3) ticks = hiS - lo;
  else if (VAR == 4) ticks = loB - hiE;
  else ticks = hiE - lo;
  if (ticks > 2000) ticks = 2000;
  unsigned long long t0 = __builtin_amdgcn_s_memrealtime();
  while (__builtin_amdgcn_s_memrealtime() - t0 < ticks * 1000) __builtin_amdgcn_s_sleep(8);
#endif
}

extern "C" void kernel_launch(void* const* d_in, const int* in_sizes, int n_in, void* d_out, int out_size,
                              void* d_ws, size_t ws_size, hipStream_t stream) {
  const uint16_t* x = (const uint16_t*)d_in[0];
  char* ws = (char*)d_ws;
  uint16_t* xn = (uint16_t*)ws;
  float* K = (float*)(ws + (4 << 20));
  float* y = (float*)(ws + (12 << 20));
  uint16_t* h = (uint16_t*)(ws + (20 << 20));
  unsigned long long* tm = (unsigned long long*)(ws + (40 << 20));
  probeA<<<256, 256, 0, stream>>>((const char*)d_in[0], (const char*)d_in[10], (const char*)d_in[11], (const char*)d_in[12], tm, (unsigned*)(ws + (41 << 20)));
  probeB<<<256, 256, 0, stream>>>(tm);
  probeSpin<<<1, 64, 0, stream>>>(tm);
  k_ln<<<L, 64, 0, stream>>>(x, xn);
  k_kgen<<<dim3(L / 256, D), 256, 0, stream>>>((const uint16_t*)d_in[1], (const uint16_t*)d_in[2], (const uint16_t*)d_in[3],
                                               (const uint16_t*)d_in[4], (const uint16_t*)d_in[5], (const uint16_t*)d_in[6],
                                               (const uint16_t*)d_in[7], K);
  k_conv<<<dim3(D / 64, L / 4), 256, 0, stream>>>(xn, K, y);
  k_g1<<<dim3(F / 256, L), 256, 0, stream>>>(xn, (const uint16_t*)d_in[10], (const uint16_t*)d_in[11], h);
  k_g2<<<dim3(D / 128, L), 128, 0, stream>>>(x, xn, h, (const uint16_t*)d_in[12], y, (const uint16_t*)d_in[8], (uint16_t*)d_out);
}
```

```cpp
#include <hip/hip_runtime.h>
#include <hip/hip_bf16.h>
#include <stdint.h>

constexpr int L = 2048, D = 640, NS = 192, F = 1536;

__device__ __forceinline__ float bf2f(uint16_t v) { return __uint_as_float(((uint32_t)v) << 16); }
__device__ __forceinline__ uint16_t f2bf(float f) {
  uint32_t u = __float_as_uint(f);
  u += 0x7fffu + ((u >> 16) & 1u);
  return (uint16_t)(u >> 16);
}
__device__ __forceinline__ float rbf(float f) { return bf2f(f2bf(f)); }

__global__ void k_ln(const uint16_t* x, uint16_t* xn) {
  int row = blockIdx.x;
  int lane = threadIdx.x;
  float v[10];
  float s = 0;
  for (int i = 0; i < 10; ++i) { v[i] = bf2f(x[row * D + lane + 64 * i]); s += v[i]; }
  for (int o = 32; o; o >>= 1) s += __shfl_xor(s, o);
  float mean = s / D;
  float q = 0;
  for (int i = 0; i < 10; ++i) { float d = v[i] - mean; q += d * d; }
  for (int o = 32; o; o >>= 1) q += __shfl_xor(q, o);
  float var = rbf(q / D);
  float m = rbf(mean);
  float den = rbf(sqrtf(rbf(var + 1e-5f)));
  for (int i = 0; i < 10; ++i) xn[row * D + lane + 64 * i] = f2bf(rbf(v[i] - m) / den);
}

__global__ void k_kgen(const uint16_t* Are, const uint16_t* Aim, const uint16_t* Bre, const uint16_t* Bim,
                       const uint16_t* Cre, const uint16_t* Cim, const uint16_t* logdt, float* K) {
  int d = blockIdx.y;
  int l = blockIdx.x * 256 + threadIdx.x;
  float dt = rbf(expf(bf2f(logdt[d])));
  float lf = rbf((float)l);
  float acc = 0;
  for (int n = 0; n < NS; ++n) {
    int i = d * NS + n;
    float ar = dt * bf2f(Are[i]), ai = dt * bf2f(Aim[i]);
    float br = dt * bf2f(Bre[i]), bi = dt * bf2f(Bim[i]);
    float cr = bf2f(Cre[i]), ci = bf2f(Cim[i]);
    float wr = cr * br - ci * bi, wi = cr * bi + ci * br;
    float mag = expf(ar * lf);
    float s, c;
    sincosf(ai * lf, &s, &c);
    acc += mag * (wr * c - wi * s);
  }
  K[d * L + l] = acc;
}

__global__ void k_conv(const uint16_t* xn, const float* K, float* y) {
  int d = blockIdx.x * 64 + threadIdx.x % 64;
  int t = blockIdx.y * 4 + threadIdx.x / 64;
  float acc = 0;
  for (int s = 0; s <= t; ++s) acc += K[d * L + s] * bf2f(xn[(t - s) * D + d]);
  y[t * D + d] = acc;
}

__global__ void k_g1(const uint16_t* xn, const uint16_t* wg, const uint16_t* wu, uint16_t* h) {
  int f = blockIdx.x * 256 + threadIdx.x;
  int t = blockIdx.y;
  float g = 0, u = 0;
  for (int k = 0; k < D; ++k) {
    float a = bf2f(xn[t * D + k]);
    g += a * bf2f(wg[k * F + f]);
    u += a * bf2f(wu[k * F + f]);
  }
  g = rbf(g); u = rbf(u);
  float sg = rbf(g / (1.f + expf(-g)));
  h[t * F + f] = f2bf(sg * u);
}

__global__ void k_g2(const uint16_t* x, const uint16_t* xn, const uint16_t* h, const uint16_t* wd, const float* y,
                     const uint16_t* dskip, uint16_t* out) {
  int d = blockIdx.x * 128 + threadIdx.x;
  int t = blockIdx.y;
  float acc = 0;
  for (int k = 0; k < F; ++k) acc += bf2f(h[t * F + k]) * bf2f(wd[k * D + d]);
  float b = rbf(acc);
  float a = rbf(rbf(y[t * D + d]) + rbf(bf2f(xn[t * D + d]) * bf2f(dskip[d])));
  out[t * D + d] = f2bf(rbf(bf2f(x[t * D + d]) + a) + b);
}

#define VAR 7

typedef unsigned v4u __attribute__((ext_vector_type(4)));
__global__ __launch_bounds__(256) void probeA(const char* x, const char* wg, const char* wu, const char* wd, unsigned long long* tm, unsigned* sink) {
#if __HIP_DEVICE_COMPILE__
  unsigned long long T0 = __builtin_amdgcn_s_memrealtime();
  unsigned long long c0 = __builtin_amdgcn_s_memtime();
  int b = blockIdx.x;
  const char* base = b < 80 ? x + b * 32768 : b < 140 ? wg + (b - 80) * 32768 : b < 200 ? wu + (b - 140) * 32768 : wd + (b - 200) * 32768;
  const char* p = base + (threadIdx.x >> 6) * 8192 + (threadIdx.x & 63) * 16;
  v4u r[8];
#pragma unroll
  for (int i = 0; i < 8; ++i) r[i] = *(const v4u*)(p + i * 1024);
  asm volatile("s_waitcnt vmcnt(7)" ::: "memory");
  unsigned long long c1 = __builtin_amdgcn_s_memtime();
  asm volatile("s_waitcnt vmcnt(0)" ::: "memory");
  v4u a = r[0];
#pragma unroll
  for (int i = 1; i < 8; ++i) a ^= r[i];
  __syncthreads();
  unsigned long long c2 = __builtin_amdgcn_s_memtime();
  unsigned long long T1 = __builtin_amdgcn_s_memrealtime();
  sink[blockIdx.x * 256 + threadIdx.x] = a.x ^ a.y ^ a.z ^ a.w;
  if (threadIdx.x == 0) { unsigned long long* o = tm + blockIdx.x * 4; o[0] = T0; o[1] = T1; o[2] = c1 - c0; o[3] = c2 - c0; }
#endif
}
__global__ __launch_bounds__(256) void probeB(unsigned long long* tm) {
#if __HIP_DEVICE_COMPILE__
  unsigned long long T0 = __builtin_amdgcn_s_memrealtime();
  if (threadIdx.x == 0) tm[2048 + blockIdx.x] = T0;
#endif
}
__global__ __launch_bounds__(64) void probeSpin(unsigned long long* tm) {
#if __HIP_DEVICE_COMPILE__
  unsigned long long lo = ~0ull, hiS = 0, hiE = 0, s1 = 0, s2 = 0, loB = ~0ull;
  for (int i = 0; i < 256; ++i) {
    unsigned long long a = tm[i * 4], e = tm[i * 4 + 1];
    lo = a < lo ? a : lo; hiS = a > hiS ? a : hiS; hiE = e > hiE ? e : hiE; s1 += tm[i * 4 + 2]; s2 += tm[i * 4 + 3];
    unsigned long long bb = tm[2048 + i]; loB = bb < loB ? bb : loB;
  }
  unsigned long long ticks;
  if (VAR == 0) ticks = 0;
  else if (VAR == 1) ticks = s2 / 256 / 24;
  else if (VAR == 2) ticks = s1 / 256 / 24;
  else if (VAR == 3) ticks = hiS - lo;
  else if (VAR == 4) ticks = loB - hiE;
  else ticks = hiE - lo;
  if (ticks > 2000) ticks = 2000;
  unsigned long long t0 = __builtin_amdgcn_s_memrealtime();
  while (__builtin_amdgcn_s_memrealtime() - t0 < ticks * 1000) __builtin_amdgcn_s_sleep(8);
#endif
}

__global__ __launch_bounds__(256) void probeC(unsigned long long* tm, int off) {
#if __HIP_DEVICE_COMPILE__
  unsigned long long T0 = __builtin_amdgcn_s_memrealtime();
  if (threadIdx.x == 0) tm[off + blockIdx.x] = T0;
#endif
}
__global__ __launch_bounds__(64) void probeSpin2(unsigned long long* tm) {
#if __HIP_DEVICE_COMPILE__
  unsigned long long hiB = 0, loC = ~0ull, hiC = 0, loD = ~0ull, loA = ~0ull, hiZ = 0;
  for (int i = 0; i < 256; ++i) {
    unsigned long long b = tm[2048 + i], c = tm[3072 + i], d = tm[4096 + i], a = tm[i * 4], z = tm[5120 + i];
    hiB = b > hiB ? b : hiB; loC = c < loC ? c : loC; hiC = c > hiC ? c : hiC; loD = d < loD ? d : loD; loA = a < loA ? a : loA; hiZ = z > hiZ ? z : hiZ;
  }
  unsigned long long ticks = VAR == 6 ? loC - hiB : VAR == 7 ? loD - hiC : loA - hiZ;
  if (ticks > 1000) ticks = 1000;
  unsigned long long t0 = __builtin_amdgcn_s_memrealtime();
  while (__builtin_amdgcn_s_memrealtime() - t0 < ticks * 1000) __builtin_amdgcn_s_sleep(8);
#endif
}

extern "C" void kernel_launch(void* const* d_in, const int* in_sizes, int n_in, void* d_out, int out_size,
                              void* d_ws, size_t ws_size, hipStream_t stream) {
  const uint16_t* x = (const uint16_t*)d_in[0];
  char* ws = (char*)d_ws;
  uint16_t* xn = (uint16_t*)ws;
  float* K = (float*)(ws + (4 << 20));
  float* y = (float*)(ws + (12 << 20));
  uint16_t* h = (uint16_t*)(ws + (20 << 20));
  unsigned long long* tm = (unsigned long long*)(ws + (40 << 20));
  probeA<<<256, 256, 0, stream>>>((const char*)d_in[0], (const char*)d_in[10], (const char*)d_in[11], (const char*)d_in[12], tm, (unsigned*)(ws + (41 << 20)));
  probeB<<<256, 256, 0, stream>>>(tm);
  probeC<<<256, 256, 0, stream>>>(tm, 3072);
  probeC<<<256, 256, 0, stream>>>(tm, 4096);
  probeSpin2<<<1, 64, 0, stream>>>(tm);
  k_ln<<<L, 64, 0, stream>>>(x, xn);
  k_kgen<<<dim3(L / 256, D), 256, 0, stream>>>((const uint16_t*)d_in[1], (const uint16_t*)d_in[2], (const uint16_t*)d_in[3],
                                               (const uint16_t*)d_in[4], (const uint16_t*)d_in[5], (const uint16_t*)d_in[6],
                                               (const uint16_t*)d_in[7], K);
  k_conv<<<dim3(D / 64, L / 4), 256, 0, stream>>>(xn, K, y);
  k_g1<<<dim3(F / 256, L), 256, 0, stream>>>(xn, (const uint16_t*)d_in[10], (const uint16_t*)d_in[11], h);
  k_g2<<<dim3(D / 128, L), 128, 0, stream>>>(x, xn, h, (const uint16_t*)d_in[12], y, (const uint16_t*)d_in[8], (uint16_t*)d_out);
  probeC<<<256, 256, 0, stream>>>(tm, 5120);
}
```

```cpp
#include <hip/hip_runtime.h>
#include <hip/hip_bf16.h>
#include <stdint.h>

constexpr int L = 2048, D = 640, NS = 192, F = 1536;

__device__ __forceinline__ float bf2f(uint16_t v) { return __uint_as_float(((uint32_t)v) << 16); }
__device__ __forceinline__ uint16_t f2bf(float f) {
  uint32_t u = __float_as_uint(f);
  u += 0x7fffu + ((u >> 16) & 1u);
  return (uint16_t)(u >> 16);
}
__device__ __forceinline__ float rbf(float f) { return bf2f(f2bf(f)); }

__global__ void k_ln(const uint16_t* x, uint16_t* xn) {
  int row = blockIdx.x;
  int lane = threadIdx.x;
  float v[10];
  float s = 0;
  for (int i = 0; i < 10; ++i) { v[i] = bf2f(x[row * D + lane + 64 * i]); s += v[i]; }
  for (int o = 32; o; o >>= 1) s += __shfl_xor(s, o);
  float mean = s / D;
  float q = 0;
  for (int i = 0; i < 10; ++i) { float d = v[i] - mean; q += d * d; }
  for (int o = 32; o; o >>= 1) q += __shfl_xor(q, o);
  float var = rbf(q / D);
  float m = rbf(mean);
  float den = rbf(sqrtf(rbf(var + 1e-5f)));
  for (int i = 0; i < 10; ++i) xn[row * D + lane + 64 * i] = f2bf(rbf(v[i] - m) / den);
}

__global__ void k_kgen(const uint16_t* Are, const uint16_t* Aim, const uint16_t* Bre, const uint16_t* Bim,
                       const uint16_t* Cre, const uint16_t* Cim, const uint16_t* logdt, float* K) {
  int d = blockIdx.y;
  int l = blockIdx.x * 256 + threadIdx.x;
  float dt = rbf(expf(bf2f(logdt[d])));
  float lf = rbf((float)l);
  float acc = 0;
  for (int n = 0; n < NS; ++n) {
    int i = d * NS + n;
    float ar = dt * bf2f(Are[i]), ai = dt * bf2f(Aim[i]);
    float br = dt * bf2f(Bre[i]), bi = dt * bf2f(Bim[i]);
    float cr = bf2f(Cre[i]), ci = bf2f(Cim[i]);
    float wr = cr * br - ci * bi, wi = cr * bi + ci * br;
    float mag = expf(ar * lf);
    float s, c;
    sincosf(ai * lf, &s, &c);
    acc += mag * (wr * c - wi * s);
  }
  K[d * L + l] = acc;
}

__global__ void k_conv(const uint16_t* xn, const float* K, float* y) {
  int d = blockIdx.x * 64 + threadIdx.x % 64;
  int t = blockIdx.y * 4 + threadIdx.x / 64;
  float acc = 0;
  for (int s = 0; s <= t; ++s) acc += K[d * L + s] * bf2f(xn[(t - s) * D + d]);
  y[t * D + d] = acc;
}

__global__ void k_g1(const uint16_t* xn, const uint16_t* wg, const uint16_t* wu, uint16_t* h) {
  int f = blockIdx.x * 256 + threadIdx.x;
  int t = blockIdx.y;
  float g = 0, u = 0;
  for (int k = 0; k < D; ++k) {
    float a = bf2f(xn[t * D + k]);
    g += a * bf2f(wg[k * F + f]);
    u += a * bf2f(wu[k * F + f]);
  }
  g = rbf(g); u = rbf(u);
  float sg = rbf(g / (1.f + expf(-g)));
  h[t * F + f] = f2bf(sg * u);
}

__global__ void k_g2(const uint16_t* x, const uint16_t* xn, const uint16_t* h, const uint16_t* wd, const float* y,
                     const uint16_t* dskip, uint16_t* out) {
  int d = blockIdx.x * 128 + threadIdx.x;
  int t = blockIdx.y;
  float acc = 0;
  for (int k = 0; k < F; ++k) acc += bf2f(h[t * F + k]) * bf2f(wd[k * D + d]);
  float b = rbf(acc);
  float a = rbf(rbf(y[t * D + d]) + rbf(bf2f(xn[t * D + d]) * bf2f(dskip[d])));
  out[t * D + d] = f2bf(rbf(bf2f(x[t * D + d]) + a) + b);
}

#define VAR 0

typedef unsigned v4u __attribute__((ext_vector_type(4)));
__global__ __launch_bounds__(64) void probeA(const char* x, unsigned long long* tm, unsigned* sink) {
#if __HIP_DEVICE_COMPILE__
  unsigned long long c0, c1, c2, c3, c4, c5;
  asm volatile("s_memtime %0\n\ts_waitcnt lgkmcnt(0)" : "=s"(c0) :: "memory");
  __builtin_amdgcn_sched_barrier(0);
  const char* xx = x; unsigned long long* tt = tm;
  asm volatile("" : "+s"(xx), "+s"(tt));
  __builtin_amdgcn_sched_barrier(0);
  asm volatile("s_memtime %0\n\ts_waitcnt lgkmcnt(0)" : "=s"(c1) :: "memory");
  v4u r = *(const v4u*)(xx + blockIdx.x * 8192 + threadIdx.x * 16);
  asm volatile("s_waitcnt vmcnt(0)" : "+v"(r) :: "memory");
  asm volatile("s_memtime %0\n\ts_waitcnt lgkmcnt(0)" : "=s"(c2) :: "memory");
  v4u r2 = *(const v4u*)(xx + blockIdx.x * 8192 + 4096 + threadIdx.x * 16 + (r.x & 0));
  asm volatile("s_waitcnt vmcnt(0)" : "+v"(r2) :: "memory");
  asm volatile("s_memtime %0\n\ts_waitcnt lgkmcnt(0)" : "=s"(c3) :: "memory");
  unsigned long long d0 = 0, d1 = 0;
  for (int it = 0; it < 2; ++it) {
    asm volatile("s_memtime %0\n\ts_waitcnt lgkmcnt(0)" : "=s"(c4) :: "memory");
    asm volatile(".rept 2048\n\tv_add3_u32 %0, %0, %0, %0\n\t.endr" : "+v"(r.y));
    asm volatile("s_memtime %0\n\ts_waitcnt lgkmcnt(0)" : "=s"(c5) :: "memory");
    if (it == 0) d0 = c5 - c4; else d1 = c5 - c4;
  }
  sink[blockIdx.x * 64 + threadIdx.x] = r.x ^ r.y ^ r2.z ^ r.w;
  if (threadIdx.x == 0) { unsigned long long* o = tt + blockIdx.x * 8; o[0] = c1 - c0; o[1] = c2 - c1; o[2] = c3 - c2; o[3] = d0; o[4] = d1; }
#endif
}
__global__ __launch_bounds__(64) void probeSpin(unsigned long long* tm) {
#if __HIP_DEVICE_COMPILE__
  unsigned long long s = 0;
  for (int i = 0; i < 256; ++i) s += tm[i * 8 + VAR];
  unsigned long long ticks = s / 256 / 24;
  if (ticks > 2000) ticks = 2000;
  unsigned long long t0 = __builtin_amdgcn_s_memrealtime();
  while (__builtin_amdgcn_s_memrealtime() - t0 < ticks * 1000) __builtin_amdgcn_s_sleep(8);
#endif
}

extern "C" void kernel_launch(void* const* d_in, const int* in_sizes, int n_in, void* d_out, int out_size,
                              void* d_ws, size_t ws_size, hipStream_t stream) {
  const uint16_t* x = (const uint16_t*)d_in[0];
  char* ws = (char*)d_ws;
  uint16_t* xn = (uint16_t*)ws;
  float* K = (float*)(ws + (4 << 20));
  float* y = (float*)(ws + (12 << 20));
  uint16_t* h = (uint16_t*)(ws + (20 << 20));
  unsigned long long* tm = (unsigned long long*)(ws + (40 << 20));
  probeA<<<256, 64, 0, stream>>>((const char*)d_in[0], tm, (unsigned*)(ws + (41 << 20)));
  probeSpin<<<1, 64, 0, stream>>>(tm);
  k_ln<<<L, 64, 0, stream>>>(x, xn);
  k_kgen<<<dim3(L / 256, D), 256, 0, stream>>>((const uint16_t*)d_in[1], (const uint16_t*)d_in[2], (const uint16_t*)d_in[3],
                                               (const uint16_t*)d_in[4], (const uint16_t*)d_in[5], (const uint16_t*)d_in[6],
                                               (const uint16_t*)d_in[7], K);
  k_conv<<<dim3(D / 64, L / 4), 256, 0, stream>>>(xn, K, y);
  k_g1<<<dim3(F / 256, L), 256, 0, stream>>>(xn, (const uint16_t*)d_in[10], (const uint16_t*)d_in[11], h);
  k_g2<<<dim3(D / 128, L), 128, 0, stream>>>(x, xn, h, (const uint16_t*)d_in[12], y, (const uint16_t*)d_in[8], (uint16_t*)d_out);
}
```

```cpp
#include <hip/hip_runtime.h>
#include <stdint.h>

typedef short bf16x8 __attribute__((ext_vector_type(8)));
typedef short s16x4 __attribute__((ext_vector_type(4)));
typedef float f32x4 __attribute__((ext_vector_type(4)));
typedef float f32x2 __attribute__((ext_vector_type(2)));
typedef __bf16 bf16x2 __attribute__((ext_vector_type(2)));
typedef unsigned v4u __attribute__((ext_vector_type(4)));
typedef unsigned v2u __attribute__((ext_vector_type(2)));
typedef unsigned v8u __attribute__((ext_vector_type(8)));
typedef unsigned long long u64;
#define LAS __attribute__((address_space(3)))
#define DI __device__ __forceinline__
typedef __amdgpu_buffer_rsrc_t rsrc_t;

constexpr int L = 2048, D = 640, NS = 192, F = 1536;
constexpr int SC1 = 16, NT_ = 2;

constexpr unsigned WS_EPOCH = 0, WS_F0 = 1024, WS_F1 = 4096, WS_F2 = 8192, WS_STATS = 16384, WS_CSP = 65536, WS_U8 = 262144,
                   WS_WDT = 3u << 20, WS_H = 5u << 20, WS_AT = 12u << 20, WS_TM = 15u << 20, WS_BYTES = 16u << 20;

constexpr unsigned STAGE = 40960, XB = 24576;
constexpr unsigned LX = 3 * STAGE;
constexpr unsigned L_RAW = LX;
constexpr unsigned L_ST = L_RAW + 3 * 2048;
constexpr unsigned L_CS = L_ST + 3072;
constexpr unsigned L_MISC = L_CS + 1024;
constexpr unsigned L_U = L_MISC + 128, U_SZ = 4608;
constexpr unsigned L_LN = L_U + 3 * U_SZ, LN_RS = 1296;
constexpr unsigned L_END = L_LN + 10240;
constexpr unsigned L_WDU = L_U, L_DUMMY = L_U + 8192, L_CSP = L_U + 9216;
constexpr unsigned L_KG = 2 * STAGE, KG_SZ = 16 * (544 + 288);
constexpr unsigned L_H = STAGE, H_CS = 2112, H_SZ = 8 * H_CS;
constexpr unsigned L_K3 = L_END, L_AT = L_LN;
constexpr unsigned LDS_BYTES = 163840;
static_assert(L_END + 960 <= LDS_BYTES, "lds");
constexpr unsigned L_P2 = L_H + 3 * H_SZ + 64;
static_assert(L_P2 + 2 * 15360 <= LX, "lds partials");
#define G2AUXW SC1
#define G2AUXH SC1
#define ROT2 1
#define G2SIG 6
#define XROT 0
constexpr int SPIN_MAX = 20000;
constexpr unsigned CODE_BYTES = 47104;

#if __HIP_DEVICE_COMPILE__
extern __shared__ __attribute__((aligned(16))) char smem_[];
DI LAS char* lds(unsigned o) { return (LAS char*)smem_ + o; }
DI rsrc_t mkr(const void* p, unsigned bytes) { return __builtin_amdgcn_make_buffer_rsrc((void*)p, 0, bytes, 0x00020000); }
DI unsigned pk(float a, float b) { f32x2 f = {a, b}; return __builtin_bit_cast(unsigned, __builtin_convertvector(f, bf16x2)); }
DI unsigned pk2(f32x2 f) { return __builtin_bit_cast(unsigned, __builtin_convertvector(f, bf16x2)); }
DI float blo(unsigned v) { return __uint_as_float(v << 16); }
DI float bhi(unsigned v) { return __uint_as_float(v & 0xffff0000u); }
DI float rbf(float f) { return blo(pk(f, f)); }
DI void bar() { __builtin_amdgcn_s_barrier(); }
DI void cfence() { asm volatile("" ::: "memory"); }
template <int N> DI void wait_vm() { asm volatile("s_waitcnt vmcnt(%0)" ::"i"(N) : "memory"); }
DI void wait_lg() { asm volatile("s_waitcnt lgkmcnt(0)" ::: "memory"); }
DI bf16x8 trd(unsigned a0, unsigned a1) {
  s16x4 lo = __builtin_amdgcn_ds_read_tr16_b64_v4i16((LAS s16x4*)lds(a0));
  s16x4 hi = __builtin_amdgcn_ds_read_tr16_b64_v4i16((LAS s16x4*)lds(a1));
  return __builtin_shufflevector(lo, hi, 0, 1, 2, 3, 4, 5, 6, 7);
}
DI bf16x8 rd128(unsigned a) { return *(LAS bf16x8*)lds(a); }
DI f32x4 mfma(bf16x8 a, bf16x8 b, f32x4 c) { return __builtin_amdgcn_mfma_f32_16x16x32_bf16(a, b, c, 0, 0, 0); }
DI v4u srd(const void* p, unsigned bytes) { const u64 a = (u64)p; return v4u{(unsigned)a, (unsigned)(a >> 32) & 0xffffu, bytes, 0x00020000u}; }
template <int AUX> DI void dma(v4u r, unsigned ldsoff, unsigned voff, unsigned soff) {
  if (AUX == 0) asm volatile("s_mov_b32 m0, %0\n\ts_nop 0\n\tbuffer_load_dwordx4 %1, %2, %3 offen lds" ::"s"(ldsoff), "v"(voff), "s"(r), "s"(soff) : "memory");
  if (AUX == NT_) asm volatile("s_mov_b32 m0, %0\n\ts_nop 0\n\tbuffer_load_dwordx4 %1, %2, %3 offen nt lds" ::"s"(ldsoff), "v"(voff), "s"(r), "s"(soff) : "memory");
  if (AUX == SC1) asm volatile("s_mov_b32 m0, %0\n\ts_nop 0\n\tbuffer_load_dwordx4 %1, %2, %3 offen sc1 lds" ::"s"(ldsoff), "v"(voff), "s"(r), "s"(soff) : "memory");
}
template <int AUX> DI void st128(v4u v, rsrc_t r, unsigned off) { __builtin_amdgcn_raw_buffer_store_b128(v, r, off, 0, AUX); }
DI void touch(v4u& sink, v4u r, unsigned vo, unsigned so) {
  asm volatile("buffer_load_dwordx4 %0, %1, %2, %3 offen sc1" : "+v"(sink) : "v"(vo), "s"(r), "s"(so) : "memory");
}
DI float rowsum16(float v) {
  v += __builtin_bit_cast(float, __builtin_amdgcn_update_dpp(0, __builtin_bit_cast(int, v), 0x128, 0xf, 0xf, false));
  v += __builtin_bit_cast(float, __builtin_amdgcn_update_dpp(0, __builtin_bit_cast(int, v), 0x124, 0xf, 0xf, false));
  v += __builtin_bit_cast(float, __builtin_amdgcn_update_dpp(0, __builtin_bit_cast(int, v), 0x122, 0xf, 0xf, false));
  v += __builtin_bit_cast(float, __builtin_amdgcn_update_dpp(0, __builtin_bit_cast(int, v), 0x121, 0xf, 0xf, false));
  return v;
}
DI f32x2 cmul(f32x2 a, f32x2 b) {
  f32x2 t = a * b.xx, r;
  asm("v_pk_fma_f32 %0, %1, %2, %3 op_sel:[1,1,0] op_sel_hi:[0,1,1] neg_lo:[0,1,0]" : "=v"(r) : "v"(a), "v"(b), "v"(t));
  return r;
}
DI f32x2 cpow(float lr, float tr, float e) {
  float m = __builtin_amdgcn_exp2f(lr * e), th = __builtin_amdgcn_fractf(tr * e);
  return f32x2{m * __builtin_amdgcn_cosf(th), m * __builtin_amdgcn_sinf(th)};
}
DI v2u tag_of(void) { return *(LAS v2u*)lds(L_MISC); }
DI void put_flag(rsrc_t rw, unsigned off, v2u tag) { __builtin_amdgcn_raw_buffer_store_b64(tag, rw, off, 0, SC1); }
template <int R> DI void poll(rsrc_t rw, unsigned off, int n, v2u tag, int lane) {
#pragma unroll 1
  for (int spin = 0; spin < SPIN_MAX; ++spin) {
    bool ok = true;
#pragma unroll
    for (int r = 0; r < R; ++r) {
      int i = lane + 64 * r; i = i < n ? i : n - 1;
      v2u f = __builtin_amdgcn_raw_buffer_load_b64(rw, off + i * 8, 0, SC1);
      ok = ok && f.x == tag.x && f.y == tag.y;
    }
    if (__all(ok)) break;
    cfence();
  }
  cfence();
}
DI void lds_set(unsigned o) { cfence(); *(volatile LAS unsigned*)lds(o) = 1; }
DI void lds_wait(unsigned o) {
#pragma unroll 1
  for (int spin = 0; spin < 16 * SPIN_MAX && *(volatile LAS unsigned*)lds(o) == 0; ++spin) __builtin_amdgcn_s_sleep(1);
  cfence();
}
DI void arrive_publish(unsigned cnt, int n, rsrc_t rw, unsigned off, int lane) {
  unsigned old = 0;
  if (lane == 0) old = __hip_atomic_fetch_add((LAS unsigned*)lds(cnt), 1u, __ATOMIC_RELAXED, __HIP_MEMORY_SCOPE_WORKGROUP);
  old = __builtin_amdgcn_readfirstlane(old);
  if ((int)old == n - 1 && lane == 0) put_flag(rw, off, tag_of());
}

#define STAMP(i)
#define BAR(i) bar()
#define DUMP()

struct G1Ld { unsigned vx0, vx1, vw0, vw1; };
DI G1Ld g1_lanes(int lane) {
  const int r8 = lane >> 3, sl = lane & 7;
  return G1Ld{(unsigned)(r8 * 1280 + ((sl ^ (r8 >> 1)) << 4)), (unsigned)(r8 * 1280 + ((sl ^ (r8 >> 1) ^ 4) << 4)),
              (unsigned)(r8 * 3072 + ((sl ^ (r8 & 2)) << 4)), (unsigned)(r8 * 3072 + ((sl ^ (r8 & 2) ^ 4) << 4))};
}
template <int NT> DI void g1_issue(v4u rx, v4u rg, v4u ru, const G1Ld& v, int lw, int s, unsigned lb, int row0, int ch) {
  const unsigned xso = (row0 + lw * 16 * NT) * 1280 + s * 128, wso = ch * 128 + (lw * 32 + s * 64) * 3072;
#pragma unroll
  for (int p = 0; p < 4; ++p) {
    dma<0>(rg, lb + XB + (lw * 4 + p) * 1024, (p & 1) ? v.vw1 : v.vw0, wso + p * 8 * 3072);
    dma<0>(ru, lb + XB + 8192 + (lw * 4 + p) * 1024, (p & 1) ? v.vw1 : v.vw0, wso + p * 8 * 3072);
  }
#pragma unroll
  for (int p = 0; p < 2 * NT; ++p) dma<0>(rx, lb + (lw * 2 * NT + p) * 1024, (p & 1) ? v.vx1 : v.vx0, xso + p * 8 * 1280);
}
DI unsigned nxt(unsigned so) { return so == 2 * STAGE ? 0 : so + STAGE; }
template <int NT, int S0> DI void g1_loader(v4u rx, v4u rg, v4u ru, int lw, int lane, int row0, int ch, int rot, unsigned so) {
  constexpr int NP = 8 + 2 * NT;
  const G1Ld v = g1_lanes(lane);
  if (S0 == 1) BAR(0);
#pragma unroll 1
  for (int s = S0; s < 10; ++s) {
    if (s >= S0 + 2) wait_vm<NP>();
    if (s >= S0 + 2) BAR(s - 2);
    g1_issue<NT>(rx, rg, ru, v, lw, s + rot >= 10 ? s + rot - 10 : s + rot, so, row0, ch);
    so = nxt(so);
  }
  wait_vm<NP>(); BAR(8);
}

DI void colsum(int k, int lane) {
  const int c = lane & 15, g = lane >> 4, q = (lane >> 2) & 3, p = lane & 3;
  const bf16x8 ones = {0x3f80, 0x3f80, 0x3f80, 0x3f80, 0x3f80, 0x3f80, 0x3f80, 0x3f80};
  f32x4 r[2];
#pragma unroll
  for (int a = 0; a < 2; ++a) {
    const unsigned ad = XB + (k >> 1) * 8192 + (8 * g + q) * 128 + (((4 * (k & 1) + 2 * a + (p >> 1)) ^ (q & 2) ^ ((g & 1) << 2)) << 4) + 8 * (p & 1);
    r[a] = mfma(trd(ad + 4096, ad + 4608), ones, mfma(trd(ad, ad + 512), ones, f32x4{0.f, 0.f, 0.f, 0.f}));
  }
  f32x4 v;
#pragma unroll
  for (int e = 0; e < 4; ++e) v[e] = c ? r[1][e] : r[0][e];
  if (c < 2) *(LAS f32x4*)lds(L_CSP + k * 128 + (16 * c + 4 * g) * 4) = v;
  wait_lg();
}

template <int NT> DI void g1_main(int wid, int lane, unsigned so, f32x4 (&acc)[4][NT]) {
  const int wa = wid & 1, wt = wid >> 1;
  const int c = lane & 15, g = lane >> 4, q = (lane >> 2) & 3, p = lane & 3;
  unsigned aA[2], aB[2];
#pragma unroll
  for (int a = 0; a < 2; ++a) aA[a] = XB + (8 * g + q) * 128 + (((4 * wa + 2 * a + (p >> 1)) ^ (q & 2) ^ ((g & 1) << 2)) << 4) + 8 * (p & 1);
#pragma unroll
  for (int ks = 0; ks < 2; ++ks) aB[ks] = (16 * NT * wt + c) * 128 + (((4 * ks + g) ^ (c >> 1)) << 4);
#pragma unroll
  for (int a = 0; a < 4; ++a)
#pragma unroll
    for (int t = 0; t < NT; ++t) acc[a][t] = f32x4{0.f, 0.f, 0.f, 0.f};
  bf16x8 fa[2][4], fb[2][NT];
  auto rd = [&](int buf, int ks, unsigned o) {
#pragma unroll
    for (int a = 0; a < 4; ++a) { unsigned ad = aA[a & 1] + o + (a >> 1) * 8192 + ks * 4096; fa[buf][a] = trd(ad, ad + 512); }
#pragma unroll
    for (int t = 0; t < NT; ++t) fb[buf][t] = rd128(aB[ks] + o + t * 2048);
  };
  auto mm = [&](int buf) {
#pragma unroll
    for (int t = 0; t < NT; ++t)
#pragma unroll
      for (int a = 0; a < 4; ++a) acc[a][t] = mfma(fa[buf][a], fb[buf][t], acc[a][t]);
  };
  auto ilv = [&]() {
    constexpr int NR = 8 + NT, NM = 4 * NT, NI = NR < NM ? NR : NM;
#pragma unroll
    for (int i = 0; i < NI; ++i) { __builtin_amdgcn_sched_group_barrier(0x008, 1, 0); __builtin_amdgcn_sched_group_barrier(0x100, 1, 0); }
    if (NM > NI) __builtin_amdgcn_sched_group_barrier(0x008, NM - NI, 0);
    __builtin_amdgcn_sched_barrier(0);
  };
  BAR(0);
  STAMP(2);
  rd(0, 0, so);
#pragma unroll 1
  for (int s = 0; s < 9; ++s) {
    rd(1, 1, so); mm(0); ilv();
    so = nxt(so);
    wait_lg();
    BAR(s + 1);
    __builtin_amdgcn_sched_barrier(0);
    rd(0, 0, so); mm(1); ilv();
  }
  rd(1, 1, so); mm(0); ilv();
  mm(1);
  __builtin_amdgcn_sched_barrier(0);
}
template <int NT> DI void g1_epilogue(rsrc_t rw, int wid, int lane, int t16_0, int ch, unsigned st_off, unsigned cs_off, const f32x4 (&acc)[4][NT]) {
  const int wa = wid & 1, wt = wid >> 1, c = lane & 15, g = lane >> 4;
  f32x4 cs[4];
#pragma unroll
  for (int a = 0; a < 4; ++a) cs[a] = *(LAS f32x4*)lds(cs_off + (a >> 1) * 256 + (32 * wa + 16 * (a & 1) + 4 * g) * 4);
  f32x4 st[NT];
#pragma unroll
  for (int t = 0; t < NT; ++t) st[t] = *(LAS f32x4*)lds(st_off + (16 * NT * wt + 16 * t + c) * 16);
#pragma unroll
  for (int t = 0; t < NT; ++t) {
    const float nm = st[t][0], qq = st[t][1], rl = st[t][2];
    v4u o;
#pragma unroll
    for (int a = 0; a < 2; ++a) {
      f32x4 tg = cs[a] * nm + acc[a][t], tu = cs[2 + a] * nm + acc[2 + a][t];
      f32x4 ar = tg * rl, e;
#pragma unroll
      for (int r = 0; r < 4; ++r) e[r] = __builtin_amdgcn_exp2f(ar[r]);
      f32x4 dn = e * qq + qq, pr = tg * tu;
#pragma unroll
      for (int r = 0; r < 4; ++r) dn[r] = __builtin_amdgcn_rcpf(dn[r]);
      pr *= dn;
      o[2 * a] = pk(pr[0], pr[1]); o[2 * a + 1] = pk(pr[2], pr[3]);
    }
    st128<SC1>(o, rw, lane * 16 + WS_H + ((t16_0 + NT * wt + t) * 48 + 2 * ch + wa) * 1024);
  }
}

template <int NM> DI void kgen(const unsigned (&w)[6], float dt, int lane, unsigned tb, unsigned done, f32x4 (&acc)[2][2]) {
  constexpr int RS = NM * 256 + 32, KS = NM * 4;
  float lr[NM], tr[NM]; f32x2 W[NM];
#pragma unroll
  for (int m = 0; m < NM; ++m) {
    float v[6];
#pragma unroll
    for (int i = 0; i < 6; ++i) v[i] = m == 0 ? blo(w[i]) : bhi(w[i]);
    lr[m] = dt * v[0] * 1.44269504f; tr[m] = dt * v[1] * 0.15915494f;
    const float br = dt * v[2], bi = dt * v[3];
    W[m] = f32x2{v[4] * br - v[5] * bi, -(v[4] * bi + v[5] * br)};
  }
  asm volatile("" : "+v"(lr[0]), "+v"(W[0]));
  STAMP(13);
  unsigned wa = tb + lane * 4 * NM, fa = tb + (lane & 15) * RS + (lane >> 4) * 16;
  asm volatile("" : "+v"(wa), "+v"(fa));
  auto put = [&](int row, const f32x2* z) {
    if (NM == 2) *(LAS v2u*)lds(wa + row * RS) = v2u{pk2(z[0]), pk2(z[NM - 1])};
    else *(LAS unsigned*)lds(wa + row * RS) = pk2(z[0]);
  };
  f32x2 c[8][NM], hi[NM], t[NM], c8[NM];
#pragma unroll
  for (int m = 0; m < NM; ++m) {
    c[0][m] = f32x2{1.f, 0.f}; c[1][m] = cpow(lr[m], tr[m], 8.f);
    c[2][m] = cmul(c[1][m], c[1][m]); c[3][m] = cmul(c[2][m], c[1][m]); c[4][m] = cmul(c[2][m], c[2][m]);
    c[5][m] = cmul(c[4][m], c[1][m]); c[6][m] = cmul(c[4][m], c[2][m]); c[7][m] = cmul(c[4][m], c[3][m]);
    c8[m] = cmul(c[4][m], c[4][m]); hi[m] = c8[m];
  }
  bf16x8 fb[2][KS];
#pragma unroll
  for (int j = 0; j < 8; ++j) put(j, c[j]);
#pragma unroll
  for (int a = 1; a < 4; ++a) {
    if (a == 2) {
#pragma unroll
      for (int s = 0; s < KS; ++s) fb[0][s] = rd128(fa + s * 64);
    }
    put((8 * a) & 15, hi);
#pragma unroll
    for (int j = 1; j < 8; ++j) {
#pragma unroll
      for (int m = 0; m < NM; ++m) t[m] = cmul(hi[m], c[j][m]);
      put((8 * a + j) & 15, t);
    }
    if (a < 3) {
#pragma unroll
      for (int m = 0; m < NM; ++m) hi[m] = cmul(hi[m], c8[m]);
    }
  }
#pragma unroll
  for (int s = 0; s < KS; ++s) fb[1][s] = rd128(fa + s * 64);
  STAMP(14);
  f32x2 z1[NM], zb[NM], r0[NM], r4[NM];
#pragma unroll
  for (int m = 0; m < NM; ++m) { z1[m] = cpow(lr[m], -tr[m], 1.f); zb[m] = cpow(lr[m], -tr[m], 256.f); t[m] = W[m]; }
#pragma unroll
  for (int b = 0; b < 8; ++b) {
    put(b, t);
    if ((b & 1) == 0) {
      f32x2 u[NM];
#pragma unroll
      for (int m = 0; m < NM; ++m) { u[m] = cmul(t[m], zb[m]); if (b == 0) r0[m] = u[m]; if (b == 4) r4[m] = u[m]; }
      put(8 + b / 2, u);
    }
    if (b < 7) {
#pragma unroll
      for (int m = 0; m < NM; ++m) t[m] = cmul(t[m], z1[m]);
    }
  }
#pragma unroll
  for (int i = 0; i < 2; ++i) {
#pragma unroll
    for (int m = 0; m < NM; ++m) { r0[m] = cmul(r0[m], zb[m]); r4[m] = cmul(r4[m], zb[m]); }
    put(12 + 2 * i, r0); put(13 + 2 * i, r4);
  }
  bf16x8 fr[2][KS];
#pragma unroll
  for (int s = 0; s < KS; ++s) fr[0][s] = rd128(fa + s * 64);
#pragma unroll
  for (int i = 0; i < 5; ++i) {
#pragma unroll
    for (int m = 0; m < NM; ++m) r0[m] = cmul(r0[m], zb[m]);
    put(i, r0);
  }
  if (NM == 2) lds_set(done);
#pragma unroll
  for (int s = 0; s < KS; ++s) fr[1][s] = rd128(fa + s * 64);
  __builtin_amdgcn_sched_barrier(0);
  STAMP(15);
#pragma unroll
  for (int i = 0; i < 4; ++i) acc[i >> 1][i & 1] = f32x4{0.f, 0.f, 0.f, 0.f};
#pragma unroll
  for (int rt = 0; rt < 2; ++rt)
#pragma unroll
    for (int s = 0; s < KS; ++s) { acc[rt][0] = mfma(fr[rt][s], fb[0][s], acc[rt][0]); acc[rt][1] = mfma(fr[rt][s], fb[1][s], acc[rt][1]); }
}
struct LnOut { s16x4 tr[2][10]; v2u st; };
DI void ln8(rsrc_t rx, int tb, int lane, unsigned tile, LnOut& out) {
  const int r4 = lane >> 4, i = lane & 15;
  v4u raw[2][5];
#pragma unroll
  for (int h = 0; h < 2; ++h)
#pragma unroll
    for (int k = 0; k < 5; ++k) raw[h][k] = __builtin_amdgcn_raw_buffer_load_b128(rx, r4 * 1280 + i * 16 + k * 256, (tb * 8 + h * 4) * 1280, 0);
  const unsigned ta = tile + ((lane >> 2) & 3) * LN_RS + (16 * r4 + 4 * (lane & 3)) * 2;
#pragma unroll
  for (int h = 0; h < 2; ++h) {
    if (h == 0) { asm volatile("" : "+v"(raw[0][4])); STAMP(13); asm volatile("" : "+v"(raw[1][4])); STAMP(15); } else STAMP(14);
    float s1 = 0.f, s2 = 0.f, s3 = 0.f, s4 = 0.f;
    const bf16x2 one2 = __builtin_bit_cast(bf16x2, 0x3f803f80u);
#pragma unroll
    for (int k = 0; k < 10; ++k) {
      const unsigned w0 = raw[h][k >> 1][2 * (k & 1)], w1 = raw[h][k >> 1][2 * (k & 1) + 1];
      const bf16x2 p0 = __builtin_bit_cast(bf16x2, w0), p1 = __builtin_bit_cast(bf16x2, w1);
      s1 = __builtin_amdgcn_fdot2_f32_bf16(p0, one2, s1, false); s2 = __builtin_amdgcn_fdot2_f32_bf16(p0, p0, s2, false);
      s3 = __builtin_amdgcn_fdot2_f32_bf16(p1, one2, s3, false); s4 = __builtin_amdgcn_fdot2_f32_bf16(p1, p1, s4, false);
    }
    const float mean = rowsum16(s1 + s3) * (1.f / D);
    const float var = rbf(fmaxf(rowsum16(s2 + s4) * (1.f / D) - mean * mean, 0.f));
    const float m = rbf(mean), den = rbf(sqrtf(rbf(var + 1e-5f)));
    const float ri = __builtin_amdgcn_rcpf(den), nb = -m * ri;
    if (h == 0 || (i & 1)) out.st = v2u{__float_as_uint(m), __float_as_uint(den)};
#pragma unroll
    for (int k = 0; k < 5; ++k) {
      v4u o;
#pragma unroll
      for (int e = 0; e < 4; ++e) { const unsigned w = raw[h][k][e]; o[e] = pk2(f32x2{blo(w), bhi(w)} * ri + nb); }
      *(LAS v4u*)lds(tile + r4 * LN_RS + i * 16 + k * 256) = o;
    }
    cfence();
#pragma unroll
    for (int it = 0; it < 10; ++it) out.tr[h][it] = __builtin_amdgcn_ds_read_tr16_b64_v4i16((LAS s16x4*)lds(ta + it * 128));
  }
}
DI void ln8_store(rsrc_t rw, int tb, int lane, const LnOut& in) {
  if ((lane & 15) < 2) __builtin_amdgcn_raw_buffer_store_b64(in.st, rw, ((lane & 1) * 4 + (lane >> 4)) * 8, WS_STATS + tb * 128, SC1);
#pragma unroll
  for (int it = 0; it < 10; ++it) {
    bf16x8 o = __builtin_shufflevector(in.tr[0][it], in.tr[1][it], 0, 1, 2, 3, 4, 5, 6, 7);
    st128<SC1>(__builtin_bit_cast(v4u, o), rw, lane * 16 + it * 1024 + WS_U8 + tb * 10240);
  }
}

DI unsigned prm(unsigned hi, unsigned lo, unsigned sel) { return __builtin_amdgcn_perm(hi, lo, sel); }
DI unsigned colo(int col) { return (col & 15) * 16 + ((col >> 4) & 1) * 8; }
template <int S> DI v4u kchunk(unsigned raw, int lane) {
  const int i = lane & 31, up = lane >> 5;
  if (S == 0) {
    const v2u a0 = *(LAS v2u*)lds(raw + colo(i) + 512 * up);
    const v2u a1 = *(LAS v2u*)lds(raw + (up ? (i == 31 ? 768 : 512 + colo(i + 1)) : 256 + colo(i)));
    const unsigned s0 = up ? 0x03020504u : 0x05040706u, s1 = up ? 0x01000100u : 0x05040706u, s2 = up ? 0x03020504u : 0x05040706u,
                   s3 = up ? 0x01000100u : 0x01000302u;
    return v4u{prm(up ? a1.x : a1.y, a0.y, s0), prm(a1.x, a0.y, s1), prm(a0.y, a0.x, s2), prm(0, a0.x, s3)};
  } else {
    const unsigned b0 = *(LAS unsigned*)lds(raw + 768 + colo(i) + 4 * up);
    const unsigned b1 = *(LAS unsigned*)lds(raw + (lane == 63 ? 1024 : 768 + colo(i + 1) + 4 * ((lane + 1) >> 5)));
    return v4u{prm(0, b1, 0x01000100), prm(0, b0, 0x03020302), prm(0, b0, 0x01000302), prm(0, b0, 0x01000100)};
  }
}
DI unsigned vmov(unsigned x) { unsigned r; asm("v_mov_b32 %0, %1" : "=v"(r) : "v"(x)); return r; }
DI void hwrite(v4u x, v4u y, unsigned hb, int pos) {
  v8u w = __builtin_shufflevector(x, y, 0, 1, 2, 3, 4, 5, 6, 7), e, o0, o1;
  asm volatile("" : "+v"(w));
#pragma unroll
  for (int k = 0; k < 6; ++k) {
    e[k] = vmov(w[k + 1]);
    o0[k] = __builtin_amdgcn_alignbit(w[k + 1], w[k], 16);
    o1[k] = __builtin_amdgcn_alignbit(w[k + 2], w[k + 1], 16);
    asm volatile("" : "+v"(o1[k]));
  }
  asm volatile("" : "+v"(e), "+v"(o0), "+v"(o1));
  const unsigned a = hb + pos * 16;
  *(LAS v4u*)lds(a + 7 * H_CS) = __builtin_shufflevector(w, w, 0, 1, 2, 3);
  *(LAS v4u*)lds(a + 3 * H_CS) = __builtin_shufflevector(w, w, 2, 3, 4, 5);
  *(LAS v4u*)lds(a + 5 * H_CS) = __builtin_shufflevector(e, e, 0, 1, 2, 3);
  *(LAS v4u*)lds(a + 1 * H_CS) = __builtin_shufflevector(e, e, 2, 3, 4, 5);
  *(LAS v4u*)lds(a + 6 * H_CS) = __builtin_shufflevector(o0, o0, 0, 1, 2, 3);
  *(LAS v4u*)lds(a + 2 * H_CS) = __builtin_shufflevector(o0, o0, 2, 3, 4, 5);
  *(LAS v4u*)lds(a + 4 * H_CS) = __builtin_shufflevector(o1, o1, 0, 1, 2, 3);
  *(LAS v4u*)lds(a + 0 * H_CS) = __builtin_shufflevector(o1, o1, 2, 3, 4, 5);
}
template <int S> DI void hbuild(unsigned raw, unsigned hb, int lane, v4u& carry) {
  const v4u x = kchunk<S>(raw, lane);
  v4u y;
#pragma unroll
  for (int k = 0; k < 4; ++k) {
    y[k] = __builtin_amdgcn_update_dpp(carry[k], x[k], 0x138, 0xf, 0xf, false);
    carry[k] = __builtin_amdgcn_readlane(x[k], 63);
  }
  hwrite(x, y, hb, 1 + 64 * S + lane);
}
DI void htail(unsigned raw, unsigned hb, unsigned k3, int lane, v4u carry) {
  const unsigned kk = *(LAS uint16_t*)lds(raw + 1024);
  const int n0 = 128 - 2 * lane, n1 = n0 - 1;
  const unsigned c0 = *(LAS uint16_t*)lds(raw + 1024 + (n0 == 128 ? 256 : colo(n0) + 2 * (n0 >> 5)));
  const unsigned c1 = *(LAS uint16_t*)lds(raw + 1024 + colo(n1) + 2 * (n1 >> 5));
  *(LAS unsigned*)lds(k3 + lane * 4) = c0 | (c1 << 16);
  if (lane < 16) *(LAS unsigned*)lds(k3 + 256 + lane * 4) = 0;
  const unsigned k2 = kk | (kk << 16), m0 = lane == 0 ? ~0u : 0u, m1 = lane == 1 ? ~0u : 0u;
  if (lane < 3) hwrite(v4u{0, (kk << 16) & m0, k2 & m0, k2 & m0},
                       v4u{carry[0] & m0, (carry[1] & m0) | ((kk << 16) & m1), (carry[2] & m0) | (k2 & m1), (carry[3] & m0) | (k2 & m1)}, hb, 129 + lane);
}

DI void conv(rsrc_t rw, int cw, int d, int lane) {
  const unsigned hb = L_H + cw * H_SZ, ub = L_U + cw * U_SZ;
  const int c = lane & 15, g = lane >> 4;
  unsigned aa = hb + (c & 7) * H_CS + (3 + (c >> 3) - g) * 16, ab = ub + 416 + 32 * c + 16 * g;
  f32x4 acc[8] = {};
  bf16x8 fa[2][4], fb[2][8];
  auto rd = [&](int buf, int oa, int ob) {
#pragma unroll
    for (int k = 0; k < 4; ++k) fa[buf][k] = rd128(aa + oa + k * 512);
#pragma unroll
    for (int e = 7; e >= 0; --e) fb[buf][e] = rd128(ab + ob + e * 512);
  };
  auto mm = [&](int buf) {
#pragma unroll
    for (int k = 0; k < 4; ++k)
#pragma unroll
      for (int e = 7 - k; e >= 0; --e) acc[e + k] = mfma(fa[buf][k], fb[buf][e], acc[e + k]);
  };
  auto ilv = [&]() {
#pragma unroll
    for (int i = 0; i < 12; ++i) { __builtin_amdgcn_sched_group_barrier(0x008, 1, 0); __builtin_amdgcn_sched_group_barrier(0x100, 1, 0); }
    __builtin_amdgcn_sched_group_barrier(0x008, 14, 0);
    __builtin_amdgcn_sched_barrier(0);
  };
  asm volatile("" : "+v"(aa), "+v"(ab));
  rd(0, 0, 64);
  {
    const bf16x8 fx = rd128(aa + 2048);
    bf16x8 ue[4], kt[4];
#pragma unroll
    for (int f = 0; f < 4; ++f) ue[f] = rd128(ab + 48 + f * 512);
    const unsigned k3 = L_K3 + cw * 320 + (254 - 2 * c + 8 * g) * 2;
#pragma unroll
    for (int f = 0; f < 4; ++f) {
      v4u t;
#pragma unroll
      for (int e = 0; e < 4; ++e) t[e] = *(LAS unsigned*)lds(k3 - (4 + f) * 64 + e * 4);
      kt[f] = __builtin_bit_cast(bf16x8, t);
    }
    bf16x8 b9, b7;
#pragma unroll
    for (int jj = 0; jj < 8; ++jj) {
      const int dl = 8 * g + jj - 8 - c;
      b9[jj] = (dl >= -4 && dl <= 4) ? 0x3f80 : 0; b7[jj] = (dl >= -3 && dl <= 3) ? 0x3f80 : 0;
    }
#pragma unroll
    for (int f = 0; f < 4; ++f) {
      acc[4 + f] = mfma(fx, fb[0][f], acc[4 + f]);
      const f32x4 ve = mfma(ue[f], b9, f32x4{0.f, 0.f, 0.f, 0.f}), vo = mfma(fb[0][f], b7, f32x4{0.f, 0.f, 0.f, 0.f});
      const bf16x8 vf = __builtin_bit_cast(bf16x8, v4u{pk(ve[0], vo[0]), pk(ve[1], vo[1]), pk(ve[2], vo[2]), pk(ve[3], vo[3])});
#pragma unroll
      for (int n = 4 + f; n < 8; ++n) acc[n] = mfma(vf, kt[n - f - 4], acc[n]);
    }
    __builtin_amdgcn_sched_barrier(0);
  }
  STAMP(8);
#pragma unroll 1
  for (int r = 0; r < 3; ++r) {
    rd(1, 64, 0); mm(0); ilv();
    aa += 128; ab -= 128;
    asm volatile("" : "+v"(aa), "+v"(ab));
    rd(0, 0, 64); mm(1); ilv();
  }
  rd(1, 64, 0); mm(0); ilv();
  mm(1);
#pragma unroll
  for (int n = 0; n < 8; ++n)
    __builtin_amdgcn_raw_buffer_store_b64(v2u{pk(acc[n][0], acc[n][1]), pk(acc[n][2], acc[n][3])}, rw, 32 * c + 8 * g + 512 * n, WS_AT + d * 4096, SC1);
}
#endif

__global__ __launch_bounds__(512, 2) void fused_s4_ffn(
    const uint16_t* __restrict__ x, const uint16_t* __restrict__ Are, const uint16_t* __restrict__ Aim, const uint16_t* __restrict__ Bre,
    const uint16_t* __restrict__ Bim, const uint16_t* __restrict__ Cre, const uint16_t* __restrict__ Cim, const uint16_t* __restrict__ logdt,
    const uint16_t* __restrict__ dskip, const uint16_t* __restrict__ wg, const uint16_t* __restrict__ wu, const uint16_t* __restrict__ wd,
    uint16_t* __restrict__ out, void* ws) {
#if __HIP_DEVICE_COMPILE__
  const int wid = __builtin_amdgcn_readfirstlane(threadIdx.x >> 6), lane = threadIdx.x & 63;
  const int b = blockIdx.x, xcd = b & 7, j = b >> 3;
  const bool normal = j < 30;
  const int rl = j % 5, ti = 5 * (xcd & 1) + rl;
  const int mm_ = 2 * (xcd >> 1) + (xcd & 1), par = j - 30;
  const int ch = normal ? 6 * (xcd >> 1) + j / 5 : 3 * mm_ + 2 * par;
  const int ch2 = 3 * mm_ + 1;
  const int nch = normal ? (j < 20 ? 3 : 2) : 0;
  const int d0 = 80 * xcd + (j < 20 ? 3 * j : 2 * j + 20);
  const int t64 = 4 * xcd + (j >> 3), dj = j & 7;
  const int rot = XROT ? (ti + (5 * (j / 5) + 1) / 3) % 10 : ti;
  const int rot2 = (3 * (j >> 3) + (3 * dj + 1) / 2) % 12;
  auto kst = [&](int s) { return s + rot2 >= 12 ? s + rot2 - 12 : s + rot2; };
  auto pbase = [](int w) -> unsigned { return w < 2 ? w * 15360 : L_P2 + (w - 2) * 15360; };
  const rsrc_t rw = mkr(ws, WS_BYTES), rx = mkr(x, L * D * 2);
  STAMP(0);
  if (wid == 6) {
    u64 pc; asm volatile("s_getpc_b64 %0" : "=s"(pc));
    dma<0>(srd((const void*)pc, CODE_BYTES), L_DUMMY, lane * 16, j * 1024);
    dma<0>(srd((const void*)pc, CODE_BYTES), L_DUMMY, lane * 16, 32768 + j * 1024);
  }
  if (wid == 7) { *(LAS v4u*)lds(L_MISC + 16 + (lane & 3) * 16) = v4u{0, 0, 0, 0}; wait_lg(); }
  asm volatile("" ::"s"(x), "s"(Are), "s"(Aim), "s"(Bre), "s"(Bim), "s"(Cre), "s"(Cim), "s"(logdt), "s"(dskip), "s"(wg), "s"(wu), "s"(wd), "s"(out), "s"(ws));
  unsigned pw[6], ldw_ = 0, dsw_ = 0;
  const int kd = d0 + (wid & 3);
  if ((wid & 3) < nch) {
    const uint16_t* const par6[6] = {Are, Aim, Bre, Bim, Cre, Cim};
    const unsigned vo = wid >= 4 ? lane * 4 : 256 + (lane >> 1) * 4;
#pragma unroll
    for (int i = 0; i < 6; ++i) pw[i] = __builtin_amdgcn_raw_buffer_load_b32(mkr(par6[i], D * NS * 2), vo, kd * NS * 2, NT_);
    ldw_ = ((const __attribute__((address_space(4))) unsigned*)logdt)[kd >> 1];
    dsw_ = ((const __attribute__((address_space(4))) unsigned*)dskip)[kd >> 1];
  }
  cfence(); bar(); cfence();
  STAMP(11);
  const float dt = rbf(expf((kd & 1) ? bhi(ldw_) : blo(ldw_)));
  const v4u sg = srd(wg, D * F * 2), su = srd(wu, D * F * 2), sx = srd(x, L * D * 2), sw = srd(ws, WS_BYTES);

  if (wid < 4) {
    if (wid < nch) {
      f32x4 ka[2][2];
#pragma unroll
      for (int i = 0; i < 6; ++i) pw[i] = (lane & 1) ? pw[i] >> 16 : pw[i];
      kgen<1>(pw, dt, lane, L_KG + wid * KG_SZ + 16 * 544, 0, ka);
#pragma unroll
      for (int i = 0; i < 4; ++i) *(LAS f32x4*)lds(L_KG + wid * KG_SZ + 16 * 544 + i * 1024 + lane * 16) = ka[i >> 1][i & 1];
      wait_lg();
      lds_set(L_MISC + 16 + wid * 4);
      STAMP(12);
      if (wid < 2) {
        const G1Ld v = g1_lanes(lane);
        lds_wait(L_MISC + 68); lds_wait(L_MISC + 72);
        if (nch == 3) lds_wait(L_MISC + 76);
        STAMP(10);
        g1_issue<6>(sx, sg, su, v, wid, rot, 0, 192 * ti, ch);
        wait_vm<0>();
      }
    } else if (wid == 3 && normal) {
      const G1Ld v = g1_lanes(lane);
      v4u sink = {0, 0, 0, 0};
#pragma unroll 1
      for (int p = 0; p < 8; ++p) {
        touch(sink, sg, v.vw0, ch * 128 + (rot * 64 + p * 8) * 3072);
        touch(sink, su, v.vw0, ch * 128 + (rot * 64 + p * 8) * 3072);
        const int sl = rot + (p >> 2);
        touch(sink, sx, v.vx0, (192 * ti + 32 * (j / 5) + 8 * (p & 3)) * 1280 + (sl == 10 ? 0 : sl) * 128);
      }
      const v4u sd = srd(wd, F * D * 2);
#pragma unroll 1
      for (int p = 0; p < 8; ++p) dma<NT_>(sd, L_WDU + p * 1024, (lane >> 3) * 1280 + (lane & 7) * 16, (ch * 64 + p * 8) * 1280 + ti * 128);
      asm volatile("s_waitcnt vmcnt(0)" : "+v"(sink)::"memory");
    }
    wait_lg();
    STAMP(1);
    if (normal) {
      f32x4 acc[4][6];
      g1_main<6>(wid, lane, 0, acc);
      BAR(12);
      STAMP(3);
      g1_epilogue<6>(rw, wid, lane, 12 * ti, ch, L_ST, L_CS, acc);
    } else {
      f32x4 acc4[4][4], acc2[4][2];
      g1_main<4>(wid, lane, 0, acc4);
      g1_main<2>(wid, lane, STAGE, acc2);
      BAR(12);
      STAMP(3);
      g1_epilogue<4>(rw, wid, lane, 120, ch, L_ST, L_CS, acc4);
      g1_epilogue<2>(rw, wid, lane, 120 + 4 * par, ch2, L_ST + par * 1024, L_CS + 512, acc2);
    }
    STAMP(4);
    lds_set(L_MISC + 48 + wid * 4);
    wait_vm<0>();
    __builtin_amdgcn_s_waitcnt(0x0F70);
    STAMP(5);
    arrive_publish(L_MISC + 8, 4, rw, WS_F1 + (normal ? ti * 24 + ch : 240 + 2 * mm_ + par) * 8, lane);
    const unsigned wso = WS_WDT + (12 * wid * 40 + 5 * dj) * 1024, hso = WS_H + (4 * t64 * 48 + 12 * wid) * 1024;
    const unsigned vo0 = lane * 16, vo1 = lane * 16 + 4096, pw = pbase(wid) + lane * 16;
    v4u wr[5][5], hr[3][4];
#pragma unroll
    for (int i = 0; i < 25; ++i) asm volatile("" : "=v"(wr[i / 5][i % 5]));
#pragma unroll
    for (int i = 0; i < 12; ++i) asm volatile("" : "=v"(hr[i / 4][i % 4]));
    auto ldw = [&](int s) {
      const unsigned so = wso + kst(s) * 40960;
      asm volatile("buffer_load_dwordx4 %0, %5, %7, %8 offen sc1\n\tbuffer_load_dwordx4 %1, %5, %7, %8 offen offset:1024 sc1\n\t"
                   "buffer_load_dwordx4 %2, %5, %7, %8 offen offset:2048 sc1\n\tbuffer_load_dwordx4 %3, %5, %7, %8 offen offset:3072 sc1\n\t"
                   "buffer_load_dwordx4 %4, %6, %7, %8 offen sc1"
                   : "+v"(wr[s % 5][0]), "+v"(wr[s % 5][1]), "+v"(wr[s % 5][2]), "+v"(wr[s % 5][3]), "+v"(wr[s % 5][4])
                   : "v"(vo0), "v"(vo1), "s"(sw), "s"(so) : "memory");
    };
    auto ldh = [&](int s) {
      const unsigned so = hso + kst(s) * 1024;
      asm volatile("buffer_load_dwordx4 %0, %4, %5, %6 offen sc1\n\tbuffer_load_dwordx4 %1, %4, %5, %7 offen sc1\n\t"
                   "buffer_load_dwordx4 %2, %4, %5, %8 offen sc1\n\tbuffer_load_dwordx4 %3, %4, %5, %9 offen sc1"
                   : "+v"(hr[s % 3][0]), "+v"(hr[s % 3][1]), "+v"(hr[s % 3][2]), "+v"(hr[s % 3][3])
                   : "v"(vo0), "s"(sw), "s"(so), "s"(so + 49152), "s"(so + 98304), "s"(so + 147456) : "memory");
    };
#pragma unroll
    for (int s = 0; s < 5; ++s) ldw(s);
    lds_wait(L_MISC + 32);
    STAMP(6);
#pragma unroll
    for (int s = 0; s < 3; ++s) ldh(s);
    f32x4 acc[4][5];
#pragma unroll
    for (int s = 0; s < 12; ++s) {
      constexpr int YN[12] = {8, 13, 18, 23, 23, 18, 13, 8, 8, 8, 4, 0};
      asm volatile("s_waitcnt vmcnt(%9)"
                   : "+v"(hr[s % 3][0]), "+v"(hr[s % 3][1]), "+v"(hr[s % 3][2]), "+v"(hr[s % 3][3]), "+v"(wr[s % 5][0]), "+v"(wr[s % 5][1]),
                     "+v"(wr[s % 5][2]), "+v"(wr[s % 5][3]), "+v"(wr[s % 5][4])
                   : "i"(YN[s]) : "memory");
#pragma unroll
      for (int t = 0; t < 4; ++t)
#pragma unroll
        for (int dd = 0; dd < 5; ++dd)
          acc[t][dd] = mfma(__builtin_bit_cast(bf16x8, wr[s % 5][dd]), __builtin_bit_cast(bf16x8, hr[s % 3][t]), s == 0 ? f32x4{0.f, 0.f, 0.f, 0.f} : acc[t][dd]);
      __builtin_amdgcn_sched_barrier(0);
      if (s == G2SIG && wid == 0) lds_set(L_MISC + 36);
      if (s + 3 < 12) ldh(s + 3);
      if (s < 4) ldw(s + 5);
      if (s >= 4 && s < 7) {
#pragma unroll
        for (int t = 0; t < 5; ++t) wr[s % 5][t] = *(LAS v4u*)lds(pw + (s - 4) * 5120 + t * 1024);
      }
      __builtin_amdgcn_sched_barrier(0);
    }
    STAMP(7);
    const int c = lane & 15, g = lane >> 4;
    const unsigned oo = ((4 * t64 + wid) * 16 + c) * 1280 + (80 * dj + 4 * g) * 2;
    v2u xr[5];
#pragma unroll
    for (int dd = 0; dd < 5; ++dd) xr[dd] = __builtin_amdgcn_raw_buffer_load_b64(rx, oo + dd * 32, 0, 0);
    f32x4 own[5];
#pragma unroll
    for (int t = 0; t < 4; ++t) {
      if (t == wid) {
#pragma unroll
        for (int dd = 0; dd < 5; ++dd) own[dd] = acc[t][dd];
      } else {
        const unsigned o = pw + (t > wid ? t - 1 : t) * 5120;
#pragma unroll
        for (int dd = 0; dd < 5; ++dd) *(LAS f32x4*)lds(o + dd * 1024) = acc[t][dd];
      }
    }
    wait_lg();
    BAR(11);
    STAMP(8);
#pragma unroll
    for (int k = 1; k < 4; ++k) {
      const int w = (wid + k) & 3;
      const unsigned o = pbase(w) + lane * 16 + (wid > w ? wid - 1 : wid) * 5120;
#pragma unroll
      for (int dd = 0; dd < 5; ++dd) own[dd] += *(LAS f32x4*)lds(o + dd * 1024);
    }
    const rsrc_t ro = mkr(out, L * D * 2);
    const unsigned at = L_AT + (4 * g + ((lane >> 2) & 3)) * 128 + (16 * wid + 4 * (lane & 3)) * 2;
#pragma unroll
    for (int dd = 0; dd < 5; ++dd) {
      const v2u a = __builtin_bit_cast(v2u, __builtin_amdgcn_ds_read_tr16_b64_v4i16((LAS s16x4*)lds(at + dd * 2048)));
      const float o0 = own[dd][0] + blo(a.x) + blo(xr[dd].x), o1 = own[dd][1] + bhi(a.x) + bhi(xr[dd].x);
      const float o2 = own[dd][2] + blo(a.y) + blo(xr[dd].y), o3 = own[dd][3] + bhi(a.y) + bhi(xr[dd].y);
      __builtin_amdgcn_raw_buffer_store_b64(v2u{pk(o0, o1), pk(o2, o3)}, ro, oo + dd * 32, 0, 0);
    }
    STAMP(9);
  } else if (wid < 7) {
    const int cw = wid - 4;
    v4u carry = {0, 0, 0, 0}, sink = {0, 0, 0, 0}, kraw[2] = {};
    auto warm_wd = [&]() {
#pragma unroll 1
      for (int i = 0; i < 6; ++i) {
        const int w = 2 * cw + (i >= 3), s = i >= 3 ? i - 3 : i;
        const unsigned so = WS_WDT + ((12 * w + kst(9 + s)) * 40 + 5 * dj) * 1024;
#pragma unroll
        for (int t = 0; t < 5; ++t) touch(sink, sw, lane * 16, so + t * 1024);
      }
      wait_vm<30>();
    };
    if (normal) {
      LnOut lo2;
      const bool ln2 = cw == 2 && (j == 20 || j == 21);
      if (cw < nch) {
        f32x4 ka[2][2];
        kgen<2>(pw, dt, lane, L_KG + cw * KG_SZ, L_MISC + 68 + cw * 4, ka);
        STAMP(12);
        lds_wait(L_MISC + 16 + cw * 4);
#pragma unroll
        for (int i = 0; i < 4; ++i) ka[i >> 1][i & 1] += *(LAS f32x4*)lds(L_KG + cw * KG_SZ + 16 * 544 + i * 1024 + lane * 16);
        if (lane == 0) ka[0][0][0] += (kd & 1) ? bhi(dsw_) : blo(dsw_);
#pragma unroll
        for (int rt = 0; rt < 2; ++rt)
          kraw[rt] = v4u{pk(ka[rt][0][0], ka[rt][0][1]), pk(ka[rt][0][2], ka[rt][0][3]), pk(ka[rt][1][0], ka[rt][1][1]), pk(ka[rt][1][2], ka[rt][1][3])};
      } else if (ln2) {
        ln8(rx, 240 + 2 * xcd + j - 20, lane, L_KG + 2 * KG_SZ, lo2);
      }
      wait_lg();
      STAMP(1);
      if (cw < 2) {
        g1_loader<6, 1>(sx, sg, su, cw, lane, 192 * ti, ch, rot, STAGE);
        warm_wd(); BAR(9);
      } else {
        BAR(0);
        colsum(0, lane); colsum(1, lane);
        BAR(1);
        if (ln2) ln8_store(rw, 240 + 2 * xcd + j - 20, lane, lo2);
        BAR(2);
        wait_vm<0>();
#pragma unroll 1
        for (int k = 3; k < 10; ++k) BAR(k);
      }
      if (cw < nch) {
        const unsigned hb = L_H + cw * H_SZ;
        __builtin_amdgcn_s_sleep(4);
#pragma unroll
        for (int rt = 0; rt < 2; ++rt) *(LAS v4u*)lds(L_RAW + cw * 2048 + rt * 1024 + lane * 16) = kraw[rt];
        if (lane < 9) *(LAS v4u*)lds(hb + lane * H_CS) = v4u{0, 0, 0, 0};
        hbuild<0>(L_RAW + cw * 2048, hb, lane, carry);
      }
      BAR(12);
      if (cw < nch) {
        hbuild<1>(L_RAW + cw * 2048, L_H + cw * H_SZ, lane, carry);
        htail(L_RAW + cw * 2048, L_H + cw * H_SZ, L_K3 + cw * 320, lane, carry);
      }
    } else if (cw < 2) {
      g1_loader<4, 0>(sx, sg, su, cw, lane, 1920, ch, 0, 0);
      wait_vm<0>(); BAR(9);
      g1_loader<2, 0>(sx, sg, su, cw, lane, 1920 + 64 * par, ch2, 0, STAGE);
      warm_wd(); BAR(9);
      BAR(12);
    } else {
#pragma unroll 1
      for (int s = 0; s < 21; ++s) bar();
    }
    asm volatile("s_waitcnt vmcnt(0)" : "+v"(sink)::"memory");
    STAMP(4);
    if (cw < nch) {
      lds_wait(L_MISC + 48 + cw * 4);
      lds_wait(L_MISC + 64);
      STAMP(5);
      __builtin_amdgcn_s_setprio(2);
      conv(rw, cw, d0 + cw, lane);
      STAMP(6);
      wait_vm<0>();
      arrive_publish(L_MISC + 12, nch, rw, WS_F2 + (xcd * 32 + j) * 8, lane);
    }
    STAMP(7);
    BAR(11);
  } else {
    const v2u e = __builtin_amdgcn_raw_buffer_load_b64(rw, WS_EPOCH, 0, SC1);
    const int tb = 120 * (xcd & 1) + 30 * (xcd >> 1) + j;
    LnOut lo;
    if (normal) ln8(rx, tb, lane, L_LN, lo);
    const u64 tg = (((u64)e.y << 32) | e.x) + 0x9E3779B97F4A7C15ull;
    const v2u tag = {(unsigned)tg, (unsigned)(tg >> 32)};
    if (lane == 0) *(LAS v4u*)lds(L_MISC) = v4u{tag.x, tag.y, 0, 0};
    wait_lg();
    STAMP(1);
    BAR(0);
    if (normal) { colsum(2, lane); colsum(3, lane); }
    BAR(1);
    if (normal) {
      ln8_store(rw, tb, lane, lo);
      if (lane < 32) st128<SC1>(*(LAS v4u*)lds(L_CSP + lane * 16), rw, lane * 16 + WS_CSP + (ch * 10 + ti) * 512);
      const unsigned a0 = L_WDU + (4 * (lane >> 4) + ((lane >> 2) & 3)) * 128 + 8 * (lane & 3);
#pragma unroll
      for (int p = 0; p < 8; ++p) {
        const unsigned a = a0 + (p >> 2) * 4096 + (p & 3) * 32;
        st128<SC1>(__builtin_bit_cast(v4u, trd(a, a + 2048)), rw, lane * 16 + WS_WDT + ((2 * ch + (p >> 2)) * 40 + 4 * ti + (p & 3)) * 1024);
      }
    }
    BAR(2); BAR(3);
    wait_vm<0>();
    if (lane == 0) put_flag(rw, WS_F0 + b * 8, tag);
    BAR(4); BAR(5); BAR(6);
    if (!normal) {
#pragma unroll 1
      for (int s = 0; s < 10; ++s) bar();
    }
    poll<4>(rw, WS_F0, 256, tag, lane);
    STAMP(2);
    if (b == 0 && lane == 0) put_flag(rw, WS_EPOCH, tag);
    const int row0 = normal ? 192 * ti : 1920;
    v2u st[3], cp[2][10];
#pragma unroll
    for (int k = 0; k < 3; ++k) { const int r = row0 + lane + 64 * k; st[k] = __builtin_amdgcn_raw_buffer_load_b64(rw, (r >> 3) * 128 + (r & 7) * 8, WS_STATS, SC1); }
#pragma unroll
    for (int k = 0; k < 10; ++k) cp[0][k] = __builtin_amdgcn_raw_buffer_load_b64(rw, lane * 8 + k * 512, WS_CSP + ch * 5120, SC1);
    if (!normal) {
#pragma unroll
      for (int k = 0; k < 10; ++k) cp[1][k] = __builtin_amdgcn_raw_buffer_load_b64(rw, lane * 8 + k * 512, WS_CSP + ch2 * 5120, SC1);
    }
    BAR(7);
    v4u ur[16];
    if ((lane & 3) < nch) {
#pragma unroll
      for (int p = 0; p < 16; ++p) ur[p] = __builtin_amdgcn_raw_buffer_load_b128(rw, (lane >> 2) * 10240 + (lane & 3) * 16, WS_U8 + p * 163840 + d0 * 16, SC1);
    }
    BAR(8);
    f32x4 so[3];
#pragma unroll
    for (int k = 0; k < 3; ++k) {
      const float m = __uint_as_float(st[k].x), den = __uint_as_float(st[k].y);
      so[k] = f32x4{-m, den * den, -1.44269504f * __builtin_amdgcn_rcpf(den), 0.f};
    }
    f32x2 co[2];
#pragma unroll
    for (int h = 0; h < 2; ++h) {
      float c0 = 0.f, c1 = 0.f;
      if (h == 0 || !normal) {
#pragma unroll
        for (int k = 0; k < 10; ++k) { c0 += __uint_as_float(cp[h][k].x); asm volatile("" : "+v"(c0)); c1 += __uint_as_float(cp[h][k].y); }
      }
      co[h] = f32x2{c0, c1};
    }
    asm volatile("" : "+v"(so[0]), "+v"(so[1]), "+v"(so[2]), "+v"(co[0]), "+v"(co[1]));
    BAR(9);
    __builtin_amdgcn_s_sleep(4);
#pragma unroll
    for (int k = 0; k < 3; ++k) *(LAS f32x4*)lds(L_ST + (lane + 64 * k) * 16) = so[k];
#pragma unroll
    for (int h = 0; h < 2; ++h) *(LAS f32x2*)lds(L_CS + h * 512 + lane * 8) = co[h];
    wait_lg();
    BAR(12);
    if (lane < 32) {
#pragma unroll
      for (int k = 0; k < 3; ++k) *(LAS v4u*)lds(L_U + k * U_SZ + lane * 16) = v4u{0, 0, 0, 0};
    }
    if ((lane & 3) < nch) {
#pragma unroll
      for (int p = 0; p < 16; ++p) *(LAS v4u*)lds(L_U + (lane & 3) * U_SZ + 512 + p * 256 + (lane >> 2) * 16) = ur[p];
    }
    wait_lg();
    lds_set(L_MISC + 64);
    lds_wait(L_MISC + 60);
    STAMP(4);
#pragma unroll 1
    for (int i = 0; i < 12; ++i) {
      const int w = i / 3, s = i - 3 * w;
      const unsigned so = WS_WDT + ((12 * w + kst(9 + s)) * 40 + 5 * dj) * 1024, lo = pbase(w) + s * 5120;
#pragma unroll
      for (int t = 0; t < 5; ++t) dma<SC1>(sw, lo + t * 1024, lane * 16, so + t * 1024);
    }
    wait_vm<0>();
    STAMP(8);
    poll<1>(rw, WS_F1 + (t64 < 30 ? (t64 / 3) * 192 : 1920), t64 < 30 ? 24 : 16, tag, lane);
    lds_set(L_MISC + 32);
    STAMP(5);
    lds_wait(L_MISC + 36);
    poll<1>(rw, WS_F2 + dj * 256, 30, tag, lane);
    STAMP(6);
#pragma unroll
    for (int p = 0; p < 10; ++p) dma<SC1>(sw, L_AT + p * 1024, (lane >> 3) * 4096 + (lane & 7) * 16, WS_AT + (80 * dj + p * 8) * 4096 + t64 * 128);
    wait_vm<0>();
    STAMP(7);
    BAR(11);
  }
  DUMP();
#endif
}

extern "C" void kernel_launch(void* const* d_in, const int* in_sizes, int n_in, void* d_out, int out_size, void* d_ws, size_t ws_size,
                              hipStream_t stream) {
  (void)hipFuncSetAttribute((const void*)fused_s4_ffn, hipFuncAttributeMaxDynamicSharedMemorySize, LDS_BYTES);
  fused_s4_ffn<<<256, 512, LDS_BYTES, stream>>>(
      (const uint16_t*)d_in[0], (const uint16_t*)d_in[1], (const uint16_t*)d_in[2], (const uint16_t*)d_in[3], (const uint16_t*)d_in[4],
      (const uint16_t*)d_in[5], (const uint16_t*)d_in[6], (const uint16_t*)d_in[7], (const uint16_t*)d_in[8], (const uint16_t*)d_in[10],
      (const uint16_t*)d_in[11], (const uint16_t*)d_in[12], (uint16_t*)d_out, d_ws);
}
```

```cpp
#define BENCH
#define PKIND 1
#define PW 0
#define PS 0
#include <hip/hip_runtime.h>
#include <stdint.h>

typedef short bf16x8 __attribute__((ext_vector_type(8)));
typedef short s16x4 __attribute__((ext_vector_type(4)));
typedef float f32x4 __attribute__((ext_vector_type(4)));
typedef float f32x2 __attribute__((ext_vector_type(2)));
typedef __bf16 bf16x2 __attribute__((ext_vector_type(2)));
typedef unsigned v4u __attribute__((ext_vector_type(4)));
typedef unsigned v2u __attribute__((ext_vector_type(2)));
typedef unsigned v8u __attribute__((ext_vector_type(8)));
typedef unsigned long long u64;
#define LAS __attribute__((address_space(3)))
#define DI __device__ __forceinline__
typedef __amdgpu_buffer_rsrc_t rsrc_t;

constexpr int L = 2048, D = 640, NS = 192, F = 1536;
constexpr int SC1 = 16, NT_ = 2;

constexpr unsigned WS_EPOCH = 0, WS_F0 = 1024, WS_F1 = 4096, WS_F2 = 8192, WS_STATS = 16384, WS_CSP = 65536, WS_U8 = 262144,
                   WS_WDT = 3u << 20, WS_H = 5u << 20, WS_AT = 12u << 20, WS_TM = 15u << 20, WS_BYTES = 16u << 20;

constexpr unsigned STAGE = 40960, XB = 24576;
constexpr unsigned LX = 3 * STAGE;
constexpr unsigned L_RAW = LX;
constexpr unsigned L_ST = L_RAW + 3 * 2048;
constexpr unsigned L_CS = L_ST + 3072;
constexpr unsigned L_MISC = L_CS + 1024;
constexpr unsigned L_U = L_MISC + 128, U_SZ = 4608;
constexpr unsigned L_LN = L_U + 3 * U_SZ, LN_RS = 1296;
constexpr unsigned L_END = L_LN + 10240;
constexpr unsigned L_WDU = L_U, L_DUMMY = L_U + 8192, L_CSP = L_U + 9216;
constexpr unsigned L_KG = 2 * STAGE, KG_SZ = 16 * (544 + 288);
constexpr unsigned L_H = STAGE, H_CS = 2112, H_SZ = 8 * H_CS;
constexpr unsigned L_K3 = L_END, L_AT = L_LN;
constexpr unsigned LDS_BYTES = 163840;
static_assert(L_END + 960 <= LDS_BYTES, "lds");
constexpr unsigned L_P2 = L_H + 3 * H_SZ + 64;
static_assert(L_P2 + 2 * 15360 <= LX, "lds partials");
#define G2AUXW SC1
#define G2AUXH SC1
#define ROT2 1
#define G2SIG 6
#define XROT 0
constexpr int SPIN_MAX = 20000;
constexpr unsigned CODE_BYTES = 47104;

#if __HIP_DEVICE_COMPILE__
extern __shared__ __attribute__((aligned(16))) char smem_[];
DI LAS char* lds(unsigned o) { return (LAS char*)smem_ + o; }
DI rsrc_t mkr(const void* p, unsigned bytes) { return __builtin_amdgcn_make_buffer_rsrc((void*)p, 0, bytes, 0x00020000); }
DI unsigned pk(float a, float b) { f32x2 f = {a, b}; return __builtin_bit_cast(unsigned, __builtin_convertvector(f, bf16x2)); }
DI unsigned pk2(f32x2 f) { return __builtin_bit_cast(unsigned, __builtin_convertvector(f, bf16x2)); }
DI float blo(unsigned v) { return __uint_as_float(v << 16); }
DI float bhi(unsigned v) { return __uint_as_float(v & 0xffff0000u); }
DI float rbf(float f) { return blo(pk(f, f)); }
DI void bar() { __builtin_amdgcn_s_barrier(); }
DI void cfence() { asm volatile("" ::: "memory"); }
template <int N> DI void wait_vm() { asm volatile("s_waitcnt vmcnt(%0)" ::"i"(N) : "memory"); }
DI void wait_lg() { asm volatile("s_waitcnt lgkmcnt(0)" ::: "memory"); }
DI bf16x8 trd(unsigned a0, unsigned a1) {
  s16x4 lo = __builtin_amdgcn_ds_read_tr16_b64_v4i16((LAS s16x4*)lds(a0));
  s16x4 hi = __builtin_amdgcn_ds_read_tr16_b64_v4i16((LAS s16x4*)lds(a1));
  return __builtin_shufflevector(lo, hi, 0, 1, 2, 3, 4, 5, 6, 7);
}
DI bf16x8 rd128(unsigned a) { return *(LAS bf16x8*)lds(a); }
DI f32x4 mfma(bf16x8 a, bf16x8 b, f32x4 c) { return __builtin_amdgcn_mfma_f32_16x16x32_bf16(a, b, c, 0, 0, 0); }
DI v4u srd(const void* p, unsigned bytes) { const u64 a = (u64)p; return v4u{(unsigned)a, (unsigned)(a >> 32) & 0xffffu, bytes, 0x00020000u}; }
template <int AUX> DI void dma(v4u r, unsigned ldsoff, unsigned voff, unsigned soff) {
  if (AUX == 0) asm volatile("s_mov_b32 m0, %0\n\ts_nop 0\n\tbuffer_load_dwordx4 %1, %2, %3 offen lds" ::"s"(ldsoff), "v"(voff), "s"(r), "s"(soff) : "memory");
  if (AUX == NT_) asm volatile("s_mov_b32 m0, %0\n\ts_nop 0\n\tbuffer_load_dwordx4 %1, %2, %3 offen nt lds" ::"s"(ldsoff), "v"(voff), "s"(r), "s"(soff) : "memory");
  if (AUX == SC1) asm volatile("s_mov_b32 m0, %0\n\ts_nop 0\n\tbuffer_load_dwordx4 %1, %2, %3 offen sc1 lds" ::"s"(ldsoff), "v"(voff), "s"(r), "s"(soff) : "memory");
}
template <int AUX> DI void st128(v4u v, rsrc_t r, unsigned off) { __builtin_amdgcn_raw_buffer_store_b128(v, r, off, 0, AUX); }
DI void touch(v4u& sink, v4u r, unsigned vo, unsigned so) {
  asm volatile("buffer_load_dwordx4 %0, %1, %2, %3 offen sc1" : "+v"(sink) : "v"(vo), "s"(r), "s"(so) : "memory");
}
DI float rowsum16(float v) {
  v += __builtin_bit_cast(float, __builtin_amdgcn_update_dpp(0, __builtin_bit_cast(int, v), 0x128, 0xf, 0xf, false));
  v += __builtin_bit_cast(float, __builtin_amdgcn_update_dpp(0, __builtin_bit_cast(int, v), 0x124, 0xf, 0xf, false));
  v += __builtin_bit_cast(float, __builtin_amdgcn_update_dpp(0, __builtin_bit_cast(int, v), 0x122, 0xf, 0xf, false));
  v += __builtin_bit_cast(float, __builtin_amdgcn_update_dpp(0, __builtin_bit_cast(int, v), 0x121, 0xf, 0xf, false));
  return v;
}
DI f32x2 cmul(f32x2 a, f32x2 b) {
  f32x2 t = a * b.xx, r;
  asm("v_pk_fma_f32 %0, %1, %2, %3 op_sel:[1,1,0] op_sel_hi:[0,1,1] neg_lo:[0,1,0]" : "=v"(r) : "v"(a), "v"(b), "v"(t));
  return r;
}
DI f32x2 cpow(float lr, float tr, float e) {
  float m = __builtin_amdgcn_exp2f(lr * e), th = __builtin_amdgcn_fractf(tr * e);
  return f32x2{m * __builtin_amdgcn_cosf(th), m * __builtin_amdgcn_sinf(th)};
}
DI v2u tag_of(void) { return *(LAS v2u*)lds(L_MISC); }
DI void put_flag(rsrc_t rw, unsigned off, v2u tag) { __builtin_amdgcn_raw_buffer_store_b64(tag, rw, off, 0, SC1); }
template <int R> DI void poll(rsrc_t rw, unsigned off, int n, v2u tag, int lane) {
#pragma unroll 1
  for (int spin = 0; spin < SPIN_MAX; ++spin) {
    bool ok = true;
#pragma unroll
    for (int r = 0; r < R; ++r) {
      int i = lane + 64 * r; i = i < n ? i : n - 1;
      v2u f = __builtin_amdgcn_raw_buffer_load_b64(rw, off + i * 8, 0, SC1);
      ok = ok && f.x == tag.x && f.y == tag.y;
    }
    if (__all(ok)) break;
    cfence();
  }
  cfence();
}
DI void lds_set(unsigned o) { cfence(); *(volatile LAS unsigned*)lds(o) = 1; }
DI void lds_wait(unsigned o) {
#pragma unroll 1
  for (int spin = 0; spin < 16 * SPIN_MAX && *(volatile LAS unsigned*)lds(o) == 0; ++spin) __builtin_amdgcn_s_sleep(1);
  cfence();
}
DI void arrive_publish(unsigned cnt, int n, rsrc_t rw, unsigned off, int lane) {
  unsigned old = 0;
  if (lane == 0) old = __hip_atomic_fetch_add((LAS unsigned*)lds(cnt), 1u, __ATOMIC_RELAXED, __HIP_MEMORY_SCOPE_WORKGROUP);
  old = __builtin_amdgcn_readfirstlane(old);
  if ((int)old == n - 1 && lane == 0) put_flag(rw, off, tag_of());
}

DI void stamp_(int i) {
  u64 c; asm volatile("s_memtime %0\n\ts_waitcnt lgkmcnt(0)" : "=s"(c) :: "memory");
  asm volatile("s_mov_b32 m0, %1\n\tv_writelane_b32 v255, %0, m0" :: "s"((unsigned)c), "s"(i) : "memory", "v255");
}
#define STAMP(i) stamp_(i)
#define BAR(i) bar()
#define DUMP() do { u64 t_ = __builtin_amdgcn_s_memrealtime(); unsigned sv_; asm volatile("v_mov_b32 %0, v255" : "=v"(sv_)); \
    unsigned* p_ = (unsigned*)((char*)ws + WS_TM + (blockIdx.x * 8 + wid) * 256); \
    if (lane < 40) p_[lane] = sv_; if (lane == 0) { ((u64*)p_)[24] = T0_; ((u64*)p_)[25] = t_; } } while (0)

struct G1Ld { unsigned vx0, vx1, vw0, vw1; };
DI G1Ld g1_lanes(int lane) {
  const int r8 = lane >> 3, sl = lane & 7;
  return G1Ld{(unsigned)(r8 * 1280 + ((sl ^ (r8 >> 1)) << 4)), (unsigned)(r8 * 1280 + ((sl ^ (r8 >> 1) ^ 4) << 4)),
              (unsigned)(r8 * 3072 + ((sl ^ (r8 & 2)) << 4)), (unsigned)(r8 * 3072 + ((sl ^ (r8 & 2) ^ 4) << 4))};
}
template <int NT> DI void g1_issue(v4u rx, v4u rg, v4u ru, const G1Ld& v, int lw, int s, unsigned lb, int row0, int ch) {
  const unsigned xso = (row0 + lw * 16 * NT) * 1280 + s * 128, wso = ch * 128 + (lw * 32 + s * 64) * 3072;
#pragma unroll
  for (int p = 0; p < 4; ++p) {
    dma<0>(rg, lb + XB + (lw * 4 + p) * 1024, (p & 1) ? v.vw1 : v.vw0, wso + p * 8 * 3072);
    dma<0>(ru, lb + XB + 8192 + (lw * 4 + p) * 1024, (p & 1) ? v.vw1 : v.vw0, wso + p * 8 * 3072);
  }
#pragma unroll
  for (int p = 0; p < 2 * NT; ++p) dma<0>(rx, lb + (lw * 2 * NT + p) * 1024, (p & 1) ? v.vx1 : v.vx0, xso + p * 8 * 1280);
}
DI unsigned nxt(unsigned so) { return so == 2 * STAGE ? 0 : so + STAGE; }
template <int NT, int S0> DI void g1_loader(v4u rx, v4u rg, v4u ru, int lw, int lane, int row0, int ch, int rot, unsigned so) {
  constexpr int NP = 8 + 2 * NT;
  const G1Ld v = g1_lanes(lane);
  if (S0 == 1) BAR(0);
#pragma unroll 1
  for (int s = S0; s < 10; ++s) {
    if (s >= S0 + 2) wait_vm<NP>();
    if (s >= S0 + 2) BAR(s - 2);
    g1_issue<NT>(rx, rg, ru, v, lw, s + rot >= 10 ? s + rot - 10 : s + rot, so, row0, ch);
    so = nxt(so);
  }
  wait_vm<NP>(); BAR(8);
}

DI void colsum(int k, int lane) {
  const int c = lane & 15, g = lane >> 4, q = (lane >> 2) & 3, p = lane & 3;
  const bf16x8 ones = {0x3f80, 0x3f80, 0x3f80, 0x3f80, 0x3f80, 0x3f80, 0x3f80, 0x3f80};
  f32x4 r[2];
#pragma unroll
  for (int a = 0; a < 2; ++a) {
    const unsigned ad = XB + (k >> 1) * 8192 + (8 * g + q) * 128 + (((4 * (k & 1) + 2 * a + (p >> 1)) ^ (q & 2) ^ ((g & 1) << 2)) << 4) + 8 * (p & 1);
    r[a] = mfma(trd(ad + 4096, ad + 4608), ones, mfma(trd(ad, ad + 512), ones, f32x4{0.f, 0.f, 0.f, 0.f}));
  }
  f32x4 v;
#pragma unroll
  for (int e = 0; e < 4; ++e) v[e] = c ? r[1][e] : r[0][e];
  if (c < 2) *(LAS f32x4*)lds(L_CSP + k * 128 + (16 * c + 4 * g) * 4) = v;
  wait_lg();
}

template <int NT> DI void g1_main(int wid, int lane, unsigned so, f32x4 (&acc)[4][NT]) {
  const int wa = wid & 1, wt = wid >> 1;
  const int c = lane & 15, g = lane >> 4, q = (lane >> 2) & 3, p = lane & 3;
  unsigned aA[2], aB[2];
#pragma unroll
  for (int a = 0; a < 2; ++a) aA[a] = XB + (8 * g + q) * 128 + (((4 * wa + 2 * a + (p >> 1)) ^ (q & 2) ^ ((g & 1) << 2)) << 4) + 8 * (p & 1);
#pragma unroll
  for (int ks = 0; ks < 2; ++ks) aB[ks] = (16 * NT * wt + c) * 128 + (((4 * ks + g) ^ (c >> 1)) << 4);
#pragma unroll
  for (int a = 0; a < 4; ++a)
#pragma unroll
    for (int t = 0; t < NT; ++t) acc[a][t] = f32x4{0.f, 0.f, 0.f, 0.f};
  bf16x8 fa[2][4], fb[2][NT];
  auto rd = [&](int buf, int ks, unsigned o) {
#pragma unroll
    for (int a = 0; a < 4; ++a) { unsigned ad = aA[a & 1] + o + (a >> 1) * 8192 + ks * 4096; fa[buf][a] = trd(ad, ad + 512); }
#pragma unroll
    for (int t = 0; t < NT; ++t) fb[buf][t] = rd128(aB[ks] + o + t * 2048);
  };
  auto mm = [&](int buf) {
#pragma unroll
    for (int t = 0; t < NT; ++t)
#pragma unroll
      for (int a = 0; a < 4; ++a) acc[a][t] = mfma(fa[buf][a], fb[buf][t], acc[a][t]);
  };
  auto ilv = [&]() {
    constexpr int NR = 8 + NT, NM = 4 * NT, NI = NR < NM ? NR : NM;
#pragma unroll
    for (int i = 0; i < NI; ++i) { __builtin_amdgcn_sched_group_barrier(0x008, 1, 0); __builtin_amdgcn_sched_group_barrier(0x100, 1, 0); }
    if (NM > NI) __builtin_amdgcn_sched_group_barrier(0x008, NM - NI, 0);
    __builtin_amdgcn_sched_barrier(0);
  };
  BAR(0);
  STAMP(2);
  rd(0, 0, so);
#pragma unroll 1
  for (int s = 0; s < 9; ++s) {
    rd(1, 1, so); mm(0); ilv();
    so = nxt(so);
    wait_lg();
    BAR(s + 1);
    __builtin_amdgcn_sched_barrier(0);
    rd(0, 0, so); mm(1); ilv();
  }
  rd(1, 1, so); mm(0); ilv();
  mm(1);
  __builtin_amdgcn_sched_barrier(0);
}
template <int NT> DI void g1_epilogue(rsrc_t rw, int wid, int lane, int t16_0, int ch, unsigned st_off, unsigned cs_off, const f32x4 (&acc)[4][NT]) {
  const int wa = wid & 1, wt = wid >> 1, c = lane & 15, g = lane >> 4;
  f32x4 cs[4];
#pragma unroll
  for (int a = 0; a < 4; ++a) cs[a] = *(LAS f32x4*)lds(cs_off + (a >> 1) * 256 + (32 * wa + 16 * (a & 1) + 4 * g) * 4);
  f32x4 st[NT];
#pragma unroll
  for (int t = 0; t < NT; ++t) st[t] = *(LAS f32x4*)lds(st_off + (16 * NT * wt + 16 * t + c) * 16);
#pragma unroll
  for (int t = 0; t < NT; ++t) {
    const float nm = st[t][0], qq = st[t][1], rl = st[t][2];
    v4u o;
#pragma unroll
    for (int a = 0; a < 2; ++a) {
      f32x4 tg = cs[a] * nm + acc[a][t], tu = cs[2 + a] * nm + acc[2 + a][t];
      f32x4 ar = tg * rl, e;
#pragma unroll
      for (int r = 0; r < 4; ++r) e[r] = __builtin_amdgcn_exp2f(ar[r]);
      f32x4 dn = e * qq + qq, pr = tg * tu;
#pragma unroll
      for (int r = 0; r < 4; ++r) dn[r] = __builtin_amdgcn_rcpf(dn[r]);
      pr *= dn;
      o[2 * a] = pk(pr[0], pr[1]); o[2 * a + 1] = pk(pr[2], pr[3]);
    }
    st128<SC1>(o, rw, lane * 16 + WS_H + ((t16_0 + NT * wt + t) * 48 + 2 * ch + wa) * 1024);
  }
}

template <int NM> DI void kgen(const unsigned (&w)[6], float dt, int lane, unsigned tb, unsigned done, f32x4 (&acc)[2][2]) {
  constexpr int RS = NM * 256 + 32, KS = NM * 4;
  float lr[NM], tr[NM]; f32x2 W[NM];
#pragma unroll
  for (int m = 0; m < NM; ++m) {
    float v[6];
#pragma unroll
    for (int i = 0; i < 6; ++i) v[i] = m == 0 ? blo(w[i]) : bhi(w[i]);
    lr[m] = dt * v[0] * 1.44269504f; tr[m] = dt * v[1] * 0.15915494f;
    const float br = dt * v[2], bi = dt * v[3];
    W[m] = f32x2{v[4] * br - v[5] * bi, -(v[4] * bi + v[5] * br)};
  }
  asm volatile("" : "+v"(lr[0]), "+v"(W[0]));
  STAMP(13);
  unsigned wa = tb + lane * 4 * NM, fa = tb + (lane & 15) * RS + (lane >> 4) * 16;
  asm volatile("" : "+v"(wa), "+v"(fa));
  auto put = [&](int row, const f32x2* z) {
    if (NM == 2) *(LAS v2u*)lds(wa + row * RS) = v2u{pk2(z[0]), pk2(z[NM - 1])};
    else *(LAS unsigned*)lds(wa + row * RS) = pk2(z[0]);
  };
  f32x2 c[8][NM], hi[NM], t[NM], c8[NM];
#pragma unroll
  for (int m = 0; m < NM; ++m) {
    c[0][m] = f32x2{1.f, 0.f}; c[1][m] = cpow(lr[m], tr[m], 8.f);
    c[2][m] = cmul(c[1][m], c[1][m]); c[3][m] = cmul(c[2][m], c[1][m]); c[4][m] = cmul(c[2][m], c[2][m]);
    c[5][m] = cmul(c[4][m], c[1][m]); c[6][m] = cmul(c[4][m], c[2][m]); c[7][m] = cmul(c[4][m], c[3][m]);
    c8[m] = cmul(c[4][m], c[4][m]); hi[m] = c8[m];
  }
  bf16x8 fb[2][KS];
#pragma unroll
  for (int j = 0; j < 8; ++j) put(j, c[j]);
#pragma unroll
  for (int a = 1; a < 4; ++a) {
    if (a == 2) {
#pragma unroll
      for (int s = 0; s < KS; ++s) fb[0][s] = rd128(fa + s * 64);
    }
    put((8 * a) & 15, hi);
#pragma unroll
    for (int j = 1; j < 8; ++j) {
#pragma unroll
      for (int m = 0; m < NM; ++m) t[m] = cmul(hi[m], c[j][m]);
      put((8 * a + j) & 15, t);
    }
    if (a < 3) {
#pragma unroll
      for (int m = 0; m < NM; ++m) hi[m] = cmul(hi[m], c8[m]);
    }
  }
#pragma unroll
  for (int s = 0; s < KS; ++s) fb[1][s] = rd128(fa + s * 64);
  STAMP(14);
  f32x2 z1[NM], zb[NM], r0[NM], r4[NM];
#pragma unroll
  for (int m = 0; m < NM; ++m) { z1[m] = cpow(lr[m], -tr[m], 1.f); zb[m] = cpow(lr[m], -tr[m], 256.f); t[m] = W[m]; }
#pragma unroll
  for (int b = 0; b < 8; ++b) {
    put(b, t);
    if ((b & 1) == 0) {
      f32x2 u[NM];
#pragma unroll
      for (int m = 0; m < NM; ++m) { u[m] = cmul(t[m], zb[m]); if (b == 0) r0[m] = u[m]; if (b == 4) r4[m] = u[m]; }
      put(8 + b / 2, u);
    }
    if (b < 7) {
#pragma unroll
      for (int m = 0; m < NM; ++m) t[m] = cmul(t[m], z1[m]);
    }
  }
#pragma unroll
  for (int i = 0; i < 2; ++i) {
#pragma unroll
    for (int m = 0; m < NM; ++m) { r0[m] = cmul(r0[m], zb[m]); r4[m] = cmul(r4[m], zb[m]); }
    put(12 + 2 * i, r0); put(13 + 2 * i, r4);
  }
  bf16x8 fr[2][KS];
#pragma unroll
  for (int s = 0; s < KS; ++s) fr[0][s] = rd128(fa + s * 64);
#pragma unroll
  for (int i = 0; i < 5; ++i) {
#pragma unroll
    for (int m = 0; m < NM; ++m) r0[m] = cmul(r0[m], zb[m]);
    put(i, r0);
  }
  if (NM == 2) lds_set(done);
#pragma unroll
  for (int s = 0; s < KS; ++s) fr[1][s] = rd128(fa + s * 64);
  __builtin_amdgcn_sched_barrier(0);
  STAMP(15);
#pragma unroll
  for (int i = 0; i < 4; ++i) acc[i >> 1][i & 1] = f32x4{0.f, 0.f, 0.f, 0.f};
#pragma unroll
  for (int rt = 0; rt < 2; ++rt)
#pragma unroll
    for (int s = 0; s < KS; ++s) { acc[rt][0] = mfma(fr[rt][s], fb[0][s], acc[rt][0]); acc[rt][1] = mfma(fr[rt][s], fb[1][s], acc[rt][1]); }
}
struct LnOut { s16x4 tr[2][10]; v2u st; };
DI void ln8(rsrc_t rx, int tb, int lane, unsigned tile, LnOut& out) {
  const int r4 = lane >> 4, i = lane & 15;
  v4u raw[2][5];
#pragma unroll
  for (int h = 0; h < 2; ++h)
#pragma unroll
    for (int k = 0; k < 5; ++k) raw[h][k] = __builtin_amdgcn_raw_buffer_load_b128(rx, r4 * 1280 + i * 16 + k * 256, (tb * 8 + h * 4) * 1280, 0);
  const unsigned ta = tile + ((lane >> 2) & 3) * LN_RS + (16 * r4 + 4 * (lane & 3)) * 2;
#pragma unroll
  for (int h = 0; h < 2; ++h) {
    if (h == 0) { asm volatile("" : "+v"(raw[0][4])); STAMP(13); asm volatile("" : "+v"(raw[1][4])); STAMP(15); } else STAMP(14);
    float s1 = 0.f, s2 = 0.f, s3 = 0.f, s4 = 0.f;
    const bf16x2 one2 = __builtin_bit_cast(bf16x2, 0x3f803f80u);
#pragma unroll
    for (int k = 0; k < 10; ++k) {
      const unsigned w0 = raw[h][k >> 1][2 * (k & 1)], w1 = raw[h][k >> 1][2 * (k & 1) + 1];
      const bf16x2 p0 = __builtin_bit_cast(bf16x2, w0), p1 = __builtin_bit_cast(bf16x2, w1);
      s1 = __builtin_amdgcn_fdot2_f32_bf16(p0, one2, s1, false); s2 = __builtin_amdgcn_fdot2_f32_bf16(p0, p0, s2, false);
      s3 = __builtin_amdgcn_fdot2_f32_bf16(p1, one2, s3, false); s4 = __builtin_amdgcn_fdot2_f32_bf16(p1, p1, s4, false);
    }
    const float mean = rowsum16(s1 + s3) * (1.f / D);
    const float var = rbf(fmaxf(rowsum16(s2 + s4) * (1.f / D) - mean * mean, 0.f));
    const float m = rbf(mean), den = rbf(sqrtf(rbf(var + 1e-5f)));
    const float ri = __builtin_amdgcn_rcpf(den), nb = -m * ri;
    if (h == 0 || (i & 1)) out.st = v2u{__float_as_uint(m), __float_as_uint(den)};
#pragma unroll
    for (int k = 0; k < 5; ++k) {
      v4u o;
#pragma unroll
      for (int e = 0; e < 4; ++e) { const unsigned w = raw[h][k][e]; o[e] = pk2(f32x2{blo(w), bhi(w)} * ri + nb); }
      *(LAS v4u*)lds(tile + r4 * LN_RS + i * 16 + k * 256) = o;
    }
    cfence();
#pragma unroll
    for (int it = 0; it < 10; ++it) out.tr[h][it] = __builtin_amdgcn_ds_read_tr16_b64_v4i16((LAS s16x4*)lds(ta + it * 128));
  }
}
DI void ln8_store(rsrc_t rw, int tb, int lane, const LnOut& in) {
  if ((lane & 15) < 2) __builtin_amdgcn_raw_buffer_store_b64(in.st, rw, ((lane & 1) * 4 + (lane >> 4)) * 8, WS_STATS + tb * 128, SC1);
#pragma unroll
  for (int it = 0; it < 10; ++it) {
    bf16x8 o = __builtin_shufflevector(in.tr[0][it], in.tr[1][it], 0, 1, 2, 3, 4, 5, 6, 7);
    st128<SC1>(__builtin_bit_cast(v4u, o), rw, lane * 16 + it * 1024 + WS_U8 + tb * 10240);
  }
}

DI unsigned prm(unsigned hi, unsigned lo, unsigned sel) { return __builtin_amdgcn_perm(hi, lo, sel); }
DI unsigned colo(int col) { return (col & 15) * 16 + ((col >> 4) & 1) * 8; }
template <int S> DI v4u kchunk(unsigned raw, int lane) {
  const int i = lane & 31, up = lane >> 5;
  if (S == 0) {
    const v2u a0 = *(LAS v2u*)lds(raw + colo(i) + 512 * up);
    const v2u a1 = *(LAS v2u*)lds(raw + (up ? (i == 31 ? 768 : 512 + colo(i + 1)) : 256 + colo(i)));
    const unsigned s0 = up ? 0x03020504u : 0x05040706u, s1 = up ? 0x01000100u : 0x05040706u, s2 = up ? 0x03020504u : 0x05040706u,
                   s3 = up ? 0x01000100u : 0x01000302u;
    return v4u{prm(up ? a1.x : a1.y, a0.y, s0), prm(a1.x, a0.y, s1), prm(a0.y, a0.x, s2), prm(0, a0.x, s3)};
  } else {
    const unsigned b0 = *(LAS unsigned*)lds(raw + 768 + colo(i) + 4 * up);
    const unsigned b1 = *(LAS unsigned*)lds(raw + (lane == 63 ? 1024 : 768 + colo(i + 1) + 4 * ((lane + 1) >> 5)));
    return v4u{prm(0, b1, 0x01000100), prm(0, b0, 0x03020302), prm(0, b0, 0x01000302), prm(0, b0, 0x01000100)};
  }
}
DI unsigned vmov(unsigned x) { unsigned r; asm("v_mov_b32 %0, %1" : "=v"(r) : "v"(x)); return r; }
DI void hwrite(v4u x, v4u y, unsigned hb, int pos) {
  v8u w = __builtin_shufflevector(x, y, 0, 1, 2, 3, 4, 5, 6, 7), e, o0, o1;
  asm volatile("" : "+v"(w));
#pragma unroll
  for (int k = 0; k < 6; ++k) {
    e[k] = vmov(w[k + 1]);
    o0[k] = __builtin_amdgcn_alignbit(w[k + 1], w[k], 16);
    o1[k] = __builtin_amdgcn_alignbit(w[k + 2], w[k + 1], 16);
    asm volatile("" : "+v"(o1[k]));
  }
  asm volatile("" : "+v"(e), "+v"(o0), "+v"(o1));
  const unsigned a = hb + pos * 16;
  *(LAS v4u*)lds(a + 7 * H_CS) = __builtin_shufflevector(w, w, 0, 1, 2, 3);
  *(LAS v4u*)lds(a + 3 * H_CS) = __builtin_shufflevector(w, w, 2, 3, 4, 5);
  *(LAS v4u*)lds(a + 5 * H_CS) = __builtin_shufflevector(e, e, 0, 1, 2, 3);
  *(LAS v4u*)lds(a + 1 * H_CS) = __builtin_shufflevector(e, e, 2, 3, 4, 5);
  *(LAS v4u*)lds(a + 6 * H_CS) = __builtin_shufflevector(o0, o0, 0, 1, 2, 3);
  *(LAS v4u*)lds(a + 2 * H_CS) = __builtin_shufflevector(o0, o0, 2, 3, 4, 5);
  *(LAS v4u*)lds(a + 4 * H_CS) = __builtin_shufflevector(o1, o1, 0, 1, 2, 3);
  *(LAS v4u*)lds(a + 0 * H_CS) = __builtin_shufflevector(o1, o1, 2, 3, 4, 5);
}
template <int S> DI void hbuild(unsigned raw, unsigned hb, int lane, v4u& carry) {
  const v4u x = kchunk<S>(raw, lane);
  v4u y;
#pragma unroll
  for (int k = 0; k < 4; ++k) {
    y[k] = __builtin_amdgcn_update_dpp(carry[k], x[k], 0x138, 0xf, 0xf, false);
    carry[k] = __builtin_amdgcn_readlane(x[k], 63);
  }
  hwrite(x, y, hb, 1 + 64 * S + lane);
}
DI void htail(unsigned raw, unsigned hb, unsigned k3, int lane, v4u carry) {
  const unsigned kk = *(LAS uint16_t*)lds(raw + 1024);
  const int n0 = 128 - 2 * lane, n1 = n0 - 1;
  const unsigned c0 = *(LAS uint16_t*)lds(raw + 1024 + (n0 == 128 ? 256 : colo(n0) + 2 * (n0 >> 5)));
  const unsigned c1 = *(LAS uint16_t*)lds(raw + 1024 + colo(n1) + 2 * (n1 >> 5));
  *(LAS unsigned*)lds(k3 + lane * 4) = c0 | (c1 << 16);
  if (lane < 16) *(LAS unsigned*)lds(k3 + 256 + lane * 4) = 0;
  const unsigned k2 = kk | (kk << 16), m0 = lane == 0 ? ~0u : 0u, m1 = lane == 1 ? ~0u : 0u;
  if (lane < 3) hwrite(v4u{0, (kk << 16) & m0, k2 & m0, k2 & m0},
                       v4u{carry[0] & m0, (carry[1] & m0) | ((kk << 16) & m1), (carry[2] & m0) | (k2 & m1), (carry[3] & m0) | (k2 & m1)}, hb, 129 + lane);
}

DI void conv(rsrc_t rw, int cw, int d, int lane) {
  const unsigned hb = L_H + cw * H_SZ, ub = L_U + cw * U_SZ;
  const int c = lane & 15, g = lane >> 4;
  unsigned aa = hb + (c & 7) * H_CS + (3 + (c >> 3) - g) * 16, ab = ub + 416 + 32 * c + 16 * g;
  f32x4 acc[8] = {};
  bf16x8 fa[2][4], fb[2][8];
  auto rd = [&](int buf, int oa, int ob) {
#pragma unroll
    for (int k = 0; k < 4; ++k) fa[buf][k] = rd128(aa + oa + k * 512);
#pragma unroll
    for (int e = 7; e >= 0; --e) fb[buf][e] = rd128(ab + ob + e * 512);
  };
  auto mm = [&](int buf) {
#pragma unroll
    for (int k = 0; k < 4; ++k)
#pragma unroll
      for (int e = 7 - k; e >= 0; --e) acc[e + k] = mfma(fa[buf][k], fb[buf][e], acc[e + k]);
  };
  auto ilv = [&]() {
#pragma unroll
    for (int i = 0; i < 12; ++i) { __builtin_amdgcn_sched_group_barrier(0x008, 1, 0); __builtin_amdgcn_sched_group_barrier(0x100, 1, 0); }
    __builtin_amdgcn_sched_group_barrier(0x008, 14, 0);
    __builtin_amdgcn_sched_barrier(0);
  };
  asm volatile("" : "+v"(aa), "+v"(ab));
  rd(0, 0, 64);
  {
    const bf16x8 fx = rd128(aa + 2048);
    bf16x8 ue[4], kt[4];
#pragma unroll
    for (int f = 0; f < 4; ++f) ue[f] = rd128(ab + 48 + f * 512);
    const unsigned k3 = L_K3 + cw * 320 + (254 - 2 * c + 8 * g) * 2;
#pragma unroll
    for (int f = 0; f < 4; ++f) {
      v4u t;
#pragma unroll
      for (int e = 0; e < 4; ++e) t[e] = *(LAS unsigned*)lds(k3 - (4 + f) * 64 + e * 4);
      kt[f] = __builtin_bit_cast(bf16x8, t);
    }
    bf16x8 b9, b7;
#pragma unroll
    for (int jj = 0; jj < 8; ++jj) {
      const int dl = 8 * g + jj - 8 - c;
      b9[jj] = (dl >= -4 && dl <= 4) ? 0x3f80 : 0; b7[jj] = (dl >= -3 && dl <= 3) ? 0x3f80 : 0;
    }
#pragma unroll
    for (int f = 0; f < 4; ++f) {
      acc[4 + f] = mfma(fx, fb[0][f], acc[4 + f]);
      const f32x4 ve = mfma(ue[f], b9, f32x4{0.f, 0.f, 0.f, 0.f}), vo = mfma(fb[0][f], b7, f32x4{0.f, 0.f, 0.f, 0.f});
      const bf16x8 vf = __builtin_bit_cast(bf16x8, v4u{pk(ve[0], vo[0]), pk(ve[1], vo[1]), pk(ve[2], vo[2]), pk(ve[3], vo[3])});
#pragma unroll
      for (int n = 4 + f; n < 8; ++n) acc[n] = mfma(vf, kt[n - f - 4], acc[n]);
    }
    __builtin_amdgcn_sched_barrier(0);
  }
  STAMP(8);
#pragma unroll 1
  for (int r = 0; r < 3; ++r) {
    rd(1, 64, 0); mm(0); ilv();
    aa += 128; ab -= 128;
    asm volatile("" : "+v"(aa), "+v"(ab));
    rd(0, 0, 64); mm(1); ilv();
  }
  rd(1, 64, 0); mm(0); ilv();
  mm(1);
#pragma unroll
  for (int n = 0; n < 8; ++n)
    __builtin_amdgcn_raw_buffer_store_b64(v2u{pk(acc[n][0], acc[n][1]), pk(acc[n][2], acc[n][3])}, rw, 32 * c + 8 * g + 512 * n, WS_AT + d * 4096, SC1);
}
#endif

__global__ __launch_bounds__(512, 2) void fused_s4_ffn(
    const uint16_t* __restrict__ x, const uint16_t* __restrict__ Are, const uint16_t* __restrict__ Aim, const uint16_t* __restrict__ Bre,
    const uint16_t* __restrict__ Bim, const uint16_t* __restrict__ Cre, const uint16_t* __restrict__ Cim, const uint16_t* __restrict__ logdt,
    const uint16_t* __restrict__ dskip, const uint16_t* __restrict__ wg, const uint16_t* __restrict__ wu, const uint16_t* __restrict__ wd,
    uint16_t* __restrict__ out, void* ws) {
#if __HIP_DEVICE_COMPILE__
  const int wid = __builtin_amdgcn_readfirstlane(threadIdx.x >> 6), lane = threadIdx.x & 63;
  const int b = blockIdx.x, xcd = b & 7, j = b >> 3;
  const bool normal = j < 30;
  const int rl = j % 5, ti = 5 * (xcd & 1) + rl;
  const int mm_ = 2 * (xcd >> 1) + (xcd & 1), par = j - 30;
  const int ch = normal ? 6 * (xcd >> 1) + j / 5 : 3 * mm_ + 2 * par;
  const int ch2 = 3 * mm_ + 1;
  const int nch = normal ? (j < 20 ? 3 : 2) : 0;
  const int d0 = 80 * xcd + (j < 20 ? 3 * j : 2 * j + 20);
  const int t64 = 4 * xcd + (j >> 3), dj = j & 7;
  const int rot = XROT ? (ti + (5 * (j / 5) + 1) / 3) % 10 : ti;
  const int rot2 = (3 * (j >> 3) + (3 * dj + 1) / 2) % 12;
  auto kst = [&](int s) { return s + rot2 >= 12 ? s + rot2 - 12 : s + rot2; };
  auto pbase = [](int w) -> unsigned { return w < 2 ? w * 15360 : L_P2 + (w - 2) * 15360; };
  const rsrc_t rw = mkr(ws, WS_BYTES), rx = mkr(x, L * D * 2);
  const u64 T0_ = __builtin_amdgcn_s_memrealtime();
  STAMP(0);
  if (wid == 6) {
    u64 pc; asm volatile("s_getpc_b64 %0" : "=s"(pc));
    dma<0>(srd((const void*)pc, CODE_BYTES), L_DUMMY, lane * 16, j * 1024);
    dma<0>(srd((const void*)pc, CODE_BYTES), L_DUMMY, lane * 16, 32768 + j * 1024);
  }
  if (wid == 7) { *(LAS v4u*)lds(L_MISC + 16 + (lane & 3) * 16) = v4u{0, 0, 0, 0}; wait_lg(); }
  asm volatile("" ::"s"(x), "s"(Are), "s"(Aim), "s"(Bre), "s"(Bim), "s"(Cre), "s"(Cim), "s"(logdt), "s"(dskip), "s"(wg), "s"(wu), "s"(wd), "s"(out), "s"(ws));
  unsigned pw[6], ldw_ = 0, dsw_ = 0;
  const int kd = d0 + (wid & 3);
  if ((wid & 3) < nch) {
    const uint16_t* const par6[6] = {Are, Aim, Bre, Bim, Cre, Cim};
    const unsigned vo = wid >= 4 ? lane * 4 : 256 + (lane >> 1) * 4;
#pragma unroll
    for (int i = 0; i < 6; ++i) pw[i] = __builtin_amdgcn_raw_buffer_load_b32(mkr(par6[i], D * NS * 2), vo, kd * NS * 2, NT_);
    ldw_ = ((const __attribute__((address_space(4))) unsigned*)logdt)[kd >> 1];
    dsw_ = ((const __attribute__((address_space(4))) unsigned*)dskip)[kd >> 1];
  }
  cfence(); bar(); cfence();
  STAMP(11);
  const float dt = rbf(expf((kd & 1) ? bhi(ldw_) : blo(ldw_)));
  const v4u sg = srd(wg, D * F * 2), su = srd(wu, D * F * 2), sx = srd(x, L * D * 2), sw = srd(ws, WS_BYTES);

  if (wid < 4) {
    if (wid < nch) {
      f32x4 ka[2][2];
#pragma unroll
      for (int i = 0; i < 6; ++i) pw[i] = (lane & 1) ? pw[i] >> 16 : pw[i];
      kgen<1>(pw, dt, lane, L_KG + wid * KG_SZ + 16 * 544, 0, ka);
#pragma unroll
      for (int i = 0; i < 4; ++i) *(LAS f32x4*)lds(L_KG + wid * KG_SZ + 16 * 544 + i * 1024 + lane * 16) = ka[i >> 1][i & 1];
      wait_lg();
      lds_set(L_MISC + 16 + wid * 4);
      STAMP(12);
      if (wid < 2) {
        const G1Ld v = g1_lanes(lane);
        lds_wait(L_MISC + 68); lds_wait(L_MISC + 72);
        if (nch == 3) lds_wait(L_MISC + 76);
        STAMP(10);
        g1_issue<6>(sx, sg, su, v, wid, rot, 0, 192 * ti, ch);
        wait_vm<0>();
      }
    } else if (wid == 3 && normal) {
      const G1Ld v = g1_lanes(lane);
      v4u sink = {0, 0, 0, 0};
#pragma unroll 1
      for (int p = 0; p < 8; ++p) {
        touch(sink, sg, v.vw0, ch * 128 + (rot * 64 + p * 8) * 3072);
        touch(sink, su, v.vw0, ch * 128 + (rot * 64 + p * 8) * 3072);
        const int sl = rot + (p >> 2);
        touch(sink, sx, v.vx0, (192 * ti + 32 * (j / 5) + 8 * (p & 3)) * 1280 + (sl == 10 ? 0 : sl) * 128);
      }
      const v4u sd = srd(wd, F * D * 2);
#pragma unroll 1
      for (int p = 0; p < 8; ++p) dma<NT_>(sd, L_WDU + p * 1024, (lane >> 3) * 1280 + (lane & 7) * 16, (ch * 64 + p * 8) * 1280 + ti * 128);
      asm volatile("s_waitcnt vmcnt(0)" : "+v"(sink)::"memory");
    }
    wait_lg();
    STAMP(1);
    if (normal) {
      f32x4 acc[4][6];
      g1_main<6>(wid, lane, 0, acc);
      BAR(12);
      STAMP(3);
      g1_epilogue<6>(rw, wid, lane, 12 * ti, ch, L_ST, L_CS, acc);
    } else {
      f32x4 acc4[4][4], acc2[4][2];
      g1_main<4>(wid, lane, 0, acc4);
      g1_main<2>(wid, lane, STAGE, acc2);
      BAR(12);
      STAMP(3);
      g1_epilogue<4>(rw, wid, lane, 120, ch, L_ST, L_CS, acc4);
      g1_epilogue<2>(rw, wid, lane, 120 + 4 * par, ch2, L_ST + par * 1024, L_CS + 512, acc2);
    }
    STAMP(4);
    lds_set(L_MISC + 48 + wid * 4);
    wait_vm<0>();
    __builtin_amdgcn_s_waitcnt(0x0F70);
    STAMP(5);
    arrive_publish(L_MISC + 8, 4, rw, WS_F1 + (normal ? ti * 24 + ch : 240 + 2 * mm_ + par) * 8, lane);
    const unsigned wso = WS_WDT + (12 * wid * 40 + 5 * dj) * 1024, hso = WS_H + (4 * t64 * 48 + 12 * wid) * 1024;
    const unsigned vo0 = lane * 16, vo1 = lane * 16 + 4096, pw = pbase(wid) + lane * 16;
    v4u wr[5][5], hr[3][4];
#pragma unroll
    for (int i = 0; i < 25; ++i) asm volatile("" : "=v"(wr[i / 5][i % 5]));
#pragma unroll
    for (int i = 0; i < 12; ++i) asm volatile("" : "=v"(hr[i / 4][i % 4]));
    auto ldw = [&](int s) {
      const unsigned so = wso + kst(s) * 40960;
      asm volatile("buffer_load_dwordx4 %0, %5, %7, %8 offen sc1\n\tbuffer_load_dwordx4 %1, %5, %7, %8 offen offset:1024 sc1\n\t"
                   "buffer_load_dwordx4 %2, %5, %7, %8 offen offset:2048 sc1\n\tbuffer_load_dwordx4 %3, %5, %7, %8 offen offset:3072 sc1\n\t"
                   "buffer_load_dwordx4 %4, %6, %7, %8 offen sc1"
                   : "+v"(wr[s % 5][0]), "+v"(wr[s % 5][1]), "+v"(wr[s % 5][2]), "+v"(wr[s % 5][3]), "+v"(wr[s % 5][4])
                   : "v"(vo0), "v"(vo1), "s"(sw), "s"(so) : "memory");
    };
    auto ldh = [&](int s) {
      const unsigned so = hso + kst(s) * 1024;
      asm volatile("buffer_load_dwordx4 %0, %4, %5, %6 offen sc1\n\tbuffer_load_dwordx4 %1, %4, %5, %7 offen sc1\n\t"
                   "buffer_load_dwordx4 %2, %4, %5, %8 offen sc1\n\tbuffer_load_dwordx4 %3, %4, %5, %9 offen sc1"
                   : "+v"(hr[s % 3][0]), "+v"(hr[s % 3][1]), "+v"(hr[s % 3][2]), "+v"(hr[s % 3][3])
                   : "v"(vo0), "s"(sw), "s"(so), "s"(so + 49152), "s"(so + 98304), "s"(so + 147456) : "memory");
    };
#pragma unroll
    for (int s = 0; s < 5; ++s) ldw(s);
    lds_wait(L_MISC + 32);
    STAMP(6);
#pragma unroll
    for (int s = 0; s < 3; ++s) ldh(s);
    f32x4 acc[4][5];
#pragma unroll
    for (int s = 0; s < 12; ++s) {
      constexpr int YN[12] = {8, 13, 18, 23, 23, 18, 13, 8, 8, 8, 4, 0};
      asm volatile("s_waitcnt vmcnt(%9)"
                   : "+v"(hr[s % 3][0]), "+v"(hr[s % 3][1]), "+v"(hr[s % 3][2]), "+v"(hr[s % 3][3]), "+v"(wr[s % 5][0]), "+v"(wr[s % 5][1]),
                     "+v"(wr[s % 5][2]), "+v"(wr[s % 5][3]), "+v"(wr[s % 5][4])
                   : "i"(YN[s]) : "memory");
#pragma unroll
      for (int t = 0; t < 4; ++t)
#pragma unroll
        for (int dd = 0; dd < 5; ++dd)
          acc[t][dd] = mfma(__builtin_bit_cast(bf16x8, wr[s % 5][dd]), __builtin_bit_cast(bf16x8, hr[s % 3][t]), s == 0 ? f32x4{0.f, 0.f, 0.f, 0.f} : acc[t][dd]);
      __builtin_amdgcn_sched_barrier(0);
      if (s == G2SIG && wid == 0) lds_set(L_MISC + 36);
      if (s + 3 < 12) ldh(s + 3);
      if (s < 4) ldw(s + 5);
      if (s >= 4 && s < 7) {
#pragma unroll
        for (int t = 0; t < 5; ++t) wr[s % 5][t] = *(LAS v4u*)lds(pw + (s - 4) * 5120 + t * 1024);
      }
      __builtin_amdgcn_sched_barrier(0);
    }
    STAMP(7);
    const int c = lane & 15, g = lane >> 4;
    const unsigned oo = ((4 * t64 + wid) * 16 + c) * 1280 + (80 * dj + 4 * g) * 2;
    v2u xr[5];
#pragma unroll
    for (int dd = 0; dd < 5; ++dd) xr[dd] = __builtin_amdgcn_raw_buffer_load_b64(rx, oo + dd * 32, 0, 0);
    f32x4 own[5];
#pragma unroll
    for (int t = 0; t < 4; ++t) {
      if (t == wid) {
#pragma unroll
        for (int dd = 0; dd < 5; ++dd) own[dd] = acc[t][dd];
      } else {
        const unsigned o = pw + (t > wid ? t - 1 : t) * 5120;
#pragma unroll
        for (int dd = 0; dd < 5; ++dd) *(LAS f32x4*)lds(o + dd * 1024) = acc[t][dd];
      }
    }
    wait_lg();
    BAR(11);
    STAMP(8);
#pragma unroll
    for (int k = 1; k < 4; ++k) {
      const int w = (wid + k) & 3;
      const unsigned o = pbase(w) + lane * 16 + (wid > w ? wid - 1 : wid) * 5120;
#pragma unroll
      for (int dd = 0; dd < 5; ++dd) own[dd] += *(LAS f32x4*)lds(o + dd * 1024);
    }
    const rsrc_t ro = mkr(out, L * D * 2);
    const unsigned at = L_AT + (4 * g + ((lane >> 2) & 3)) * 128 + (16 * wid + 4 * (lane & 3)) * 2;
#pragma unroll
    for (int dd = 0; dd < 5; ++dd) {
      const v2u a = __builtin_bit_cast(v2u, __builtin_amdgcn_ds_read_tr16_b64_v4i16((LAS s16x4*)lds(at + dd * 2048)));
      const float o0 = own[dd][0] + blo(a.x) + blo(xr[dd].x), o1 = own[dd][1] + bhi(a.x) + bhi(xr[dd].x);
      const float o2 = own[dd][2] + blo(a.y) + blo(xr[dd].y), o3 = own[dd][3] + bhi(a.y) + bhi(xr[dd].y);
      __builtin_amdgcn_raw_buffer_store_b64(v2u{pk(o0, o1), pk(o2, o3)}, ro, oo + dd * 32, 0, 0);
    }
    STAMP(9);
  } else if (wid < 7) {
    const int cw = wid - 4;
    v4u carry = {0, 0, 0, 0}, sink = {0, 0, 0, 0}, kraw[2] = {};
    auto warm_wd = [&]() {
#pragma unroll 1
      for (int i = 0; i < 6; ++i) {
        const int w = 2 * cw + (i >= 3), s = i >= 3 ? i - 3 : i;
        const unsigned so = WS_WDT + ((12 * w + kst(9 + s)) * 40 + 5 * dj) * 1024;
#pragma unroll
        for (int t = 0; t < 5; ++t) touch(sink, sw, lane * 16, so + t * 1024);
      }
      wait_vm<30>();
    };
    if (normal) {
      LnOut lo2;
      const bool ln2 = cw == 2 && (j == 20 || j == 21);
      if (cw < nch) {
        f32x4 ka[2][2];
        kgen<2>(pw, dt, lane, L_KG + cw * KG_SZ, L_MISC + 68 + cw * 4, ka);
        STAMP(12);
        lds_wait(L_MISC + 16 + cw * 4);
#pragma unroll
        for (int i = 0; i < 4; ++i) ka[i >> 1][i & 1] += *(LAS f32x4*)lds(L_KG + cw * KG_SZ + 16 * 544 + i * 1024 + lane * 16);
        if (lane == 0) ka[0][0][0] += (kd & 1) ? bhi(dsw_) : blo(dsw_);
#pragma unroll
        for (int rt = 0; rt < 2; ++rt)
          kraw[rt] = v4u{pk(ka[rt][0][0], ka[rt][0][1]), pk(ka[rt][0][2], ka[rt][0][3]), pk(ka[rt][1][0], ka[rt][1][1]), pk(ka[rt][1][2], ka[rt][1][3])};
      } else if (ln2) {
        ln8(rx, 240 + 2 * xcd + j - 20, lane, L_KG + 2 * KG_SZ, lo2);
      }
      wait_lg();
      STAMP(1);
      if (cw < 2) {
        g1_loader<6, 1>(sx, sg, su, cw, lane, 192 * ti, ch, rot, STAGE);
        warm_wd(); BAR(9);
      } else {
        BAR(0);
        colsum(0, lane); colsum(1, lane);
        BAR(1);
        if (ln2) ln8_store(rw, 240 + 2 * xcd + j - 20, lane, lo2);
        BAR(2);
        wait_vm<0>();
#pragma unroll 1
        for (int k = 3; k < 10; ++k) BAR(k);
      }
      if (cw < nch) {
        const unsigned hb = L_H + cw * H_SZ;
        __builtin_amdgcn_s_sleep(4);
#pragma unroll
        for (int rt = 0; rt < 2; ++rt) *(LAS v4u*)lds(L_RAW + cw * 2048 + rt * 1024 + lane * 16) = kraw[rt];
        if (lane < 9) *(LAS v4u*)lds(hb + lane * H_CS) = v4u{0, 0, 0, 0};
        hbuild<0>(L_RAW + cw * 2048, hb, lane, carry);
      }
      BAR(12);
      if (cw < nch) {
        hbuild<1>(L_RAW + cw * 2048, L_H + cw * H_SZ, lane, carry);
        htail(L_RAW + cw * 2048, L_H + cw * H_SZ, L_K3 + cw * 320, lane, carry);
      }
    } else if (cw < 2) {
      g1_loader<4, 0>(sx, sg, su, cw, lane, 1920, ch, 0, 0);
      wait_vm<0>(); BAR(9);
      g1_loader<2, 0>(sx, sg, su, cw, lane, 1920 + 64 * par, ch2, 0, STAGE);
      warm_wd(); BAR(9);
      BAR(12);
    } else {
#pragma unroll 1
      for (int s = 0; s < 21; ++s) bar();
    }
    asm volatile("s_waitcnt vmcnt(0)" : "+v"(sink)::"memory");
    STAMP(4);
    if (cw < nch) {
      lds_wait(L_MISC + 48 + cw * 4);
      lds_wait(L_MISC + 64);
      STAMP(5);
      __builtin_amdgcn_s_setprio(2);
      conv(rw, cw, d0 + cw, lane);
      STAMP(6);
      wait_vm<0>();
      arrive_publish(L_MISC + 12, nch, rw, WS_F2 + (xcd * 32 + j) * 8, lane);
    }
    STAMP(7);
    BAR(11);
  } else {
    const v2u e = __builtin_amdgcn_raw_buffer_load_b64(rw, WS_EPOCH, 0, SC1);
    const int tb = 120 * (xcd & 1) + 30 * (xcd >> 1) + j;
    LnOut lo;
    if (normal) ln8(rx, tb, lane, L_LN, lo);
    const u64 tg = (((u64)e.y << 32) | e.x) + 0x9E3779B97F4A7C15ull;
    const v2u tag = {(unsigned)tg, (unsigned)(tg >> 32)};
    if (lane == 0) *(LAS v4u*)lds(L_MISC) = v4u{tag.x, tag.y, 0, 0};
    wait_lg();
    STAMP(1);
    BAR(0);
    if (normal) { colsum(2, lane); colsum(3, lane); }
    BAR(1);
    if (normal) {
      ln8_store(rw, tb, lane, lo);
      if (lane < 32) st128<SC1>(*(LAS v4u*)lds(L_CSP + lane * 16), rw, lane * 16 + WS_CSP + (ch * 10 + ti) * 512);
      const unsigned a0 = L_WDU + (4 * (lane >> 4) + ((lane >> 2) & 3)) * 128 + 8 * (lane & 3);
#pragma unroll
      for (int p = 0; p < 8; ++p) {
        const unsigned a = a0 + (p >> 2) * 4096 + (p & 3) * 32;
        st128<SC1>(__builtin_bit_cast(v4u, trd(a, a + 2048)), rw, lane * 16 + WS_WDT + ((2 * ch + (p >> 2)) * 40 + 4 * ti + (p & 3)) * 1024);
      }
    }
    BAR(2); BAR(3);
    wait_vm<0>();
    if (lane == 0) put_flag(rw, WS_F0 + b * 8, tag);
    BAR(4); BAR(5); BAR(6);
    if (!normal) {
#pragma unroll 1
      for (int s = 0; s < 10; ++s) bar();
    }
    poll<4>(rw, WS_F0, 256, tag, lane);
    STAMP(2);
    if (b == 0 && lane == 0) put_flag(rw, WS_EPOCH, tag);
    const int row0 = normal ? 192 * ti : 1920;
    v2u st[3], cp[2][10];
#pragma unroll
    for (int k = 0; k < 3; ++k) { const int r = row0 + lane + 64 * k; st[k] = __builtin_amdgcn_raw_buffer_load_b64(rw, (r >> 3) * 128 + (r & 7) * 8, WS_STATS, SC1); }
#pragma unroll
    for (int k = 0; k < 10; ++k) cp[0][k] = __builtin_amdgcn_raw_buffer_load_b64(rw, lane * 8 + k * 512, WS_CSP + ch * 5120, SC1);
    if (!normal) {
#pragma unroll
      for (int k = 0; k < 10; ++k) cp[1][k] = __builtin_amdgcn_raw_buffer_load_b64(rw, lane * 8 + k * 512, WS_CSP + ch2 * 5120, SC1);
    }
    BAR(7);
    v4u ur[16];
    if ((lane & 3) < nch) {
#pragma unroll
      for (int p = 0; p < 16; ++p) ur[p] = __builtin_amdgcn_raw_buffer_load_b128(rw, (lane >> 2) * 10240 + (lane & 3) * 16, WS_U8 + p * 163840 + d0 * 16, SC1);
    }
    BAR(8);
    f32x4 so[3];
#pragma unroll
    for (int k = 0; k < 3; ++k) {
      const float m = __uint_as_float(st[k].x), den = __uint_as_float(st[k].y);
      so[k] = f32x4{-m, den * den, -1.44269504f * __builtin_amdgcn_rcpf(den), 0.f};
    }
    f32x2 co[2];
#pragma unroll
    for (int h = 0; h < 2; ++h) {
      float c0 = 0.f, c1 = 0.f;
      if (h == 0 || !normal) {
#pragma unroll
        for (int k = 0; k < 10; ++k) { c0 += __uint_as_float(cp[h][k].x); asm volatile("" : "+v"(c0)); c1 += __uint_as_float(cp[h][k].y); }
      }
      co[h] = f32x2{c0, c1};
    }
    asm volatile("" : "+v"(so[0]), "+v"(so[1]), "+v"(so[2]), "+v"(co[0]), "+v"(co[1]));
    BAR(9);
    __builtin_amdgcn_s_sleep(4);
#pragma unroll
    for (int k = 0; k < 3; ++k) *(LAS f32x4*)lds(L_ST + (lane + 64 * k) * 16) = so[k];
#pragma unroll
    for (int h = 0; h < 2; ++h) *(LAS f32x2*)lds(L_CS + h * 512 + lane * 8) = co[h];
    wait_lg();
    BAR(12);
    if (lane < 32) {
#pragma unroll
      for (int k = 0; k < 3; ++k) *(LAS v4u*)lds(L_U + k * U_SZ + lane * 16) = v4u{0, 0, 0, 0};
    }
    if ((lane & 3) < nch) {
#pragma unroll
      for (int p = 0; p < 16; ++p) *(LAS v4u*)lds(L_U + (lane & 3) * U_SZ + 512 + p * 256 + (lane >> 2) * 16) = ur[p];
    }
    wait_lg();
    lds_set(L_MISC + 64);
    lds_wait(L_MISC + 60);
    STAMP(4);
#pragma unroll 1
    for (int i = 0; i < 12; ++i) {
      const int w = i / 3, s = i - 3 * w;
      const unsigned so = WS_WDT + ((12 * w + kst(9 + s)) * 40 + 5 * dj) * 1024, lo = pbase(w) + s * 5120;
#pragma unroll
      for (int t = 0; t < 5; ++t) dma<SC1>(sw, lo + t * 1024, lane * 16, so + t * 1024);
    }
    wait_vm<0>();
    STAMP(8);
    poll<1>(rw, WS_F1 + (t64 < 30 ? (t64 / 3) * 192 : 1920), t64 < 30 ? 24 : 16, tag, lane);
    lds_set(L_MISC + 32);
    STAMP(5);
    lds_wait(L_MISC + 36);
    poll<1>(rw, WS_F2 + dj * 256, 30, tag, lane);
    STAMP(6);
#pragma unroll
    for (int p = 0; p < 10; ++p) dma<SC1>(sw, L_AT + p * 1024, (lane >> 3) * 4096 + (lane & 7) * 16, WS_AT + (80 * dj + p * 8) * 4096 + t64 * 128);
    wait_vm<0>();
    STAMP(7);
    BAR(11);
  }
  DUMP();
#endif
}

__global__ __launch_bounds__(64) void probeSpin(const unsigned* t) {
#if __HIP_DEVICE_COMPILE__
  unsigned long long lo = ~0ull, hiS = 0, hiE = 0, sum = 0;
  for (int b = 0; b < 256; ++b) {
    const unsigned* p = t + (b * 8) * 64;
    unsigned long long s0 = *(const unsigned long long*)(p + 48), e0 = *(const unsigned long long*)(p + 50);
    lo = s0 < lo ? s0 : lo; hiS = s0 > hiS ? s0 : hiS; hiE = e0 > hiE ? e0 : hiE;
    if (b < 240) sum += (unsigned)(t[(b * 8 + PW) * 64 + PS] - p[0]);
  }
  unsigned long long ticks = PKIND == 0 ? (hiE - lo) * 100 : PKIND == 1 ? (hiS - lo) * 100 : PKIND == 2 ? sum / 240 * 10 : 0;
  if (ticks > 1000000) ticks = 1000000;
  unsigned long long t0 = __builtin_amdgcn_s_memrealtime();
  while (__builtin_amdgcn_s_memrealtime() - t0 < ticks) __builtin_amdgcn_s_sleep(8);
#endif
}

extern "C" void kernel_launch(void* const* d_in, const int* in_sizes, int n_in, void* d_out, int out_size, void* d_ws, size_t ws_size,
                              hipStream_t stream) {
  (void)hipFuncSetAttribute((const void*)fused_s4_ffn, hipFuncAttributeMaxDynamicSharedMemorySize, LDS_BYTES);
  fused_s4_ffn<<<256, 512, LDS_BYTES, stream>>>(
      (const uint16_t*)d_in[0], (const uint16_t*)d_in[1], (const uint16_t*)d_in[2], (const uint16_t*)d_in[3], (const uint16_t*)d_in[4],
      (const uint16_t*)d_in[5], (const uint16_t*)d_in[6], (const uint16_t*)d_in[7], (const uint16_t*)d_in[8], (const uint16_t*)d_in[10],
      (const uint16_t*)d_in[11], (const uint16_t*)d_in[12], (uint16_t*)d_out, d_ws);
  probeSpin<<<1, 64, 0, stream>>>((const unsigned*)((char*)d_ws + WS_TM));
}
```

```cpp
#define BENCH
#define PKIND 4
#define PW 0
#define PS 0
#include <hip/hip_runtime.h>
#include <stdint.h>

typedef short bf16x8 __attribute__((ext_vector_type(8)));
typedef short s16x4 __attribute__((ext_vector_type(4)));
typedef float f32x4 __attribute__((ext_vector_type(4)));
typedef float f32x2 __attribute__((ext_vector_type(2)));
typedef __bf16 bf16x2 __attribute__((ext_vector_type(2)));
typedef unsigned v4u __attribute__((ext_vector_type(4)));
typedef unsigned v2u __attribute__((ext_vector_type(2)));
typedef unsigned v8u __attribute__((ext_vector_type(8)));
typedef unsigned long long u64;
#define LAS __attribute__((address_space(3)))
#define DI __device__ __forceinline__
typedef __amdgpu_buffer_rsrc_t rsrc_t;

constexpr int L = 2048, D = 640, NS = 192, F = 1536;
constexpr int SC1 = 16, NT_ = 2;

constexpr unsigned WS_EPOCH = 0, WS_F0 = 1024, WS_F1 = 4096, WS_F2 = 8192, WS_STATS = 16384, WS_CSP = 65536, WS_U8 = 262144,
                   WS_WDT = 3u << 20, WS_H = 5u << 20, WS_AT = 12u << 20, WS_TM = 15u << 20, WS_BYTES = 16u << 20;

constexpr unsigned STAGE = 40960, XB = 24576;
constexpr unsigned LX = 3 * STAGE;
constexpr unsigned L_RAW = LX;
constexpr unsigned L_ST = L_RAW + 3 * 2048;
constexpr unsigned L_CS = L_ST + 3072;
constexpr unsigned L_MISC = L_CS + 1024;
constexpr unsigned L_U = L_MISC + 128, U_SZ = 4608;
constexpr unsigned L_LN = L_U + 3 * U_SZ, LN_RS = 1296;
constexpr unsigned L_END = L_LN + 10240;
constexpr unsigned L_WDU = L_U, L_DUMMY = L_U + 8192, L_CSP = L_U + 9216;
constexpr unsigned L_KG = 2 * STAGE, KG_SZ = 16 * (544 + 288);
constexpr unsigned L_H = STAGE, H_CS = 2112, H_SZ = 8 * H_CS;
constexpr unsigned L_K3 = L_END, L_AT = L_LN;
constexpr unsigned LDS_BYTES = 163840;
static_assert(L_END + 960 <= LDS_BYTES, "lds");
constexpr unsigned L_P2 = L_H + 3 * H_SZ + 64;
static_assert(L_P2 + 2 * 15360 <= LX, "lds partials");
#define G2AUXW SC1
#define G2AUXH SC1
#define ROT2 1
#define G2SIG 6
#define XROT 0
constexpr int SPIN_MAX = 20000;
constexpr unsigned CODE_BYTES = 47104;

#if __HIP_DEVICE_COMPILE__
extern __shared__ __attribute__((aligned(16))) char smem_[];
DI LAS char* lds(unsigned o) { return (LAS char*)smem_ + o; }
DI rsrc_t mkr(const void* p, unsigned bytes) { return __builtin_amdgcn_make_buffer_rsrc((void*)p, 0, bytes, 0x00020000); }
DI unsigned pk(float a, float b) { f32x2 f = {a, b}; return __builtin_bit_cast(unsigned, __builtin_convertvector(f, bf16x2)); }
DI unsigned pk2(f32x2 f) { return __builtin_bit_cast(unsigned, __builtin_convertvector(f, bf16x2)); }
DI float blo(unsigned v) { return __uint_as_float(v << 16); }
DI float bhi(unsigned v) { return __uint_as_float(v & 0xffff0000u); }
DI float rbf(float f) { return blo(pk(f, f)); }
DI void bar() { __builtin_amdgcn_s_barrier(); }
DI void cfence() { asm volatile("" ::: "memory"); }
template <int N> DI void wait_vm() { asm volatile("s_waitcnt vmcnt(%0)" ::"i"(N) : "memory"); }
DI void wait_lg() { asm volatile("s_waitcnt lgkmcnt(0)" ::: "memory"); }
DI bf16x8 trd(unsigned a0, unsigned a1) {
  s16x4 lo = __builtin_amdgcn_ds_read_tr16_b64_v4i16((LAS s16x4*)lds(a0));
  s16x4 hi = __builtin_amdgcn_ds_read_tr16_b64_v4i16((LAS s16x4*)lds(a1));
  return __builtin_shufflevector(lo, hi, 0, 1, 2, 3, 4, 5, 6, 7);
}
DI bf16x8 rd128(unsigned a) { return *(LAS bf16x8*)lds(a); }
DI f32x4 mfma(bf16x8 a, bf16x8 b, f32x4 c) { return __builtin_amdgcn_mfma_f32_16x16x32_bf16(a, b, c, 0, 0, 0); }
DI v4u srd(const void* p, unsigned bytes) { const u64 a = (u64)p; return v4u{(unsigned)a, (unsigned)(a >> 32) & 0xffffu, bytes, 0x00020000u}; }
template <int AUX> DI void dma(v4u r, unsigned ldsoff, unsigned voff, unsigned soff) {
  if (AUX == 0) asm volatile("s_mov_b32 m0, %0\n\ts_nop 0\n\tbuffer_load_dwordx4 %1, %2, %3 offen lds" ::"s"(ldsoff), "v"(voff), "s"(r), "s"(soff) : "memory");
  if (AUX == NT_) asm volatile("s_mov_b32 m0, %0\n\ts_nop 0\n\tbuffer_load_dwordx4 %1, %2, %3 offen nt lds" ::"s"(ldsoff), "v"(voff), "s"(r), "s"(soff) : "memory");
  if (AUX == SC1) asm volatile("s_mov_b32 m0, %0\n\ts_nop 0\n\tbuffer_load_dwordx4 %1, %2, %3 offen sc1 lds" ::"s"(ldsoff), "v"(voff), "s"(r), "s"(soff) : "memory");
}
template <int AUX> DI void st128(v4u v, rsrc_t r, unsigned off) { __builtin_amdgcn_raw_buffer_store_b128(v, r, off, 0, AUX); }
DI void touch(v4u& sink, v4u r, unsigned vo, unsigned so) {
  asm volatile("buffer_load_dwordx4 %0, %1, %2, %3 offen sc1" : "+v"(sink) : "v"(vo), "s"(r), "s"(so) : "memory");
}
DI float rowsum16(float v) {
  v += __builtin_bit_cast(float, __builtin_amdgcn_update_dpp(0, __builtin_bit_cast(int, v), 0x128, 0xf, 0xf, false));
  v += __builtin_bit_cast(float, __builtin_amdgcn_update_dpp(0, __builtin_bit_cast(int, v), 0x124, 0xf, 0xf, false));
  v += __builtin_bit_cast(float, __builtin_amdgcn_update_dpp(0, __builtin_bit_cast(int, v), 0x122, 0xf, 0xf, false));
  v += __builtin_bit_cast(float, __builtin_amdgcn_update_dpp(0, __builtin_bit_cast(int, v), 0x121, 0xf, 0xf, false));
  return v;
}
DI f32x2 cmul(f32x2 a, f32x2 b) {
  f32x2 t = a * b.xx, r;
  asm("v_pk_fma_f32 %0, %1, %2, %3 op_sel:[1,1,0] op_sel_hi:[0,1,1] neg_lo:[0,1,0]" : "=v"(r) : "v"(a), "v"(b), "v"(t));
  return r;
}
DI f32x2 cpow(float lr, float tr, float e) {
  float m = __builtin_amdgcn_exp2f(lr * e), th = __builtin_amdgcn_fractf(tr * e);
  return f32x2{m * __builtin_amdgcn_cosf(th), m * __builtin_amdgcn_sinf(th)};
}
DI v2u tag_of(void) { return *(LAS v2u*)lds(L_MISC); }
DI void put_flag(rsrc_t rw, unsigned off, v2u tag) { __builtin_amdgcn_raw_buffer_store_b64(tag, rw, off, 0, SC1); }
template <int R> DI void poll(rsrc_t rw, unsigned off, int n, v2u tag, int lane) {
#pragma unroll 1
  for (int spin = 0; spin < SPIN_MAX; ++spin) {
    bool ok = true;
#pragma unroll
    for (int r = 0; r < R; ++r) {
      int i = lane + 64 * r; i = i < n ? i : n - 1;
      v2u f = __builtin_amdgcn_raw_buffer_load_b64(rw, off + i * 8, 0, SC1);
      ok = ok && f.x == tag.x && f.y == tag.y;
    }
    if (__all(ok)) break;
    cfence();
  }
  cfence();
}
DI void lds_set(unsigned o) { cfence(); *(volatile LAS unsigned*)lds(o) = 1; }
DI void lds_wait(unsigned o) {
#pragma unroll 1
  for (int spin = 0; spin < 16 * SPIN_MAX && *(volatile LAS unsigned*)lds(o) == 0; ++spin) __builtin_amdgcn_s_sleep(1);
  cfence();
}
DI void arrive_publish(unsigned cnt, int n, rsrc_t rw, unsigned off, int lane) {
  unsigned old = 0;
  if (lane == 0) old = __hip_atomic_fetch_add((LAS unsigned*)lds(cnt), 1u, __ATOMIC_RELAXED, __HIP_MEMORY_SCOPE_WORKGROUP);
  old = __builtin_amdgcn_readfirstlane(old);
  if ((int)old == n - 1 && lane == 0) put_flag(rw, off, tag_of());
}

DI void stamp_(int i) {
  u64 c; asm volatile("s_memtime %0\n\ts_waitcnt lgkmcnt(0)" : "=s"(c) :: "memory");
  asm volatile("s_mov_b32 m0, %1\n\tv_writelane_b32 v255, %0, m0" :: "s"((unsigned)c), "s"(i) : "memory", "v255");
}
#define STAMP(i) stamp_(i)
#define BAR(i) bar()
#define DUMP() do { u64 t_ = __builtin_amdgcn_s_memrealtime(); unsigned sv_; asm volatile("v_mov_b32 %0, v255" : "=v"(sv_)); \
    unsigned* p_ = (unsigned*)((char*)ws + WS_TM + (blockIdx.x * 8 + wid) * 256); \
    if (lane < 40) p_[lane] = sv_; if (lane == 0) { ((u64*)p_)[24] = T0_; ((u64*)p_)[25] = t_; } } while (0)

struct G1Ld { unsigned vx0, vx1, vw0, vw1; };
DI G1Ld g1_lanes(int lane) {
  const int r8 = lane >> 3, sl = lane & 7;
  return G1Ld{(unsigned)(r8 * 1280 + ((sl ^ (r8 >> 1)) << 4)), (unsigned)(r8 * 1280 + ((sl ^ (r8 >> 1) ^ 4) << 4)),
              (unsigned)(r8 * 3072 + ((sl ^ (r8 & 2)) << 4)), (unsigned)(r8 * 3072 + ((sl ^ (r8 & 2) ^ 4) << 4))};
}
template <int NT> DI void g1_issue(v4u rx, v4u rg, v4u ru, const G1Ld& v, int lw, int s, unsigned lb, int row0, int ch) {
  const unsigned xso = (row0 + lw * 16 * NT) * 1280 + s * 128, wso = ch * 128 + (lw * 32 + s * 64) * 3072;
#pragma unroll
  for (int p = 0; p < 4; ++p) {
    dma<0>(rg, lb + XB + (lw * 4 + p) * 1024, (p & 1) ? v.vw1 : v.vw0, wso + p * 8 * 3072);
    dma<0>(ru, lb + XB + 8192 + (lw * 4 + p) * 1024, (p & 1) ? v.vw1 : v.vw0, wso + p * 8 * 3072);
  }
#pragma unroll
  for (int p = 0; p < 2 * NT; ++p) dma<0>(rx, lb + (lw * 2 * NT + p) * 1024, (p & 1) ? v.vx1 : v.vx0, xso + p * 8 * 1280);
}
DI unsigned nxt(unsigned so) { return so == 2 * STAGE ? 0 : so + STAGE; }
template <int NT, int S0> DI void g1_loader(v4u rx, v4u rg, v4u ru, int lw, int lane, int row0, int ch, int rot, unsigned so) {
  constexpr int NP = 8 + 2 * NT;
  const G1Ld v = g1_lanes(lane);
  if (S0 == 1) BAR(0);
#pragma unroll 1
  for (int s = S0; s < 10; ++s) {
    if (s >= S0 + 2) wait_vm<NP>();
    if (s >= S0 + 2) BAR(s - 2);
    g1_issue<NT>(rx, rg, ru, v, lw, s + rot >= 10 ? s + rot - 10 : s + rot, so, row0, ch);
    so = nxt(so);
  }
  wait_vm<NP>(); BAR(8);
}

DI void colsum(int k, int lane) {
  const int c = lane & 15, g = lane >> 4, q = (lane >> 2) & 3, p = lane & 3;
  const bf16x8 ones = {0x3f80, 0x3f80, 0x3f80, 0x3f80, 0x3f80, 0x3f80, 0x3f80, 0x3f80};
  f32x4 r[2];
#pragma unroll
  for (int a = 0; a < 2; ++a) {
    const unsigned ad = XB + (k >> 1) * 8192 + (8 * g + q) * 128 + (((4 * (k & 1) + 2 * a + (p >> 1)) ^ (q & 2) ^ ((g & 1) << 2)) << 4) + 8 * (p & 1);
    r[a] = mfma(trd(ad + 4096, ad + 4608), ones, mfma(trd(ad, ad + 512), ones, f32x4{0.f, 0.f, 0.f, 0.f}));
  }
  f32x4 v;
#pragma unroll
  for (int e = 0; e < 4; ++e) v[e] = c ? r[1][e] : r[0][e];
  if (c < 2) *(LAS f32x4*)lds(L_CSP + k * 128 + (16 * c + 4 * g) * 4) = v;
  wait_lg();
}

template <int NT> DI void g1_main(int wid, int lane, unsigned so, f32x4 (&acc)[4][NT]) {
  const int wa = wid & 1, wt = wid >> 1;
  const int c = lane & 15, g = lane >> 4, q = (lane >> 2) & 3, p = lane & 3;
  unsigned aA[2], aB[2];
#pragma unroll
  for (int a = 0; a < 2; ++a) aA[a] = XB + (8 * g + q) * 128 + (((4 * wa + 2 * a + (p >> 1)) ^ (q & 2) ^ ((g & 1) << 2)) << 4) + 8 * (p & 1);
#pragma unroll
  for (int ks = 0; ks < 2; ++ks) aB[ks] = (16 * NT * wt + c) * 128 + (((4 * ks + g) ^ (c >> 1)) << 4);
#pragma unroll
  for (int a = 0; a < 4; ++a)
#pragma unroll
    for (int t = 0; t < NT; ++t) acc[a][t] = f32x4{0.f, 0.f, 0.f, 0.f};
  bf16x8 fa[2][4], fb[2][NT];
  auto rd = [&](int buf, int ks, unsigned o) {
#pragma unroll
    for (int a = 0; a < 4; ++a) { unsigned ad = aA[a & 1] + o + (a >> 1) * 8192 + ks * 4096; fa[buf][a] = trd(ad, ad + 512); }
#pragma unroll
    for (int t = 0; t < NT; ++t) fb[buf][t] = rd128(aB[ks] + o + t * 2048);
  };
  auto mm = [&](int buf) {
#pragma unroll
    for (int t = 0; t < NT; ++t)
#pragma unroll
      for (int a = 0; a < 4; ++a) acc[a][t] = mfma(fa[buf][a], fb[buf][t], acc[a][t]);
  };
  auto ilv = [&]() {
    constexpr int NR = 8 + NT, NM = 4 * NT, NI = NR < NM ? NR : NM;
#pragma unroll
    for (int i = 0; i < NI; ++i) { __builtin_amdgcn_sched_group_barrier(0x008, 1, 0); __builtin_amdgcn_sched_group_barrier(0x100, 1, 0); }
    if (NM > NI) __builtin_amdgcn_sched_group_barrier(0x008, NM - NI, 0);
    __builtin_amdgcn_sched_barrier(0);
  };
  BAR(0);
  STAMP(2);
  rd(0, 0, so);
#pragma unroll 1
  for (int s = 0; s < 9; ++s) {
    rd(1, 1, so); mm(0); ilv();
    so = nxt(so);
    wait_lg();
    BAR(s + 1);
    __builtin_amdgcn_sched_barrier(0);
    rd(0, 0, so); mm(1); ilv();
  }
  rd(1, 1, so); mm(0); ilv();
  mm(1);
  __builtin_amdgcn_sched_barrier(0);
}
template <int NT> DI void g1_epilogue(rsrc_t rw, int wid, int lane, int t16_0, int ch, unsigned st_off, unsigned cs_off, const f32x4 (&acc)[4][NT]) {
  const int wa = wid & 1, wt = wid >> 1, c = lane & 15, g = lane >> 4;
  f32x4 cs[4];
#pragma unroll
  for (int a = 0; a < 4; ++a) cs[a] = *(LAS f32x4*)lds(cs_off + (a >> 1) * 256 + (32 * wa + 16 * (a & 1) + 4 * g) * 4);
  f32x4 st[NT];
#pragma unroll
  for (int t = 0; t < NT; ++t) st[t] = *(LAS f32x4*)lds(st_off + (16 * NT * wt + 16 * t + c) * 16);
#pragma unroll
  for (int t = 0; t < NT; ++t) {
    const float nm = st[t][0], qq = st[t][1], rl = st[t][2];
    v4u o;
#pragma unroll
    for (int a = 0; a < 2; ++a) {
      f32x4 tg = cs[a] * nm + acc[a][t], tu = cs[2 + a] * nm + acc[2 + a][t];
      f32x4 ar = tg * rl, e;
#pragma unroll
      for (int r = 0; r < 4; ++r) e[r] = __builtin_amdgcn_exp2f(ar[r]);
      f32x4 dn = e * qq + qq, pr = tg * tu;
#pragma unroll
      for (int r = 0; r < 4; ++r) dn[r] = __builtin_amdgcn_rcpf(dn[r]);
      pr *= dn;
      o[2 * a] = pk(pr[0], pr[1]); o[2 * a + 1] = pk(pr[2], pr[3]);
    }
    st128<SC1>(o, rw, lane * 16 + WS_H + ((t16_0 + NT * wt + t) * 48 + 2 * ch + wa) * 1024);
  }
}

template <int NM> DI void kgen(const unsigned (&w)[6], float dt, int lane, unsigned tb, unsigned done, f32x4 (&acc)[2][2]) {
  constexpr int RS = NM * 256 + 32, KS = NM * 4;
  float lr[NM], tr[NM]; f32x2 W[NM];
#pragma unroll
  for (int m = 0; m < NM; ++m) {
    float v[6];
#pragma unroll
    for (int i = 0; i < 6; ++i) v[i] = m == 0 ? blo(w[i]) : bhi(w[i]);
    lr[m] = dt * v[0] * 1.44269504f; tr[m] = dt * v[1] * 0.15915494f;
    const float br = dt * v[2], bi = dt * v[3];
    W[m] = f32x2{v[4] * br - v[5] * bi, -(v[4] * bi + v[5] * br)};
  }
  asm volatile("" : "+v"(lr[0]), "+v"(W[0]));
  STAMP(13);
  unsigned wa = tb + lane * 4 * NM, fa = tb + (lane & 15) * RS + (lane >> 4) * 16;
  asm volatile("" : "+v"(wa), "+v"(fa));
  auto put = [&](int row, const f32x2* z) {
    if (NM == 2) *(LAS v2u*)lds(wa + row * RS) = v2u{pk2(z[0]), pk2(z[NM - 1])};
    else *(LAS unsigned*)lds(wa + row * RS) = pk2(z[0]);
  };
  f32x2 c[8][NM], hi[NM], t[NM], c8[NM];
#pragma unroll
  for (int m = 0; m < NM; ++m) {
    c[0][m] = f32x2{1.f, 0.f}; c[1][m] = cpow(lr[m], tr[m], 8.f);
    c[2][m] = cmul(c[1][m], c[1][m]); c[3][m] = cmul(c[2][m], c[1][m]); c[4][m] = cmul(c[2][m], c[2][m]);
    c[5][m] = cmul(c[4][m], c[1][m]); c[6][m] = cmul(c[4][m], c[2][m]); c[7][m] = cmul(c[4][m], c[3][m]);
    c8[m] = cmul(c[4][m], c[4][m]); hi[m] = c8[m];
  }
  bf16x8 fb[2][KS];
#pragma unroll
  for (int j = 0; j < 8; ++j) put(j, c[j]);
#pragma unroll
  for (int a = 1; a < 4; ++a) {
    if (a == 2) {
#pragma unroll
      for (int s = 0; s < KS; ++s) fb[0][s] = rd128(fa + s * 64);
    }
    put((8 * a) & 15, hi);
#pragma unroll
    for (int j = 1; j < 8; ++j) {
#pragma unroll
      for (int m = 0; m < NM; ++m) t[m] = cmul(hi[m], c[j][m]);
      put((8 * a + j) & 15, t);
    }
    if (a < 3) {
#pragma unroll
      for (int m = 0; m < NM; ++m) hi[m] = cmul(hi[m], c8[m]);
    }
  }
#pragma unroll
  for (int s = 0; s < KS; ++s) fb[1][s] = rd128(fa + s * 64);
  STAMP(14);
  f32x2 z1[NM], zb[NM], r0[NM], r4[NM];
#pragma unroll
  for (int m = 0; m < NM; ++m) { z1[m] = cpow(lr[m], -tr[m], 1.f); zb[m] = cpow(lr[m], -tr[m], 256.f); t[m] = W[m]; }
#pragma unroll
  for (int b = 0; b < 8; ++b) {
    put(b, t);
    if ((b & 1) == 0) {
      f32x2 u[NM];
#pragma unroll
      for (int m = 0; m < NM; ++m) { u[m] = cmul(t[m], zb[m]); if (b == 0) r0[m] = u[m]; if (b == 4) r4[m] = u[m]; }
      put(8 + b / 2, u);
    }
    if (b < 7) {
#pragma unroll
      for (int m = 0; m < NM; ++m) t[m] = cmul(t[m], z1[m]);
    }
  }
#pragma unroll
  for (int i = 0; i < 2; ++i) {
#pragma unroll
    for (int m = 0; m < NM; ++m) { r0[m] = cmul(r0[m], zb[m]); r4[m] = cmul(r4[m], zb[m]); }
    put(12 + 2 * i, r0); put(13 + 2 * i, r4);
  }
  bf16x8 fr[2][KS];
#pragma unroll
  for (int s = 0; s < KS; ++s) fr[0][s] = rd128(fa + s * 64);
#pragma unroll
  for (int i = 0; i < 5; ++i) {
#pragma unroll
    for (int m = 0; m < NM; ++m) r0[m] = cmul(r0[m], zb[m]);
    put(i, r0);
  }
  if (NM == 2) lds_set(done);
#pragma unroll
  for (int s = 0; s < KS; ++s) fr[1][s] = rd128(fa + s * 64);
  __builtin_amdgcn_sched_barrier(0);
  STAMP(15);
#pragma unroll
  for (int i = 0; i < 4; ++i) acc[i >> 1][i & 1] = f32x4{0.f, 0.f, 0.f, 0.f};
#pragma unroll
  for (int rt = 0; rt < 2; ++rt)
#pragma unroll
    for (int s = 0; s < KS; ++s) { acc[rt][0] = mfma(fr[rt][s], fb[0][s], acc[rt][0]); acc[rt][1] = mfma(fr[rt][s], fb[1][s], acc[rt][1]); }
}
struct LnOut { s16x4 tr[2][10]; v2u st; };
DI void ln8(rsrc_t rx, int tb, int lane, unsigned tile, LnOut& out) {
  const int r4 = lane >> 4, i = lane & 15;
  v4u raw[2][5];
#pragma unroll
  for (int h = 0; h < 2; ++h)
#pragma unroll
    for (int k = 0; k < 5; ++k) raw[h][k] = __builtin_amdgcn_raw_buffer_load_b128(rx, r4 * 1280 + i * 16 + k * 256, (tb * 8 + h * 4) * 1280, 0);
  const unsigned ta = tile + ((lane >> 2) & 3) * LN_RS + (16 * r4 + 4 * (lane & 3)) * 2;
#pragma unroll
  for (int h = 0; h < 2; ++h) {
    if (h == 0) { asm volatile("" : "+v"(raw[0][4])); STAMP(13); asm volatile("" : "+v"(raw[1][4])); STAMP(15); } else STAMP(14);
    float s1 = 0.f, s2 = 0.f, s3 = 0.f, s4 = 0.f;
    const bf16x2 one2 = __builtin_bit_cast(bf16x2, 0x3f803f80u);
#pragma unroll
    for (int k = 0; k < 10; ++k) {
      const unsigned w0 = raw[h][k >> 1][2 * (k & 1)], w1 = raw[h][k >> 1][2 * (k & 1) + 1];
      const bf16x2 p0 = __builtin_bit_cast(bf16x2, w0), p1 = __builtin_bit_cast(bf16x2, w1);
      s1 = __builtin_amdgcn_fdot2_f32_bf16(p0, one2, s1, false); s2 = __builtin_amdgcn_fdot2_f32_bf16(p0, p0, s2, false);
      s3 = __builtin_amdgcn_fdot2_f32_bf16(p1, one2, s3, false); s4 = __builtin_amdgcn_fdot2_f32_bf16(p1, p1, s4, false);
    }
    const float mean = rowsum16(s1 + s3) * (1.f / D);
    const float var = rbf(fmaxf(rowsum16(s2 + s4) * (1.f / D) - mean * mean, 0.f));
    const float m = rbf(mean), den = rbf(sqrtf(rbf(var + 1e-5f)));
    const float ri = __builtin_amdgcn_rcpf(den), nb = -m * ri;
    if (h == 0 || (i & 1)) out.st = v2u{__float_as_uint(m), __float_as_uint(den)};
#pragma unroll
    for (int k = 0; k < 5; ++k) {
      v4u o;
#pragma unroll
      for (int e = 0; e < 4; ++e) { const unsigned w = raw[h][k][e]; o[e] = pk2(f32x2{blo(w), bhi(w)} * ri + nb); }
      *(LAS v4u*)lds(tile + r4 * LN_RS + i * 16 + k * 256) = o;
    }
    cfence();
#pragma unroll
    for (int it = 0; it < 10; ++it) out.tr[h][it] = __builtin_amdgcn_ds_read_tr16_b64_v4i16((LAS s16x4*)lds(ta + it * 128));
  }
}
DI void ln8_store(rsrc_t rw, int tb, int lane, const LnOut& in) {
  if ((lane & 15) < 2) __builtin_amdgcn_raw_buffer_store_b64(in.st, rw, ((lane & 1) * 4 + (lane >> 4)) * 8, WS_STATS + tb * 128, SC1);
#pragma unroll
  for (int it = 0; it < 10; ++it) {
    bf16x8 o = __builtin_shufflevector(in.tr[0][it], in.tr[1][it], 0, 1, 2, 3, 4, 5, 6, 7);
    st128<SC1>(__builtin_bit_cast(v4u, o), rw, lane * 16 + it * 1024 + WS_U8 + tb * 10240);
  }
}

DI unsigned prm(unsigned hi, unsigned lo, unsigned sel) { return __builtin_amdgcn_perm(hi, lo, sel); }
DI unsigned colo(int col) { return (col & 15) * 16 + ((col >> 4) & 1) * 8; }
template <int S> DI v4u kchunk(unsigned raw, int lane) {
  const int i = lane & 31, up = lane >> 5;
  if (S == 0) {
    const v2u a0 = *(LAS v2u*)lds(raw + colo(i) + 512 * up);
    const v2u a1 = *(LAS v2u*)lds(raw + (up ? (i == 31 ? 768 : 512 + colo(i + 1)) : 256 + colo(i)));
    const unsigned s0 = up ? 0x03020504u : 0x05040706u, s1 = up ? 0x01000100u : 0x05040706u, s2 = up ? 0x03020504u : 0x05040706u,
                   s3 = up ? 0x01000100u : 0x01000302u;
    return v4u{prm(up ? a1.x : a1.y, a0.y, s0), prm(a1.x, a0.y, s1), prm(a0.y, a0.x, s2), prm(0, a0.x, s3)};
  } else {
    const unsigned b0 = *(LAS unsigned*)lds(raw + 768 + colo(i) + 4 * up);
    const unsigned b1 = *(LAS unsigned*)lds(raw + (lane == 63 ? 1024 : 768 + colo(i + 1) + 4 * ((lane + 1) >> 5)));
    return v4u{prm(0, b1, 0x01000100), prm(0, b0, 0x03020302), prm(0, b0, 0x01000302), prm(0, b0, 0x01000100)};
  }
}
DI unsigned vmov(unsigned x) { unsigned r; asm("v_mov_b32 %0, %1" : "=v"(r) : "v"(x)); return r; }
DI void hwrite(v4u x, v4u y, unsigned hb, int pos) {
  v8u w = __builtin_shufflevector(x, y, 0, 1, 2, 3, 4, 5, 6, 7), e, o0, o1;
  asm volatile("" : "+v"(w));
#pragma unroll
  for (int k = 0; k < 6; ++k) {
    e[k] = vmov(w[k + 1]);
    o0[k] = __builtin_amdgcn_alignbit(w[k + 1], w[k], 16);
    o1[k] = __builtin_amdgcn_alignbit(w[k + 2], w[k + 1], 16);
    asm volatile("" : "+v"(o1[k]));
  }
  asm volatile("" : "+v"(e), "+v"(o0), "+v"(o1));
  const unsigned a = hb + pos * 16;
  *(LAS v4u*)lds(a + 7 * H_CS) = __builtin_shufflevector(w, w, 0, 1, 2, 3);
  *(LAS v4u*)lds(a + 3 * H_CS) = __builtin_shufflevector(w, w, 2, 3, 4, 5);
  *(LAS v4u*)lds(a + 5 * H_CS) = __builtin_shufflevector(e, e, 0, 1, 2, 3);
  *(LAS v4u*)lds(a + 1 * H_CS) = __builtin_shufflevector(e, e, 2, 3, 4, 5);
  *(LAS v4u*)lds(a + 6 * H_CS) = __builtin_shufflevector(o0, o0, 0, 1, 2, 3);
  *(LAS v4u*)lds(a + 2 * H_CS) = __builtin_shufflevector(o0, o0, 2, 3, 4, 5);
  *(LAS v4u*)lds(a + 4 * H_CS) = __builtin_shufflevector(o1, o1, 0, 1, 2, 3);
  *(LAS v4u*)lds(a + 0 * H_CS) = __builtin_shufflevector(o1, o1, 2, 3, 4, 5);
}
template <int S> DI void hbuild(unsigned raw, unsigned hb, int lane, v4u& carry) {
  const v4u x = kchunk<S>(raw, lane);
  v4u y;
#pragma unroll
  for (int k = 0; k < 4; ++k) {
    y[k] = __builtin_amdgcn_update_dpp(carry[k], x[k], 0x138, 0xf, 0xf, false);
    carry[k] = __builtin_amdgcn_readlane(x[k], 63);
  }
  hwrite(x, y, hb, 1 + 64 * S + lane);
}
DI void htail(unsigned raw, unsigned hb, unsigned k3, int lane, v4u carry) {
  const unsigned kk = *(LAS uint16_t*)lds(raw + 1024);
  const int n0 = 128 - 2 * lane, n1 = n0 - 1;
  const unsigned c0 = *(LAS uint16_t*)lds(raw + 1024 + (n0 == 128 ? 256 : colo(n0) + 2 * (n0 >> 5)));
  const unsigned c1 = *(LAS uint16_t*)lds(raw + 1024 + colo(n1) + 2 * (n1 >> 5));
  *(LAS unsigned*)lds(k3 + lane * 4) = c0 | (c1 << 16);
  if (lane < 16) *(LAS unsigned*)lds(k3 + 256 + lane * 4) = 0;
  const unsigned k2 = kk | (kk << 16), m0 = lane == 0 ? ~0u : 0u, m1 = lane == 1 ? ~0u : 0u;
  if (lane < 3) hwrite(v4u{0, (kk << 16) & m0, k2 & m0, k2 & m0},
                       v4u{carry[0] & m0, (carry[1] & m0) | ((kk << 16) & m1), (carry[2] & m0) | (k2 & m1), (carry[3] & m0) | (k2 & m1)}, hb, 129 + lane);
}

DI void conv(rsrc_t rw, int cw, int d, int lane) {
  const unsigned hb = L_H + cw * H_SZ, ub = L_U + cw * U_SZ;
  const int c = lane & 15, g = lane >> 4;
  unsigned aa = hb + (c & 7) * H_CS + (3 + (c >> 3) - g) * 16, ab = ub + 416 + 32 * c + 16 * g;
  f32x4 acc[8] = {};
  bf16x8 fa[2][4], fb[2][8];
  auto rd = [&](int buf, int oa, int ob) {
#pragma unroll
    for (int k = 0; k < 4; ++k) fa[buf][k] = rd128(aa + oa + k * 512);
#pragma unroll
    for (int e = 7; e >= 0; --e) fb[buf][e] = rd128(ab + ob + e * 512);
  };
  auto mm = [&](int buf) {
#pragma unroll
    for (int k = 0; k < 4; ++k)
#pragma unroll
      for (int e = 7 - k; e >= 0; --e) acc[e + k] = mfma(fa[buf][k], fb[buf][e], acc[e + k]);
  };
  auto ilv = [&]() {
#pragma unroll
    for (int i = 0; i < 12; ++i) { __builtin_amdgcn_sched_group_barrier(0x008, 1, 0); __builtin_amdgcn_sched_group_barrier(0x100, 1, 0); }
    __builtin_amdgcn_sched_group_barrier(0x008, 14, 0);
    __builtin_amdgcn_sched_barrier(0);
  };
  asm volatile("" : "+v"(aa), "+v"(ab));
  rd(0, 0, 64);
  {
    const bf16x8 fx = rd128(aa + 2048);
    bf16x8 ue[4], kt[4];
#pragma unroll
    for (int f = 0; f < 4; ++f) ue[f] = rd128(ab + 48 + f * 512);
    const unsigned k3 = L_K3 + cw * 320 + (254 - 2 * c + 8 * g) * 2;
#pragma unroll
    for (int f = 0; f < 4; ++f) {
      v4u t;
#pragma unroll
      for (int e = 0; e < 4; ++e) t[e] = *(LAS unsigned*)lds(k3 - (4 + f) * 64 + e * 4);
      kt[f] = __builtin_bit_cast(bf16x8, t);
    }
    bf16x8 b9, b7;
#pragma unroll
    for (int jj = 0; jj < 8; ++jj) {
      const int dl = 8 * g + jj - 8 - c;
      b9[jj] = (dl >= -4 && dl <= 4) ? 0x3f80 : 0; b7[jj] = (dl >= -3 && dl <= 3) ? 0x3f80 : 0;
    }
#pragma unroll
    for (int f = 0; f < 4; ++f) {
      acc[4 + f] = mfma(fx, fb[0][f], acc[4 + f]);
      const f32x4 ve = mfma(ue[f], b9, f32x4{0.f, 0.f, 0.f, 0.f}), vo = mfma(fb[0][f], b7, f32x4{0.f, 0.f, 0.f, 0.f});
      const bf16x8 vf = __builtin_bit_cast(bf16x8, v4u{pk(ve[0], vo[0]), pk(ve[1], vo[1]), pk(ve[2], vo[2]), pk(ve[3], vo[3])});
#pragma unroll
      for (int n = 4 + f; n < 8; ++n) acc[n] = mfma(vf, kt[n - f - 4], acc[n]);
    }
    __builtin_amdgcn_sched_barrier(0);
  }
  STAMP(8);
#pragma unroll 1
  for (int r = 0; r < 3; ++r) {
    rd(1, 64, 0); mm(0); ilv();
    aa += 128; ab -= 128;
    asm volatile("" : "+v"(aa), "+v"(ab));
    rd(0, 0, 64); mm(1); ilv();
  }
  rd(1, 64, 0); mm(0); ilv();
  mm(1);
#pragma unroll
  for (int n = 0; n < 8; ++n)
    __builtin_amdgcn_raw_buffer_store_b64(v2u{pk(acc[n][0], acc[n][1]), pk(acc[n][2], acc[n][3])}, rw, 32 * c + 8 * g + 512 * n, WS_AT + d * 4096, SC1);
}
#endif

__global__ __launch_bounds__(512, 2) void fused_s4_ffn(
    const uint16_t* __restrict__ x, const uint16_t* __restrict__ Are, const uint16_t* __restrict__ Aim, const uint16_t* __restrict__ Bre,
    const uint16_t* __restrict__ Bim, const uint16_t* __restrict__ Cre, const uint16_t* __restrict__ Cim, const uint16_t* __restrict__ logdt,
    const uint16_t* __restrict__ dskip, const uint16_t* __restrict__ wg, const uint16_t* __restrict__ wu, const uint16_t* __restrict__ wd,
    uint16_t* __restrict__ out, void* ws) {
#if __HIP_DEVICE_COMPILE__
  const int wid = __builtin_amdgcn_readfirstlane(threadIdx.x >> 6), lane = threadIdx.x & 63;
  const int b = blockIdx.x, xcd = b & 7, j = b >> 3;
  const bool normal = j < 30;
  const int rl = j % 5, ti = 5 * (xcd & 1) + rl;
  const int mm_ = 2 * (xcd >> 1) + (xcd & 1), par = j - 30;
  const int ch = normal ? 6 * (xcd >> 1) + j / 5 : 3 * mm_ + 2 * par;
  const int ch2 = 3 * mm_ + 1;
  const int nch = normal ? (j < 20 ? 3 : 2) : 0;
  const int d0 = 80 * xcd + (j < 20 ? 3 * j : 2 * j + 20);
  const int t64 = 4 * xcd + (j >> 3), dj = j & 7;
  const int rot = XROT ? (ti + (5 * (j / 5) + 1) / 3) % 10 : ti;
  const int rot2 = (3 * (j >> 3) + (3 * dj + 1) / 2) % 12;
  auto kst = [&](int s) { return s + rot2 >= 12 ? s + rot2 - 12 : s + rot2; };
  auto pbase = [](int w) -> unsigned { return w < 2 ? w * 15360 : L_P2 + (w - 2) * 15360; };
  const rsrc_t rw = mkr(ws, WS_BYTES), rx = mkr(x, L * D * 2);
  const u64 T0_ = __builtin_amdgcn_s_memrealtime();
  STAMP(0);
  if (wid == 6) {
    u64 pc; asm volatile("s_getpc_b64 %0" : "=s"(pc));
    dma<0>(srd((const void*)pc, CODE_BYTES), L_DUMMY, lane * 16, j * 1024);
    dma<0>(srd((const void*)pc, CODE_BYTES), L_DUMMY, lane * 16, 32768 + j * 1024);
  }
  if (wid == 7) { *(LAS v4u*)lds(L_MISC + 16 + (lane & 3) * 16) = v4u{0, 0, 0, 0}; wait_lg(); }
  asm volatile("" ::"s"(x), "s"(Are), "s"(Aim), "s"(Bre), "s"(Bim), "s"(Cre), "s"(Cim), "s"(logdt), "s"(dskip), "s"(wg), "s"(wu), "s"(wd), "s"(out), "s"(ws));
  unsigned pw[6], ldw_ = 0, dsw_ = 0;
  const int kd = d0 + (wid & 3);
  if ((wid & 3) < nch) {
    const uint16_t* const par6[6] = {Are, Aim, Bre, Bim, Cre, Cim};
    const unsigned vo = wid >= 4 ? lane * 4 : 256 + (lane >> 1) * 4;
#pragma unroll
    for (int i = 0; i < 6; ++i) pw[i] = __builtin_amdgcn_raw_buffer_load_b32(mkr(par6[i], D * NS * 2), vo, kd * NS * 2, NT_);
    ldw_ = ((const __attribute__((address_space(4))) unsigned*)logdt)[kd >> 1];
    dsw_ = ((const __attribute__((address_space(4))) unsigned*)dskip)[kd >> 1];
  }
  cfence(); bar(); cfence();
  STAMP(11);
  const float dt = rbf(expf((kd & 1) ? bhi(ldw_) : blo(ldw_)));
  const v4u sg = srd(wg, D * F * 2), su = srd(wu, D * F * 2), sx = srd(x, L * D * 2), sw = srd(ws, WS_BYTES);

  if (wid < 4) {
    if (wid < nch) {
      f32x4 ka[2][2];
#pragma unroll
      for (int i = 0; i < 6; ++i) pw[i] = (lane & 1) ? pw[i] >> 16 : pw[i];
      kgen<1>(pw, dt, lane, L_KG + wid * KG_SZ + 16 * 544, 0, ka);
#pragma unroll
      for (int i = 0; i < 4; ++i) *(LAS f32x4*)lds(L_KG + wid * KG_SZ + 16 * 544 + i * 1024 + lane * 16) = ka[i >> 1][i & 1];
      wait_lg();
      lds_set(L_MISC + 16 + wid * 4);
      STAMP(12);
      if (wid < 2) {
        const G1Ld v = g1_lanes(lane);
        lds_wait(L_MISC + 68); lds_wait(L_MISC + 72);
        if (nch == 3) lds_wait(L_MISC + 76);
        STAMP(10);
        g1_issue<6>(sx, sg, su, v, wid, rot, 0, 192 * ti, ch);
        wait_vm<0>();
      }
    } else if (wid == 3 && normal) {
      const G1Ld v = g1_lanes(lane);
      v4u sink = {0, 0, 0, 0};
#pragma unroll 1
      for (int p = 0; p < 8; ++p) {
        touch(sink, sg, v.vw0, ch * 128 + (rot * 64 + p * 8) * 3072);
        touch(sink, su, v.vw0, ch * 128 + (rot * 64 + p * 8) * 3072);
        const int sl = rot + (p >> 2);
        touch(sink, sx, v.vx0, (192 * ti + 32 * (j / 5) + 8 * (p & 3)) * 1280 + (sl == 10 ? 0 : sl) * 128);
      }
      const v4u sd = srd(wd, F * D * 2);
#pragma unroll 1
      for (int p = 0; p < 8; ++p) dma<NT_>(sd, L_WDU + p * 1024, (lane >> 3) * 1280 + (lane & 7) * 16, (ch * 64 + p * 8) * 1280 + ti * 128);
      asm volatile("s_waitcnt vmcnt(0)" : "+v"(sink)::"memory");
    }
    wait_lg();
    STAMP(1);
    if (normal) {
      f32x4 acc[4][6];
      g1_main<6>(wid, lane, 0, acc);
      BAR(12);
      STAMP(3);
      g1_epilogue<6>(rw, wid, lane, 12 * ti, ch, L_ST, L_CS, acc);
    } else {
      f32x4 acc4[4][4], acc2[4][2];
      g1_main<4>(wid, lane, 0, acc4);
      g1_main<2>(wid, lane, STAGE, acc2);
      BAR(12);
      STAMP(3);
      g1_epilogue<4>(rw, wid, lane, 120, ch, L_ST, L_CS, acc4);
      g1_epilogue<2>(rw, wid, lane, 120 + 4 * par, ch2, L_ST + par * 1024, L_CS + 512, acc2);
    }
    STAMP(4);
    lds_set(L_MISC + 48 + wid * 4);
    wait_vm<0>();
    __builtin_amdgcn_s_waitcnt(0x0F70);
    STAMP(5);
    arrive_publish(L_MISC + 8, 4, rw, WS_F1 + (normal ? ti * 24 + ch : 240 + 2 * mm_ + par) * 8, lane);
    const unsigned wso = WS_WDT + (12 * wid * 40 + 5 * dj) * 1024, hso = WS_H + (4 * t64 * 48 + 12 * wid) * 1024;
    const unsigned vo0 = lane * 16, vo1 = lane * 16 + 4096, pw = pbase(wid) + lane * 16;
    v4u wr[5][5], hr[3][4];
#pragma unroll
    for (int i = 0; i < 25; ++i) asm volatile("" : "=v"(wr[i / 5][i % 5]));
#pragma unroll
    for (int i = 0; i < 12; ++i) asm volatile("" : "=v"(hr[i / 4][i % 4]));
    auto ldw = [&](int s) {
      const unsigned so = wso + kst(s) * 40960;
      asm volatile("buffer_load_dwordx4 %0, %5, %7, %8 offen sc1\n\tbuffer_load_dwordx4 %1, %5, %7, %8 offen offset:1024 sc1\n\t"
                   "buffer_load_dwordx4 %2, %5, %7, %8 offen offset:2048 sc1\n\tbuffer_load_dwordx4 %3, %5, %7, %8 offen offset:3072 sc1\n\t"
                   "buffer_load_dwordx4 %4, %6, %7, %8 offen sc1"
                   : "+v"(wr[s % 5][0]), "+v"(wr[s % 5][1]), "+v"(wr[s % 5][2]), "+v"(wr[s % 5][3]), "+v"(wr[s % 5][4])
                   : "v"(vo0), "v"(vo1), "s"(sw), "s"(so) : "memory");
    };
    auto ldh = [&](int s) {
      const unsigned so = hso + kst(s) * 1024;
      asm volatile("buffer_load_dwordx4 %0, %4, %5, %6 offen sc1\n\tbuffer_load_dwordx4 %1, %4, %5, %7 offen sc1\n\t"
                   "buffer_load_dwordx4 %2, %4, %5, %8 offen sc1\n\tbuffer_load_dwordx4 %3, %4, %5, %9 offen sc1"
                   : "+v"(hr[s % 3][0]), "+v"(hr[s % 3][1]), "+v"(hr[s % 3][2]), "+v"(hr[s % 3][3])
                   : "v"(vo0), "s"(sw), "s"(so), "s"(so + 49152), "s"(so + 98304), "s"(so + 147456) : "memory");
    };
#pragma unroll
    for (int s = 0; s < 5; ++s) ldw(s);
    lds_wait(L_MISC + 32);
    STAMP(6);
#pragma unroll
    for (int s = 0; s < 3; ++s) ldh(s);
    f32x4 acc[4][5];
#pragma unroll
    for (int s = 0; s < 12; ++s) {
      constexpr int YN[12] = {8, 13, 18, 23, 23, 18, 13, 8, 8, 8, 4, 0};
      asm volatile("s_waitcnt vmcnt(%9)"
                   : "+v"(hr[s % 3][0]), "+v"(hr[s % 3][1]), "+v"(hr[s % 3][2]), "+v"(hr[s % 3][3]), "+v"(wr[s % 5][0]), "+v"(wr[s % 5][1]),
                     "+v"(wr[s % 5][2]), "+v"(wr[s % 5][3]), "+v"(wr[s % 5][4])
                   : "i"(YN[s]) : "memory");
#pragma unroll
      for (int t = 0; t < 4; ++t)
#pragma unroll
        for (int dd = 0; dd < 5; ++dd)
          acc[t][dd] = mfma(__builtin_bit_cast(bf16x8, wr[s % 5][dd]), __builtin_bit_cast(bf16x8, hr[s % 3][t]), s == 0 ? f32x4{0.f, 0.f, 0.f, 0.f} : acc[t][dd]);
      __builtin_amdgcn_sched_barrier(0);
      if (s == G2SIG && wid == 0) lds_set(L_MISC + 36);
      if (s + 3 < 12) ldh(s + 3);
      if (s < 4) ldw(s + 5);
      if (s >= 4 && s < 7) {
#pragma unroll
        for (int t = 0; t < 5; ++t) wr[s % 5][t] = *(LAS v4u*)lds(pw + (s - 4) * 5120 + t * 1024);
      }
      __builtin_amdgcn_sched_barrier(0);
    }
    STAMP(7);
    const int c = lane & 15, g = lane >> 4;
    const unsigned oo = ((4 * t64 + wid) * 16 + c) * 1280 + (80 * dj + 4 * g) * 2;
    v2u xr[5];
#pragma unroll
    for (int dd = 0; dd < 5; ++dd) xr[dd] = __builtin_amdgcn_raw_buffer_load_b64(rx, oo + dd * 32, 0, 0);
    f32x4 own[5];
#pragma unroll
    for (int t = 0; t < 4; ++t) {
      if (t == wid) {
#pragma unroll
        for (int dd = 0; dd < 5; ++dd) own[dd] = acc[t][dd];
      } else {
        const unsigned o = pw + (t > wid ? t - 1 : t) * 5120;
#pragma unroll
        for (int dd = 0; dd < 5; ++dd) *(LAS f32x4*)lds(o + dd * 1024) = acc[t][dd];
      }
    }
    wait_lg();
    BAR(11);
    STAMP(8);
#pragma unroll
    for (int k = 1; k < 4; ++k) {
      const int w = (wid + k) & 3;
      const unsigned o = pbase(w) + lane * 16 + (wid > w ? wid - 1 : wid) * 5120;
#pragma unroll
      for (int dd = 0; dd < 5; ++dd) own[dd] += *(LAS f32x4*)lds(o + dd * 1024);
    }
    const rsrc_t ro = mkr(out, L * D * 2);
    const unsigned at = L_AT + (4 * g + ((lane >> 2) & 3)) * 128 + (16 * wid + 4 * (lane & 3)) * 2;
#pragma unroll
    for (int dd = 0; dd < 5; ++dd) {
      const v2u a = __builtin_bit_cast(v2u, __builtin_amdgcn_ds_read_tr16_b64_v4i16((LAS s16x4*)lds(at + dd * 2048)));
      const float o0 = own[dd][0] + blo(a.x) + blo(xr[dd].x), o1 = own[dd][1] + bhi(a.x) + bhi(xr[dd].x);
      const float o2 = own[dd][2] + blo(a.y) + blo(xr[dd].y), o3 = own[dd][3] + bhi(a.y) + bhi(xr[dd].y);
      __builtin_amdgcn_raw_buffer_store_b64(v2u{pk(o0, o1), pk(o2, o3)}, ro, oo + dd * 32, 0, 0);
    }
    STAMP(9);
  } else if (wid < 7) {
    const int cw = wid - 4;
    v4u carry = {0, 0, 0, 0}, sink = {0, 0, 0, 0}, kraw[2] = {};
    auto warm_wd = [&]() {
#pragma unroll 1
      for (int i = 0; i < 6; ++i) {
        const int w = 2 * cw + (i >= 3), s = i >= 3 ? i - 3 : i;
        const unsigned so = WS_WDT + ((12 * w + kst(9 + s)) * 40 + 5 * dj) * 1024;
#pragma unroll
        for (int t = 0; t < 5; ++t) touch(sink, sw, lane * 16, so + t * 1024);
      }
      wait_vm<30>();
    };
    if (normal) {
      LnOut lo2;
      const bool ln2 = cw == 2 && (j == 20 || j == 21);
      if (cw < nch) {
        f32x4 ka[2][2];
        kgen<2>(pw, dt, lane, L_KG + cw * KG_SZ, L_MISC + 68 + cw * 4, ka);
        STAMP(12);
        lds_wait(L_MISC + 16 + cw * 4);
#pragma unroll
        for (int i = 0; i < 4; ++i) ka[i >> 1][i & 1] += *(LAS f32x4*)lds(L_KG + cw * KG_SZ + 16 * 544 + i * 1024 + lane * 16);
        if (lane == 0) ka[0][0][0] += (kd & 1) ? bhi(dsw_) : blo(dsw_);
#pragma unroll
        for (int rt = 0; rt < 2; ++rt)
          kraw[rt] = v4u{pk(ka[rt][0][0], ka[rt][0][1]), pk(ka[rt][0][2], ka[rt][0][3]), pk(ka[rt][1][0], ka[rt][1][1]), pk(ka[rt][1][2], ka[rt][1][3])};
      } else if (ln2) {
        ln8(rx, 240 + 2 * xcd + j - 20, lane, L_KG + 2 * KG_SZ, lo2);
      }
      wait_lg();
      STAMP(1);
      if (cw < 2) {
        g1_loader<6, 1>(sx, sg, su, cw, lane, 192 * ti, ch, rot, STAGE);
        warm_wd(); BAR(9);
      } else {
        BAR(0);
        colsum(0, lane); colsum(1, lane);
        BAR(1);
        if (ln2) ln8_store(rw, 240 + 2 * xcd + j - 20, lane, lo2);
        BAR(2);
        wait_vm<0>();
#pragma unroll 1
        for (int k = 3; k < 10; ++k) BAR(k);
      }
      if (cw < nch) {
        const unsigned hb = L_H + cw * H_SZ;
        __builtin_amdgcn_s_sleep(4);
#pragma unroll
        for (int rt = 0; rt < 2; ++rt) *(LAS v4u*)lds(L_RAW + cw * 2048 + rt * 1024 + lane * 16) = kraw[rt];
        if (lane < 9) *(LAS v4u*)lds(hb + lane * H_CS) = v4u{0, 0, 0, 0};
        hbuild<0>(L_RAW + cw * 2048, hb, lane, carry);
      }
      BAR(12);
      if (cw < nch) {
        hbuild<1>(L_RAW + cw * 2048, L_H + cw * H_SZ, lane, carry);
        htail(L_RAW + cw * 2048, L_H + cw * H_SZ, L_K3 + cw * 320, lane, carry);
      }
    } else if (cw < 2) {
      g1_loader<4, 0>(sx, sg, su, cw, lane, 1920, ch, 0, 0);
      wait_vm<0>(); BAR(9);
      g1_loader<2, 0>(sx, sg, su, cw, lane, 1920 + 64 * par, ch2, 0, STAGE);
      warm_wd(); BAR(9);
      BAR(12);
    } else {
#pragma unroll 1
      for (int s = 0; s < 21; ++s) bar();
    }
    asm volatile("s_waitcnt vmcnt(0)" : "+v"(sink)::"memory");
    STAMP(4);
    if (cw < nch) {
      lds_wait(L_MISC + 48 + cw * 4);
      lds_wait(L_MISC + 64);
      STAMP(5);
      __builtin_amdgcn_s_setprio(2);
      conv(rw, cw, d0 + cw, lane);
      STAMP(6);
      wait_vm<0>();
      arrive_publish(L_MISC + 12, nch, rw, WS_F2 + (xcd * 32 + j) * 8, lane);
    }
    STAMP(7);
    BAR(11);
  } else {
    const v2u e = __builtin_amdgcn_raw_buffer_load_b64(rw, WS_EPOCH, 0, SC1);
    const int tb = 120 * (xcd & 1) + 30 * (xcd >> 1) + j;
    LnOut lo;
    if (normal) ln8(rx, tb, lane, L_LN, lo);
    const u64 tg = (((u64)e.y << 32) | e.x) + 0x9E3779B97F4A7C15ull;
    const v2u tag = {(unsigned)tg, (unsigned)(tg >> 32)};
    if (lane == 0) *(LAS v4u*)lds(L_MISC) = v4u{tag.x, tag.y, 0, 0};
    wait_lg();
    STAMP(1);
    BAR(0);
    if (normal) { colsum(2, lane); colsum(3, lane); }
    BAR(1);
    if (normal) {
      ln8_store(rw, tb, lane, lo);
      if (lane < 32) st128<SC1>(*(LAS v4u*)lds(L_CSP + lane * 16), rw, lane * 16 + WS_CSP + (ch * 10 + ti) * 512);
      const unsigned a0 = L_WDU + (4 * (lane >> 4) + ((lane >> 2) & 3)) * 128 + 8 * (lane & 3);
#pragma unroll
      for (int p = 0; p < 8; ++p) {
        const unsigned a = a0 + (p >> 2) * 4096 + (p & 3) * 32;
        st128<SC1>(__builtin_bit_cast(v4u, trd(a, a + 2048)), rw, lane * 16 + WS_WDT + ((2 * ch + (p >> 2)) * 40 + 4 * ti + (p & 3)) * 1024);
      }
    }
    BAR(2); BAR(3);
    wait_vm<0>();
    if (lane == 0) put_flag(rw, WS_F0 + b * 8, tag);
    BAR(4); BAR(5); BAR(6);
    if (!normal) {
#pragma unroll 1
      for (int s = 0; s < 10; ++s) bar();
    }
    poll<4>(rw, WS_F0, 256, tag, lane);
    STAMP(2);
    if (b == 0 && lane == 0) put_flag(rw, WS_EPOCH, tag);
    const int row0 = normal ? 192 * ti : 1920;
    v2u st[3], cp[2][10];
#pragma unroll
    for (int k = 0; k < 3; ++k) { const int r = row0 + lane + 64 * k; st[k] = __builtin_amdgcn_raw_buffer_load_b64(rw, (r >> 3) * 128 + (r & 7) * 8, WS_STATS, SC1); }
#pragma unroll
    for (int k = 0; k < 10; ++k) cp[0][k] = __builtin_amdgcn_raw_buffer_load_b64(rw, lane * 8 + k * 512, WS_CSP + ch * 5120, SC1);
    if (!normal) {
#pragma unroll
      for (int k = 0; k < 10; ++k) cp[1][k] = __builtin_amdgcn_raw_buffer_load_b64(rw, lane * 8 + k * 512, WS_CSP + ch2 * 5120, SC1);
    }
    BAR(7);
    v4u ur[16];
    if ((lane & 3) < nch) {
#pragma unroll
      for (int p = 0; p < 16; ++p) ur[p] = __builtin_amdgcn_raw_buffer_load_b128(rw, (lane >> 2) * 10240 + (lane & 3) * 16, WS_U8 + p * 163840 + d0 * 16, SC1);
    }
    BAR(8);
    f32x4 so[3];
#pragma unroll
    for (int k = 0; k < 3; ++k) {
      const float m = __uint_as_float(st[k].x), den = __uint_as_float(st[k].y);
      so[k] = f32x4{-m, den * den, -1.44269504f * __builtin_amdgcn_rcpf(den), 0.f};
    }
    f32x2 co[2];
#pragma unroll
    for (int h = 0; h < 2; ++h) {
      float c0 = 0.f, c1 = 0.f;
      if (h == 0 || !normal) {
#pragma unroll
        for (int k = 0; k < 10; ++k) { c0 += __uint_as_float(cp[h][k].x); asm volatile("" : "+v"(c0)); c1 += __uint_as_float(cp[h][k].y); }
      }
      co[h] = f32x2{c0, c1};
    }
    asm volatile("" : "+v"(so[0]), "+v"(so[1]), "+v"(so[2]), "+v"(co[0]), "+v"(co[1]));
    BAR(9);
    __builtin_amdgcn_s_sleep(4);
#pragma unroll
    for (int k = 0; k < 3; ++k) *(LAS f32x4*)lds(L_ST + (lane + 64 * k) * 16) = so[k];
#pragma unroll
    for (int h = 0; h < 2; ++h) *(LAS f32x2*)lds(L_CS + h * 512 + lane * 8) = co[h];
    wait_lg();
    BAR(12);
    if (lane < 32) {
#pragma unroll
      for (int k = 0; k < 3; ++k) *(LAS v4u*)lds(L_U + k * U_SZ + lane * 16) = v4u{0, 0, 0, 0};
    }
    if ((lane & 3) < nch) {
#pragma unroll
      for (int p = 0; p < 16; ++p) *(LAS v4u*)lds(L_U + (lane & 3) * U_SZ + 512 + p * 256 + (lane >> 2) * 16) = ur[p];
    }
    wait_lg();
    lds_set(L_MISC + 64);
    lds_wait(L_MISC + 60);
    STAMP(4);
#pragma unroll 1
    for (int i = 0; i < 12; ++i) {
      const int w = i / 3, s = i - 3 * w;
      const unsigned so = WS_WDT + ((12 * w + kst(9 + s)) * 40 + 5 * dj) * 1024, lo = pbase(w) + s * 5120;
#pragma unroll
      for (int t = 0; t < 5; ++t) dma<SC1>(sw, lo + t * 1024, lane * 16, so + t * 1024);
    }
    wait_vm<0>();
    STAMP(8);
    poll<1>(rw, WS_F1 + (t64 < 30 ? (t64 / 3) * 192 : 1920), t64 < 30 ? 24 : 16, tag, lane);
    lds_set(L_MISC + 32);
    STAMP(5);
    lds_wait(L_MISC + 36);
    poll<1>(rw, WS_F2 + dj * 256, 30, tag, lane);
    STAMP(6);
#pragma unroll
    for (int p = 0; p < 10; ++p) dma<SC1>(sw, L_AT + p * 1024, (lane >> 3) * 4096 + (lane & 7) * 16, WS_AT + (80 * dj + p * 8) * 4096 + t64 * 128);
    wait_vm<0>();
    STAMP(7);
    BAR(11);
  }
  DUMP();
#endif
}

__global__ __launch_bounds__(64) void probeSpin(const unsigned* t) {
#if __HIP_DEVICE_COMPILE__
  const unsigned long long tstart = __builtin_amdgcn_s_memrealtime();
  if (PKIND == 4) return;
  unsigned long long lo = ~0ull, hiS = 0, hiE = 0, sum = 0;
  for (int b = 0; b < 256; ++b) {
    const unsigned* p = t + (b * 8) * 64;
    unsigned long long s0 = *(const unsigned long long*)(p + 48), e0 = *(const unsigned long long*)(p + 50);
    lo = s0 < lo ? s0 : lo; hiS = s0 > hiS ? s0 : hiS; hiE = e0 > hiE ? e0 : hiE;
    if (b < 240) sum += (unsigned)(t[(b * 8 + PW) * 64 + PS] - p[0]);
  }
  unsigned long long ticks = PKIND == 0 ? (hiE - lo) * 100 : PKIND == 1 ? (hiS - lo) * 100 : PKIND == 2 ? sum / 240 * 10 : PKIND == 3 ? (tstart - hiE) * 100 : PKIND == 5 ? (lo - *(const unsigned long long*)(t + 200000)) * 100 : 0;
  if (ticks > 1000000) ticks = 1000000;
  unsigned long long t0 = __builtin_amdgcn_s_memrealtime();
  while (__builtin_amdgcn_s_memrealtime() - t0 < ticks) __builtin_amdgcn_s_sleep(8);
#endif
}

extern "C" void kernel_launch(void* const* d_in, const int* in_sizes, int n_in, void* d_out, int out_size, void* d_ws, size_t ws_size,
                              hipStream_t stream) {
  (void)hipFuncSetAttribute((const void*)fused_s4_ffn, hipFuncAttributeMaxDynamicSharedMemorySize, LDS_BYTES);
  fused_s4_ffn<<<256, 512, LDS_BYTES, stream>>>(
      (const uint16_t*)d_in[0], (const uint16_t*)d_in[1], (const uint16_t*)d_in[2], (const uint16_t*)d_in[3], (const uint16_t*)d_in[4],
      (const uint16_t*)d_in[5], (const uint16_t*)d_in[6], (const uint16_t*)d_in[7], (const uint16_t*)d_in[8], (const uint16_t*)d_in[10],
      (const uint16_t*)d_in[11], (const uint16_t*)d_in[12], (uint16_t*)d_out, d_ws);
  probeSpin<<<1, 64, 0, stream>>>((const unsigned*)((char*)d_ws + WS_TM));
}
```

```cpp
#define BIGV 1
#include <hip/hip_runtime.h>
#include <stdint.h>

typedef short bf16x8 __attribute__((ext_vector_type(8)));
typedef short s16x4 __attribute__((ext_vector_type(4)));
typedef float f32x4 __attribute__((ext_vector_type(4)));
typedef float f32x2 __attribute__((ext_vector_type(2)));
typedef __bf16 bf16x2 __attribute__((ext_vector_type(2)));
typedef unsigned v4u __attribute__((ext_vector_type(4)));
typedef unsigned v2u __attribute__((ext_vector_type(2)));
typedef unsigned v8u __attribute__((ext_vector_type(8)));
typedef unsigned long long u64;
#define LAS __attribute__((address_space(3)))
#define DI __device__ __forceinline__
typedef __amdgpu_buffer_rsrc_t rsrc_t;

constexpr int L = 2048, D = 640, NS = 192, F = 1536;
constexpr int SC1 = 16, NT_ = 2;

constexpr unsigned WS_EPOCH = 0, WS_F0 = 1024, WS_F1 = 4096, WS_F2 = 8192, WS_STATS = 16384, WS_CSP = 65536, WS_U8 = 262144,
                   WS_WDT = 3u << 20, WS_H = 5u << 20, WS_AT = 12u << 20, WS_TM = 15u << 20, WS_BYTES = 16u << 20;

constexpr unsigned STAGE = 40960, XB = 24576;
constexpr unsigned LX = 3 * STAGE;
constexpr unsigned L_RAW = LX;
constexpr unsigned L_ST = L_RAW + 3 * 2048;
constexpr unsigned L_CS = L_ST + 3072;
constexpr unsigned L_MISC = L_CS + 1024;
constexpr unsigned L_U = L_MISC + 128, U_SZ = 4608;
constexpr unsigned L_LN = L_U + 3 * U_SZ, LN_RS = 1296;
constexpr unsigned L_END = L_LN + 10240;
constexpr unsigned L_WDU = L_U, L_DUMMY = L_U + 8192, L_CSP = L_U + 9216;
constexpr unsigned L_KG = 2 * STAGE, KG_SZ = 16 * (544 + 288);
constexpr unsigned L_H = STAGE, H_CS = 2112, H_SZ = 8 * H_CS;
constexpr unsigned L_K3 = L_END, L_AT = L_LN;
constexpr unsigned LDS_BYTES = 163840;
static_assert(L_END + 960 <= LDS_BYTES, "lds");
constexpr unsigned L_P2 = L_H + 3 * H_SZ + 64;
static_assert(L_P2 + 2 * 15360 <= LX, "lds partials");
#define G2AUXW SC1
#define G2AUXH SC1
#define ROT2 1
#define G2SIG 6
#define XROT 0
constexpr int SPIN_MAX = 20000;
constexpr unsigned CODE_BYTES = 47104;

#if __HIP_DEVICE_COMPILE__
extern __shared__ __attribute__((aligned(16))) char smem_[];
DI LAS char* lds(unsigned o) { return (LAS char*)smem_ + o; }
DI rsrc_t mkr(const void* p, unsigned bytes) { return __builtin_amdgcn_make_buffer_rsrc((void*)p, 0, bytes, 0x00020000); }
DI unsigned pk(float a, float b) { f32x2 f = {a, b}; return __builtin_bit_cast(unsigned, __builtin_convertvector(f, bf16x2)); }
DI unsigned pk2(f32x2 f) { return __builtin_bit_cast(unsigned, __builtin_convertvector(f, bf16x2)); }
DI float blo(unsigned v) { return __uint_as_float(v << 16); }
DI float bhi(unsigned v) { return __uint_as_float(v & 0xffff0000u); }
DI float rbf(float f) { return blo(pk(f, f)); }
DI void bar() { __builtin_amdgcn_s_barrier(); }
DI void cfence() { asm volatile("" ::: "memory"); }
template <int N> DI void wait_vm() { asm volatile("s_waitcnt vmcnt(%0)" ::"i"(N) : "memory"); }
DI void wait_lg() { asm volatile("s_waitcnt lgkmcnt(0)" ::: "memory"); }
DI bf16x8 trd(unsigned a0, unsigned a1) {
  s16x4 lo = __builtin_amdgcn_ds_read_tr16_b64_v4i16((LAS s16x4*)lds(a0));
  s16x4 hi = __builtin_amdgcn_ds_read_tr16_b64_v4i16((LAS s16x4*)lds(a1));
  return __builtin_shufflevector(lo, hi, 0, 1, 2, 3, 4, 5, 6, 7);
}
DI bf16x8 rd128(unsigned a) { return *(LAS bf16x8*)lds(a); }
DI f32x4 mfma(bf16x8 a, bf16x8 b, f32x4 c) { return __builtin_amdgcn_mfma_f32_16x16x32_bf16(a, b, c, 0, 0, 0); }
DI v4u srd(const void* p, unsigned bytes) { const u64 a = (u64)p; return v4u{(unsigned)a, (unsigned)(a >> 32) & 0xffffu, bytes, 0x00020000u}; }
template <int AUX> DI void dma(v4u r, unsigned ldsoff, unsigned voff, unsigned soff) {
  if (AUX == 0) asm volatile("s_mov_b32 m0, %0\n\ts_nop 0\n\tbuffer_load_dwordx4 %1, %2, %3 offen lds" ::"s"(ldsoff), "v"(voff), "s"(r), "s"(soff) : "memory");
  if (AUX == NT_) asm volatile("s_mov_b32 m0, %0\n\ts_nop 0\n\tbuffer_load_dwordx4 %1, %2, %3 offen nt lds" ::"s"(ldsoff), "v"(voff), "s"(r), "s"(soff) : "memory");
  if (AUX == SC1) asm volatile("s_mov_b32 m0, %0\n\ts_nop 0\n\tbuffer_load_dwordx4 %1, %2, %3 offen sc1 lds" ::"s"(ldsoff), "v"(voff), "s"(r), "s"(soff) : "memory");
}
template <int AUX> DI void st128(v4u v, rsrc_t r, unsigned off) { __builtin_amdgcn_raw_buffer_store_b128(v, r, off, 0, AUX); }
DI void touch(v4u& sink, v4u r, unsigned vo, unsigned so) {
  asm volatile("buffer_load_dwordx4 %0, %1, %2, %3 offen sc1" : "+v"(sink) : "v"(vo), "s"(r), "s"(so) : "memory");
}
DI float rowsum16(float v) {
  v += __builtin_bit_cast(float, __builtin_amdgcn_update_dpp(0, __builtin_bit_cast(int, v), 0x128, 0xf, 0xf, false));
  v += __builtin_bit_cast(float, __builtin_amdgcn_update_dpp(0, __builtin_bit_cast(int, v), 0x124, 0xf, 0xf, false));
  v += __builtin_bit_cast(float, __builtin_amdgcn_update_dpp(0, __builtin_bit_cast(int, v), 0x122, 0xf, 0xf, false));
  v += __builtin_bit_cast(float, __builtin_amdgcn_update_dpp(0, __builtin_bit_cast(int, v), 0x121, 0xf, 0xf, false));
  return v;
}
DI f32x2 cmul(f32x2 a, f32x2 b) {
  f32x2 t = a * b.xx, r;
  asm("v_pk_fma_f32 %0, %1, %2, %3 op_sel:[1,1,0] op_sel_hi:[0,1,1] neg_lo:[0,1,0]" : "=v"(r) : "v"(a), "v"(b), "v"(t));
  return r;
}
DI f32x2 cpow(float lr, float tr, float e) {
  float m = __builtin_amdgcn_exp2f(lr * e), th = __builtin_amdgcn_fractf(tr * e);
  return f32x2{m * __builtin_amdgcn_cosf(th), m * __builtin_amdgcn_sinf(th)};
}
DI v2u tag_of(void) { return *(LAS v2u*)lds(L_MISC); }
DI void put_flag(rsrc_t rw, unsigned off, v2u tag) { __builtin_amdgcn_raw_buffer_store_b64(tag, rw, off, 0, SC1); }
template <int R> DI void poll(rsrc_t rw, unsigned off, int n, v2u tag, int lane) {
#pragma unroll 1
  for (int spin = 0; spin < SPIN_MAX; ++spin) {
    bool ok = true;
#pragma unroll
    for (int r = 0; r < R; ++r) {
      int i = lane + 64 * r; i = i < n ? i : n - 1;
      v2u f = __builtin_amdgcn_raw_buffer_load_b64(rw, off + i * 8, 0, SC1);
      ok = ok && f.x == tag.x && f.y == tag.y;
    }
    if (__all(ok)) break;
    cfence();
  }
  cfence();
}
DI void lds_set(unsigned o) { cfence(); *(volatile LAS unsigned*)lds(o) = 1; }
DI void lds_wait(unsigned o) {
#pragma unroll 1
  for (int spin = 0; spin < 16 * SPIN_MAX && *(volatile LAS unsigned*)lds(o) == 0; ++spin) __builtin_amdgcn_s_sleep(1);
  cfence();
}
DI void arrive_publish(unsigned cnt, int n, rsrc_t rw, unsigned off, int lane) {
  unsigned old = 0;
  if (lane == 0) old = __hip_atomic_fetch_add((LAS unsigned*)lds(cnt), 1u, __ATOMIC_RELAXED, __HIP_MEMORY_SCOPE_WORKGROUP);
  old = __builtin_amdgcn_readfirstlane(old);
  if ((int)old == n - 1 && lane == 0) put_flag(rw, off, tag_of());
}

#define STAMP(i)
#define BAR(i) bar()
#define DUMP()

struct G1Ld { unsigned vx0, vx1, vw0, vw1; };
DI G1Ld g1_lanes(int lane) {
  const int r8 = lane >> 3, sl = lane & 7;
  return G1Ld{(unsigned)(r8 * 1280 + ((sl ^ (r8 >> 1)) << 4)), (unsigned)(r8 * 1280 + ((sl ^ (r8 >> 1) ^ 4) << 4)),
              (unsigned)(r8 * 3072 + ((sl ^ (r8 & 2)) << 4)), (unsigned)(r8 * 3072 + ((sl ^ (r8 & 2) ^ 4) << 4))};
}
template <int NT> DI void g1_issue(v4u rx, v4u rg, v4u ru, const G1Ld& v, int lw, int s, unsigned lb, int row0, int ch) {
  const unsigned xso = (row0 + lw * 16 * NT) * 1280 + s * 128, wso = ch * 128 + (lw * 32 + s * 64) * 3072;
#pragma unroll
  for (int p = 0; p < 4; ++p) {
    dma<0>(rg, lb + XB + (lw * 4 + p) * 1024, (p & 1) ? v.vw1 : v.vw0, wso + p * 8 * 3072);
    dma<0>(ru, lb + XB + 8192 + (lw * 4 + p) * 1024, (p & 1) ? v.vw1 : v.vw0, wso + p * 8 * 3072);
  }
#pragma unroll
  for (int p = 0; p < 2 * NT; ++p) dma<0>(rx, lb + (lw * 2 * NT + p) * 1024, (p & 1) ? v.vx1 : v.vx0, xso + p * 8 * 1280);
}
DI unsigned nxt(unsigned so) { return so == 2 * STAGE ? 0 : so + STAGE; }
template <int NT, int S0> DI void g1_loader(v4u rx, v4u rg, v4u ru, int lw, int lane, int row0, int ch, int rot, unsigned so) {
  constexpr int NP = 8 + 2 * NT;
  const G1Ld v = g1_lanes(lane);
  if (S0 == 1) BAR(0);
#pragma unroll 1
  for (int s = S0; s < 10; ++s) {
    if (s >= S0 + 2) wait_vm<NP>();
    if (s >= S0 + 2) BAR(s - 2);
    g1_issue<NT>(rx, rg, ru, v, lw, s + rot >= 10 ? s + rot - 10 : s + rot, so, row0, ch);
    so = nxt(so);
  }
  wait_vm<NP>(); BAR(8);
}

DI void colsum(int k, int lane) {
  const int c = lane & 15, g = lane >> 4, q = (lane >> 2) & 3, p = lane & 3;
  const bf16x8 ones = {0x3f80, 0x3f80, 0x3f80, 0x3f80, 0x3f80, 0x3f80, 0x3f80, 0x3f80};
  f32x4 r[2];
#pragma unroll
  for (int a = 0; a < 2; ++a) {
    const unsigned ad = XB + (k >> 1) * 8192 + (8 * g + q) * 128 + (((4 * (k & 1) + 2 * a + (p >> 1)) ^ (q & 2) ^ ((g & 1) << 2)) << 4) + 8 * (p & 1);
    r[a] = mfma(trd(ad + 4096, ad + 4608), ones, mfma(trd(ad, ad + 512), ones, f32x4{0.f, 0.f, 0.f, 0.f}));
  }
  f32x4 v;
#pragma unroll
  for (int e = 0; e < 4; ++e) v[e] = c ? r[1][e] : r[0][e];
  if (c < 2) *(LAS f32x4*)lds(L_CSP + k * 128 + (16 * c + 4 * g) * 4) = v;
  wait_lg();
}

template <int NT> DI void g1_main(int wid, int lane, unsigned so, f32x4 (&acc)[4][NT]) {
  const int wa = wid & 1, wt = wid >> 1;
  const int c = lane & 15, g = lane >> 4, q = (lane >> 2) & 3, p = lane & 3;
  unsigned aA[2], aB[2];
#pragma unroll
  for (int a = 0; a < 2; ++a) aA[a] = XB + (8 * g + q) * 128 + (((4 * wa + 2 * a + (p >> 1)) ^ (q & 2) ^ ((g & 1) << 2)) << 4) + 8 * (p & 1);
#pragma unroll
  for (int ks = 0; ks < 2; ++ks) aB[ks] = (16 * NT * wt + c) * 128 + (((4 * ks + g) ^ (c >> 1)) << 4);
#pragma unroll
  for (int a = 0; a < 4; ++a)
#pragma unroll
    for (int t = 0; t < NT; ++t) acc[a][t] = f32x4{0.f, 0.f, 0.f, 0.f};
  bf16x8 fa[2][4], fb[2][NT];
  auto rd = [&](int buf, int ks, unsigned o) {
#pragma unroll
    for (int a = 0; a < 4; ++a) { unsigned ad = aA[a & 1] + o + (a >> 1) * 8192 + ks * 4096; fa[buf][a] = trd(ad, ad + 512); }
#pragma unroll
    for (int t = 0; t < NT; ++t) fb[buf][t] = rd128(aB[ks] + o + t * 2048);
  };
  auto mm = [&](int buf) {
#pragma unroll
    for (int t = 0; t < NT; ++t)
#pragma unroll
      for (int a = 0; a < 4; ++a) acc[a][t] = mfma(fa[buf][a], fb[buf][t], acc[a][t]);
  };
  auto ilv = [&]() {
    constexpr int NR = 8 + NT, NM = 4 * NT, NI = NR < NM ? NR : NM;
#pragma unroll
    for (int i = 0; i < NI; ++i) { __builtin_amdgcn_sched_group_barrier(0x008, 1, 0); __builtin_amdgcn_sched_group_barrier(0x100, 1, 0); }
    if (NM > NI) __builtin_amdgcn_sched_group_barrier(0x008, NM - NI, 0);
    __builtin_amdgcn_sched_barrier(0);
  };
  BAR(0);
  STAMP(2);
  rd(0, 0, so);
#pragma unroll 1
  for (int s = 0; s < 9; ++s) {
    rd(1, 1, so); mm(0); ilv();
    so = nxt(so);
    wait_lg();
    BAR(s + 1);
    __builtin_amdgcn_sched_barrier(0);
    rd(0, 0, so); mm(1); ilv();
  }
  rd(1, 1, so); mm(0); ilv();
  mm(1);
  __builtin_amdgcn_sched_barrier(0);
}
template <int NT> DI void g1_epilogue(rsrc_t rw, int wid, int lane, int t16_0, int ch, unsigned st_off, unsigned cs_off, const f32x4 (&acc)[4][NT]) {
  const int wa = wid & 1, wt = wid >> 1, c = lane & 15, g = lane >> 4;
  f32x4 cs[4];
#pragma unroll
  for (int a = 0; a < 4; ++a) cs[a] = *(LAS f32x4*)lds(cs_off + (a >> 1) * 256 + (32 * wa + 16 * (a & 1) + 4 * g) * 4);
  f32x4 st[NT];
#pragma unroll
  for (int t = 0; t < NT; ++t) st[t] = *(LAS f32x4*)lds(st_off + (16 * NT * wt + 16 * t + c) * 16);
#pragma unroll
  for (int t = 0; t < NT; ++t) {
    const float nm = st[t][0], qq = st[t][1], rl = st[t][2];
    v4u o;
#pragma unroll
    for (int a = 0; a < 2; ++a) {
      f32x4 tg = cs[a] * nm + acc[a][t], tu = cs[2 + a] * nm + acc[2 + a][t];
      f32x4 ar = tg * rl, e;
#pragma unroll
      for (int r = 0; r < 4; ++r) e[r] = __builtin_amdgcn_exp2f(ar[r]);
      f32x4 dn = e * qq + qq, pr = tg * tu;
#pragma unroll
      for (int r = 0; r < 4; ++r) dn[r] = __builtin_amdgcn_rcpf(dn[r]);
      pr *= dn;
      o[2 * a] = pk(pr[0], pr[1]); o[2 * a + 1] = pk(pr[2], pr[3]);
    }
    st128<SC1>(o, rw, lane * 16 + WS_H + ((t16_0 + NT * wt + t) * 48 + 2 * ch + wa) * 1024);
  }
}

template <int NM> DI void kgen(const unsigned (&w)[6], float dt, int lane, unsigned tb, unsigned done, f32x4 (&acc)[2][2]) {
  constexpr int RS = NM * 256 + 32, KS = NM * 4;
  float lr[NM], tr[NM]; f32x2 W[NM];
#pragma unroll
  for (int m = 0; m < NM; ++m) {
    float v[6];
#pragma unroll
    for (int i = 0; i < 6; ++i) v[i] = m == 0 ? blo(w[i]) : bhi(w[i]);
    lr[m] = dt * v[0] * 1.44269504f; tr[m] = dt * v[1] * 0.15915494f;
    const float br = dt * v[2], bi = dt * v[3];
    W[m] = f32x2{v[4] * br - v[5] * bi, -(v[4] * bi + v[5] * br)};
  }
  asm volatile("" : "+v"(lr[0]), "+v"(W[0]));
  STAMP(13);
  unsigned wa = tb + lane * 4 * NM, fa = tb + (lane & 15) * RS + (lane >> 4) * 16;
  asm volatile("" : "+v"(wa), "+v"(fa));
  auto put = [&](int row, const f32x2* z) {
    if (NM == 2) *(LAS v2u*)lds(wa + row * RS) = v2u{pk2(z[0]), pk2(z[NM - 1])};
    else *(LAS unsigned*)lds(wa + row * RS) = pk2(z[0]);
  };
  f32x2 c[8][NM], hi[NM], t[NM], c8[NM];
#pragma unroll
  for (int m = 0; m < NM; ++m) {
    c[0][m] = f32x2{1.f, 0.f}; c[1][m] = cpow(lr[m], tr[m], 8.f);
    c[2][m] = cmul(c[1][m], c[1][m]); c[3][m] = cmul(c[2][m], c[1][m]); c[4][m] = cmul(c[2][m], c[2][m]);
    c[5][m] = cmul(c[4][m], c[1][m]); c[6][m] = cmul(c[4][m], c[2][m]); c[7][m] = cmul(c[4][m], c[3][m]);
    c8[m] = cmul(c[4][m], c[4][m]); hi[m] = c8[m];
  }
  bf16x8 fb[2][KS];
#pragma unroll
  for (int j = 0; j < 8; ++j) put(j, c[j]);
#pragma unroll
  for (int a = 1; a < 4; ++a) {
    if (a == 2) {
#pragma unroll
      for (int s = 0; s < KS; ++s) fb[0][s] = rd128(fa + s * 64);
    }
    put((8 * a) & 15, hi);
#pragma unroll
    for (int j = 1; j < 8; ++j) {
#pragma unroll
      for (int m = 0; m < NM; ++m) t[m] = cmul(hi[m], c[j][m]);
      put((8 * a + j) & 15, t);
    }
    if (a < 3) {
#pragma unroll
      for (int m = 0; m < NM; ++m) hi[m] = cmul(hi[m], c8[m]);
    }
  }
#pragma unroll
  for (int s = 0; s < KS; ++s) fb[1][s] = rd128(fa + s * 64);
  STAMP(14);
  f32x2 z1[NM], zb[NM], r0[NM], r4[NM];
#pragma unroll
  for (int m = 0; m < NM; ++m) { z1[m] = cpow(lr[m], -tr[m], 1.f); zb[m] = cpow(lr[m], -tr[m], 256.f); t[m] = W[m]; }
#pragma unroll
  for (int b = 0; b < 8; ++b) {
    put(b, t);
    if ((b & 1) == 0) {
      f32x2 u[NM];
#pragma unroll
      for (int m = 0; m < NM; ++m) { u[m] = cmul(t[m], zb[m]); if (b == 0) r0[m] = u[m]; if (b == 4) r4[m] = u[m]; }
      put(8 + b / 2, u);
    }
    if (b < 7) {
#pragma unroll
      for (int m = 0; m < NM; ++m) t[m] = cmul(t[m], z1[m]);
    }
  }
#pragma unroll
  for (int i = 0; i < 2; ++i) {
#pragma unroll
    for (int m = 0; m < NM; ++m) { r0[m] = cmul(r0[m], zb[m]); r4[m] = cmul(r4[m], zb[m]); }
    put(12 + 2 * i, r0); put(13 + 2 * i, r4);
  }
  bf16x8 fr[2][KS];
#pragma unroll
  for (int s = 0; s < KS; ++s) fr[0][s] = rd128(fa + s * 64);
#pragma unroll
  for (int i = 0; i < 5; ++i) {
#pragma unroll
    for (int m = 0; m < NM; ++m) r0[m] = cmul(r0[m], zb[m]);
    put(i, r0);
  }
  if (NM == 2) lds_set(done);
#pragma unroll
  for (int s = 0; s < KS; ++s) fr[1][s] = rd128(fa + s * 64);
  __builtin_amdgcn_sched_barrier(0);
  STAMP(15);
#pragma unroll
  for (int i = 0; i < 4; ++i) acc[i >> 1][i & 1] = f32x4{0.f, 0.f, 0.f, 0.f};
#pragma unroll
  for (int rt = 0; rt < 2; ++rt)
#pragma unroll
    for (int s = 0; s < KS; ++s) { acc[rt][0] = mfma(fr[rt][s], fb[0][s], acc[rt][0]); acc[rt][1] = mfma(fr[rt][s], fb[1][s], acc[rt][1]); }
}
struct LnOut { s16x4 tr[2][10]; v2u st; };
DI void ln8(rsrc_t rx, int tb, int lane, unsigned tile, LnOut& out) {
  const int r4 = lane >> 4, i = lane & 15;
  v4u raw[2][5];
#pragma unroll
  for (int h = 0; h < 2; ++h)
#pragma unroll
    for (int k = 0; k < 5; ++k) raw[h][k] = __builtin_amdgcn_raw_buffer_load_b128(rx, r4 * 1280 + i * 16 + k * 256, (tb * 8 + h * 4) * 1280, 0);
  const unsigned ta = tile + ((lane >> 2) & 3) * LN_RS + (16 * r4 + 4 * (lane & 3)) * 2;
#pragma unroll
  for (int h = 0; h < 2; ++h) {
    if (h == 0) { asm volatile("" : "+v"(raw[0][4])); STAMP(13); asm volatile("" : "+v"(raw[1][4])); STAMP(15); } else STAMP(14);
    float s1 = 0.f, s2 = 0.f, s3 = 0.f, s4 = 0.f;
    const bf16x2 one2 = __builtin_bit_cast(bf16x2, 0x3f803f80u);
#pragma unroll
    for (int k = 0; k < 10; ++k) {
      const unsigned w0 = raw[h][k >> 1][2 * (k & 1)], w1 = raw[h][k >> 1][2 * (k & 1) + 1];
      const bf16x2 p0 = __builtin_bit_cast(bf16x2, w0), p1 = __builtin_bit_cast(bf16x2, w1);
      s1 = __builtin_amdgcn_fdot2_f32_bf16(p0, one2, s1, false); s2 = __builtin_amdgcn_fdot2_f32_bf16(p0, p0, s2, false);
      s3 = __builtin_amdgcn_fdot2_f32_bf16(p1, one2, s3, false); s4 = __builtin_amdgcn_fdot2_f32_bf16(p1, p1, s4, false);
    }
    const float mean = rowsum16(s1 + s3) * (1.f / D);
    const float var = rbf(fmaxf(rowsum16(s2 + s4) * (1.f / D) - mean * mean, 0.f));
    const float m = rbf(mean), den = rbf(sqrtf(rbf(var + 1e-5f)));
    const float ri = __builtin_amdgcn_rcpf(den), nb = -m * ri;
    if (h == 0 || (i & 1)) out.st = v2u{__float_as_uint(m), __float_as_uint(den)};
#pragma unroll
    for (int k = 0; k < 5; ++k) {
      v4u o;
#pragma unroll
      for (int e = 0; e < 4; ++e) { const unsigned w = raw[h][k][e]; o[e] = pk2(f32x2{blo(w), bhi(w)} * ri + nb); }
      *(LAS v4u*)lds(tile + r4 * LN_RS + i * 16 + k * 256) = o;
    }
    cfence();
#pragma unroll
    for (int it = 0; it < 10; ++it) out.tr[h][it] = __builtin_amdgcn_ds_read_tr16_b64_v4i16((LAS s16x4*)lds(ta + it * 128));
  }
}
DI void ln8_store(rsrc_t rw, int tb, int lane, const LnOut& in) {
  if ((lane & 15) < 2) __builtin_amdgcn_raw_buffer_store_b64(in.st, rw, ((lane & 1) * 4 + (lane >> 4)) * 8, WS_STATS + tb * 128, SC1);
#pragma unroll
  for (int it = 0; it < 10; ++it) {
    bf16x8 o = __builtin_shufflevector(in.tr[0][it], in.tr[1][it], 0, 1, 2, 3, 4, 5, 6, 7);
    st128<SC1>(__builtin_bit_cast(v4u, o), rw, lane * 16 + it * 1024 + WS_U8 + tb * 10240);
  }
}

DI unsigned prm(unsigned hi, unsigned lo, unsigned sel) { return __builtin_amdgcn_perm(hi, lo, sel); }
DI unsigned colo(int col) { return (col & 15) * 16 + ((col >> 4) & 1) * 8; }
template <int S> DI v4u kchunk(unsigned raw, int lane) {
  const int i = lane & 31, up = lane >> 5;
  if (S == 0) {
    const v2u a0 = *(LAS v2u*)lds(raw + colo(i) + 512 * up);
    const v2u a1 = *(LAS v2u*)lds(raw + (up ? (i == 31 ? 768 : 512 + colo(i + 1)) : 256 + colo(i)));
    const unsigned s0 = up ? 0x03020504u : 0x05040706u, s1 = up ? 0x01000100u : 0x05040706u, s2 = up ? 0x03020504u : 0x05040706u,
                   s3 = up ? 0x01000100u : 0x01000302u;
    return v4u{prm(up ? a1.x : a1.y, a0.y, s0), prm(a1.x, a0.y, s1), prm(a0.y, a0.x, s2), prm(0, a0.x, s3)};
  } else {
    const unsigned b0 = *(LAS unsigned*)lds(raw + 768 + colo(i) + 4 * up);
    const unsigned b1 = *(LAS unsigned*)lds(raw + (lane == 63 ? 1024 : 768 + colo(i + 1) + 4 * ((lane + 1) >> 5)));
    return v4u{prm(0, b1, 0x01000100), prm(0, b0, 0x03020302), prm(0, b0, 0x01000302), prm(0, b0, 0x01000100)};
  }
}
DI unsigned vmov(unsigned x) { unsigned r; asm("v_mov_b32 %0, %1" : "=v"(r) : "v"(x)); return r; }
DI void hwrite(v4u x, v4u y, unsigned hb, int pos) {
  v8u w = __builtin_shufflevector(x, y, 0, 1, 2, 3, 4, 5, 6, 7), e, o0, o1;
  asm volatile("" : "+v"(w));
#pragma unroll
  for (int k = 0; k < 6; ++k) {
    e[k] = vmov(w[k + 1]);
    o0[k] = __builtin_amdgcn_alignbit(w[k + 1], w[k], 16);
    o1[k] = __builtin_amdgcn_alignbit(w[k + 2], w[k + 1], 16);
    asm volatile("" : "+v"(o1[k]));
  }
  asm volatile("" : "+v"(e), "+v"(o0), "+v"(o1));
  const unsigned a = hb + pos * 16;
  *(LAS v4u*)lds(a + 7 * H_CS) = __builtin_shufflevector(w, w, 0, 1, 2, 3);
  *(LAS v4u*)lds(a + 3 * H_CS) = __builtin_shufflevector(w, w, 2, 3, 4, 5);
  *(LAS v4u*)lds(a + 5 * H_CS) = __builtin_shufflevector(e, e, 0, 1, 2, 3);
  *(LAS v4u*)lds(a + 1 * H_CS) = __builtin_shufflevector(e, e, 2, 3, 4, 5);
  *(LAS v4u*)lds(a + 6 * H_CS) = __builtin_shufflevector(o0, o0, 0, 1, 2, 3);
  *(LAS v4u*)lds(a + 2 * H_CS) = __builtin_shufflevector(o0, o0, 2, 3, 4, 5);
  *(LAS v4u*)lds(a + 4 * H_CS) = __builtin_shufflevector(o1, o1, 0, 1, 2, 3);
  *(LAS v4u*)lds(a + 0 * H_CS) = __builtin_shufflevector(o1, o1, 2, 3, 4, 5);
}
template <int S> DI void hbuild(unsigned raw, unsigned hb, int lane, v4u& carry) {
  const v4u x = kchunk<S>(raw, lane);
  v4u y;
#pragma unroll
  for (int k = 0; k < 4; ++k) {
    y[k] = __builtin_amdgcn_update_dpp(carry[k], x[k], 0x138, 0xf, 0xf, false);
    carry[k] = __builtin_amdgcn_readlane(x[k], 63);
  }
  hwrite(x, y, hb, 1 + 64 * S + lane);
}
DI void htail(unsigned raw, unsigned hb, unsigned k3, int lane, v4u carry) {
  const unsigned kk = *(LAS uint16_t*)lds(raw + 1024);
  const int n0 = 128 - 2 * lane, n1 = n0 - 1;
  const unsigned c0 = *(LAS uint16_t*)lds(raw + 1024 + (n0 == 128 ? 256 : colo(n0) + 2 * (n0 >> 5)));
  const unsigned c1 = *(LAS uint16_t*)lds(raw + 1024 + colo(n1) + 2 * (n1 >> 5));
  *(LAS unsigned*)lds(k3 + lane * 4) = c0 | (c1 << 16);
  if (lane < 16) *(LAS unsigned*)lds(k3 + 256 + lane * 4) = 0;
  const unsigned k2 = kk | (kk << 16), m0 = lane == 0 ? ~0u : 0u, m1 = lane == 1 ? ~0u : 0u;
  if (lane < 3) hwrite(v4u{0, (kk << 16) & m0, k2 & m0, k2 & m0},
                       v4u{carry[0] & m0, (carry[1] & m0) | ((kk << 16) & m1), (carry[2] & m0) | (k2 & m1), (carry[3] & m0) | (k2 & m1)}, hb, 129 + lane);
}

DI void conv(rsrc_t rw, int cw, int d, int lane) {
  const unsigned hb = L_H + cw * H_SZ, ub = L_U + cw * U_SZ;
  const int c = lane & 15, g = lane >> 4;
  unsigned aa = hb + (c & 7) * H_CS + (3 + (c >> 3) - g) * 16, ab = ub + 416 + 32 * c + 16 * g;
  f32x4 acc[8] = {};
  bf16x8 fa[2][4], fb[2][8];
  auto rd = [&](int buf, int oa, int ob) {
#pragma unroll
    for (int k = 0; k < 4; ++k) fa[buf][k] = rd128(aa + oa + k * 512);
#pragma unroll
    for (int e = 7; e >= 0; --e) fb[buf][e] = rd128(ab + ob + e * 512);
  };
  auto mm = [&](int buf) {
#pragma unroll
    for (int k = 0; k < 4; ++k)
#pragma unroll
      for (int e = 7 - k; e >= 0; --e) acc[e + k] = mfma(fa[buf][k], fb[buf][e], acc[e + k]);
  };
  auto ilv = [&]() {
#pragma unroll
    for (int i = 0; i < 12; ++i) { __builtin_amdgcn_sched_group_barrier(0x008, 1, 0); __builtin_amdgcn_sched_group_barrier(0x100, 1, 0); }
    __builtin_amdgcn_sched_group_barrier(0x008, 14, 0);
    __builtin_amdgcn_sched_barrier(0);
  };
  asm volatile("" : "+v"(aa), "+v"(ab));
  rd(0, 0, 64);
  {
    const bf16x8 fx = rd128(aa + 2048);
    bf16x8 ue[4], kt[4];
#pragma unroll
    for (int f = 0; f < 4; ++f) ue[f] = rd128(ab + 48 + f * 512);
    const unsigned k3 = L_K3 + cw * 320 + (254 - 2 * c + 8 * g) * 2;
#pragma unroll
    for (int f = 0; f < 4; ++f) {
      v4u t;
#pragma unroll
      for (int e = 0; e < 4; ++e) t[e] = *(LAS unsigned*)lds(k3 - (4 + f) * 64 + e * 4);
      kt[f] = __builtin_bit_cast(bf16x8, t);
    }
    bf16x8 b9, b7;
#pragma unroll
    for (int jj = 0; jj < 8; ++jj) {
      const int dl = 8 * g + jj - 8 - c;
      b9[jj] = (dl >= -4 && dl <= 4) ? 0x3f80 : 0; b7[jj] = (dl >= -3 && dl <= 3) ? 0x3f80 : 0;
    }
#pragma unroll
    for (int f = 0; f < 4; ++f) {
      acc[4 + f] = mfma(fx, fb[0][f], acc[4 + f]);
      const f32x4 ve = mfma(ue[f], b9, f32x4{0.f, 0.f, 0.f, 0.f}), vo = mfma(fb[0][f], b7, f32x4{0.f, 0.f, 0.f, 0.f});
      const bf16x8 vf = __builtin_bit_cast(bf16x8, v4u{pk(ve[0], vo[0]), pk(ve[1], vo[1]), pk(ve[2], vo[2]), pk(ve[3], vo[3])});
#pragma unroll
      for (int n = 4 + f; n < 8; ++n) acc[n] = mfma(vf, kt[n - f - 4], acc[n]);
    }
    __builtin_amdgcn_sched_barrier(0);
  }
  STAMP(8);
#pragma unroll 1
  for (int r = 0; r < 3; ++r) {
    rd(1, 64, 0); mm(0); ilv();
    aa += 128; ab -= 128;
    asm volatile("" : "+v"(aa), "+v"(ab));
    rd(0, 0, 64); mm(1); ilv();
  }
  rd(1, 64, 0); mm(0); ilv();
  mm(1);
#pragma unroll
  for (int n = 0; n < 8; ++n)
    __builtin_amdgcn_raw_buffer_store_b64(v2u{pk(acc[n][0], acc[n][1]), pk(acc[n][2], acc[n][3])}, rw, 32 * c + 8 * g + 512 * n, WS_AT + d * 4096, SC1);
}
#endif

__global__ __launch_bounds__(512, 2) void fused_s4_ffn(
    const uint16_t* __restrict__ x, const uint16_t* __restrict__ Are, const uint16_t* __restrict__ Aim, const uint16_t* __restrict__ Bre,
    const uint16_t* __restrict__ Bim, const uint16_t* __restrict__ Cre, const uint16_t* __restrict__ Cim, const uint16_t* __restrict__ logdt,
    const uint16_t* __restrict__ dskip, const uint16_t* __restrict__ wg, const uint16_t* __restrict__ wu, const uint16_t* __restrict__ wd,
    uint16_t* __restrict__ out, void* ws) {
#if __HIP_DEVICE_COMPILE__
  const int wid = __builtin_amdgcn_readfirstlane(threadIdx.x >> 6), lane = threadIdx.x & 63;
  const int b = blockIdx.x, xcd = b & 7, j = b >> 3;
  const bool normal = j < 30;
  const int rl = j % 5, ti = 5 * (xcd & 1) + rl;
  const int mm_ = 2 * (xcd >> 1) + (xcd & 1), par = j - 30;
  const int ch = normal ? 6 * (xcd >> 1) + j / 5 : 3 * mm_ + 2 * par;
  const int ch2 = 3 * mm_ + 1;
  const int nch = normal ? (j < 20 ? 3 : 2) : 0;
  const int d0 = 80 * xcd + (j < 20 ? 3 * j : 2 * j + 20);
  const int t64 = 4 * xcd + (j >> 3), dj = j & 7;
  const int rot = XROT ? (ti + (5 * (j / 5) + 1) / 3) % 10 : ti;
  const int rot2 = (3 * (j >> 3) + (3 * dj + 1) / 2) % 12;
  auto kst = [&](int s) { return s + rot2 >= 12 ? s + rot2 - 12 : s + rot2; };
  auto pbase = [](int w) -> unsigned { return w < 2 ? w * 15360 : L_P2 + (w - 2) * 15360; };
  const rsrc_t rw = mkr(ws, WS_BYTES), rx = mkr(x, L * D * 2);
  STAMP(0);
  if (wid == 6) {
    u64 pc; asm volatile("s_getpc_b64 %0" : "=s"(pc));
    dma<0>(srd((const void*)pc, CODE_BYTES), L_DUMMY, lane * 16, j * 1024);
    dma<0>(srd((const void*)pc, CODE_BYTES), L_DUMMY, lane * 16, 32768 + j * 1024);
  }
  if (wid == 7) { *(LAS v4u*)lds(L_MISC + 16 + (lane & 3) * 16) = v4u{0, 0, 0, 0}; wait_lg(); }
  asm volatile("" ::"s"(x), "s"(Are), "s"(Aim), "s"(Bre), "s"(Bim), "s"(Cre), "s"(Cim), "s"(logdt), "s"(dskip), "s"(wg), "s"(wu), "s"(wd), "s"(out), "s"(ws));
  unsigned pw[6], ldw_ = 0, dsw_ = 0;
  const int kd = d0 + (wid & 3);
  if ((wid & 3) < nch) {
    const uint16_t* const par6[6] = {Are, Aim, Bre, Bim, Cre, Cim};
    const unsigned vo = wid >= 4 ? lane * 4 : 256 + (lane >> 1) * 4;
#pragma unroll
    for (int i = 0; i < 6; ++i) pw[i] = __builtin_amdgcn_raw_buffer_load_b32(mkr(par6[i], D * NS * 2), vo, kd * NS * 2, NT_);
    ldw_ = ((const __attribute__((address_space(4))) unsigned*)logdt)[kd >> 1];
    dsw_ = ((const __attribute__((address_space(4))) unsigned*)dskip)[kd >> 1];
  }
  cfence(); bar(); cfence();
  STAMP(11);
  const float dt = rbf(expf((kd & 1) ? bhi(ldw_) : blo(ldw_)));
  const v4u sg = srd(wg, D * F * 2), su = srd(wu, D * F * 2), sx = srd(x, L * D * 2), sw = srd(ws, WS_BYTES);

  if (wid < 4) {
    if (wid < nch) {
      f32x4 ka[2][2];
#pragma unroll
      for (int i = 0; i < 6; ++i) pw[i] = (lane & 1) ? pw[i] >> 16 : pw[i];
      kgen<1>(pw, dt, lane, L_KG + wid * KG_SZ + 16 * 544, 0, ka);
#pragma unroll
      for (int i = 0; i < 4; ++i) *(LAS f32x4*)lds(L_KG + wid * KG_SZ + 16 * 544 + i * 1024 + lane * 16) = ka[i >> 1][i & 1];
      wait_lg();
      lds_set(L_MISC + 16 + wid * 4);
      STAMP(12);
      if (wid < 2) {
        const G1Ld v = g1_lanes(lane);
        lds_wait(L_MISC + 68); lds_wait(L_MISC + 72);
        if (nch == 3) lds_wait(L_MISC + 76);
        STAMP(10);
        g1_issue<6>(sx, sg, su, v, wid, rot, 0, 192 * ti, ch);
        wait_vm<0>();
      }
    } else if (wid == 3 && normal) {
      const G1Ld v = g1_lanes(lane);
      v4u sink = {0, 0, 0, 0};
#pragma unroll 1
      for (int p = 0; p < 8; ++p) {
        touch(sink, sg, v.vw0, ch * 128 + (rot * 64 + p * 8) * 3072);
        touch(sink, su, v.vw0, ch * 128 + (rot * 64 + p * 8) * 3072);
        const int sl = rot + (p >> 2);
        touch(sink, sx, v.vx0, (192 * ti + 32 * (j / 5) + 8 * (p & 3)) * 1280 + (sl == 10 ? 0 : sl) * 128);
      }
      const v4u sd = srd(wd, F * D * 2);
#pragma unroll 1
      for (int p = 0; p < 8; ++p) dma<NT_>(sd, L_WDU + p * 1024, (lane >> 3) * 1280 + (lane & 7) * 16, (ch * 64 + p * 8) * 1280 + ti * 128);
      asm volatile("s_waitcnt vmcnt(0)" : "+v"(sink)::"memory");
    }
    wait_lg();
    STAMP(1);
    if (normal) {
      f32x4 acc[4][6];
      g1_main<6>(wid, lane, 0, acc);
      BAR(12);
      STAMP(3);
      g1_epilogue<6>(rw, wid, lane, 12 * ti, ch, L_ST, L_CS, acc);
    } else {
      f32x4 acc4[4][4], acc2[4][2];
      g1_main<4>(wid, lane, 0, acc4);
      g1_main<2>(wid, lane, STAGE, acc2);
      BAR(12);
      STAMP(3);
      g1_epilogue<4>(rw, wid, lane, 120, ch, L_ST, L_CS, acc4);
      g1_epilogue<2>(rw, wid, lane, 120 + 4 * par, ch2, L_ST + par * 1024, L_CS + 512, acc2);
    }
    STAMP(4);
    lds_set(L_MISC + 48 + wid * 4);
    wait_vm<0>();
    __builtin_amdgcn_s_waitcnt(0x0F70);
    STAMP(5);
    arrive_publish(L_MISC + 8, 4, rw, WS_F1 + (normal ? ti * 24 + ch : 240 + 2 * mm_ + par) * 8, lane);
    const unsigned wso = WS_WDT + (12 * wid * 40 + 5 * dj) * 1024, hso = WS_H + (4 * t64 * 48 + 12 * wid) * 1024;
    const unsigned vo0 = lane * 16, vo1 = lane * 16 + 4096, pw = pbase(wid) + lane * 16;
    v4u wr[5][5], hr[3][4];
#pragma unroll
    for (int i = 0; i < 25; ++i) asm volatile("" : "=v"(wr[i / 5][i % 5]));
#pragma unroll
    for (int i = 0; i < 12; ++i) asm volatile("" : "=v"(hr[i / 4][i % 4]));
    auto ldw = [&](int s) {
      const unsigned so = wso + kst(s) * 40960;
      asm volatile("buffer_load_dwordx4 %0, %5, %7, %8 offen sc1\n\tbuffer_load_dwordx4 %1, %5, %7, %8 offen offset:1024 sc1\n\t"
                   "buffer_load_dwordx4 %2, %5, %7, %8 offen offset:2048 sc1\n\tbuffer_load_dwordx4 %3, %5, %7, %8 offen offset:3072 sc1\n\t"
                   "buffer_load_dwordx4 %4, %6, %7, %8 offen sc1"
                   : "+v"(wr[s % 5][0]), "+v"(wr[s % 5][1]), "+v"(wr[s % 5][2]), "+v"(wr[s % 5][3]), "+v"(wr[s % 5][4])
                   : "v"(vo0), "v"(vo1), "s"(sw), "s"(so) : "memory");
    };
    auto ldh = [&](int s) {
      const unsigned so = hso + kst(s) * 1024;
      asm volatile("buffer_load_dwordx4 %0, %4, %5, %6 offen sc1\n\tbuffer_load_dwordx4 %1, %4, %5, %7 offen sc1\n\t"
                   "buffer_load_dwordx4 %2, %4, %5, %8 offen sc1\n\tbuffer_load_dwordx4 %3, %4, %5, %9 offen sc1"
                   : "+v"(hr[s % 3][0]), "+v"(hr[s % 3][1]), "+v"(hr[s % 3][2]), "+v"(hr[s % 3][3])
                   : "v"(vo0), "s"(sw), "s"(so), "s"(so + 49152), "s"(so + 98304), "s"(so + 147456) : "memory");
    };
#pragma unroll
    for (int s = 0; s < 5; ++s) ldw(s);
    lds_wait(L_MISC + 32);
    STAMP(6);
#pragma unroll
    for (int s = 0; s < 3; ++s) ldh(s);
    f32x4 acc[4][5];
#pragma unroll
    for (int s = 0; s < 12; ++s) {
      constexpr int YN[12] = {8, 13, 18, 23, 23, 18, 13, 8, 8, 8, 4, 0};
      asm volatile("s_waitcnt vmcnt(%9)"
                   : "+v"(hr[s % 3][0]), "+v"(hr[s % 3][1]), "+v"(hr[s % 3][2]), "+v"(hr[s % 3][3]), "+v"(wr[s % 5][0]), "+v"(wr[s % 5][1]),
                     "+v"(wr[s % 5][2]), "+v"(wr[s % 5][3]), "+v"(wr[s % 5][4])
                   : "i"(YN[s]) : "memory");
#pragma unroll
      for (int t = 0; t < 4; ++t)
#pragma unroll
        for (int dd = 0; dd < 5; ++dd)
          acc[t][dd] = mfma(__builtin_bit_cast(bf16x8, wr[s % 5][dd]), __builtin_bit_cast(bf16x8, hr[s % 3][t]), s == 0 ? f32x4{0.f, 0.f, 0.f, 0.f} : acc[t][dd]);
      __builtin_amdgcn_sched_barrier(0);
      if (s == G2SIG && wid == 0) lds_set(L_MISC + 36);
      if (s + 3 < 12) ldh(s + 3);
      if (s < 4) ldw(s + 5);
      if (s >= 4 && s < 7) {
#pragma unroll
        for (int t = 0; t < 5; ++t) wr[s % 5][t] = *(LAS v4u*)lds(pw + (s - 4) * 5120 + t * 1024);
      }
      __builtin_amdgcn_sched_barrier(0);
    }
    STAMP(7);
    const int c = lane & 15, g = lane >> 4;
    const unsigned oo = ((4 * t64 + wid) * 16 + c) * 1280 + (80 * dj + 4 * g) * 2;
    v2u xr[5];
#pragma unroll
    for (int dd = 0; dd < 5; ++dd) xr[dd] = __builtin_amdgcn_raw_buffer_load_b64(rx, oo + dd * 32, 0, 0);
    f32x4 own[5];
#pragma unroll
    for (int t = 0; t < 4; ++t) {
      if (t == wid) {
#pragma unroll
        for (int dd = 0; dd < 5; ++dd) own[dd] = acc[t][dd];
      } else {
        const unsigned o = pw + (t > wid ? t - 1 : t) * 5120;
#pragma unroll
        for (int dd = 0; dd < 5; ++dd) *(LAS f32x4*)lds(o + dd * 1024) = acc[t][dd];
      }
    }
    wait_lg();
    BAR(11);
    STAMP(8);
#pragma unroll
    for (int k = 1; k < 4; ++k) {
      const int w = (wid + k) & 3;
      const unsigned o = pbase(w) + lane * 16 + (wid > w ? wid - 1 : wid) * 5120;
#pragma unroll
      for (int dd = 0; dd < 5; ++dd) own[dd] += *(LAS f32x4*)lds(o + dd * 1024);
    }
    const rsrc_t ro = mkr(out, L * D * 2);
    const unsigned at = L_AT + (4 * g + ((lane >> 2) & 3)) * 128 + (16 * wid + 4 * (lane & 3)) * 2;
#pragma unroll
    for (int dd = 0; dd < 5; ++dd) {
      const v2u a = __builtin_bit_cast(v2u, __builtin_amdgcn_ds_read_tr16_b64_v4i16((LAS s16x4*)lds(at + dd * 2048)));
      const float o0 = own[dd][0] + blo(a.x) + blo(xr[dd].x), o1 = own[dd][1] + bhi(a.x) + bhi(xr[dd].x);
      const float o2 = own[dd][2] + blo(a.y) + blo(xr[dd].y), o3 = own[dd][3] + bhi(a.y) + bhi(xr[dd].y);
      __builtin_amdgcn_raw_buffer_store_b64(v2u{pk(o0, o1), pk(o2, o3)}, ro, oo + dd * 32, 0, 0);
    }
    STAMP(9);
  } else if (wid < 7) {
    const int cw = wid - 4;
    v4u carry = {0, 0, 0, 0}, sink = {0, 0, 0, 0}, kraw[2] = {};
    auto warm_wd = [&]() {
#pragma unroll 1
      for (int i = 0; i < 6; ++i) {
        const int w = 2 * cw + (i >= 3), s = i >= 3 ? i - 3 : i;
        const unsigned so = WS_WDT + ((12 * w + kst(9 + s)) * 40 + 5 * dj) * 1024;
#pragma unroll
        for (int t = 0; t < 5; ++t) touch(sink, sw, lane * 16, so + t * 1024);
      }
      wait_vm<30>();
    };
    if (normal) {
      LnOut lo2;
      const bool ln2 = cw == 2 && (j == 20 || j == 21);
      if (cw < nch) {
        f32x4 ka[2][2];
        kgen<2>(pw, dt, lane, L_KG + cw * KG_SZ, L_MISC + 68 + cw * 4, ka);
        STAMP(12);
        lds_wait(L_MISC + 16 + cw * 4);
#pragma unroll
        for (int i = 0; i < 4; ++i) ka[i >> 1][i & 1] += *(LAS f32x4*)lds(L_KG + cw * KG_SZ + 16 * 544 + i * 1024 + lane * 16);
        if (lane == 0) ka[0][0][0] += (kd & 1) ? bhi(dsw_) : blo(dsw_);
#pragma unroll
        for (int rt = 0; rt < 2; ++rt)
          kraw[rt] = v4u{pk(ka[rt][0][0], ka[rt][0][1]), pk(ka[rt][0][2], ka[rt][0][3]), pk(ka[rt][1][0], ka[rt][1][1]), pk(ka[rt][1][2], ka[rt][1][3])};
      } else if (ln2) {
        ln8(rx, 240 + 2 * xcd + j - 20, lane, L_KG + 2 * KG_SZ, lo2);
      }
      wait_lg();
      STAMP(1);
      if (cw < 2) {
        g1_loader<6, 1>(sx, sg, su, cw, lane, 192 * ti, ch, rot, STAGE);
        warm_wd(); BAR(9);
      } else {
        BAR(0);
        colsum(0, lane); colsum(1, lane);
        BAR(1);
        if (ln2) ln8_store(rw, 240 + 2 * xcd + j - 20, lane, lo2);
        BAR(2);
        wait_vm<0>();
#pragma unroll 1
        for (int k = 3; k < 10; ++k) BAR(k);
      }
      if (cw < nch) {
        const unsigned hb = L_H + cw * H_SZ;
        __builtin_amdgcn_s_sleep(4);
#pragma unroll
        for (int rt = 0; rt < 2; ++rt) *(LAS v4u*)lds(L_RAW + cw * 2048 + rt * 1024 + lane * 16) = kraw[rt];
        if (lane < 9) *(LAS v4u*)lds(hb + lane * H_CS) = v4u{0, 0, 0, 0};
        hbuild<0>(L_RAW + cw * 2048, hb, lane, carry);
      }
      BAR(12);
      if (cw < nch) {
        hbuild<1>(L_RAW + cw * 2048, L_H + cw * H_SZ, lane, carry);
        htail(L_RAW + cw * 2048, L_H + cw * H_SZ, L_K3 + cw * 320, lane, carry);
      }
    } else if (cw < 2) {
      g1_loader<4, 0>(sx, sg, su, cw, lane, 1920, ch, 0, 0);
      wait_vm<0>(); BAR(9);
      g1_loader<2, 0>(sx, sg, su, cw, lane, 1920 + 64 * par, ch2, 0, STAGE);
      warm_wd(); BAR(9);
      BAR(12);
    } else {
#pragma unroll 1
      for (int s = 0; s < 21; ++s) bar();
    }
    asm volatile("s_waitcnt vmcnt(0)" : "+v"(sink)::"memory");
    STAMP(4);
    if (cw < nch) {
      lds_wait(L_MISC + 48 + cw * 4);
      lds_wait(L_MISC + 64);
      STAMP(5);
      __builtin_amdgcn_s_setprio(2);
      conv(rw, cw, d0 + cw, lane);
      STAMP(6);
      wait_vm<0>();
      arrive_publish(L_MISC + 12, nch, rw, WS_F2 + (xcd * 32 + j) * 8, lane);
    }
    STAMP(7);
    BAR(11);
  } else {
    const v2u e = __builtin_amdgcn_raw_buffer_load_b64(rw, WS_EPOCH, 0, SC1);
    const int tb = 120 * (xcd & 1) + 30 * (xcd >> 1) + j;
    LnOut lo;
    if (normal) ln8(rx, tb, lane, L_LN, lo);
    const u64 tg = (((u64)e.y << 32) | e.x) + 0x9E3779B97F4A7C15ull;
    const v2u tag = {(unsigned)tg, (unsigned)(tg >> 32)};
    if (lane == 0) *(LAS v4u*)lds(L_MISC) = v4u{tag.x, tag.y, 0, 0};
    wait_lg();
    STAMP(1);
    BAR(0);
    if (normal) { colsum(2, lane); colsum(3, lane); }
    BAR(1);
    if (normal) {
      ln8_store(rw, tb, lane, lo);
      if (lane < 32) st128<SC1>(*(LAS v4u*)lds(L_CSP + lane * 16), rw, lane * 16 + WS_CSP + (ch * 10 + ti) * 512);
      const unsigned a0 = L_WDU + (4 * (lane >> 4) + ((lane >> 2) & 3)) * 128 + 8 * (lane & 3);
#pragma unroll
      for (int p = 0; p < 8; ++p) {
        const unsigned a = a0 + (p >> 2) * 4096 + (p & 3) * 32;
        st128<SC1>(__builtin_bit_cast(v4u, trd(a, a + 2048)), rw, lane * 16 + WS_WDT + ((2 * ch + (p >> 2)) * 40 + 4 * ti + (p & 3)) * 1024);
      }
    }
    BAR(2); BAR(3);
    wait_vm<0>();
    if (lane == 0) put_flag(rw, WS_F0 + b * 8, tag);
    BAR(4); BAR(5); BAR(6);
    if (!normal) {
#pragma unroll 1
      for (int s = 0; s < 10; ++s) bar();
    }
    poll<4>(rw, WS_F0, 256, tag, lane);
    STAMP(2);
    if (b == 0 && lane == 0) put_flag(rw, WS_EPOCH, tag);
    const int row0 = normal ? 192 * ti : 1920;
    v2u st[3], cp[2][10];
#pragma unroll
    for (int k = 0; k < 3; ++k) { const int r = row0 + lane + 64 * k; st[k] = __builtin_amdgcn_raw_buffer_load_b64(rw, (r >> 3) * 128 + (r & 7) * 8, WS_STATS, SC1); }
#pragma unroll
    for (int k = 0; k < 10; ++k) cp[0][k] = __builtin_amdgcn_raw_buffer_load_b64(rw, lane * 8 + k * 512, WS_CSP + ch * 5120, SC1);
    if (!normal) {
#pragma unroll
      for (int k = 0; k < 10; ++k) cp[1][k] = __builtin_amdgcn_raw_buffer_load_b64(rw, lane * 8 + k * 512, WS_CSP + ch2 * 5120, SC1);
    }
    BAR(7);
    v4u ur[16];
    if ((lane & 3) < nch) {
#pragma unroll
      for (int p = 0; p < 16; ++p) ur[p] = __builtin_amdgcn_raw_buffer_load_b128(rw, (lane >> 2) * 10240 + (lane & 3) * 16, WS_U8 + p * 163840 + d0 * 16, SC1);
    }
    BAR(8);
    f32x4 so[3];
#pragma unroll
    for (int k = 0; k < 3; ++k) {
      const float m = __uint_as_float(st[k].x), den = __uint_as_float(st[k].y);
      so[k] = f32x4{-m, den * den, -1.44269504f * __builtin_amdgcn_rcpf(den), 0.f};
    }
    f32x2 co[2];
#pragma unroll
    for (int h = 0; h < 2; ++h) {
      float c0 = 0.f, c1 = 0.f;
      if (h == 0 || !normal) {
#pragma unroll
        for (int k = 0; k < 10; ++k) { c0 += __uint_as_float(cp[h][k].x); asm volatile("" : "+v"(c0)); c1 += __uint_as_float(cp[h][k].y); }
      }
      co[h] = f32x2{c0, c1};
    }
    asm volatile("" : "+v"(so[0]), "+v"(so[1]), "+v"(so[2]), "+v"(co[0]), "+v"(co[1]));
    BAR(9);
    __builtin_amdgcn_s_sleep(4);
#pragma unroll
    for (int k = 0; k < 3; ++k) *(LAS f32x4*)lds(L_ST + (lane + 64 * k) * 16) = so[k];
#pragma unroll
    for (int h = 0; h < 2; ++h) *(LAS f32x2*)lds(L_CS + h * 512 + lane * 8) = co[h];
    wait_lg();
    BAR(12);
    if (lane < 32) {
#pragma unroll
      for (int k = 0; k < 3; ++k) *(LAS v4u*)lds(L_U + k * U_SZ + lane * 16) = v4u{0, 0, 0, 0};
    }
    if ((lane & 3) < nch) {
#pragma unroll
      for (int p = 0; p < 16; ++p) *(LAS v4u*)lds(L_U + (lane & 3) * U_SZ + 512 + p * 256 + (lane >> 2) * 16) = ur[p];
    }
    wait_lg();
    lds_set(L_MISC + 64);
    lds_wait(L_MISC + 60);
    STAMP(4);
#pragma unroll 1
    for (int i = 0; i < 12; ++i) {
      const int w = i / 3, s = i - 3 * w;
      const unsigned so = WS_WDT + ((12 * w + kst(9 + s)) * 40 + 5 * dj) * 1024, lo = pbase(w) + s * 5120;
#pragma unroll
      for (int t = 0; t < 5; ++t) dma<SC1>(sw, lo + t * 1024, lane * 16, so + t * 1024);
    }
    wait_vm<0>();
    STAMP(8);
    poll<1>(rw, WS_F1 + (t64 < 30 ? (t64 / 3) * 192 : 1920), t64 < 30 ? 24 : 16, tag, lane);
    lds_set(L_MISC + 32);
    STAMP(5);
    lds_wait(L_MISC + 36);
    poll<1>(rw, WS_F2 + dj * 256, 30, tag, lane);
    STAMP(6);
#pragma unroll
    for (int p = 0; p < 10; ++p) dma<SC1>(sw, L_AT + p * 1024, (lane >> 3) * 4096 + (lane & 7) * 16, WS_AT + (80 * dj + p * 8) * 4096 + t64 * 128);
    wait_vm<0>();
    STAMP(7);
    BAR(11);
  }
  DUMP();
#endif
}

__global__ void probeFirst(unsigned long long* p) {
#if __HIP_DEVICE_COMPILE__
  if (threadIdx.x == 0) p[blockIdx.x] = __builtin_amdgcn_s_memrealtime();
#endif
}
__global__ __launch_bounds__(512, 2) void probeBig(unsigned long long* p) {
#if __HIP_DEVICE_COMPILE__
  unsigned long long t = __builtin_amdgcn_s_memrealtime();
  if (BIGV) asm volatile("v_mov_b32 v250, 0" ::: "v250");
  if (threadIdx.x == 0) p[blockIdx.x] = t;
#endif
}
__global__ __launch_bounds__(64) void probeSpin(const unsigned long long* t) {
#if __HIP_DEVICE_COMPILE__
  unsigned long long lo = ~0ull;
  for (int k = 0; k < 4; ++k) { unsigned long long v = t[1024 + threadIdx.x * 4 + k]; lo = v < lo ? v : lo; }
  for (int o = 32; o; o >>= 1) { unsigned long long v = __shfl_xor(lo, o); lo = v < lo ? v : lo; }
  unsigned long long ticks = (lo - t[0]) * 100;
  if (ticks > 1000000) ticks = 1000000;
  unsigned long long t0 = __builtin_amdgcn_s_memrealtime();
  while (__builtin_amdgcn_s_memrealtime() - t0 < ticks) __builtin_amdgcn_s_sleep(8);
#endif
}

extern "C" void kernel_launch(void* const* d_in, const int* in_sizes, int n_in, void* d_out, int out_size, void* d_ws, size_t ws_size,
                              hipStream_t stream) {
  (void)hipFuncSetAttribute((const void*)fused_s4_ffn, hipFuncAttributeMaxDynamicSharedMemorySize, LDS_BYTES);
  unsigned long long* tm = (unsigned long long*)((char*)d_ws + WS_TM);
  (void)hipFuncSetAttribute((const void*)probeBig, hipFuncAttributeMaxDynamicSharedMemorySize, LDS_BYTES);
  probeBig<<<256, 512, LDS_BYTES, stream>>>(tm + 1024);
  fused_s4_ffn<<<256, 512, LDS_BYTES, stream>>>(
      (const uint16_t*)d_in[0], (const uint16_t*)d_in[1], (const uint16_t*)d_in[2], (const uint16_t*)d_in[3], (const uint16_t*)d_in[4],
      (const uint16_t*)d_in[5], (const uint16_t*)d_in[6], (const uint16_t*)d_in[7], (const uint16_t*)d_in[8], (const uint16_t*)d_in[10],
      (const uint16_t*)d_in[11], (const uint16_t*)d_in[12], (uint16_t*)d_out, d_ws);
}
```

```cpp
#include <hip/hip_runtime.h>
#include <stdint.h>

typedef short bf16x8 __attribute__((ext_vector_type(8)));
typedef short s16x4 __attribute__((ext_vector_type(4)));
typedef float f32x4 __attribute__((ext_vector_type(4)));
typedef float f32x2 __attribute__((ext_vector_type(2)));
typedef __bf16 bf16x2 __attribute__((ext_vector_type(2)));
typedef unsigned v4u __attribute__((ext_vector_type(4)));
typedef unsigned v2u __attribute__((ext_vector_type(2)));
typedef unsigned v8u __attribute__((ext_vector_type(8)));
typedef unsigned long long u64;
#define LAS __attribute__((address_space(3)))
#define DI __device__ __forceinline__
typedef __amdgpu_buffer_rsrc_t rsrc_t;

constexpr int L = 2048, D = 640, NS = 192, F = 1536;
constexpr int SC1 = 16, NT_ = 2;

constexpr unsigned WS_EPOCH = 0, WS_F0 = 1024, WS_F3 = 4096, WS_F2 = 8192, WS_STATS = 16384, WS_F1 = 49152, WS_CSP = 65536, WS_U8 = 262144,
                   WS_WDT = 3u << 20, WS_H = 5u << 20, WS_AT = 12u << 20, WS_TM = 15u << 20, WS_BYTES = 16u << 20;

constexpr unsigned STAGE = 40960, XB = 24576;
constexpr unsigned LX = 3 * STAGE;
constexpr unsigned L_LN = LX, LN_RS = 1296, L_AT = LX;
constexpr unsigned L_RAW = LX + 10240;
constexpr unsigned L_ST = L_RAW + 3 * 2048;
constexpr unsigned L_CS = L_ST + 3072;
constexpr unsigned L_MISC = L_CS + 1024;
constexpr unsigned L_U = L_MISC + 128, U_SZ = 4608;
constexpr unsigned L_K3 = L_U + 3 * U_SZ;
constexpr unsigned L_END = L_K3 + 960;
constexpr unsigned L_WDU = L_U, L_DUMMY = L_U + 8192;
constexpr unsigned L_KG = STAGE, KG_SZ = 16 * (544 + 288);
constexpr unsigned L_H = 2 * STAGE, H_CS = 2112, H_SZ = 8 * H_CS;
constexpr unsigned LDS_BYTES = 163840;
static_assert(L_END <= LDS_BYTES && L_H + 3 * H_SZ + 16 <= L_RAW, "lds");
#define G2AUXW SC1
#define G2AUXH SC1
#define ROT2 0
#define G2SIG 3
#define XROT 0
#define TA 20
#define TB 20
#define LNB 0
#define PDELAY 3
#define CPRIO 2
#define PWAIT 14
#define NEARLY 8
#define NSTG 4
#define GPRIO 0
constexpr int SPIN_MAX = 20000;
constexpr unsigned CODE_BYTES = 46592;

#if __HIP_DEVICE_COMPILE__
extern __shared__ __attribute__((aligned(16))) char smem_[];
DI LAS char* lds(unsigned o) { return (LAS char*)smem_ + o; }
DI rsrc_t mkr(const void* p, unsigned bytes) { return __builtin_amdgcn_make_buffer_rsrc((void*)p, 0, bytes, 0x00020000); }
DI unsigned pk(float a, float b) { f32x2 f = {a, b}; return __builtin_bit_cast(unsigned, __builtin_convertvector(f, bf16x2)); }
DI unsigned pk2(f32x2 f) { return __builtin_bit_cast(unsigned, __builtin_convertvector(f, bf16x2)); }
DI float blo(unsigned v) { return __uint_as_float(v << 16); }
DI float bhi(unsigned v) { return __uint_as_float(v & 0xffff0000u); }
DI float rbf(float f) { return blo(pk(f, f)); }
DI void bar() { __builtin_amdgcn_s_barrier(); }
DI void cfence() { asm volatile("" ::: "memory"); }
template <int N> DI void wait_vm() { asm volatile("s_waitcnt vmcnt(%0)" ::"i"(N) : "memory"); }
DI void wait_lg() { asm volatile("s_waitcnt lgkmcnt(0)" ::: "memory"); }
DI bf16x8 trd(unsigned a0, unsigned a1) {
  s16x4 lo = __builtin_amdgcn_ds_read_tr16_b64_v4i16((LAS s16x4*)lds(a0));
  s16x4 hi = __builtin_amdgcn_ds_read_tr16_b64_v4i16((LAS s16x4*)lds(a1));
  return __builtin_shufflevector(lo, hi, 0, 1, 2, 3, 4, 5, 6, 7);
}
DI bf16x8 rd128(unsigned a) { return *(LAS bf16x8*)lds(a); }
DI f32x4 mfma(bf16x8 a, bf16x8 b, f32x4 c) { return __builtin_amdgcn_mfma_f32_16x16x32_bf16(a, b, c, 0, 0, 0); }
DI v4u srd(const void* p, unsigned bytes) { const u64 a = (u64)p; return v4u{(unsigned)a, (unsigned)(a >> 32) & 0xffffu, bytes, 0x00020000u}; }
template <int AUX> DI void dma(v4u r, unsigned ldsoff, unsigned voff, unsigned soff) {
  if (AUX == 0) asm volatile("s_mov_b32 m0, %0\n\ts_nop 0\n\tbuffer_load_dwordx4 %1, %2, %3 offen lds" ::"s"(ldsoff), "v"(voff), "s"(r), "s"(soff) : "memory");
  if (AUX == NT_) asm volatile("s_mov_b32 m0, %0\n\ts_nop 0\n\tbuffer_load_dwordx4 %1, %2, %3 offen nt lds" ::"s"(ldsoff), "v"(voff), "s"(r), "s"(soff) : "memory");
  if (AUX == SC1) asm volatile("s_mov_b32 m0, %0\n\ts_nop 0\n\tbuffer_load_dwordx4 %1, %2, %3 offen sc1 lds" ::"s"(ldsoff), "v"(voff), "s"(r), "s"(soff) : "memory");
}
template <int AUX> DI void st128(v4u v, rsrc_t r, unsigned off) { __builtin_amdgcn_raw_buffer_store_b128(v, r, off, 0, AUX); }
DI void touch(v4u& sink, v4u r, unsigned vo, unsigned so) {
  asm volatile("buffer_load_dwordx4 %0, %1, %2, %3 offen sc1" : "+v"(sink) : "v"(vo), "s"(r), "s"(so) : "memory");
}
DI float rowsum16(float v) {
  v += __builtin_bit_cast(float, __builtin_amdgcn_update_dpp(0, __builtin_bit_cast(int, v), 0x128, 0xf, 0xf, false));
  v += __builtin_bit_cast(float, __builtin_amdgcn_update_dpp(0, __builtin_bit_cast(int, v), 0x124, 0xf, 0xf, false));
  v += __builtin_bit_cast(float, __builtin_amdgcn_update_dpp(0, __builtin_bit_cast(int, v), 0x122, 0xf, 0xf, false));
  v += __builtin_bit_cast(float, __builtin_amdgcn_update_dpp(0, __builtin_bit_cast(int, v), 0x121, 0xf, 0xf, false));
  return v;
}
DI f32x2 cmul(f32x2 a, f32x2 b) {
  f32x2 t = a * b.xx, r;
  asm("v_pk_fma_f32 %0, %1, %2, %3 op_sel:[1,1,0] op_sel_hi:[0,1,1] neg_lo:[0,1,0]" : "=v"(r) : "v"(a), "v"(b), "v"(t));
  return r;
}
DI f32x2 cpow(float lr, float tr, float e) {
  float m = __builtin_amdgcn_exp2f(lr * e), th = __builtin_amdgcn_fractf(tr * e);
  return f32x2{m * __builtin_amdgcn_cosf(th), m * __builtin_amdgcn_sinf(th)};
}
DI v2u tag_of(void) { return *(LAS v2u*)lds(L_MISC); }
DI v2u epoch_tag(rsrc_t rw) {
  const v2u e = __builtin_amdgcn_raw_buffer_load_b64(rw, WS_EPOCH, 0, SC1);
  const u64 t = (((u64)e.y << 32) | e.x) + 0x9E3779B97F4A7C15ull;
  return v2u{(unsigned)t, (unsigned)(t >> 32)};
}
DI void put_flag(rsrc_t rw, unsigned off, v2u tag) { __builtin_amdgcn_raw_buffer_store_b64(tag, rw, off, 0, SC1); }
template <int R> DI void poll(rsrc_t rw, unsigned off, int n, v2u tag, int lane) {
#pragma unroll 1
  for (int spin = 0; spin < SPIN_MAX; ++spin) {
    bool ok = true;
#pragma unroll
    for (int r = 0; r < R; ++r) {
      int i = lane + 64 * r; i = i < n ? i : n - 1;
      v2u f = __builtin_amdgcn_raw_buffer_load_b64(rw, off + i * 8, 0, SC1);
      ok = ok && f.x == tag.x && f.y == tag.y;
    }
    if (__all(ok)) break;
    cfence();
  }
  cfence();
}
struct Poll4 { v2u f[4]; };
DI void poll4_issue(rsrc_t rw, unsigned off, int n, int lane, Poll4& p) {
#pragma unroll
  for (int r = 0; r < 4; ++r) { int i = lane + 64 * r; i = i < n ? i : n - 1; p.f[r] = __builtin_amdgcn_raw_buffer_load_b64(rw, off + i * 8, 0, SC1); }
  cfence();
}
DI void poll4_finish(rsrc_t rw, unsigned off, int n, v2u tag, int lane, const Poll4& p) {
  bool ok = true;
#pragma unroll
  for (int r = 0; r < 4; ++r) ok = ok && p.f[r].x == tag.x && p.f[r].y == tag.y;
  if (!__all(ok)) poll<4>(rw, off, n, tag, lane);
  cfence();
}
DI void lds_set(unsigned o) { cfence(); *(volatile LAS unsigned*)lds(o) = 1; }
DI void lds_wait(unsigned o) {
#pragma unroll 1
  for (int spin = 0; spin < 16 * SPIN_MAX && *(volatile LAS unsigned*)lds(o) == 0; ++spin) __builtin_amdgcn_s_sleep(1);
  cfence();
}
DI void lds_wait4(unsigned o) {
#pragma unroll 1
  for (int spin = 0; spin < 16 * SPIN_MAX; ++spin) {
    const v4u f = *(volatile LAS v4u*)lds(o);
    if (f[0] & f[1] & f[2] & f[3]) break;
    __builtin_amdgcn_s_sleep(1);
  }
  cfence();
}

#define RSTAMP(i)
#define STAMP(i)
#define BAR(i) bar()
#define DUMP()

struct G1Ld { unsigned vx0, vx1, vw0, vw1; };
DI G1Ld g1_lanes(int lane) {
  const int r8 = lane >> 3, sl = lane & 7;
  return G1Ld{(unsigned)(r8 * 1280 + ((sl ^ (r8 >> 1)) << 4)), (unsigned)(r8 * 1280 + ((sl ^ (r8 >> 1) ^ 4) << 4)),
              (unsigned)(r8 * 3072 + ((sl ^ (r8 & 2)) << 4)), (unsigned)(r8 * 3072 + ((sl ^ (r8 & 2) ^ 4) << 4))};
}
template <int NT> DI void g1_issue(v4u rx, v4u rg, v4u ru, const G1Ld& v, int lw, int s, unsigned lb, int row0, int ch) {
  const unsigned xso = (row0 + lw * 16 * NT) * 1280 + s * 128, wso = ch * 128 + (lw * 32 + s * 64) * 3072;
#pragma unroll
  for (int p = 0; p < 4; ++p) {
    dma<0>(rg, lb + XB + (lw * 4 + p) * 1024, (p & 1) ? v.vw1 : v.vw0, wso + p * 8 * 3072);
    dma<0>(ru, lb + XB + 8192 + (lw * 4 + p) * 1024, (p & 1) ? v.vw1 : v.vw0, wso + p * 8 * 3072);
  }
#pragma unroll
  for (int p = 0; p < 2 * NT; ++p) dma<0>(rx, lb + (lw * 2 * NT + p) * 1024, (p & 1) ? v.vx1 : v.vx0, xso + p * 8 * 1280);
}
DI unsigned nxt(unsigned so) { return so == 0 ? 2 * STAGE : so - STAGE; }
template <int NT, int S0> DI void g1_loader(v4u rx, v4u rg, v4u ru, int lw, int lane, int row0, int ch, int rot, unsigned so) {
  constexpr int NP = 8 + 2 * NT;
  const G1Ld v = g1_lanes(lane);
#pragma unroll 1
  for (int s = S0; s < 10; ++s) {
    if (s >= S0 + 2) wait_vm<NP>();
    if (s >= 2) BAR(s - 2);
    g1_issue<NT>(rx, rg, ru, v, lw, s + rot >= 10 ? s + rot - 10 : s + rot, so, row0, ch);
    so = nxt(so);
  }
  wait_vm<NP>(); BAR(8);
}

DI void colsum(rsrc_t rw, unsigned off, int k, int lane) {
  const int c = lane & 15, g = lane >> 4, q = (lane >> 2) & 3, p = lane & 3;
  const bf16x8 ones = {0x3f80, 0x3f80, 0x3f80, 0x3f80, 0x3f80, 0x3f80, 0x3f80, 0x3f80};
  f32x4 r[2];
#pragma unroll
  for (int a = 0; a < 2; ++a) {
    const unsigned ad = XB + (k >> 1) * 8192 + (8 * g + q) * 128 + (((4 * (k & 1) + 2 * a + (p >> 1)) ^ (q & 2) ^ ((g & 1) << 2)) << 4) + 8 * (p & 1);
    r[a] = mfma(trd(ad + 4096, ad + 4608), ones, mfma(trd(ad, ad + 512), ones, f32x4{0.f, 0.f, 0.f, 0.f}));
  }
  f32x4 v;
#pragma unroll
  for (int e = 0; e < 4; ++e) v[e] = c ? r[1][e] : r[0][e];
  if (c < 2) st128<SC1>(__builtin_bit_cast(v4u, v), rw, off + k * 128 + (16 * c + 4 * g) * 4);
}

template <int NT> DI void g1_main(int wid, int lane, unsigned so, f32x4 (&acc)[4][NT]) {
  const int wa = wid & 1, wt = wid >> 1;
  const int c = lane & 15, g = lane >> 4, q = (lane >> 2) & 3, p = lane & 3;
  unsigned aA[2], aB[2];
#pragma unroll
  for (int a = 0; a < 2; ++a) aA[a] = XB + (8 * g + q) * 128 + (((4 * wa + 2 * a + (p >> 1)) ^ (q & 2) ^ ((g & 1) << 2)) << 4) + 8 * (p & 1);
#pragma unroll
  for (int ks = 0; ks < 2; ++ks) aB[ks] = (16 * NT * wt + c) * 128 + (((4 * ks + g) ^ (c >> 1)) << 4);
#pragma unroll
  for (int a = 0; a < 4; ++a)
#pragma unroll
    for (int t = 0; t < NT; ++t) acc[a][t] = f32x4{0.f, 0.f, 0.f, 0.f};
  bf16x8 fa[2][4], fb[2][NT];
  auto rd = [&](int buf, int ks, unsigned o) {
#pragma unroll
    for (int a = 0; a < 4; ++a) { unsigned ad = aA[a & 1] + o + (a >> 1) * 8192 + ks * 4096; fa[buf][a] = trd(ad, ad + 512); }
#pragma unroll
    for (int t = 0; t < NT; ++t) fb[buf][t] = rd128(aB[ks] + o + t * 2048);
  };
  auto mm = [&](int buf) {
#pragma unroll
    for (int t = 0; t < NT; ++t)
#pragma unroll
      for (int a = 0; a < 4; ++a) acc[a][t] = mfma(fa[buf][a], fb[buf][t], acc[a][t]);
  };
  auto ilv = [&]() {
    constexpr int NR = 8 + NT, NM = 4 * NT, NI = NR < NM ? NR : NM;
#pragma unroll
    for (int i = 0; i < NI; ++i) { __builtin_amdgcn_sched_group_barrier(0x008, 1, 0); __builtin_amdgcn_sched_group_barrier(0x100, 1, 0); }
    if (NM > NI) __builtin_amdgcn_sched_group_barrier(0x008, NM - NI, 0);
    __builtin_amdgcn_sched_barrier(0);
  };
  BAR(0);
  STAMP(2); RSTAMP(31);
  rd(0, 0, so);
#pragma unroll 1
  for (int s = 0; s < 9; ++s) {
    rd(1, 1, so); mm(0); ilv();
    so = nxt(so);
    asm volatile("s_waitcnt vmcnt(0) lgkmcnt(0)" ::: "memory");
    BAR(s + 1);
    __builtin_amdgcn_sched_barrier(0);
    rd(0, 0, so); mm(1); ilv();
  }
  rd(1, 1, so); mm(0); ilv();
  mm(1);
  __builtin_amdgcn_sched_barrier(0);
}
template <int NT, bool LAST> DI void g1_epilogue(rsrc_t rw, int wid, int lane, int t16_0, int ch, unsigned st_off, unsigned cs_off, const f32x4 (&acc)[4][NT]) {
  const int wa = wid & 1, wt = wid >> 1, c = lane & 15, g = lane >> 4;
  f32x4 cs[4];
#pragma unroll
  for (int a = 0; a < 4; ++a) cs[a] = *(LAS f32x4*)lds(cs_off + (a >> 1) * 256 + (32 * wa + 16 * (a & 1) + 4 * g) * 4);
  f32x4 st[NT];
#pragma unroll
  for (int t = 0; t < NT; ++t) st[t] = *(LAS f32x4*)lds(st_off + (16 * NT * wt + 16 * t + c) * 16);
#pragma unroll
  for (int t = 0; t < NT; ++t) {
    const float nm = st[t][0], qq = st[t][1], rl = st[t][2];
    v4u o;
#pragma unroll
    for (int a = 0; a < 2; ++a) {
      f32x4 tg = cs[a] * nm + acc[a][t], tu = cs[2 + a] * nm + acc[2 + a][t];
      f32x4 ar = tg * rl, e;
#pragma unroll
      for (int r = 0; r < 4; ++r) e[r] = __builtin_amdgcn_exp2f(ar[r]);
      f32x4 dn = e * qq + qq, pr = tg * tu;
#pragma unroll
      for (int r = 0; r < 4; ++r) dn[r] = __builtin_amdgcn_rcpf(dn[r]);
      pr *= dn;
      if (LAST && t == NT - 1 && a == 1) { asm volatile("" : "+v"(pr)); lds_set(L_MISC + 48 + wid * 4); }
      o[2 * a] = pk(pr[0], pr[1]); o[2 * a + 1] = pk(pr[2], pr[3]);
    }
    st128<SC1>(o, rw, lane * 16 + WS_H + ((t16_0 + NT * wt + t) * 48 + 2 * ch + wa) * 1024);
  }
}

template <int NM> DI void kgen(const unsigned (&w)[6], float dt, int lane, unsigned tb, unsigned done, f32x4 (&acc)[2][2]) {
  constexpr int RS = NM * 256 + 32, KS = NM * 4;
  float lr[NM], tr[NM]; f32x2 W[NM];
#pragma unroll
  for (int m = 0; m < NM; ++m) {
    float v[6];
#pragma unroll
    for (int i = 0; i < 6; ++i) v[i] = m == 0 ? blo(w[i]) : bhi(w[i]);
    lr[m] = dt * v[0] * 1.44269504f; tr[m] = dt * v[1] * 0.15915494f;
    const float br = dt * v[2], bi = dt * v[3];
    W[m] = f32x2{v[4] * br - v[5] * bi, -(v[4] * bi + v[5] * br)};
  }
  asm volatile("" : "+v"(lr[0]), "+v"(W[0]));
  STAMP(13);
  unsigned wa = tb + lane * 4 * NM, fa = tb + (lane & 15) * RS + (lane >> 4) * 16;
  asm volatile("" : "+v"(wa), "+v"(fa));
  auto put = [&](int row, const f32x2* z) {
    if (NM == 2) *(LAS v2u*)lds(wa + row * RS) = v2u{pk2(z[0]), pk2(z[NM - 1])};
    else *(LAS unsigned*)lds(wa + row * RS) = pk2(z[0]);
  };
  f32x2 c[8][NM], hi[NM], t[NM], c8[NM];
#pragma unroll
  for (int m = 0; m < NM; ++m) {
    c[0][m] = f32x2{1.f, 0.f}; c[1][m] = cpow(lr[m], tr[m], 8.f);
    c[2][m] = cmul(c[1][m], c[1][m]); c[3][m] = cmul(c[2][m], c[1][m]); c[4][m] = cmul(c[2][m], c[2][m]);
    c[5][m] = cmul(c[4][m], c[1][m]); c[6][m] = cmul(c[4][m], c[2][m]); c[7][m] = cmul(c[4][m], c[3][m]);
    c8[m] = cmul(c[4][m], c[4][m]); hi[m] = c8[m];
  }
  bf16x8 fb[2][KS];
#pragma unroll
  for (int j = 0; j < 8; ++j) put(j, c[j]);
#pragma unroll
  for (int a = 1; a < 4; ++a) {
    if (a == 2) {
#pragma unroll
      for (int s = 0; s < KS; ++s) fb[0][s] = rd128(fa + s * 64);
    }
    put((8 * a) & 15, hi);
#pragma unroll
    for (int j = 1; j < 8; ++j) {
#pragma unroll
      for (int m = 0; m < NM; ++m) t[m] = cmul(hi[m], c[j][m]);
      put((8 * a + j) & 15, t);
    }
    if (a < 3) {
#pragma unroll
      for (int m = 0; m < NM; ++m) hi[m] = cmul(hi[m], c8[m]);
    }
  }
#pragma unroll
  for (int s = 0; s < KS; ++s) fb[1][s] = rd128(fa + s * 64);
  STAMP(14);
  f32x2 z1[NM], zb[NM], r0[NM], r4[NM];
#pragma unroll
  for (int m = 0; m < NM; ++m) { z1[m] = cpow(lr[m], -tr[m], 1.f); zb[m] = cpow(lr[m], -tr[m], 256.f); t[m] = W[m]; }
#pragma unroll
  for (int b = 0; b < 8; ++b) {
    put(b, t);
    if ((b & 1) == 0) {
      f32x2 u[NM];
#pragma unroll
      for (int m = 0; m < NM; ++m) { u[m] = cmul(t[m], zb[m]); if (b == 0) r0[m] = u[m]; if (b == 4) r4[m] = u[m]; }
      put(8 + b / 2, u);
    }
    if (b < 7) {
#pragma unroll
      for (int m = 0; m < NM; ++m) t[m] = cmul(t[m], z1[m]);
    }
  }
#pragma unroll
  for (int i = 0; i < 2; ++i) {
#pragma unroll
    for (int m = 0; m < NM; ++m) { r0[m] = cmul(r0[m], zb[m]); r4[m] = cmul(r4[m], zb[m]); }
    put(12 + 2 * i, r0); put(13 + 2 * i, r4);
  }
  bf16x8 fr[2][KS];
#pragma unroll
  for (int s = 0; s < KS; ++s) fr[0][s] = rd128(fa + s * 64);
#pragma unroll
  for (int i = 0; i < 5; ++i) {
#pragma unroll
    for (int m = 0; m < NM; ++m) r0[m] = cmul(r0[m], zb[m]);
    put(i, r0);
  }
  if (NM == 2) lds_set(done);
#pragma unroll
  for (int s = 0; s < KS; ++s) fr[1][s] = rd128(fa + s * 64);
  __builtin_amdgcn_sched_barrier(0);
  STAMP(15);
#pragma unroll
  for (int i = 0; i < 4; ++i) acc[i >> 1][i & 1] = f32x4{0.f, 0.f, 0.f, 0.f};
#pragma unroll
  for (int rt = 0; rt < 2; ++rt)
#pragma unroll
    for (int s = 0; s < KS; ++s) { acc[rt][0] = mfma(fr[rt][s], fb[0][s], acc[rt][0]); acc[rt][1] = mfma(fr[rt][s], fb[1][s], acc[rt][1]); }
}
struct LnOut { s16x4 tr[2][10]; v2u st; };
DI void ln8(rsrc_t rx, int tb, int lane, unsigned tile, LnOut& out) {
  const int r4 = lane >> 4, i = lane & 15;
  v4u raw[2][5];
#pragma unroll
  for (int h = 0; h < 2; ++h)
#pragma unroll
    for (int k = 0; k < 5; ++k) raw[h][k] = __builtin_amdgcn_raw_buffer_load_b128(rx, r4 * 1280 + i * 16 + k * 256, (tb * 8 + h * 4) * 1280, 0);
  const unsigned ta = tile + ((lane >> 2) & 3) * LN_RS + (16 * r4 + 4 * (lane & 3)) * 2;
#pragma unroll
  for (int h = 0; h < 2; ++h) {
    if (h == 0) { asm volatile("" : "+v"(raw[0][4])); STAMP(13); asm volatile("" : "+v"(raw[1][4])); STAMP(15); } else STAMP(14);
    float s1 = 0.f, s2 = 0.f, s3 = 0.f, s4 = 0.f;
    const bf16x2 one2 = __builtin_bit_cast(bf16x2, 0x3f803f80u);
#pragma unroll
    for (int k = 0; k < 10; ++k) {
      const unsigned w0 = raw[h][k >> 1][2 * (k & 1)], w1 = raw[h][k >> 1][2 * (k & 1) + 1];
      const bf16x2 p0 = __builtin_bit_cast(bf16x2, w0), p1 = __builtin_bit_cast(bf16x2, w1);
      s1 = __builtin_amdgcn_fdot2_f32_bf16(p0, one2, s1, false); s2 = __builtin_amdgcn_fdot2_f32_bf16(p0, p0, s2, false);
      s3 = __builtin_amdgcn_fdot2_f32_bf16(p1, one2, s3, false); s4 = __builtin_amdgcn_fdot2_f32_bf16(p1, p1, s4, false);
    }
    const float mean = rowsum16(s1 + s3) * (1.f / D);
    const float var = rbf(fmaxf(rowsum16(s2 + s4) * (1.f / D) - mean * mean, 0.f));
    const float m = rbf(mean), den = rbf(sqrtf(rbf(var + 1e-5f)));
    const float ri = __builtin_amdgcn_rcpf(den), nb = -m * ri;
    if (h == 0 || (i & 1)) out.st = v2u{__float_as_uint(m), __float_as_uint(den)};
#pragma unroll
    for (int k = 0; k < 5; ++k) {
      v4u o;
#pragma unroll
      for (int e = 0; e < 4; ++e) { const unsigned w = raw[h][k][e]; o[e] = pk2(f32x2{blo(w), bhi(w)} * ri + nb); }
      *(LAS v4u*)lds(tile + r4 * LN_RS + i * 16 + k * 256) = o;
    }
    cfence();
#pragma unroll
    for (int it = 0; it < 10; ++it) out.tr[h][it] = __builtin_amdgcn_ds_read_tr16_b64_v4i16((LAS s16x4*)lds(ta + it * 128));
  }
}
DI void ln8_store(rsrc_t rw, int tb, int lane, const LnOut& in) {
  if ((lane & 15) < 2) __builtin_amdgcn_raw_buffer_store_b64(in.st, rw, ((lane & 1) * 4 + (lane >> 4)) * 8, WS_STATS + tb * 128, SC1);
#pragma unroll
  for (int it = 0; it < 10; ++it) {
    bf16x8 o = __builtin_shufflevector(in.tr[0][it], in.tr[1][it], 0, 1, 2, 3, 4, 5, 6, 7);
    st128<SC1>(__builtin_bit_cast(v4u, o), rw, lane * 16 + it * 1024 + WS_U8 + tb * 10240);
  }
}

DI unsigned prm(unsigned hi, unsigned lo, unsigned sel) { return __builtin_amdgcn_perm(hi, lo, sel); }
DI unsigned colo(int col) { return (col & 15) * 16 + ((col >> 4) & 1) * 8; }
template <int S> DI v4u kchunk(unsigned raw, int lane) {
  const int i = lane & 31, up = lane >> 5;
  if (S == 0) {
    const v2u a0 = *(LAS v2u*)lds(raw + colo(i) + 512 * up);
    const v2u a1 = *(LAS v2u*)lds(raw + (up ? (i == 31 ? 768 : 512 + colo(i + 1)) : 256 + colo(i)));
    const unsigned s0 = up ? 0x03020504u : 0x05040706u, s1 = up ? 0x01000100u : 0x05040706u, s2 = up ? 0x03020504u : 0x05040706u,
                   s3 = up ? 0x01000100u : 0x01000302u;
    return v4u{prm(up ? a1.x : a1.y, a0.y, s0), prm(a1.x, a0.y, s1), prm(a0.y, a0.x, s2), prm(0, a0.x, s3)};
  } else {
    const unsigned b0 = *(LAS unsigned*)lds(raw + 768 + colo(i) + 4 * up);
    const unsigned b1 = *(LAS unsigned*)lds(raw + (lane == 63 ? 1024 : 768 + colo(i + 1) + 4 * ((lane + 1) >> 5)));
    return v4u{prm(0, b1, 0x01000100), prm(0, b0, 0x03020302), prm(0, b0, 0x01000302), prm(0, b0, 0x01000100)};
  }
}
DI unsigned vmov(unsigned x) { unsigned r; asm("v_mov_b32 %0, %1" : "=v"(r) : "v"(x)); return r; }
struct HWin { v8u w, e, o0, o1; };
DI void hwin(v4u x, v4u y, HWin& h) {
  h.w = __builtin_shufflevector(x, y, 0, 1, 2, 3, 4, 5, 6, 7);
  asm volatile("" : "+v"(h.w));
#pragma unroll
  for (int k = 0; k < 6; ++k) {
    h.e[k] = vmov(h.w[k + 1]);
    h.o0[k] = __builtin_amdgcn_alignbit(h.w[k + 1], h.w[k], 16);
    h.o1[k] = __builtin_amdgcn_alignbit(h.w[k + 2], h.w[k + 1], 16);
    asm volatile("" : "+v"(h.o1[k]));
  }
  asm volatile("" : "+v"(h.e), "+v"(h.o0), "+v"(h.o1));
}
DI void hwrite(const HWin& h, unsigned a) {
  *(LAS v4u*)lds(a + 7 * H_CS) = __builtin_shufflevector(h.w, h.w, 0, 1, 2, 3);
  *(LAS v4u*)lds(a + 3 * H_CS) = __builtin_shufflevector(h.w, h.w, 2, 3, 4, 5);
  *(LAS v4u*)lds(a + 5 * H_CS) = __builtin_shufflevector(h.e, h.e, 0, 1, 2, 3);
  *(LAS v4u*)lds(a + 1 * H_CS) = __builtin_shufflevector(h.e, h.e, 2, 3, 4, 5);
  *(LAS v4u*)lds(a + 6 * H_CS) = __builtin_shufflevector(h.o0, h.o0, 0, 1, 2, 3);
  *(LAS v4u*)lds(a + 2 * H_CS) = __builtin_shufflevector(h.o0, h.o0, 2, 3, 4, 5);
  *(LAS v4u*)lds(a + 4 * H_CS) = __builtin_shufflevector(h.o1, h.o1, 0, 1, 2, 3);
  *(LAS v4u*)lds(a + 0 * H_CS) = __builtin_shufflevector(h.o1, h.o1, 2, 3, 4, 5);
}
struct ConvPre { HWin h[3]; unsigned k3; bf16x8 kt[4], b9, b7; };
DI void conv_pre(unsigned raw, int lane, ConvPre& p) {
  const v4u x0 = kchunk<0>(raw, lane), x1 = kchunk<1>(raw, lane);
  const unsigned kk = *(LAS uint16_t*)lds(raw + 1024);
  const int n0 = 128 - 2 * lane, n1 = n0 - 1;
  const unsigned c0 = *(LAS uint16_t*)lds(raw + 1024 + (n0 == 128 ? 256 : colo(n0) + 2 * (n0 >> 5)));
  const unsigned c1 = *(LAS uint16_t*)lds(raw + 1024 + colo(n1) + 2 * (n1 >> 5));
  p.k3 = c0 | (c1 << 16);
  v4u y0, y1, cl;
#pragma unroll
  for (int k = 0; k < 4; ++k) {
    y0[k] = __builtin_amdgcn_update_dpp(0u, x0[k], 0x138, 0xf, 0xf, false);
    y1[k] = __builtin_amdgcn_update_dpp((unsigned)__builtin_amdgcn_readlane(x0[k], 63), x1[k], 0x138, 0xf, 0xf, false);
    cl[k] = __builtin_amdgcn_readlane(x1[k], 63);
  }
  hwin(x0, y0, p.h[0]);
  hwin(x1, y1, p.h[1]);
  const unsigned k2 = kk | (kk << 16), m0 = lane == 0 ? ~0u : 0u, m1 = lane == 1 ? ~0u : 0u;
  hwin(v4u{0, (kk << 16) & m0, k2 & m0, k2 & m0},
       v4u{cl[0] & m0, (cl[1] & m0) | ((kk << 16) & m1), (cl[2] & m0) | (k2 & m1), (cl[3] & m0) | (k2 & m1)}, p.h[2]);
  asm volatile("" : "+v"(p.k3));
}
DI void conv_image(ConvPre& p, unsigned hb, unsigned k3, int lane) {
  const int c = lane & 15, g = lane >> 4;
#pragma unroll
  for (int jj = 0; jj < 8; ++jj) {
    const int dl = 8 * g + jj - 8 - c;
    p.b9[jj] = (dl >= -4 && dl <= 4) ? 0x3f80 : 0; p.b7[jj] = (dl >= -3 && dl <= 3) ? 0x3f80 : 0;
  }
  asm volatile("" : "+v"(p.b9), "+v"(p.b7));
  *(LAS unsigned*)lds(k3 + lane * 4) = p.k3;
  if (lane < 16) *(LAS unsigned*)lds(k3 + 256 + lane * 4) = 0;
  const unsigned ka = k3 + (254 - 2 * c + 8 * g) * 2;
#pragma unroll
  for (int f = 0; f < 4; ++f) {
    v4u t;
#pragma unroll
    for (int e = 0; e < 4; ++e) t[e] = *(LAS unsigned*)lds(ka - (4 + f) * 64 + e * 4);
    p.kt[f] = __builtin_bit_cast(bf16x8, t);
  }
  hwrite(p.h[0], hb + (1 + lane) * 16);
  hwrite(p.h[1], hb + (65 + lane) * 16);
  if (lane < 5) hwrite(p.h[2], hb + (lane < 3 ? 129 + lane : lane == 3 ? 0 : 132) * 16);
}

DI void conv(rsrc_t rw, int cw, int d, int lane, const ConvPre& p, unsigned go) {
  const unsigned hb = L_H + cw * H_SZ, ub = L_U + cw * U_SZ;
  const int c = lane & 15, g = lane >> 4;
  unsigned aa = hb + (c & 7) * H_CS + (3 + (c >> 3) - g) * 16, ab = ub + 32 + 32 * c + 16 * g;
  f32x4 acc[8] = {};
  bf16x8 fa[2][4], fb[2][8];
  auto rd = [&](int buf, int oa, int ob) {
#pragma unroll
    for (int k = 0; k < 4; ++k) fa[buf][k] = rd128(aa + oa + k * 512);
#pragma unroll
    for (int e = 7; e >= 0; --e) fb[buf][e] = rd128(ab + ob + e * 512);
  };
  auto mm = [&](int buf) {
#pragma unroll
    for (int k = 0; k < 4; ++k)
#pragma unroll
      for (int e = 7 - k; e >= 0; --e) acc[e + k] = mfma(fa[buf][k], fb[buf][e], acc[e + k]);
  };
  auto ilv = [&]() {
#pragma unroll
    for (int i = 0; i < 12; ++i) { __builtin_amdgcn_sched_group_barrier(0x008, 1, 0); __builtin_amdgcn_sched_group_barrier(0x100, 1, 0); }
    __builtin_amdgcn_sched_group_barrier(0x008, 14, 0);
    __builtin_amdgcn_sched_barrier(0);
  };
  asm volatile("" : "+v"(aa), "+v"(ab));
  rd(0, 0, 448);
  {
    const bf16x8 fx = rd128(aa + 2048);
    bf16x8 ue[4];
#pragma unroll
    for (int f = 0; f < 4; ++f) ue[f] = rd128(ab + 432 + f * 512);
#pragma unroll 1
    for (int spin = 0; spin < 64 * SPIN_MAX && *(volatile LAS unsigned*)lds(go) == 0; ++spin) {}
    cfence();
    STAMP(5);
    __builtin_amdgcn_s_setprio(CPRIO);
#pragma unroll
    for (int f = 0; f < 4; ++f) {
      acc[4 + f] = mfma(fx, fb[0][f], acc[4 + f]);
      const f32x4 ve = mfma(ue[f], p.b9, f32x4{0.f, 0.f, 0.f, 0.f}), vo = mfma(fb[0][f], p.b7, f32x4{0.f, 0.f, 0.f, 0.f});
      const bf16x8 vf = __builtin_bit_cast(bf16x8, v4u{pk(ve[0], vo[0]), pk(ve[1], vo[1]), pk(ve[2], vo[2]), pk(ve[3], vo[3])});
#pragma unroll
      for (int n = 4 + f; n < 8; ++n) acc[n] = mfma(vf, p.kt[n - f - 4], acc[n]);
    }
    __builtin_amdgcn_sched_barrier(0);
  }
  STAMP(8);
#pragma unroll
  for (int r = 0; r < 7; ++r) { rd((r + 1) & 1, 64 * (r + 1), 448 - 64 * (r + 1)); mm(r & 1); ilv(); }
  mm(1);
#pragma unroll
  for (int n = 0; n < 8; ++n)
    __builtin_amdgcn_raw_buffer_store_b64(v2u{pk(acc[n][0], acc[n][1]), pk(acc[n][2], acc[n][3])}, rw, 32 * c + 8 * g + 512 * n, WS_AT + d * 4096, SC1);
}
#endif

__global__ __launch_bounds__(512, 2) void fused_s4_ffn(
    const uint16_t* __restrict__ x, const uint16_t* __restrict__ Are, const uint16_t* __restrict__ Aim, const uint16_t* __restrict__ Bre,
    const uint16_t* __restrict__ Bim, const uint16_t* __restrict__ Cre, const uint16_t* __restrict__ Cim, const uint16_t* __restrict__ logdt,
    const uint16_t* __restrict__ dskip, const uint16_t* __restrict__ wg, const uint16_t* __restrict__ wu, const uint16_t* __restrict__ wd,
    uint16_t* __restrict__ out, void* ws) {
#if __HIP_DEVICE_COMPILE__
  const int wid = __builtin_amdgcn_readfirstlane(threadIdx.x >> 6), lane = threadIdx.x & 63;
  const int b = blockIdx.x, xcd = b & 7, j = b >> 3;
  const bool normal = j < 30;
  const int rl = j % 5, ti = 5 * (xcd & 1) + rl;
  const int mm_ = 2 * (xcd >> 1) + (xcd & 1), par = j - 30;
  const int ch = normal ? 6 * (xcd >> 1) + j / 5 : 3 * mm_ + 2 * par;
  const int ch2 = 3 * mm_ + 1;
  const int nch = normal ? (j < 20 ? 3 : 2) : 0;
  const int d0 = 80 * xcd + (j < 20 ? 3 * j : 2 * j + 20);
  const int t64 = 8 * (xcd >> 1) + (j >> 2), dj = 4 * (xcd & 1) + (j & 3);
  const int rot = XROT ? (ti + (5 * (j / 5) + 1) / 3) % 10 : ti;
  const int rot2 = ROT2 * (j & 3);
  auto kst = [&](int s) { return s + rot2 >= 12 ? s + rot2 - 12 : s + rot2; };
  auto pbase = [](int w) -> unsigned { return w * 20480; };
  const rsrc_t rw = mkr(ws, WS_BYTES), rx = mkr(x, L * D * 2);
  STAMP(0);
  if (wid == 6) {
    u64 pc; asm volatile("s_getpc_b64 %0" : "=s"(pc));
    dma<0>(srd((const void*)pc, CODE_BYTES), L_DUMMY, lane * 16, j * 1024);
    dma<0>(srd((const void*)pc, CODE_BYTES), L_DUMMY, lane * 16, 32768 + j * 1024);
  }
  if (wid == 7) { if (lane < 7) *(LAS v4u*)lds(L_MISC + 16 + lane * 16) = v4u{0, 0, 0, 0}; wait_lg(); }
  asm volatile("" ::"s"(x), "s"(Are), "s"(Aim), "s"(Bre), "s"(Bim), "s"(Cre), "s"(Cim), "s"(logdt), "s"(dskip), "s"(wg), "s"(wu), "s"(wd), "s"(out), "s"(ws));
  unsigned pw[6], ldw_ = 0, dsw_ = 0;
  const int kd = d0 + (wid & 3);
  if ((wid & 3) < nch) {
    const uint16_t* const par6[6] = {Are, Aim, Bre, Bim, Cre, Cim};
    const unsigned vo = wid >= 4 ? lane * 4 : 256 + (lane >> 1) * 4;
#pragma unroll
    for (int i = 0; i < 6; ++i) pw[i] = __builtin_amdgcn_raw_buffer_load_b32(mkr(par6[i], D * NS * 2), vo, kd * NS * 2, NT_);
    ldw_ = ((const __attribute__((address_space(4))) unsigned*)logdt)[kd >> 1];
    dsw_ = ((const __attribute__((address_space(4))) unsigned*)dskip)[kd >> 1];
  }
  cfence(); bar(); cfence();
  STAMP(11);
  const float dt = rbf(expf((kd & 1) ? bhi(ldw_) : blo(ldw_)));
  const v4u sg = srd(wg, D * F * 2), su = srd(wu, D * F * 2), sx = srd(x, L * D * 2), sw = srd(ws, WS_BYTES);

  if (wid < 4) {
    if (wid < nch) {
      f32x4 ka[2][2];
#pragma unroll
      for (int i = 0; i < 6; ++i) pw[i] = (lane & 1) ? pw[i] >> 16 : pw[i];
      kgen<1>(pw, dt, lane, L_KG + wid * KG_SZ + 16 * 544, 0, ka);
#pragma unroll
      for (int i = 0; i < 4; ++i) *(LAS f32x4*)lds(L_KG + wid * KG_SZ + 16 * 544 + i * 1024 + lane * 16) = ka[i >> 1][i & 1];
      wait_lg();
      lds_set(L_MISC + 16 + wid * 4);
      STAMP(12);
    } else if (wid == 3 && normal) {
      const v2u tag = epoch_tag(rw);
      const v4u sd = srd(wd, F * D * 2);
#pragma unroll 1
      for (int p = 0; p < 8; ++p) dma<NT_>(sd, L_WDU + p * 1024, (lane >> 3) * 1280 + (lane & 7) * 16, (ch * 64 + p * 8) * 1280 + ti * 128);
      const G1Ld v = g1_lanes(lane);
      v4u sink = {0, 0, 0, 0};
#pragma unroll 1
      for (int p = 0; p < 8; ++p) {
        touch(sink, sg, v.vw0, ch * 128 + (rot * 64 + p * 8) * 3072);
        touch(sink, su, v.vw0, ch * 128 + (rot * 64 + p * 8) * 3072);
      }
      wait_vm<16>();
      const unsigned a0 = L_WDU + (4 * (lane >> 4) + ((lane >> 2) & 3)) * 128 + 8 * (lane & 3);
#pragma unroll
      for (int p = 0; p < 8; ++p) {
        const unsigned a = a0 + (p >> 2) * 4096 + (p & 3) * 32;
        st128<SC1>(__builtin_bit_cast(v4u, trd(a, a + 2048)), rw, lane * 16 + WS_WDT + ((2 * ch + (p >> 2)) * 40 + 4 * ti + (p & 3)) * 1024);
      }
      STAMP(12);
      asm volatile("s_waitcnt vmcnt(0)" : "+v"(sink)::"memory");
      if (lane == 0) put_flag(rw, WS_F3 + (ti * 24 + ch) * 8, tag);
    }
    if (normal) {
      const G1Ld v = g1_lanes(lane);
      if (wid < 2) {
        lds_wait(L_MISC + 68); lds_wait(L_MISC + 72);
        if (nch == 3) lds_wait(L_MISC + 76);
      } else {
        lds_wait(L_MISC + 40 + (wid & 1) * 4);
      }
      STAMP(10);
      g1_issue<6>(sx, sg, su, v, wid & 1, wid < 2 ? rot : rot == 9 ? 0 : rot + 1, wid < 2 ? 0 : 2 * STAGE, 192 * ti, ch);
      if (wid < 2) { lds_set(L_MISC + 40 + wid * 4); wait_vm<0>(); }
    }
    wait_lg();
    STAMP(1);
    if (normal) {
      f32x4 acc[4][6];
      g1_main<6>(wid, lane, 0, acc);
      lds_wait(L_MISC + 96);
      STAMP(3);
      g1_epilogue<6, true>(rw, wid, lane, 12 * ti, ch, L_ST, L_CS, acc);
    } else {
      f32x4 acc4[4][4], acc2[4][2];
      g1_main<4>(wid, lane, 0, acc4);
      g1_main<2>(wid, lane, 2 * STAGE, acc2);
      lds_wait(L_MISC + 96);
      STAMP(3);
      g1_epilogue<4, false>(rw, wid, lane, 120, ch, L_ST, L_CS, acc4);
      g1_epilogue<2, true>(rw, wid, lane, 120 + 4 * par, ch2, L_ST + par * 1024, L_CS + 512, acc2);
    }
    STAMP(4); RSTAMP(32);
    const v2u tag = tag_of();

    const unsigned wso = WS_WDT + (12 * wid * 40 + 5 * dj) * 1024, hso = WS_H + (4 * t64 * 48 + 12 * wid) * 1024;
    const unsigned vo0 = lane * 16, vo1 = lane * 16 + 4096, pw = pbase(wid) + lane * 16;
    v4u wr[5][5], hr[3][4];
#pragma unroll
    for (int i = 0; i < 25; ++i) asm volatile("" : "=v"(wr[i / 5][i % 5]));
#pragma unroll
    for (int i = 0; i < 12; ++i) asm volatile("" : "=v"(hr[i / 4][i % 4]));
    auto ld1 = [&](v4u& dst, unsigned vo, unsigned so, int imm) {
      if (imm == 0) asm volatile("buffer_load_dwordx4 %0, %1, %2, %3 offen sc1" : "+v"(dst) : "v"(vo), "s"(sw), "s"(so) : "memory");
      if (imm == 1) asm volatile("buffer_load_dwordx4 %0, %1, %2, %3 offen offset:1024 sc1" : "+v"(dst) : "v"(vo), "s"(sw), "s"(so) : "memory");
      if (imm == 2) asm volatile("buffer_load_dwordx4 %0, %1, %2, %3 offen offset:2048 sc1" : "+v"(dst) : "v"(vo), "s"(sw), "s"(so) : "memory");
      if (imm == 3) asm volatile("buffer_load_dwordx4 %0, %1, %2, %3 offen offset:3072 sc1" : "+v"(dst) : "v"(vo), "s"(sw), "s"(so) : "memory");
    };
    auto ldw = [&](int s, int dd) { ld1(wr[s % 5][dd], dd < 4 ? vo0 : vo1, wso + kst(s) * 40960, dd & 3); };
    unsigned ht[4];
#pragma unroll
    for (int t = 0; t < 4; ++t) ht[t] = hso + ((wid + 1 + t) & 3) * 49152;
    auto ldh = [&](int s, int t) { ld1(hr[s % 3][t], vo0, ht[t] + kst(s) * 1024, 0); };
#pragma unroll
    for (int i = 0; i < NEARLY; ++i) ldw(i / 5, i % 5);
    wait_vm<NEARLY>();
    STAMP(5); RSTAMP(33);
    if (lane == 0) put_flag(rw, WS_F1 + ((normal ? ti * 24 + ch : 240 + 2 * mm_ + par) * 4 + wid) * 8, tag);
#pragma unroll
    for (int i = NEARLY; i < 25; ++i) ldw(i / 5, i % 5);
    lds_wait4(L_MISC + 48);
#pragma unroll 1
    for (int s = 0; s < NSTG; ++s) {
      const unsigned so = wso + kst(12 - NSTG + s) * 40960, lo = pbase(wid) + s * 5120;
#pragma unroll
      for (int t = 0; t < 5; ++t) dma<SC1>(sw, lo + t * 1024, vo0, so + t * 1024);
    }
    wait_vm<PWAIT>();
    lds_set(L_MISC + 80 + wid * 4);
    lds_wait(L_MISC + 32);
    STAMP(6);
#pragma unroll
    for (int i = 0; i < 12; ++i) ldh(i / 4, i % 4);
    f32x4 acc[4][5];
    const int c = lane & 15, g = lane >> 4;
    const unsigned oo = ((4 * t64 + wid) * 16 + c) * 1280 + (80 * dj + 4 * g) * 2;
    v2u xr[5];
    auto mm1 = [&](int s, int t, int dd) {
      if (s == 0) asm volatile("v_mfma_f32_16x16x32_bf16 %0, %1, %2, 0" : "=&v"(acc[t][dd]) : "v"(wr[0][dd]), "v"(hr[0][t]));
      else asm volatile("v_mfma_f32_16x16x32_bf16 %0, %1, %2, %0" : "+v"(acc[t][dd]) : "v"(wr[s % 5][dd]), "v"(hr[s % 3][t]));
    };
#pragma unroll
    for (int s = 0; s < 10; ++s) {
      auto nld = [](int i) constexpr { return (i < 9 ? 4 : 0) + (i < 7 - NSTG ? 5 : 0); };
      const int yn = s == 0 ? 8 : s == 1 ? 4 + nld(0) : nld(s - 2) + nld(s - 1);
      if (s == 12 - NSTG) wait_lg();
      asm volatile("s_waitcnt vmcnt(%0)" ::"i"(yn) : "memory");
#pragma unroll
      for (int t = 0; t < 4; ++t) {
#pragma unroll
        for (int dd = 0; dd < 5; ++dd) {
          mm1(s, t, dd);
          if (t == 3 && s < 7 - NSTG) ldw(s + 5, dd);
          if (t == 3 && s >= 7 - NSTG && s < 7)
            asm volatile("ds_read_b128 %0, %1 offset:%2" : "+v"(wr[s % 5][dd]) : "v"(pw), "i"((s - 7 + NSTG) * 5120 + dd * 1024) : "memory");
        }
        if (s + 3 < 12) ldh(s + 3, t);
      }
      if (s == G2SIG && wid == 0) lds_set(L_MISC + 36);
    }
    asm volatile("s_waitcnt vmcnt(0) lgkmcnt(0)" ::: "memory");
#pragma unroll
    for (int dd = 0; dd < 5; ++dd) xr[dd] = __builtin_amdgcn_raw_buffer_load_b64(rx, oo + dd * 32, 0, 0);
#pragma unroll
    for (int t = 0; t < 4; ++t) {
#pragma unroll
      for (int i = 0; i < 10; ++i) {
        mm1(10 + i / 5, t, i % 5);
        if (t > 0 && i >= 3 && i < 8)
          asm volatile("ds_write_b128 %0, %1 offset:%2" ::"v"(pw), "v"(acc[t - 1][i - 3]), "i"((t - 1) * 5120 + (i - 3) * 1024) : "memory");
      }
    }
    asm volatile("s_nop 7" : "+v"(acc[3][0]), "+v"(acc[3][1]), "+v"(acc[3][2]), "+v"(acc[3][3]), "+v"(acc[3][4]));
    STAMP(7);
    wait_lg();
    BAR(11);
    STAMP(8);
    f32x4 own[5];
#pragma unroll
    for (int dd = 0; dd < 5; ++dd) own[dd] = acc[3][dd];
#pragma unroll
    for (int k = 1; k < 4; ++k) {
      const unsigned o = pbase((wid + k) & 3) + lane * 16 + (3 - k) * 5120;
#pragma unroll
      for (int dd = 0; dd < 5; ++dd) own[dd] += *(LAS f32x4*)lds(o + dd * 1024);
    }
    const rsrc_t ro = mkr(out, L * D * 2);
    const unsigned at = L_AT + (4 * g + ((lane >> 2) & 3)) * 128 + (16 * wid + 4 * (lane & 3)) * 2;
#pragma unroll
    for (int dd = 0; dd < 5; ++dd) {
      const v2u a = __builtin_bit_cast(v2u, __builtin_amdgcn_ds_read_tr16_b64_v4i16((LAS s16x4*)lds(at + dd * 2048)));
      const float o0 = own[dd][0] + blo(a.x) + blo(xr[dd].x), o1 = own[dd][1] + bhi(a.x) + bhi(xr[dd].x);
      const float o2 = own[dd][2] + blo(a.y) + blo(xr[dd].y), o3 = own[dd][3] + bhi(a.y) + bhi(xr[dd].y);
      __builtin_amdgcn_raw_buffer_store_b64(v2u{pk(o0, o1), pk(o2, o3)}, ro, oo + dd * 32, 0, 0);
    }
    STAMP(9);
  } else if (wid < 7) {
    const int cw = wid - 4;
    ConvPre pre;
    v4u sink = {0, 0, 0, 0};
    auto warm_wd = [&]() {
#pragma unroll 1
      for (int i = 0; i < 3; ++i) {
        const unsigned so = WS_WDT + ((6 * (j >> 2) + 3 * cw + i) * 40 + 5 * dj) * 1024;
#pragma unroll
        for (int t = 0; t < 5; ++t) touch(sink, sw, lane * 16, so + t * 1024);
      }
      wait_vm<15>();
    };
    if (normal) {
      LnOut lo2;
      const bool ln2 = cw == 2 && (j == 20 || j == 21);
      if (cw < nch) {
        f32x4 ka[2][2];
        kgen<2>(pw, dt, lane, L_KG + cw * KG_SZ, L_MISC + 68 + cw * 4, ka);
        STAMP(12);
        lds_wait(L_MISC + 16 + cw * 4);
#pragma unroll
        for (int i = 0; i < 4; ++i) ka[i >> 1][i & 1] += *(LAS f32x4*)lds(L_KG + cw * KG_SZ + 16 * 544 + i * 1024 + lane * 16);
        if (lane == 0) ka[0][0][0] += (kd & 1) ? bhi(dsw_) : blo(dsw_);
#pragma unroll
        for (int rt = 0; rt < 2; ++rt)
          *(LAS v4u*)lds(L_RAW + cw * 2048 + rt * 1024 + lane * 16) =
              v4u{pk(ka[rt][0][0], ka[rt][0][1]), pk(ka[rt][0][2], ka[rt][0][3]), pk(ka[rt][1][0], ka[rt][1][1]), pk(ka[rt][1][2], ka[rt][1][3])};
        STAMP(3);
        conv_pre(L_RAW + cw * 2048, lane, pre);
      } else if (ln2) {
        ln8(rx, 240 + 2 * xcd + j - 20, lane, L_KG + 2 * KG_SZ, lo2);
      }
      wait_lg();
      STAMP(1);
      if (cw < 2) {
        g1_loader<6, 2>(sx, sg, su, cw, lane, 192 * ti, ch, rot, STAGE);
        warm_wd();
      } else {
        BAR(0);
        colsum(rw, WS_CSP + (ch * 10 + ti) * 512, 0, lane); colsum(rw, WS_CSP + (ch * 10 + ti) * 512, 1, lane);
        if (LNB == 0 && ln2) ln8_store(rw, 240 + 2 * xcd + j - 20, lane, lo2);
        BAR(1);
        if (LNB == 1 && ln2) ln8_store(rw, 240 + 2 * xcd + j - 20, lane, lo2);
        if (LNB == 1) BAR(2);
        wait_vm<0>();
        if (LNB == 0) BAR(2);
        BAR(3); BAR(4);
        Poll4 p3;
        poll4_issue(rw, WS_F3, 240, lane, p3);
        BAR(5);
        poll4_finish(rw, WS_F3, 240, tag_of(), lane, p3);
        BAR(6); BAR(7); BAR(8);
      }
      BAR(9);
      if (cw < nch) conv_image(pre, L_H + cw * H_SZ, L_K3 + cw * 320, lane);
    } else if (cw < 2) {
      g1_loader<4, 0>(sx, sg, su, cw, lane, 1920, ch, 5 * (xcd & 1), 0);
      wait_vm<0>(); BAR(9);
      g1_loader<2, 0>(sx, sg, su, cw, lane, 1920 + 64 * par, ch2, 5 * (xcd & 1), 2 * STAGE);
      warm_wd(); BAR(9);
    } else {
#pragma unroll 1
      for (int s = 0; s < 20; ++s) bar();
    }
    asm volatile("s_waitcnt vmcnt(0)" : "+v"(sink)::"memory");
    STAMP(4);
    if (cw < nch) {
      lds_wait(L_MISC + 64);
      conv(rw, cw, d0 + cw, lane, pre, L_MISC + 48 + cw * 4);
      STAMP(6);
      lds_set(L_MISC + 100 + cw * 4);
      wait_vm<0>();
      if (lane == 0) put_flag(rw, WS_F2 + (d0 + cw) * 8, tag_of());
    }
    STAMP(7);
    BAR(11);
  } else {
    const v2u tag = epoch_tag(rw);
    const int tb = 120 * (xcd & 1) + 30 * (xcd >> 1) + j;
    LnOut lo;
    if (normal) ln8(rx, tb, lane, L_LN, lo);
    if (lane == 0) *(LAS v4u*)lds(L_MISC) = v4u{tag.x, tag.y, 0, 0};
    wait_lg();
    STAMP(1);
    BAR(0);
    if (normal) {
      colsum(rw, WS_CSP + (ch * 10 + ti) * 512, 2, lane); colsum(rw, WS_CSP + (ch * 10 + ti) * 512, 3, lane);
      if (LNB == 0) ln8_store(rw, tb, lane, lo);
    }
    BAR(1);
    if (LNB == 1 && normal) ln8_store(rw, tb, lane, lo);
    BAR(2);
    if (LNB == 1) BAR(3);
    wait_vm<0>();
    if (lane == 0) put_flag(rw, WS_F0 + b * 8, tag);
    if (LNB == 0) BAR(3);
    BAR(4); BAR(5);
    if (!normal) {
#pragma unroll 1
      for (int s = 0; s < 10; ++s) bar();
    }
    Poll4 p0;
    poll4_issue(rw, WS_F0, 256, lane, p0);
    BAR(6);
    poll4_finish(rw, WS_F0, 256, tag, lane, p0);
    if (!normal) poll<4>(rw, WS_F3, 240, tag, lane);
    STAMP(2);
    if (b == 0 && lane == 0) put_flag(rw, WS_EPOCH, tag);
    const int row0 = normal ? 192 * ti : 1920;
    v2u st[3], cp[2][10];
#pragma unroll
    for (int k = 0; k < 3; ++k) { const int r = row0 + lane + 64 * k; st[k] = __builtin_amdgcn_raw_buffer_load_b64(rw, (r >> 3) * 128 + (r & 7) * 8, WS_STATS, SC1); }
#pragma unroll
    for (int k = 0; k < 10; ++k) cp[0][k] = __builtin_amdgcn_raw_buffer_load_b64(rw, lane * 8 + k * 512, WS_CSP + ch * 5120, SC1);
    if (!normal) {
#pragma unroll
      for (int k = 0; k < 10; ++k) cp[1][k] = __builtin_amdgcn_raw_buffer_load_b64(rw, lane * 8 + k * 512, WS_CSP + ch2 * 5120, SC1);
    }
    cfence();
    BAR(7); BAR(8);
    f32x4 so[3];
#pragma unroll
    for (int k = 0; k < 3; ++k) {
      const float m = __uint_as_float(st[k].x), den = __uint_as_float(st[k].y);
      so[k] = f32x4{-m, den * den, -1.44269504f * __builtin_amdgcn_rcpf(den), 0.f};
    }
    f32x2 co[2];
#pragma unroll
    for (int h = 0; h < 2; ++h) {
      float c0 = 0.f, c1 = 0.f;
      if (h == 0 || !normal) {
#pragma unroll
        for (int k = 0; k < 10; ++k) { c0 += __uint_as_float(cp[h][k].x); asm volatile("" : "+v"(c0)); c1 += __uint_as_float(cp[h][k].y); }
      }
      co[h] = f32x2{c0, c1};
    }
    asm volatile("" : "+v"(so[0]), "+v"(so[1]), "+v"(so[2]), "+v"(co[0]), "+v"(co[1]));
    v4u ur[16];
    auto ldu = [&](int p) { ur[p] = __builtin_amdgcn_raw_buffer_load_b128(rw, (lane >> 2) * 10240 + (lane & 3) * 16, WS_U8 + p * 163840 + d0 * 16, SC1); };
    if ((lane & 3) < nch) {
#pragma unroll
      for (int p = 0; p < 10; ++p) ldu(p);
    }
    cfence();
    BAR(9);
    __builtin_amdgcn_s_sleep(3);
#pragma unroll
    for (int k = 0; k < 3; ++k) *(LAS f32x4*)lds(L_ST + (lane + 64 * k) * 16) = so[k];
#pragma unroll
    for (int h = 0; h < 2; ++h) *(LAS f32x2*)lds(L_CS + h * 512 + lane * 8) = co[h];
    wait_lg();
    lds_set(L_MISC + 96);
    if ((lane & 3) < nch) {
#pragma unroll
      for (int p = 10; p < 16; ++p) ldu(p);
    }
    if (lane < 32) {
#pragma unroll
      for (int k = 0; k < 3; ++k) *(LAS v4u*)lds(L_U + k * U_SZ + lane * 16) = v4u{0, 0, 0, 0};
    }
    if ((lane & 3) < nch) {
#pragma unroll
      for (int p = 0; p < 10; ++p) *(LAS v4u*)lds(L_U + (lane & 3) * U_SZ + 512 + p * 256 + (lane >> 2) * 16) = ur[p];
    }
    if ((lane & 3) < nch) {
#pragma unroll
      for (int p = 10; p < 16; ++p) *(LAS v4u*)lds(L_U + (lane & 3) * U_SZ + 512 + p * 256 + (lane >> 2) * 16) = ur[p];
    }
    wait_lg();
    lds_set(L_MISC + 64);
    STAMP(4);
    lds_wait4(L_MISC + 80);
    STAMP(8);
    poll<2>(rw, WS_F1 + (t64 < 30 ? (t64 / 3) * 768 : 7680), t64 < 30 ? 96 : 64, tag, lane);
    lds_set(L_MISC + 32);
    STAMP(5); RSTAMP(34);
    lds_wait(L_MISC + 36);
    poll<2>(rw, WS_F2 + dj * 640, 80, tag, lane);
#pragma unroll 1
    for (int k = 0; k < nch; ++k) lds_wait(L_MISC + 100 + k * 4);
    STAMP(6);
#pragma unroll
    for (int p = 0; p < 10; ++p) dma<SC1>(sw, L_AT + p * 1024, (lane >> 3) * 4096 + (lane & 7) * 16, WS_AT + (80 * dj + p * 8) * 4096 + t64 * 128);
    wait_vm<0>();
    STAMP(7);
    BAR(11);
  }
  DUMP();
#endif
}

extern "C" void kernel_launch(void* const* d_in, const int* in_sizes, int n_in, void* d_out, int out_size, void* d_ws, size_t ws_size,
                              hipStream_t stream) {
  (void)hipFuncSetAttribute((const void*)fused_s4_ffn, hipFuncAttributeMaxDynamicSharedMemorySize, LDS_BYTES);
  fused_s4_ffn<<<256, 512, LDS_BYTES, stream>>>(
      (const uint16_t*)d_in[0], (const uint16_t*)d_in[1], (const uint16_t*)d_in[2], (const uint16_t*)d_in[3], (const uint16_t*)d_in[4],
      (const uint16_t*)d_in[5], (const uint16_t*)d_in[6], (const uint16_t*)d_in[7], (const uint16_t*)d_in[8], (const uint16_t*)d_in[10],
      (const uint16_t*)d_in[11], (const uint16_t*)d_in[12], (uint16_t*)d_out, d_ws);
}
```

```cpp
#include <hip/hip_runtime.h>
#include <stdint.h>

typedef short bf16x8 __attribute__((ext_vector_type(8)));
typedef short s16x4 __attribute__((ext_vector_type(4)));
typedef float f32x4 __attribute__((ext_vector_type(4)));
typedef float f32x2 __attribute__((ext_vector_type(2)));
typedef __bf16 bf16x2 __attribute__((ext_vector_type(2)));
typedef unsigned v4u __attribute__((ext_vector_type(4)));
typedef unsigned v2u __attribute__((ext_vector_type(2)));
typedef unsigned v8u __attribute__((ext_vector_type(8)));
typedef unsigned v16u __attribute__((ext_vector_type(16)));
typedef float f32x8 __attribute__((ext_vector_type(8)));
typedef float f32x16 __attribute__((ext_vector_type(16)));
typedef unsigned long long u64;
#define LAS __attribute__((address_space(3)))
#define DI __device__ __forceinline__
typedef __amdgpu_buffer_rsrc_t rsrc_t;

constexpr int L = 2048, D = 640, NS = 192, F = 1536;
constexpr int SC1 = 16, NT_ = 2;

constexpr unsigned WS_EPOCH = 0, WS_F0 = 1024, WS_F3 = 4096, WS_F2 = 8192, WS_STATS = 16384, WS_F1 = 49152, WS_CSP = 65536, WS_U8 = 262144,
                   WS_WDT = 3u << 20, WS_H = 5u << 20, WS_AT = 12u << 20, WS_TM = 15u << 20, WS_BYTES = 16u << 20;

constexpr unsigned STAGE = 40960, XB = 24576;
constexpr unsigned LX = 3 * STAGE;
constexpr unsigned L_LN = LX, LN_RS = 1296, L_AT = LX;
constexpr unsigned L_RAW = LX + 10240;
constexpr unsigned L_ST = L_RAW + 3 * 2048;
constexpr unsigned L_CS = L_ST + 3072;
constexpr unsigned L_MISC = L_CS + 1024;
constexpr unsigned L_U = L_MISC + 128, U_SZ = 4608;
constexpr unsigned L_K3 = L_U + 3 * U_SZ;
constexpr unsigned L_END = L_K3 + 960;
constexpr unsigned L_WDU = L_U, L_DUMMY = L_U + 8192;
constexpr unsigned L_KG = STAGE, KG_SZ = 16 * (544 + 288);
constexpr unsigned L_H = 2 * STAGE, H_CS = 2112, H_SZ = 8 * H_CS;
constexpr unsigned LDS_BYTES = 163840;
static_assert(L_END <= LDS_BYTES && L_H + 3 * H_SZ + 16 <= L_RAW, "lds");
#define G2AUXW SC1
#define G2AUXH SC1
#define ROT2 0
#define G2SIG 3
#define XROT 0
#define TA 20
#define TB 20
#define LNB 0
#define PDELAY 3
#define CPRIO 2
#define PWAIT 6
#define NEARLY 8
#define NSTG 4
#define GPRIO 0
#define STGB 0
#define PACE 0
#define TRIG 1
#define NU1 0
#define HGAP 2
#define WPS 4
constexpr int SPIN_MAX = 20000;
constexpr unsigned CODE_BYTES = 55296;

constexpr int G2_NW = 5 * (7 - NSTG), G2_W0 = 1;
constexpr int g2_piece(int s, int t) { const int p = WPS * (s - G2_W0) + t; return s >= G2_W0 && t < WPS && p < G2_NW ? p : -1; }
constexpr int g2_wait(int step) {
  int n = 0, need = 0;
  for (int i = 0; i < 12; ++i) { ++n; if (i / 4 == step) need = n; }
  for (int s = 0; s < step; ++s)
    for (int t = 0; t < 4; ++t) {
      if (s + 3 < 12) { ++n; if (s + 3 == step) need = n; }
      const int p = g2_piece(s, t);
      if (p >= 0) { ++n; if (5 + p / 5 == step) need = n; }
    }
  return n - need;
}
static_assert(NSTG == 4 && G2_W0 + (G2_NW - 1) / WPS <= 5, "streamed weights must be issued two steps ahead, into slots already drained");

#if __HIP_DEVICE_COMPILE__
extern __shared__ __attribute__((aligned(16))) char smem_[];
DI LAS char* lds(unsigned o) { return (LAS char*)smem_ + o; }
DI rsrc_t mkr(const void* p, unsigned bytes) { return __builtin_amdgcn_make_buffer_rsrc((void*)p, 0, bytes, 0x00020000); }
DI unsigned pk(float a, float b) { f32x2 f = {a, b}; return __builtin_bit_cast(unsigned, __builtin_convertvector(f, bf16x2)); }
DI unsigned pk2(f32x2 f) { return __builtin_bit_cast(unsigned, __builtin_convertvector(f, bf16x2)); }
DI float blo(unsigned v) { return __uint_as_float(v << 16); }
DI float bhi(unsigned v) { return __uint_as_float(v & 0xffff0000u); }
DI float rbf(float f) { return blo(pk(f, f)); }
DI void bar() { __builtin_amdgcn_s_barrier(); }
DI void cfence() { asm volatile("" ::: "memory"); }
template <int N> DI void wait_vm() { asm volatile("s_waitcnt vmcnt(%0)" ::"i"(N) : "memory"); }
DI void wait_lg() { asm volatile("s_waitcnt lgkmcnt(0)" ::: "memory"); }
DI bf16x8 trd(unsigned a0, unsigned a1) {
  s16x4 lo = __builtin_amdgcn_ds_read_tr16_b64_v4i16((LAS s16x4*)lds(a0));
  s16x4 hi = __builtin_amdgcn_ds_read_tr16_b64_v4i16((LAS s16x4*)lds(a1));
  return __builtin_shufflevector(lo, hi, 0, 1, 2, 3, 4, 5, 6, 7);
}
DI bf16x8 rd128(unsigned a) { return *(LAS bf16x8*)lds(a); }
DI f32x4 mfma(bf16x8 a, bf16x8 b, f32x4 c) { return __builtin_amdgcn_mfma_f32_16x16x32_bf16(a, b, c, 0, 0, 0); }
DI v4u srd(const void* p, unsigned bytes) { const u64 a = (u64)p; return v4u{(unsigned)a, (unsigned)(a >> 32) & 0xffffu, bytes, 0x00020000u}; }
template <int AUX> DI void dma(v4u r, unsigned ldsoff, unsigned voff, unsigned soff) {
  if (AUX == 0) asm volatile("s_mov_b32 m0, %0\n\ts_nop 0\n\tbuffer_load_dwordx4 %1, %2, %3 offen lds" ::"s"(ldsoff), "v"(voff), "s"(r), "s"(soff) : "memory");
  if (AUX == NT_) asm volatile("s_mov_b32 m0, %0\n\ts_nop 0\n\tbuffer_load_dwordx4 %1, %2, %3 offen nt lds" ::"s"(ldsoff), "v"(voff), "s"(r), "s"(soff) : "memory");
  if (AUX == SC1) asm volatile("s_mov_b32 m0, %0\n\ts_nop 0\n\tbuffer_load_dwordx4 %1, %2, %3 offen sc1 lds" ::"s"(ldsoff), "v"(voff), "s"(r), "s"(soff) : "memory");
}
#define DMA4_(POL) asm volatile("s_mov_b32 m0, %0\n\ts_nop 0\n\tbuffer_load_dwordx4 %1, %5, %6 offen " POL "lds\n\t" \
    "buffer_load_dwordx4 %2, %5, %6 offen offset:1024 " POL "lds\n\tbuffer_load_dwordx4 %3, %5, %6 offen offset:2048 " POL "lds\n\t" \
    "buffer_load_dwordx4 %4, %5, %6 offen offset:3072 " POL "lds" ::"s"(ldsoff), "v"(v0), "v"(v1), "v"(v2), "v"(v3), "s"(r), "s"(soff) : "memory")
template <int AUX> DI void dma4(v4u r, unsigned ldsoff, unsigned v0, unsigned v1, unsigned v2, unsigned v3, unsigned soff) {
  if (AUX == 0) DMA4_("");
  if (AUX == NT_) DMA4_("nt ");
  if (AUX == SC1) DMA4_("sc1 ");
}
template <int AUX> DI void st128(v4u v, rsrc_t r, unsigned off) { __builtin_amdgcn_raw_buffer_store_b128(v, r, off, 0, AUX); }
DI void touch(v4u& sink, v4u r, unsigned vo, unsigned so) {
  asm volatile("buffer_load_dwordx4 %0, %1, %2, %3 offen sc1" : "+v"(sink) : "v"(vo), "s"(r), "s"(so) : "memory");
}
DI void touch4(v4u& sink, v4u r, unsigned vo, unsigned so) {
  asm volatile("buffer_load_dwordx4 %0, %1, %2, %3 offen sc1\n\tbuffer_load_dwordx4 %0, %1, %2, %3 offen offset:1024 sc1\n\t"
               "buffer_load_dwordx4 %0, %1, %2, %3 offen offset:2048 sc1\n\tbuffer_load_dwordx4 %0, %1, %2, %3 offen offset:3072 sc1"
               : "+v"(sink) : "v"(vo), "s"(r), "s"(so) : "memory");
}
DI void dma5(v4u r, unsigned ldsoff, unsigned vo, unsigned so) { dma4<SC1>(r, ldsoff, vo, vo, vo, vo, so); dma<SC1>(r, ldsoff + 4096, vo, so + 4096); }
DI float rowsum16(float v) {
  v += __builtin_bit_cast(float, __builtin_amdgcn_update_dpp(0, __builtin_bit_cast(int, v), 0x128, 0xf, 0xf, false));
  v += __builtin_bit_cast(float, __builtin_amdgcn_update_dpp(0, __builtin_bit_cast(int, v), 0x124, 0xf, 0xf, false));
  v += __builtin_bit_cast(float, __builtin_amdgcn_update_dpp(0, __builtin_bit_cast(int, v), 0x122, 0xf, 0xf, false));
  v += __builtin_bit_cast(float, __builtin_amdgcn_update_dpp(0, __builtin_bit_cast(int, v), 0x121, 0xf, 0xf, false));
  return v;
}
DI f32x2 cmul(f32x2 a, f32x2 b) {
  f32x2 t = a * b.xx, r;
  asm("v_pk_fma_f32 %0, %1, %2, %3 op_sel:[1,1,0] op_sel_hi:[0,1,1] neg_lo:[0,1,0]" : "=v"(r) : "v"(a), "v"(b), "v"(t));
  return r;
}
DI f32x2 cpow(float lr, float tr, float e) {
  float m = __builtin_amdgcn_exp2f(lr * e), th = __builtin_amdgcn_fractf(tr * e);
  return f32x2{m * __builtin_amdgcn_cosf(th), m * __builtin_amdgcn_sinf(th)};
}
DI v2u tag_of(void) { return *(LAS v2u*)lds(L_MISC); }
DI v2u epoch_tag(rsrc_t rw) {
  const v2u e = __builtin_amdgcn_raw_buffer_load_b64(rw, WS_EPOCH, 0, SC1);
  const u64 t = (((u64)e.y << 32) | e.x) + 0x9E3779B97F4A7C15ull;
  return v2u{(unsigned)t, (unsigned)(t >> 32)};
}
DI void put_flag(rsrc_t rw, unsigned off, v2u tag) { __builtin_amdgcn_raw_buffer_store_b64(tag, rw, off, 0, SC1); }
template <int R> DI void poll(rsrc_t rw, unsigned off, int n, v2u tag, int lane) {
#pragma unroll 1
  for (int spin = 0; spin < SPIN_MAX; ++spin) {
    bool ok = true;
#pragma unroll
    for (int r = 0; r < R; ++r) {
      int i = lane + 64 * r; i = i < n ? i : n - 1;
      v2u f = __builtin_amdgcn_raw_buffer_load_b64(rw, off + i * 8, 0, SC1);
      ok = ok && f.x == tag.x && f.y == tag.y;
    }
    if (__all(ok)) break;
    cfence();
  }
  cfence();
}
struct Poll4 { v2u f[4]; };
DI void poll4_issue(rsrc_t rw, unsigned off, int n, int lane, Poll4& p) {
#pragma unroll
  for (int r = 0; r < 4; ++r) { int i = lane + 64 * r; i = i < n ? i : n - 1; p.f[r] = __builtin_amdgcn_raw_buffer_load_b64(rw, off + i * 8, 0, SC1); }
  cfence();
}
DI void poll4_finish(rsrc_t rw, unsigned off, int n, v2u tag, int lane, const Poll4& p) {
  bool ok = true;
#pragma unroll
  for (int r = 0; r < 4; ++r) ok = ok && p.f[r].x == tag.x && p.f[r].y == tag.y;
  if (!__all(ok)) poll<4>(rw, off, n, tag, lane);
  cfence();
}
DI void lds_set(unsigned o) { cfence(); *(volatile LAS unsigned*)lds(o) = 1; }
DI void lds_wait(unsigned o) {
#pragma unroll 1
  for (int spin = 0; spin < 16 * SPIN_MAX && *(volatile LAS unsigned*)lds(o) == 0; ++spin) __builtin_amdgcn_s_sleep(1);
  cfence();
}
DI void lds_wait4(unsigned o) {
#pragma unroll 1
  for (int spin = 0; spin < 16 * SPIN_MAX; ++spin) {
    const v4u f = *(volatile LAS v4u*)lds(o);
    if (f[0] & f[1] & f[2] & f[3]) break;
    __builtin_amdgcn_s_sleep(1);
  }
  cfence();
}

#define RSTAMP(i)
#define STAMP(i)
#define BAR(i) bar()
#define DUMP()

#define G1_STAMP ""
#define G1_STAMP_CLOB
#define G1_MAIN_ASM \
  "v_mov_b32 v240, %[a0]\n\t" \
  "v_mov_b32 v241, %[a1]\n\t" \
  "v_mov_b32 v242, %[a2]\n\t" \
  "v_mov_b32 v243, %[a3]\n\t" \
  "v_add_u32 v244, 81920, %[a0]\n\t" \
  "v_add_u32 v245, 81920, %[a1]\n\t" \
  "v_add_u32 v246, 81920, %[a2]\n\t" \
  "v_add_u32 v247, 81920, %[a3]\n\t" \
  "v_add_u32 v248, 40960, %[a0]\n\t" \
  "v_add_u32 v249, 40960, %[a1]\n\t" \
  "v_add_u32 v250, 40960, %[a2]\n\t" \
  "v_add_u32 v251, 40960, %[a3]\n\t" \
  "s_mov_b32 %[cnt], 3\n\t" \
  "s_barrier\n\t" \
  G1_STAMP \
  "ds_read_b64_tr_b16 v[160:161], v240 offset:0\n\t" \
  "ds_read_b64_tr_b16 v[162:163], v240 offset:512\n\t" \
  "ds_read_b64_tr_b16 v[164:165], v241 offset:0\n\t" \
  "ds_read_b64_tr_b16 v[166:167], v241 offset:512\n\t" \
  "ds_read_b64_tr_b16 v[168:169], v240 offset:8192\n\t" \
  "ds_read_b64_tr_b16 v[170:171], v240 offset:8704\n\t" \
  "ds_read_b64_tr_b16 v[172:173], v241 offset:8192\n\t" \
  "ds_read_b64_tr_b16 v[174:175], v241 offset:8704\n\t" \
  "ds_read_b128 v[176:179], v242 offset:0\n\t" \
  "ds_read_b128 v[180:183], v242 offset:2048\n\t" \
  "ds_read_b128 v[184:187], v242 offset:4096\n\t" \
  "ds_read_b128 v[188:191], v242 offset:6144\n\t" \
  "ds_read_b128 v[192:195], v242 offset:8192\n\t" \
  "ds_read_b128 v[196:199], v242 offset:10240\n\t" \
  "s_waitcnt lgkmcnt(0)\n\t" \
  ".Lg1_%=:\n\t" \
  "v_mfma_f32_16x16x32_bf16 v[64:67], v[160:163], v[176:179], v[64:67]\n\t" \
  "ds_read_b64_tr_b16 v[200:201], v240 offset:4096\n\t" \
  "v_mfma_f32_16x16x32_bf16 v[68:71], v[164:167], v[176:179], v[68:71]\n\t" \
  "ds_read_b64_tr_b16 v[202:203], v240 offset:4608\n\t" \
  "v_mfma_f32_16x16x32_bf16 v[72:75], v[168:171], v[176:179], v[72:75]\n\t" \
  "ds_read_b64_tr_b16 v[204:205], v241 offset:4096\n\t" \
  "v_mfma_f32_16x16x32_bf16 v[76:79], v[172:175], v[176:179], v[76:79]\n\t" \
  "ds_read_b64_tr_b16 v[206:207], v241 offset:4608\n\t" \
  "v_mfma_f32_16x16x32_bf16 v[80:83], v[160:163], v[180:183], v[80:83]\n\t" \
  "ds_read_b64_tr_b16 v[208:209], v240 offset:12288\n\t" \
  "v_mfma_f32_16x16x32_bf16 v[84:87], v[164:167], v[180:183], v[84:87]\n\t" \
  "ds_read_b64_tr_b16 v[210:211], v240 offset:12800\n\t" \
  "v_mfma_f32_16x16x32_bf16 v[88:91], v[168:171], v[180:183], v[88:91]\n\t" \
  "ds_read_b64_tr_b16 v[212:213], v241 offset:12288\n\t" \
  "v_mfma_f32_16x16x32_bf16 v[92:95], v[172:175], v[180:183], v[92:95]\n\t" \
  "ds_read_b64_tr_b16 v[214:215], v241 offset:12800\n\t" \
  "v_mfma_f32_16x16x32_bf16 v[96:99], v[160:163], v[184:187], v[96:99]\n\t" \
  "ds_read_b128 v[216:219], v243 offset:0\n\t" \
  "v_mfma_f32_16x16x32_bf16 v[100:103], v[164:167], v[184:187], v[100:103]\n\t" \
  "ds_read_b128 v[220:223], v243 offset:2048\n\t" \
  "v_mfma_f32_16x16x32_bf16 v[104:107], v[168:171], v[184:187], v[104:107]\n\t" \
  "ds_read_b128 v[224:227], v243 offset:4096\n\t" \
  "v_mfma_f32_16x16x32_bf16 v[108:111], v[172:175], v[184:187], v[108:111]\n\t" \
  "ds_read_b128 v[228:231], v243 offset:6144\n\t" \
  "v_mfma_f32_16x16x32_bf16 v[112:115], v[160:163], v[188:191], v[112:115]\n\t" \
  "ds_read_b128 v[232:235], v243 offset:8192\n\t" \
  "v_mfma_f32_16x16x32_bf16 v[116:119], v[164:167], v[188:191], v[116:119]\n\t" \
  "ds_read_b128 v[236:239], v243 offset:10240\n\t" \
  "v_mfma_f32_16x16x32_bf16 v[120:123], v[168:171], v[188:191], v[120:123]\n\t" \
  "v_mfma_f32_16x16x32_bf16 v[124:127], v[172:175], v[188:191], v[124:127]\n\t" \
  "v_mfma_f32_16x16x32_bf16 v[128:131], v[160:163], v[192:195], v[128:131]\n\t" \
  "v_mfma_f32_16x16x32_bf16 v[132:135], v[164:167], v[192:195], v[132:135]\n\t" \
  "v_mfma_f32_16x16x32_bf16 v[136:139], v[168:171], v[192:195], v[136:139]\n\t" \
  "v_mfma_f32_16x16x32_bf16 v[140:143], v[172:175], v[192:195], v[140:143]\n\t" \
  "v_mfma_f32_16x16x32_bf16 v[144:147], v[160:163], v[196:199], v[144:147]\n\t" \
  "v_mfma_f32_16x16x32_bf16 v[148:151], v[164:167], v[196:199], v[148:151]\n\t" \
  "v_mfma_f32_16x16x32_bf16 v[152:155], v[168:171], v[196:199], v[152:155]\n\t" \
  "v_mfma_f32_16x16x32_bf16 v[156:159], v[172:175], v[196:199], v[156:159]\n\t" \
  "s_waitcnt vmcnt(0) lgkmcnt(0)\n\t" \
  "s_barrier\n\t" \
  "v_mfma_f32_16x16x32_bf16 v[64:67], v[200:203], v[216:219], v[64:67]\n\t" \
  "ds_read_b64_tr_b16 v[160:161], v244 offset:0\n\t" \
  "v_mfma_f32_16x16x32_bf16 v[68:71], v[204:207], v[216:219], v[68:71]\n\t" \
  "ds_read_b64_tr_b16 v[162:163], v244 offset:512\n\t" \
  "v_mfma_f32_16x16x32_bf16 v[72:75], v[208:211], v[216:219], v[72:75]\n\t" \
  "ds_read_b64_tr_b16 v[164:165], v245 offset:0\n\t" \
  "v_mfma_f32_16x16x32_bf16 v[76:79], v[212:215], v[216:219], v[76:79]\n\t" \
  "ds_read_b64_tr_b16 v[166:167], v245 offset:512\n\t" \
  "v_mfma_f32_16x16x32_bf16 v[80:83], v[200:203], v[220:223], v[80:83]\n\t" \
  "ds_read_b64_tr_b16 v[168:169], v244 offset:8192\n\t" \
  "v_mfma_f32_16x16x32_bf16 v[84:87], v[204:207], v[220:223], v[84:87]\n\t" \
  "ds_read_b64_tr_b16 v[170:171], v244 offset:8704\n\t" \
  "v_mfma_f32_16x16x32_bf16 v[88:91], v[208:211], v[220:223], v[88:91]\n\t" \
  "ds_read_b64_tr_b16 v[172:173], v245 offset:8192\n\t" \
  "v_mfma_f32_16x16x32_bf16 v[92:95], v[212:215], v[220:223], v[92:95]\n\t" \
  "ds_read_b64_tr_b16 v[174:175], v245 offset:8704\n\t" \
  "v_mfma_f32_16x16x32_bf16 v[96:99], v[200:203], v[224:227], v[96:99]\n\t" \
  "ds_read_b128 v[176:179], v246 offset:0\n\t" \
  "v_mfma_f32_16x16x32_bf16 v[100:103], v[204:207], v[224:227], v[100:103]\n\t" \
  "ds_read_b128 v[180:183], v246 offset:2048\n\t" \
  "v_mfma_f32_16x16x32_bf16 v[104:107], v[208:211], v[224:227], v[104:107]\n\t" \
  "ds_read_b128 v[184:187], v246 offset:4096\n\t" \
  "v_mfma_f32_16x16x32_bf16 v[108:111], v[212:215], v[224:227], v[108:111]\n\t" \
  "ds_read_b128 v[188:191], v246 offset:6144\n\t" \
  "v_mfma_f32_16x16x32_bf16 v[112:115], v[200:203], v[228:231], v[112:115]\n\t" \
  "ds_read_b128 v[192:195], v246 offset:8192\n\t" \
  "v_mfma_f32_16x16x32_bf16 v[116:119], v[204:207], v[228:231], v[116:119]\n\t" \
  "ds_read_b128 v[196:199], v246 offset:10240\n\t" \
  "v_mfma_f32_16x16x32_bf16 v[120:123], v[208:211], v[228:231], v[120:123]\n\t" \
  "v_mfma_f32_16x16x32_bf16 v[124:127], v[212:215], v[228:231], v[124:127]\n\t" \
  "v_mfma_f32_16x16x32_bf16 v[128:131], v[200:203], v[232:235], v[128:131]\n\t" \
  "v_mfma_f32_16x16x32_bf16 v[132:135], v[204:207], v[232:235], v[132:135]\n\t" \
  "v_mfma_f32_16x16x32_bf16 v[136:139], v[208:211], v[232:235], v[136:139]\n\t" \
  "v_mfma_f32_16x16x32_bf16 v[140:143], v[212:215], v[232:235], v[140:143]\n\t" \
  "v_mfma_f32_16x16x32_bf16 v[144:147], v[200:203], v[236:239], v[144:147]\n\t" \
  "v_mfma_f32_16x16x32_bf16 v[148:151], v[204:207], v[236:239], v[148:151]\n\t" \
  "v_mfma_f32_16x16x32_bf16 v[152:155], v[208:211], v[236:239], v[152:155]\n\t" \
  "v_mfma_f32_16x16x32_bf16 v[156:159], v[212:215], v[236:239], v[156:159]\n\t" \
  "s_waitcnt lgkmcnt(0)\n\t" \
  "v_mfma_f32_16x16x32_bf16 v[64:67], v[160:163], v[176:179], v[64:67]\n\t" \
  "ds_read_b64_tr_b16 v[200:201], v244 offset:4096\n\t" \
  "v_mfma_f32_16x16x32_bf16 v[68:71], v[164:167], v[176:179], v[68:71]\n\t" \
  "ds_read_b64_tr_b16 v[202:203], v244 offset:4608\n\t" \
  "v_mfma_f32_16x16x32_bf16 v[72:75], v[168:171], v[176:179], v[72:75]\n\t" \
  "ds_read_b64_tr_b16 v[204:205], v245 offset:4096\n\t" \
  "v_mfma_f32_16x16x32_bf16 v[76:79], v[172:175], v[176:179], v[76:79]\n\t" \
  "ds_read_b64_tr_b16 v[206:207], v245 offset:4608\n\t" \
  "v_mfma_f32_16x16x32_bf16 v[80:83], v[160:163], v[180:183], v[80:83]\n\t" \
  "ds_read_b64_tr_b16 v[208:209], v244 offset:12288\n\t" \
  "v_mfma_f32_16x16x32_bf16 v[84:87], v[164:167], v[180:183], v[84:87]\n\t" \
  "ds_read_b64_tr_b16 v[210:211], v244 offset:12800\n\t" \
  "v_mfma_f32_16x16x32_bf16 v[88:91], v[168:171], v[180:183], v[88:91]\n\t" \
  "ds_read_b64_tr_b16 v[212:213], v245 offset:12288\n\t" \
  "v_mfma_f32_16x16x32_bf16 v[92:95], v[172:175], v[180:183], v[92:95]\n\t" \
  "ds_read_b64_tr_b16 v[214:215], v245 offset:12800\n\t" \
  "v_mfma_f32_16x16x32_bf16 v[96:99], v[160:163], v[184:187], v[96:99]\n\t" \
  "ds_read_b128 v[216:219], v247 offset:0\n\t" \
  "v_mfma_f32_16x16x32_bf16 v[100:103], v[164:167], v[184:187], v[100:103]\n\t" \
  "ds_read_b128 v[220:223], v247 offset:2048\n\t" \
  "v_mfma_f32_16x16x32_bf16 v[104:107], v[168:171], v[184:187], v[104:107]\n\t" \
  "ds_read_b128 v[224:227], v247 offset:4096\n\t" \
  "v_mfma_f32_16x16x32_bf16 v[108:111], v[172:175], v[184:187], v[108:111]\n\t" \
  "ds_read_b128 v[228:231], v247 offset:6144\n\t" \
  "v_mfma_f32_16x16x32_bf16 v[112:115], v[160:163], v[188:191], v[112:115]\n\t" \
  "ds_read_b128 v[232:235], v247 offset:8192\n\t" \
  "v_mfma_f32_16x16x32_bf16 v[116:119], v[164:167], v[188:191], v[116:119]\n\t" \
  "ds_read_b128 v[236:239], v247 offset:10240\n\t" \
  "v_mfma_f32_16x16x32_bf16 v[120:123], v[168:171], v[188:191], v[120:123]\n\t" \
  "v_mfma_f32_16x16x32_bf16 v[124:127], v[172:175], v[188:191], v[124:127]\n\t" \
  "v_mfma_f32_16x16x32_bf16 v[128:131], v[160:163], v[192:195], v[128:131]\n\t" \
  "v_mfma_f32_16x16x32_bf16 v[132:135], v[164:167], v[192:195], v[132:135]\n\t" \
  "v_mfma_f32_16x16x32_bf16 v[136:139], v[168:171], v[192:195], v[136:139]\n\t" \
  "v_mfma_f32_16x16x32_bf16 v[140:143], v[172:175], v[192:195], v[140:143]\n\t" \
  "v_mfma_f32_16x16x32_bf16 v[144:147], v[160:163], v[196:199], v[144:147]\n\t" \
  "v_mfma_f32_16x16x32_bf16 v[148:151], v[164:167], v[196:199], v[148:151]\n\t" \
  "v_mfma_f32_16x16x32_bf16 v[152:155], v[168:171], v[196:199], v[152:155]\n\t" \
  "v_mfma_f32_16x16x32_bf16 v[156:159], v[172:175], v[196:199], v[156:159]\n\t" \
  "s_waitcnt vmcnt(0) lgkmcnt(0)\n\t" \
  "s_barrier\n\t" \
  "v_mfma_f32_16x16x32_bf16 v[64:67], v[200:203], v[216:219], v[64:67]\n\t" \
  "ds_read_b64_tr_b16 v[160:161], v248 offset:0\n\t" \
  "v_mfma_f32_16x16x32_bf16 v[68:71], v[204:207], v[216:219], v[68:71]\n\t" \
  "ds_read_b64_tr_b16 v[162:163], v248 offset:512\n\t" \
  "v_mfma_f32_16x16x32_bf16 v[72:75], v[208:211], v[216:219], v[72:75]\n\t" \
  "ds_read_b64_tr_b16 v[164:165], v249 offset:0\n\t" \
  "v_mfma_f32_16x16x32_bf16 v[76:79], v[212:215], v[216:219], v[76:79]\n\t" \
  "ds_read_b64_tr_b16 v[166:167], v249 offset:512\n\t" \
  "v_mfma_f32_16x16x32_bf16 v[80:83], v[200:203], v[220:223], v[80:83]\n\t" \
  "ds_read_b64_tr_b16 v[168:169], v248 offset:8192\n\t" \
  "v_mfma_f32_16x16x32_bf16 v[84:87], v[204:207], v[220:223], v[84:87]\n\t" \
  "ds_read_b64_tr_b16 v[170:171], v248 offset:8704\n\t" \
  "v_mfma_f32_16x16x32_bf16 v[88:91], v[208:211], v[220:223], v[88:91]\n\t" \
  "ds_read_b64_tr_b16 v[172:173], v249 offset:8192\n\t" \
  "v_mfma_f32_16x16x32_bf16 v[92:95], v[212:215], v[220:223], v[92:95]\n\t" \
  "ds_read_b64_tr_b16 v[174:175], v249 offset:8704\n\t" \
  "v_mfma_f32_16x16x32_bf16 v[96:99], v[200:203], v[224:227], v[96:99]\n\t" \
  "ds_read_b128 v[176:179], v250 offset:0\n\t" \
  "v_mfma_f32_16x16x32_bf16 v[100:103], v[204:207], v[224:227], v[100:103]\n\t" \
  "ds_read_b128 v[180:183], v250 offset:2048\n\t" \
  "v_mfma_f32_16x16x32_bf16 v[104:107], v[208:211], v[224:227], v[104:107]\n\t" \
  "ds_read_b128 v[184:187], v250 offset:4096\n\t" \
  "v_mfma_f32_16x16x32_bf16 v[108:111], v[212:215], v[224:227], v[108:111]\n\t" \
  "ds_read_b128 v[188:191], v250 offset:6144\n\t" \
  "v_mfma_f32_16x16x32_bf16 v[112:115], v[200:203], v[228:231], v[112:115]\n\t" \
  "ds_read_b128 v[192:195], v250 offset:8192\n\t" \
  "v_mfma_f32_16x16x32_bf16 v[116:119], v[204:207], v[228:231], v[116:119]\n\t" \
  "ds_read_b128 v[196:199], v250 offset:10240\n\t" \
  "v_mfma_f32_16x16x32_bf16 v[120:123], v[208:211], v[228:231], v[120:123]\n\t" \
  "v_mfma_f32_16x16x32_bf16 v[124:127], v[212:215], v[228:231], v[124:127]\n\t" \
  "v_mfma_f32_16x16x32_bf16 v[128:131], v[200:203], v[232:235], v[128:131]\n\t" \
  "v_mfma_f32_16x16x32_bf16 v[132:135], v[204:207], v[232:235], v[132:135]\n\t" \
  "v_mfma_f32_16x16x32_bf16 v[136:139], v[208:211], v[232:235], v[136:139]\n\t" \
  "v_mfma_f32_16x16x32_bf16 v[140:143], v[212:215], v[232:235], v[140:143]\n\t" \
  "v_mfma_f32_16x16x32_bf16 v[144:147], v[200:203], v[236:239], v[144:147]\n\t" \
  "v_mfma_f32_16x16x32_bf16 v[148:151], v[204:207], v[236:239], v[148:151]\n\t" \
  "v_mfma_f32_16x16x32_bf16 v[152:155], v[208:211], v[236:239], v[152:155]\n\t" \
  "v_mfma_f32_16x16x32_bf16 v[156:159], v[212:215], v[236:239], v[156:159]\n\t" \
  "s_waitcnt lgkmcnt(0)\n\t" \
  "v_mfma_f32_16x16x32_bf16 v[64:67], v[160:163], v[176:179], v[64:67]\n\t" \
  "ds_read_b64_tr_b16 v[200:201], v248 offset:4096\n\t" \
  "v_mfma_f32_16x16x32_bf16 v[68:71], v[164:167], v[176:179], v[68:71]\n\t" \
  "ds_read_b64_tr_b16 v[202:203], v248 offset:4608\n\t" \
  "v_mfma_f32_16x16x32_bf16 v[72:75], v[168:171], v[176:179], v[72:75]\n\t" \
  "ds_read_b64_tr_b16 v[204:205], v249 offset:4096\n\t" \
  "v_mfma_f32_16x16x32_bf16 v[76:79], v[172:175], v[176:179], v[76:79]\n\t" \
  "ds_read_b64_tr_b16 v[206:207], v249 offset:4608\n\t" \
  "v_mfma_f32_16x16x32_bf16 v[80:83], v[160:163], v[180:183], v[80:83]\n\t" \
  "ds_read_b64_tr_b16 v[208:209], v248 offset:12288\n\t" \
  "v_mfma_f32_16x16x32_bf16 v[84:87], v[164:167], v[180:183], v[84:87]\n\t" \
  "ds_read_b64_tr_b16 v[210:211], v248 offset:12800\n\t" \
  "v_mfma_f32_16x16x32_bf16 v[88:91], v[168:171], v[180:183], v[88:91]\n\t" \
  "ds_read_b64_tr_b16 v[212:213], v249 offset:12288\n\t" \
  "v_mfma_f32_16x16x32_bf16 v[92:95], v[172:175], v[180:183], v[92:95]\n\t" \
  "ds_read_b64_tr_b16 v[214:215], v249 offset:12800\n\t" \
  "v_mfma_f32_16x16x32_bf16 v[96:99], v[160:163], v[184:187], v[96:99]\n\t" \
  "ds_read_b128 v[216:219], v251 offset:0\n\t" \
  "v_mfma_f32_16x16x32_bf16 v[100:103], v[164:167], v[184:187], v[100:103]\n\t" \
  "ds_read_b128 v[220:223], v251 offset:2048\n\t" \
  "v_mfma_f32_16x16x32_bf16 v[104:107], v[168:171], v[184:187], v[104:107]\n\t" \
  "ds_read_b128 v[224:227], v251 offset:4096\n\t" \
  "v_mfma_f32_16x16x32_bf16 v[108:111], v[172:175], v[184:187], v[108:111]\n\t" \
  "ds_read_b128 v[228:231], v251 offset:6144\n\t" \
  "v_mfma_f32_16x16x32_bf16 v[112:115], v[160:163], v[188:191], v[112:115]\n\t" \
  "ds_read_b128 v[232:235], v251 offset:8192\n\t" \
  "v_mfma_f32_16x16x32_bf16 v[116:119], v[164:167], v[188:191], v[116:119]\n\t" \
  "ds_read_b128 v[236:239], v251 offset:10240\n\t" \
  "v_mfma_f32_16x16x32_bf16 v[120:123], v[168:171], v[188:191], v[120:123]\n\t" \
  "v_mfma_f32_16x16x32_bf16 v[124:127], v[172:175], v[188:191], v[124:127]\n\t" \
  "v_mfma_f32_16x16x32_bf16 v[128:131], v[160:163], v[192:195], v[128:131]\n\t" \
  "v_mfma_f32_16x16x32_bf16 v[132:135], v[164:167], v[192:195], v[132:135]\n\t" \
  "v_mfma_f32_16x16x32_bf16 v[136:139], v[168:171], v[192:195], v[136:139]\n\t" \
  "v_mfma_f32_16x16x32_bf16 v[140:143], v[172:175], v[192:195], v[140:143]\n\t" \
  "v_mfma_f32_16x16x32_bf16 v[144:147], v[160:163], v[196:199], v[144:147]\n\t" \
  "v_mfma_f32_16x16x32_bf16 v[148:151], v[164:167], v[196:199], v[148:151]\n\t" \
  "v_mfma_f32_16x16x32_bf16 v[152:155], v[168:171], v[196:199], v[152:155]\n\t" \
  "v_mfma_f32_16x16x32_bf16 v[156:159], v[172:175], v[196:199], v[156:159]\n\t" \
  "s_waitcnt vmcnt(0) lgkmcnt(0)\n\t" \
  "s_barrier\n\t" \
  "v_mfma_f32_16x16x32_bf16 v[64:67], v[200:203], v[216:219], v[64:67]\n\t" \
  "ds_read_b64_tr_b16 v[160:161], v240 offset:0\n\t" \
  "v_mfma_f32_16x16x32_bf16 v[68:71], v[204:207], v[216:219], v[68:71]\n\t" \
  "ds_read_b64_tr_b16 v[162:163], v240 offset:512\n\t" \
  "v_mfma_f32_16x16x32_bf16 v[72:75], v[208:211], v[216:219], v[72:75]\n\t" \
  "ds_read_b64_tr_b16 v[164:165], v241 offset:0\n\t" \
  "v_mfma_f32_16x16x32_bf16 v[76:79], v[212:215], v[216:219], v[76:79]\n\t" \
  "ds_read_b64_tr_b16 v[166:167], v241 offset:512\n\t" \
  "v_mfma_f32_16x16x32_bf16 v[80:83], v[200:203], v[220:223], v[80:83]\n\t" \
  "ds_read_b64_tr_b16 v[168:169], v240 offset:8192\n\t" \
  "v_mfma_f32_16x16x32_bf16 v[84:87], v[204:207], v[220:223], v[84:87]\n\t" \
  "ds_read_b64_tr_b16 v[170:171], v240 offset:8704\n\t" \
  "v_mfma_f32_16x16x32_bf16 v[88:91], v[208:211], v[220:223], v[88:91]\n\t" \
  "ds_read_b64_tr_b16 v[172:173], v241 offset:8192\n\t" \
  "v_mfma_f32_16x16x32_bf16 v[92:95], v[212:215], v[220:223], v[92:95]\n\t" \
  "ds_read_b64_tr_b16 v[174:175], v241 offset:8704\n\t" \
  "v_mfma_f32_16x16x32_bf16 v[96:99], v[200:203], v[224:227], v[96:99]\n\t" \
  "ds_read_b128 v[176:179], v242 offset:0\n\t" \
  "v_mfma_f32_16x16x32_bf16 v[100:103], v[204:207], v[224:227], v[100:103]\n\t" \
  "ds_read_b128 v[180:183], v242 offset:2048\n\t" \
  "v_mfma_f32_16x16x32_bf16 v[104:107], v[208:211], v[224:227], v[104:107]\n\t" \
  "ds_read_b128 v[184:187], v242 offset:4096\n\t" \
  "v_mfma_f32_16x16x32_bf16 v[108:111], v[212:215], v[224:227], v[108:111]\n\t" \
  "ds_read_b128 v[188:191], v242 offset:6144\n\t" \
  "v_mfma_f32_16x16x32_bf16 v[112:115], v[200:203], v[228:231], v[112:115]\n\t" \
  "ds_read_b128 v[192:195], v242 offset:8192\n\t" \
  "v_mfma_f32_16x16x32_bf16 v[116:119], v[204:207], v[228:231], v[116:119]\n\t" \
  "ds_read_b128 v[196:199], v242 offset:10240\n\t" \
  "v_mfma_f32_16x16x32_bf16 v[120:123], v[208:211], v[228:231], v[120:123]\n\t" \
  "v_mfma_f32_16x16x32_bf16 v[124:127], v[212:215], v[228:231], v[124:127]\n\t" \
  "v_mfma_f32_16x16x32_bf16 v[128:131], v[200:203], v[232:235], v[128:131]\n\t" \
  "v_mfma_f32_16x16x32_bf16 v[132:135], v[204:207], v[232:235], v[132:135]\n\t" \
  "v_mfma_f32_16x16x32_bf16 v[136:139], v[208:211], v[232:235], v[136:139]\n\t" \
  "v_mfma_f32_16x16x32_bf16 v[140:143], v[212:215], v[232:235], v[140:143]\n\t" \
  "v_mfma_f32_16x16x32_bf16 v[144:147], v[200:203], v[236:239], v[144:147]\n\t" \
  "v_mfma_f32_16x16x32_bf16 v[148:151], v[204:207], v[236:239], v[148:151]\n\t" \
  "v_mfma_f32_16x16x32_bf16 v[152:155], v[208:211], v[236:239], v[152:155]\n\t" \
  "v_mfma_f32_16x16x32_bf16 v[156:159], v[212:215], v[236:239], v[156:159]\n\t" \
  "s_sub_u32 %[cnt], %[cnt], 1\n\t" \
  "s_cmp_lg_u32 %[cnt], 0\n\t" \
  "s_waitcnt lgkmcnt(0)\n\t" \
  "s_cbranch_scc1 .Lg1_%=\n\t" \
  "v_mfma_f32_16x16x32_bf16 v[64:67], v[160:163], v[176:179], v[64:67]\n\t" \
  "ds_read_b64_tr_b16 v[200:201], v240 offset:4096\n\t" \
  "v_mfma_f32_16x16x32_bf16 v[68:71], v[164:167], v[176:179], v[68:71]\n\t" \
  "ds_read_b64_tr_b16 v[202:203], v240 offset:4608\n\t" \
  "v_mfma_f32_16x16x32_bf16 v[72:75], v[168:171], v[176:179], v[72:75]\n\t" \
  "ds_read_b64_tr_b16 v[204:205], v241 offset:4096\n\t" \
  "v_mfma_f32_16x16x32_bf16 v[76:79], v[172:175], v[176:179], v[76:79]\n\t" \
  "ds_read_b64_tr_b16 v[206:207], v241 offset:4608\n\t" \
  "v_mfma_f32_16x16x32_bf16 v[80:83], v[160:163], v[180:183], v[80:83]\n\t" \
  "ds_read_b64_tr_b16 v[208:209], v240 offset:12288\n\t" \
  "v_mfma_f32_16x16x32_bf16 v[84:87], v[164:167], v[180:183], v[84:87]\n\t" \
  "ds_read_b64_tr_b16 v[210:211], v240 offset:12800\n\t" \
  "v_mfma_f32_16x16x32_bf16 v[88:91], v[168:171], v[180:183], v[88:91]\n\t" \
  "ds_read_b64_tr_b16 v[212:213], v241 offset:12288\n\t" \
  "v_mfma_f32_16x16x32_bf16 v[92:95], v[172:175], v[180:183], v[92:95]\n\t" \
  "ds_read_b64_tr_b16 v[214:215], v241 offset:12800\n\t" \
  "v_mfma_f32_16x16x32_bf16 v[96:99], v[160:163], v[184:187], v[96:99]\n\t" \
  "ds_read_b128 v[216:219], v243 offset:0\n\t" \
  "v_mfma_f32_16x16x32_bf16 v[100:103], v[164:167], v[184:187], v[100:103]\n\t" \
  "ds_read_b128 v[220:223], v243 offset:2048\n\t" \
  "v_mfma_f32_16x16x32_bf16 v[104:107], v[168:171], v[184:187], v[104:107]\n\t" \
  "ds_read_b128 v[224:227], v243 offset:4096\n\t" \
  "v_mfma_f32_16x16x32_bf16 v[108:111], v[172:175], v[184:187], v[108:111]\n\t" \
  "ds_read_b128 v[228:231], v243 offset:6144\n\t" \
  "v_mfma_f32_16x16x32_bf16 v[112:115], v[160:163], v[188:191], v[112:115]\n\t" \
  "ds_read_b128 v[232:235], v243 offset:8192\n\t" \
  "v_mfma_f32_16x16x32_bf16 v[116:119], v[164:167], v[188:191], v[116:119]\n\t" \
  "ds_read_b128 v[236:239], v243 offset:10240\n\t" \
  "v_mfma_f32_16x16x32_bf16 v[120:123], v[168:171], v[188:191], v[120:123]\n\t" \
  "v_mfma_f32_16x16x32_bf16 v[124:127], v[172:175], v[188:191], v[124:127]\n\t" \
  "v_mfma_f32_16x16x32_bf16 v[128:131], v[160:163], v[192:195], v[128:131]\n\t" \
  "v_mfma_f32_16x16x32_bf16 v[132:135], v[164:167], v[192:195], v[132:135]\n\t" \
  "v_mfma_f32_16x16x32_bf16 v[136:139], v[168:171], v[192:195], v[136:139]\n\t" \
  "v_mfma_f32_16x16x32_bf16 v[140:143], v[172:175], v[192:195], v[140:143]\n\t" \
  "v_mfma_f32_16x16x32_bf16 v[144:147], v[160:163], v[196:199], v[144:147]\n\t" \
  "v_mfma_f32_16x16x32_bf16 v[148:151], v[164:167], v[196:199], v[148:151]\n\t" \
  "v_mfma_f32_16x16x32_bf16 v[152:155], v[168:171], v[196:199], v[152:155]\n\t" \
  "v_mfma_f32_16x16x32_bf16 v[156:159], v[172:175], v[196:199], v[156:159]\n\t" \
  "s_waitcnt lgkmcnt(0)\n\t" \
  "v_mfma_f32_16x16x32_bf16 v[64:67], v[200:203], v[216:219], v[64:67]\n\t" \
  "ds_read_b128 v[160:163], %[cs] offset:0\n\t" \
  "v_mfma_f32_16x16x32_bf16 v[68:71], v[204:207], v[216:219], v[68:71]\n\t" \
  "ds_read_b128 v[164:167], %[cs] offset:64\n\t" \
  "v_mfma_f32_16x16x32_bf16 v[72:75], v[208:211], v[216:219], v[72:75]\n\t" \
  "ds_read_b128 v[168:171], %[cs] offset:256\n\t" \
  "v_mfma_f32_16x16x32_bf16 v[76:79], v[212:215], v[216:219], v[76:79]\n\t" \
  "ds_read_b128 v[172:175], %[cs] offset:320\n\t" \
  "v_mfma_f32_16x16x32_bf16 v[80:83], v[200:203], v[220:223], v[80:83]\n\t" \
  "ds_read_b128 v[176:179], %[st] offset:0\n\t" \
  "v_mfma_f32_16x16x32_bf16 v[84:87], v[204:207], v[220:223], v[84:87]\n\t" \
  "ds_read_b128 v[180:183], %[st] offset:256\n\t" \
  "v_mfma_f32_16x16x32_bf16 v[88:91], v[208:211], v[220:223], v[88:91]\n\t" \
  "ds_read_b128 v[184:187], %[st] offset:512\n\t" \
  "v_mfma_f32_16x16x32_bf16 v[92:95], v[212:215], v[220:223], v[92:95]\n\t" \
  "ds_read_b128 v[188:191], %[st] offset:768\n\t" \
  "v_mfma_f32_16x16x32_bf16 v[96:99], v[200:203], v[224:227], v[96:99]\n\t" \
  "ds_read_b128 v[192:195], %[st] offset:1024\n\t" \
  "v_mfma_f32_16x16x32_bf16 v[100:103], v[204:207], v[224:227], v[100:103]\n\t" \
  "ds_read_b128 v[196:199], %[st] offset:1280\n\t" \
  "v_mfma_f32_16x16x32_bf16 v[104:107], v[208:211], v[224:227], v[104:107]\n\t" \
  "v_mfma_f32_16x16x32_bf16 v[108:111], v[212:215], v[224:227], v[108:111]\n\t" \
  "v_mfma_f32_16x16x32_bf16 v[112:115], v[200:203], v[228:231], v[112:115]\n\t" \
  "v_mfma_f32_16x16x32_bf16 v[116:119], v[204:207], v[228:231], v[116:119]\n\t" \
  "v_mfma_f32_16x16x32_bf16 v[120:123], v[208:211], v[228:231], v[120:123]\n\t" \
  "v_mfma_f32_16x16x32_bf16 v[124:127], v[212:215], v[228:231], v[124:127]\n\t" \
  "v_mfma_f32_16x16x32_bf16 v[128:131], v[200:203], v[232:235], v[128:131]\n\t" \
  "v_mfma_f32_16x16x32_bf16 v[132:135], v[204:207], v[232:235], v[132:135]\n\t" \
  "v_mfma_f32_16x16x32_bf16 v[136:139], v[208:211], v[232:235], v[136:139]\n\t" \
  "v_mfma_f32_16x16x32_bf16 v[140:143], v[212:215], v[232:235], v[140:143]\n\t" \
  "v_mfma_f32_16x16x32_bf16 v[144:147], v[200:203], v[236:239], v[144:147]\n\t" \
  "v_mfma_f32_16x16x32_bf16 v[148:151], v[204:207], v[236:239], v[148:151]\n\t" \
  "v_mfma_f32_16x16x32_bf16 v[152:155], v[208:211], v[236:239], v[152:155]\n\t" \
  "v_mfma_f32_16x16x32_bf16 v[156:159], v[212:215], v[236:239], v[156:159]\n\t" \
  "s_nop 7\n\t" \
  "s_waitcnt lgkmcnt(0)\n\t" \
  ""
#define G1_MAIN_CLOB "v200","v201","v202","v203","v204","v205","v206","v207","v208","v209","v210","v211","v212","v213","v214","v215","v216","v217","v218","v219","v220","v221","v222","v223","v224","v225","v226","v227","v228","v229","v230","v231","v232","v233","v234","v235","v236","v237","v238","v239","v240","v241","v242","v243","v244","v245","v246","v247","v248","v249","v250","v251"
#define CONV_FIRST_ASM \
  "ds_read_b128 v[96:99], %[aa] offset:0\n\t" \
  "ds_read_b128 v[100:103], %[aa] offset:512\n\t" \
  "ds_read_b128 v[104:107], %[aa] offset:1024\n\t" \
  "ds_read_b128 v[108:111], %[aa] offset:1536\n\t" \
  "ds_read_b128 v[140:143], %[ab] offset:4032\n\t" \
  "ds_read_b128 v[136:139], %[ab] offset:3520\n\t" \
  "ds_read_b128 v[132:135], %[ab] offset:3008\n\t" \
  "ds_read_b128 v[128:131], %[ab] offset:2496\n\t" \
  "ds_read_b128 v[124:127], %[ab] offset:1984\n\t" \
  "ds_read_b128 v[120:123], %[ab] offset:1472\n\t" \
  "ds_read_b128 v[116:119], %[ab] offset:960\n\t" \
  "ds_read_b128 v[112:115], %[ab] offset:448\n\t" \
  ""
#define CONV_MAIN_ASM \
  "v_mfma_f32_16x16x32_bf16 v[92:95], v[96:99], v[140:143], v[92:95]\n\t" \
  "ds_read_b128 v[144:147], %[aa] offset:64\n\t" \
  "v_mfma_f32_16x16x32_bf16 v[88:91], v[96:99], v[136:139], v[88:91]\n\t" \
  "ds_read_b128 v[148:151], %[aa] offset:576\n\t" \
  "v_mfma_f32_16x16x32_bf16 v[84:87], v[96:99], v[132:135], v[84:87]\n\t" \
  "ds_read_b128 v[152:155], %[aa] offset:1088\n\t" \
  "v_mfma_f32_16x16x32_bf16 v[80:83], v[96:99], v[128:131], v[80:83]\n\t" \
  "ds_read_b128 v[156:159], %[aa] offset:1600\n\t" \
  "v_mfma_f32_16x16x32_bf16 v[76:79], v[96:99], v[124:127], v[76:79]\n\t" \
  "ds_read_b128 v[188:191], %[ab] offset:3968\n\t" \
  "v_mfma_f32_16x16x32_bf16 v[72:75], v[96:99], v[120:123], v[72:75]\n\t" \
  "ds_read_b128 v[184:187], %[ab] offset:3456\n\t" \
  "v_mfma_f32_16x16x32_bf16 v[68:71], v[96:99], v[116:119], v[68:71]\n\t" \
  "ds_read_b128 v[180:183], %[ab] offset:2944\n\t" \
  "v_mfma_f32_16x16x32_bf16 v[64:67], v[96:99], v[112:115], v[64:67]\n\t" \
  "ds_read_b128 v[176:179], %[ab] offset:2432\n\t" \
  "v_mfma_f32_16x16x32_bf16 v[92:95], v[100:103], v[136:139], v[92:95]\n\t" \
  "ds_read_b128 v[172:175], %[ab] offset:1920\n\t" \
  "v_mfma_f32_16x16x32_bf16 v[88:91], v[100:103], v[132:135], v[88:91]\n\t" \
  "ds_read_b128 v[168:171], %[ab] offset:1408\n\t" \
  "v_mfma_f32_16x16x32_bf16 v[84:87], v[100:103], v[128:131], v[84:87]\n\t" \
  "ds_read_b128 v[164:167], %[ab] offset:896\n\t" \
  "v_mfma_f32_16x16x32_bf16 v[80:83], v[100:103], v[124:127], v[80:83]\n\t" \
  "ds_read_b128 v[160:163], %[ab] offset:384\n\t" \
  "v_mfma_f32_16x16x32_bf16 v[76:79], v[100:103], v[120:123], v[76:79]\n\t" \
  "v_mfma_f32_16x16x32_bf16 v[72:75], v[100:103], v[116:119], v[72:75]\n\t" \
  "v_mfma_f32_16x16x32_bf16 v[68:71], v[100:103], v[112:115], v[68:71]\n\t" \
  "v_mfma_f32_16x16x32_bf16 v[92:95], v[104:107], v[132:135], v[92:95]\n\t" \
  "v_mfma_f32_16x16x32_bf16 v[88:91], v[104:107], v[128:131], v[88:91]\n\t" \
  "v_mfma_f32_16x16x32_bf16 v[84:87], v[104:107], v[124:127], v[84:87]\n\t" \
  "v_mfma_f32_16x16x32_bf16 v[80:83], v[104:107], v[120:123], v[80:83]\n\t" \
  "v_mfma_f32_16x16x32_bf16 v[76:79], v[104:107], v[116:119], v[76:79]\n\t" \
  "v_mfma_f32_16x16x32_bf16 v[72:75], v[104:107], v[112:115], v[72:75]\n\t" \
  "v_mfma_f32_16x16x32_bf16 v[92:95], v[108:111], v[128:131], v[92:95]\n\t" \
  "v_mfma_f32_16x16x32_bf16 v[88:91], v[108:111], v[124:127], v[88:91]\n\t" \
  "v_mfma_f32_16x16x32_bf16 v[84:87], v[108:111], v[120:123], v[84:87]\n\t" \
  "v_mfma_f32_16x16x32_bf16 v[80:83], v[108:111], v[116:119], v[80:83]\n\t" \
  "v_mfma_f32_16x16x32_bf16 v[76:79], v[108:111], v[112:115], v[76:79]\n\t" \
  "s_waitcnt lgkmcnt(0)\n\t" \
  "v_mfma_f32_16x16x32_bf16 v[92:95], v[144:147], v[188:191], v[92:95]\n\t" \
  "ds_read_b128 v[96:99], %[aa] offset:128\n\t" \
  "v_mfma_f32_16x16x32_bf16 v[88:91], v[144:147], v[184:187], v[88:91]\n\t" \
  "ds_read_b128 v[100:103], %[aa] offset:640\n\t" \
  "v_mfma_f32_16x16x32_bf16 v[84:87], v[144:147], v[180:183], v[84:87]\n\t" \
  "ds_read_b128 v[104:107], %[aa] offset:1152\n\t" \
  "v_mfma_f32_16x16x32_bf16 v[80:83], v[144:147], v[176:179], v[80:83]\n\t" \
  "ds_read_b128 v[108:111], %[aa] offset:1664\n\t" \
  "v_mfma_f32_16x16x32_bf16 v[76:79], v[144:147], v[172:175], v[76:79]\n\t" \
  "ds_read_b128 v[140:143], %[ab] offset:3904\n\t" \
  "v_mfma_f32_16x16x32_bf16 v[72:75], v[144:147], v[168:171], v[72:75]\n\t" \
  "ds_read_b128 v[136:139], %[ab] offset:3392\n\t" \
  "v_mfma_f32_16x16x32_bf16 v[68:71], v[144:147], v[164:167], v[68:71]\n\t" \
  "ds_read_b128 v[132:135], %[ab] offset:2880\n\t" \
  "v_mfma_f32_16x16x32_bf16 v[64:67], v[144:147], v[160:163], v[64:67]\n\t" \
  "ds_read_b128 v[128:131], %[ab] offset:2368\n\t" \
  "v_mfma_f32_16x16x32_bf16 v[92:95], v[148:151], v[184:187], v[92:95]\n\t" \
  "ds_read_b128 v[124:127], %[ab] offset:1856\n\t" \
  "v_mfma_f32_16x16x32_bf16 v[88:91], v[148:151], v[180:183], v[88:91]\n\t" \
  "ds_read_b128 v[120:123], %[ab] offset:1344\n\t" \
  "v_mfma_f32_16x16x32_bf16 v[84:87], v[148:151], v[176:179], v[84:87]\n\t" \
  "ds_read_b128 v[116:119], %[ab] offset:832\n\t" \
  "v_mfma_f32_16x16x32_bf16 v[80:83], v[148:151], v[172:175], v[80:83]\n\t" \
  "ds_read_b128 v[112:115], %[ab] offset:320\n\t" \
  "v_mfma_f32_16x16x32_bf16 v[76:79], v[148:151], v[168:171], v[76:79]\n\t" \
  "v_mfma_f32_16x16x32_bf16 v[72:75], v[148:151], v[164:167], v[72:75]\n\t" \
  "v_mfma_f32_16x16x32_bf16 v[68:71], v[148:151], v[160:163], v[68:71]\n\t" \
  "v_mfma_f32_16x16x32_bf16 v[92:95], v[152:155], v[180:183], v[92:95]\n\t" \
  "v_mfma_f32_16x16x32_bf16 v[88:91], v[152:155], v[176:179], v[88:91]\n\t" \
  "v_mfma_f32_16x16x32_bf16 v[84:87], v[152:155], v[172:175], v[84:87]\n\t" \
  "v_mfma_f32_16x16x32_bf16 v[80:83], v[152:155], v[168:171], v[80:83]\n\t" \
  "v_mfma_f32_16x16x32_bf16 v[76:79], v[152:155], v[164:167], v[76:79]\n\t" \
  "v_mfma_f32_16x16x32_bf16 v[72:75], v[152:155], v[160:163], v[72:75]\n\t" \
  "v_mfma_f32_16x16x32_bf16 v[92:95], v[156:159], v[176:179], v[92:95]\n\t" \
  "v_mfma_f32_16x16x32_bf16 v[88:91], v[156:159], v[172:175], v[88:91]\n\t" \
  "v_mfma_f32_16x16x32_bf16 v[84:87], v[156:159], v[168:171], v[84:87]\n\t" \
  "v_mfma_f32_16x16x32_bf16 v[80:83], v[156:159], v[164:167], v[80:83]\n\t" \
  "v_mfma_f32_16x16x32_bf16 v[76:79], v[156:159], v[160:163], v[76:79]\n\t" \
  "s_waitcnt lgkmcnt(0)\n\t" \
  "v_mfma_f32_16x16x32_bf16 v[92:95], v[96:99], v[140:143], v[92:95]\n\t" \
  "ds_read_b128 v[144:147], %[aa] offset:192\n\t" \
  "v_mfma_f32_16x16x32_bf16 v[88:91], v[96:99], v[136:139], v[88:91]\n\t" \
  "ds_read_b128 v[148:151], %[aa] offset:704\n\t" \
  "v_mfma_f32_16x16x32_bf16 v[84:87], v[96:99], v[132:135], v[84:87]\n\t" \
  "ds_read_b128 v[152:155], %[aa] offset:1216\n\t" \
  "v_mfma_f32_16x16x32_bf16 v[80:83], v[96:99], v[128:131], v[80:83]\n\t" \
  "ds_read_b128 v[156:159], %[aa] offset:1728\n\t" \
  "v_mfma_f32_16x16x32_bf16 v[76:79], v[96:99], v[124:127], v[76:79]\n\t" \
  "ds_read_b128 v[188:191], %[ab] offset:3840\n\t" \
  "v_mfma_f32_16x16x32_bf16 v[72:75], v[96:99], v[120:123], v[72:75]\n\t" \
  "ds_read_b128 v[184:187], %[ab] offset:3328\n\t" \
  "v_mfma_f32_16x16x32_bf16 v[68:71], v[96:99], v[116:119], v[68:71]\n\t" \
  "ds_read_b128 v[180:183], %[ab] offset:2816\n\t" \
  "v_mfma_f32_16x16x32_bf16 v[64:67], v[96:99], v[112:115], v[64:67]\n\t" \
  "ds_read_b128 v[176:179], %[ab] offset:2304\n\t" \
  "v_mfma_f32_16x16x32_bf16 v[92:95], v[100:103], v[136:139], v[92:95]\n\t" \
  "ds_read_b128 v[172:175], %[ab] offset:1792\n\t" \
  "v_mfma_f32_16x16x32_bf16 v[88:91], v[100:103], v[132:135], v[88:91]\n\t" \
  "ds_read_b128 v[168:171], %[ab] offset:1280\n\t" \
  "v_mfma_f32_16x16x32_bf16 v[84:87], v[100:103], v[128:131], v[84:87]\n\t" \
  "ds_read_b128 v[164:167], %[ab] offset:768\n\t" \
  "v_mfma_f32_16x16x32_bf16 v[80:83], v[100:103], v[124:127], v[80:83]\n\t" \
  "ds_read_b128 v[160:163], %[ab] offset:256\n\t" \
  "v_mfma_f32_16x16x32_bf16 v[76:79], v[100:103], v[120:123], v[76:79]\n\t" \
  "v_mfma_f32_16x16x32_bf16 v[72:75], v[100:103], v[116:119], v[72:75]\n\t" \
  "v_mfma_f32_16x16x32_bf16 v[68:71], v[100:103], v[112:115], v[68:71]\n\t" \
  "v_mfma_f32_16x16x32_bf16 v[92:95], v[104:107], v[132:135], v[92:95]\n\t" \
  "v_mfma_f32_16x16x32_bf16 v[88:91], v[104:107], v[128:131], v[88:91]\n\t" \
  "v_mfma_f32_16x16x32_bf16 v[84:87], v[104:107], v[124:127], v[84:87]\n\t" \
  "v_mfma_f32_16x16x32_bf16 v[80:83], v[104:107], v[120:123], v[80:83]\n\t" \
  "v_mfma_f32_16x16x32_bf16 v[76:79], v[104:107], v[116:119], v[76:79]\n\t" \
  "v_mfma_f32_16x16x32_bf16 v[72:75], v[104:107], v[112:115], v[72:75]\n\t" \
  "v_mfma_f32_16x16x32_bf16 v[92:95], v[108:111], v[128:131], v[92:95]\n\t" \
  "v_mfma_f32_16x16x32_bf16 v[88:91], v[108:111], v[124:127], v[88:91]\n\t" \
  "v_mfma_f32_16x16x32_bf16 v[84:87], v[108:111], v[120:123], v[84:87]\n\t" \
  "v_mfma_f32_16x16x32_bf16 v[80:83], v[108:111], v[116:119], v[80:83]\n\t" \
  "v_mfma_f32_16x16x32_bf16 v[76:79], v[108:111], v[112:115], v[76:79]\n\t" \
  "s_waitcnt lgkmcnt(0)\n\t" \
  "v_mfma_f32_16x16x32_bf16 v[92:95], v[144:147], v[188:191], v[92:95]\n\t" \
  "ds_read_b128 v[96:99], %[aa] offset:256\n\t" \
  "v_mfma_f32_16x16x32_bf16 v[88:91], v[144:147], v[184:187], v[88:91]\n\t" \
  "ds_read_b128 v[100:103], %[aa] offset:768\n\t" \
  "v_mfma_f32_16x16x32_bf16 v[84:87], v[144:147], v[180:183], v[84:87]\n\t" \
  "ds_read_b128 v[104:107], %[aa] offset:1280\n\t" \
  "v_mfma_f32_16x16x32_bf16 v[80:83], v[144:147], v[176:179], v[80:83]\n\t" \
  "ds_read_b128 v[108:111], %[aa] offset:1792\n\t" \
  "v_mfma_f32_16x16x32_bf16 v[76:79], v[144:147], v[172:175], v[76:79]\n\t" \
  "ds_read_b128 v[140:143], %[ab] offset:3776\n\t" \
  "v_mfma_f32_16x16x32_bf16 v[72:75], v[144:147], v[168:171], v[72:75]\n\t" \
  "ds_read_b128 v[136:139], %[ab] offset:3264\n\t" \
  "v_mfma_f32_16x16x32_bf16 v[68:71], v[144:147], v[164:167], v[68:71]\n\t" \
  "ds_read_b128 v[132:135], %[ab] offset:2752\n\t" \
  "v_mfma_f32_16x16x32_bf16 v[64:67], v[144:147], v[160:163], v[64:67]\n\t" \
  "ds_read_b128 v[128:131], %[ab] offset:2240\n\t" \
  "v_mfma_f32_16x16x32_bf16 v[92:95], v[148:151], v[184:187], v[92:95]\n\t" \
  "ds_read_b128 v[124:127], %[ab] offset:1728\n\t" \
  "v_mfma_f32_16x16x32_bf16 v[88:91], v[148:151], v[180:183], v[88:91]\n\t" \
  "ds_read_b128 v[120:123], %[ab] offset:1216\n\t" \
  "v_mfma_f32_16x16x32_bf16 v[84:87], v[148:151], v[176:179], v[84:87]\n\t" \
  "ds_read_b128 v[116:119], %[ab] offset:704\n\t" \
  "v_mfma_f32_16x16x32_bf16 v[80:83], v[148:151], v[172:175], v[80:83]\n\t" \
  "ds_read_b128 v[112:115], %[ab] offset:192\n\t" \
  "v_mfma_f32_16x16x32_bf16 v[76:79], v[148:151], v[168:171], v[76:79]\n\t" \
  "v_mfma_f32_16x16x32_bf16 v[72:75], v[148:151], v[164:167], v[72:75]\n\t" \
  "v_mfma_f32_16x16x32_bf16 v[68:71], v[148:151], v[160:163], v[68:71]\n\t" \
  "v_mfma_f32_16x16x32_bf16 v[92:95], v[152:155], v[180:183], v[92:95]\n\t" \
  "v_mfma_f32_16x16x32_bf16 v[88:91], v[152:155], v[176:179], v[88:91]\n\t" \
  "v_mfma_f32_16x16x32_bf16 v[84:87], v[152:155], v[172:175], v[84:87]\n\t" \
  "v_mfma_f32_16x16x32_bf16 v[80:83], v[152:155], v[168:171], v[80:83]\n\t" \
  "v_mfma_f32_16x16x32_bf16 v[76:79], v[152:155], v[164:167], v[76:79]\n\t" \
  "v_mfma_f32_16x16x32_bf16 v[72:75], v[152:155], v[160:163], v[72:75]\n\t" \
  "v_mfma_f32_16x16x32_bf16 v[92:95], v[156:159], v[176:179], v[92:95]\n\t" \
  "v_mfma_f32_16x16x32_bf16 v[88:91], v[156:159], v[172:175], v[88:91]\n\t" \
  "v_mfma_f32_16x16x32_bf16 v[84:87], v[156:159], v[168:171], v[84:87]\n\t" \
  "v_mfma_f32_16x16x32_bf16 v[80:83], v[156:159], v[164:167], v[80:83]\n\t" \
  "v_mfma_f32_16x16x32_bf16 v[76:79], v[156:159], v[160:163], v[76:79]\n\t" \
  "s_waitcnt lgkmcnt(0)\n\t" \
  "v_mfma_f32_16x16x32_bf16 v[92:95], v[96:99], v[140:143], v[92:95]\n\t" \
  "ds_read_b128 v[144:147], %[aa] offset:320\n\t" \
  "v_mfma_f32_16x16x32_bf16 v[88:91], v[96:99], v[136:139], v[88:91]\n\t" \
  "ds_read_b128 v[148:151], %[aa] offset:832\n\t" \
  "v_mfma_f32_16x16x32_bf16 v[84:87], v[96:99], v[132:135], v[84:87]\n\t" \
  "ds_read_b128 v[152:155], %[aa] offset:1344\n\t" \
  "v_mfma_f32_16x16x32_bf16 v[80:83], v[96:99], v[128:131], v[80:83]\n\t" \
  "ds_read_b128 v[156:159], %[aa] offset:1856\n\t" \
  "v_mfma_f32_16x16x32_bf16 v[76:79], v[96:99], v[124:127], v[76:79]\n\t" \
  "ds_read_b128 v[188:191], %[ab] offset:3712\n\t" \
  "v_mfma_f32_16x16x32_bf16 v[72:75], v[96:99], v[120:123], v[72:75]\n\t" \
  "ds_read_b128 v[184:187], %[ab] offset:3200\n\t" \
  "v_mfma_f32_16x16x32_bf16 v[68:71], v[96:99], v[116:119], v[68:71]\n\t" \
  "ds_read_b128 v[180:183], %[ab] offset:2688\n\t" \
  "v_mfma_f32_16x16x32_bf16 v[64:67], v[96:99], v[112:115], v[64:67]\n\t" \
  "ds_read_b128 v[176:179], %[ab] offset:2176\n\t" \
  "v_mfma_f32_16x16x32_bf16 v[92:95], v[100:103], v[136:139], v[92:95]\n\t" \
  "ds_read_b128 v[172:175], %[ab] offset:1664\n\t" \
  "v_mfma_f32_16x16x32_bf16 v[88:91], v[100:103], v[132:135], v[88:91]\n\t" \
  "ds_read_b128 v[168:171], %[ab] offset:1152\n\t" \
  "v_mfma_f32_16x16x32_bf16 v[84:87], v[100:103], v[128:131], v[84:87]\n\t" \
  "ds_read_b128 v[164:167], %[ab] offset:640\n\t" \
  "v_mfma_f32_16x16x32_bf16 v[80:83], v[100:103], v[124:127], v[80:83]\n\t" \
  "ds_read_b128 v[160:163], %[ab] offset:128\n\t" \
  "v_mfma_f32_16x16x32_bf16 v[76:79], v[100:103], v[120:123], v[76:79]\n\t" \
  "v_mfma_f32_16x16x32_bf16 v[72:75], v[100:103], v[116:119], v[72:75]\n\t" \
  "v_mfma_f32_16x16x32_bf16 v[68:71], v[100:103], v[112:115], v[68:71]\n\t" \
  "v_mfma_f32_16x16x32_bf16 v[92:95], v[104:107], v[132:135], v[92:95]\n\t" \
  "v_mfma_f32_16x16x32_bf16 v[88:91], v[104:107], v[128:131], v[88:91]\n\t" \
  "v_mfma_f32_16x16x32_bf16 v[84:87], v[104:107], v[124:127], v[84:87]\n\t" \
  "v_mfma_f32_16x16x32_bf16 v[80:83], v[104:107], v[120:123], v[80:83]\n\t" \
  "v_mfma_f32_16x16x32_bf16 v[76:79], v[104:107], v[116:119], v[76:79]\n\t" \
  "v_mfma_f32_16x16x32_bf16 v[72:75], v[104:107], v[112:115], v[72:75]\n\t" \
  "v_mfma_f32_16x16x32_bf16 v[92:95], v[108:111], v[128:131], v[92:95]\n\t" \
  "v_mfma_f32_16x16x32_bf16 v[88:91], v[108:111], v[124:127], v[88:91]\n\t" \
  "v_mfma_f32_16x16x32_bf16 v[84:87], v[108:111], v[120:123], v[84:87]\n\t" \
  "v_mfma_f32_16x16x32_bf16 v[80:83], v[108:111], v[116:119], v[80:83]\n\t" \
  "v_mfma_f32_16x16x32_bf16 v[76:79], v[108:111], v[112:115], v[76:79]\n\t" \
  "s_waitcnt lgkmcnt(0)\n\t" \
  "v_mfma_f32_16x16x32_bf16 v[92:95], v[144:147], v[188:191], v[92:95]\n\t" \
  "ds_read_b128 v[96:99], %[aa] offset:384\n\t" \
  "v_mfma_f32_16x16x32_bf16 v[88:91], v[144:147], v[184:187], v[88:91]\n\t" \
  "ds_read_b128 v[100:103], %[aa] offset:896\n\t" \
  "v_mfma_f32_16x16x32_bf16 v[84:87], v[144:147], v[180:183], v[84:87]\n\t" \
  "ds_read_b128 v[104:107], %[aa] offset:1408\n\t" \
  "v_mfma_f32_16x16x32_bf16 v[80:83], v[144:147], v[176:179], v[80:83]\n\t" \
  "ds_read_b128 v[108:111], %[aa] offset:1920\n\t" \
  "v_mfma_f32_16x16x32_bf16 v[76:79], v[144:147], v[172:175], v[76:79]\n\t" \
  "ds_read_b128 v[140:143], %[ab] offset:3648\n\t" \
  "v_mfma_f32_16x16x32_bf16 v[72:75], v[144:147], v[168:171], v[72:75]\n\t" \
  "ds_read_b128 v[136:139], %[ab] offset:3136\n\t" \
  "v_mfma_f32_16x16x32_bf16 v[68:71], v[144:147], v[164:167], v[68:71]\n\t" \
  "ds_read_b128 v[132:135], %[ab] offset:2624\n\t" \
  "v_mfma_f32_16x16x32_bf16 v[64:67], v[144:147], v[160:163], v[64:67]\n\t" \
  "ds_read_b128 v[128:131], %[ab] offset:2112\n\t" \
  "v_mfma_f32_16x16x32_bf16 v[92:95], v[148:151], v[184:187], v[92:95]\n\t" \
  "ds_read_b128 v[124:127], %[ab] offset:1600\n\t" \
  "v_mfma_f32_16x16x32_bf16 v[88:91], v[148:151], v[180:183], v[88:91]\n\t" \
  "ds_read_b128 v[120:123], %[ab] offset:1088\n\t" \
  "v_mfma_f32_16x16x32_bf16 v[84:87], v[148:151], v[176:179], v[84:87]\n\t" \
  "ds_read_b128 v[116:119], %[ab] offset:576\n\t" \
  "v_mfma_f32_16x16x32_bf16 v[80:83], v[148:151], v[172:175], v[80:83]\n\t" \
  "ds_read_b128 v[112:115], %[ab] offset:64\n\t" \
  "v_mfma_f32_16x16x32_bf16 v[76:79], v[148:151], v[168:171], v[76:79]\n\t" \
  "v_mfma_f32_16x16x32_bf16 v[72:75], v[148:151], v[164:167], v[72:75]\n\t" \
  "v_mfma_f32_16x16x32_bf16 v[68:71], v[148:151], v[160:163], v[68:71]\n\t" \
  "v_mfma_f32_16x16x32_bf16 v[92:95], v[152:155], v[180:183], v[92:95]\n\t" \
  "v_mfma_f32_16x16x32_bf16 v[88:91], v[152:155], v[176:179], v[88:91]\n\t" \
  "v_mfma_f32_16x16x32_bf16 v[84:87], v[152:155], v[172:175], v[84:87]\n\t" \
  "v_mfma_f32_16x16x32_bf16 v[80:83], v[152:155], v[168:171], v[80:83]\n\t" \
  "v_mfma_f32_16x16x32_bf16 v[76:79], v[152:155], v[164:167], v[76:79]\n\t" \
  "v_mfma_f32_16x16x32_bf16 v[72:75], v[152:155], v[160:163], v[72:75]\n\t" \
  "v_mfma_f32_16x16x32_bf16 v[92:95], v[156:159], v[176:179], v[92:95]\n\t" \
  "v_mfma_f32_16x16x32_bf16 v[88:91], v[156:159], v[172:175], v[88:91]\n\t" \
  "v_mfma_f32_16x16x32_bf16 v[84:87], v[156:159], v[168:171], v[84:87]\n\t" \
  "v_mfma_f32_16x16x32_bf16 v[80:83], v[156:159], v[164:167], v[80:83]\n\t" \
  "v_mfma_f32_16x16x32_bf16 v[76:79], v[156:159], v[160:163], v[76:79]\n\t" \
  "s_waitcnt lgkmcnt(0)\n\t" \
  "v_mfma_f32_16x16x32_bf16 v[92:95], v[96:99], v[140:143], v[92:95]\n\t" \
  "ds_read_b128 v[144:147], %[aa] offset:448\n\t" \
  "v_mfma_f32_16x16x32_bf16 v[88:91], v[96:99], v[136:139], v[88:91]\n\t" \
  "ds_read_b128 v[148:151], %[aa] offset:960\n\t" \
  "v_mfma_f32_16x16x32_bf16 v[84:87], v[96:99], v[132:135], v[84:87]\n\t" \
  "ds_read_b128 v[152:155], %[aa] offset:1472\n\t" \
  "v_mfma_f32_16x16x32_bf16 v[80:83], v[96:99], v[128:131], v[80:83]\n\t" \
  "ds_read_b128 v[156:159], %[aa] offset:1984\n\t" \
  "v_mfma_f32_16x16x32_bf16 v[76:79], v[96:99], v[124:127], v[76:79]\n\t" \
  "ds_read_b128 v[188:191], %[ab] offset:3584\n\t" \
  "v_mfma_f32_16x16x32_bf16 v[72:75], v[96:99], v[120:123], v[72:75]\n\t" \
  "ds_read_b128 v[184:187], %[ab] offset:3072\n\t" \
  "v_mfma_f32_16x16x32_bf16 v[68:71], v[96:99], v[116:119], v[68:71]\n\t" \
  "ds_read_b128 v[180:183], %[ab] offset:2560\n\t" \
  "v_mfma_f32_16x16x32_bf16 v[64:67], v[96:99], v[112:115], v[64:67]\n\t" \
  "ds_read_b128 v[176:179], %[ab] offset:2048\n\t" \
  "v_mfma_f32_16x16x32_bf16 v[92:95], v[100:103], v[136:139], v[92:95]\n\t" \
  "ds_read_b128 v[172:175], %[ab] offset:1536\n\t" \
  "v_mfma_f32_16x16x32_bf16 v[88:91], v[100:103], v[132:135], v[88:91]\n\t" \
  "ds_read_b128 v[168:171], %[ab] offset:1024\n\t" \
  "v_mfma_f32_16x16x32_bf16 v[84:87], v[100:103], v[128:131], v[84:87]\n\t" \
  "ds_read_b128 v[164:167], %[ab] offset:512\n\t" \
  "v_mfma_f32_16x16x32_bf16 v[80:83], v[100:103], v[124:127], v[80:83]\n\t" \
  "ds_read_b128 v[160:163], %[ab] offset:0\n\t" \
  "v_mfma_f32_16x16x32_bf16 v[76:79], v[100:103], v[120:123], v[76:79]\n\t" \
  "v_mfma_f32_16x16x32_bf16 v[72:75], v[100:103], v[116:119], v[72:75]\n\t" \
  "v_mfma_f32_16x16x32_bf16 v[68:71], v[100:103], v[112:115], v[68:71]\n\t" \
  "v_mfma_f32_16x16x32_bf16 v[92:95], v[104:107], v[132:135], v[92:95]\n\t" \
  "v_mfma_f32_16x16x32_bf16 v[88:91], v[104:107], v[128:131], v[88:91]\n\t" \
  "v_mfma_f32_16x16x32_bf16 v[84:87], v[104:107], v[124:127], v[84:87]\n\t" \
  "v_mfma_f32_16x16x32_bf16 v[80:83], v[104:107], v[120:123], v[80:83]\n\t" \
  "v_mfma_f32_16x16x32_bf16 v[76:79], v[104:107], v[116:119], v[76:79]\n\t" \
  "v_mfma_f32_16x16x32_bf16 v[72:75], v[104:107], v[112:115], v[72:75]\n\t" \
  "v_mfma_f32_16x16x32_bf16 v[92:95], v[108:111], v[128:131], v[92:95]\n\t" \
  "v_mfma_f32_16x16x32_bf16 v[88:91], v[108:111], v[124:127], v[88:91]\n\t" \
  "v_mfma_f32_16x16x32_bf16 v[84:87], v[108:111], v[120:123], v[84:87]\n\t" \
  "v_mfma_f32_16x16x32_bf16 v[80:83], v[108:111], v[116:119], v[80:83]\n\t" \
  "v_mfma_f32_16x16x32_bf16 v[76:79], v[108:111], v[112:115], v[76:79]\n\t" \
  "s_waitcnt lgkmcnt(0)\n\t" \
  "v_mfma_f32_16x16x32_bf16 v[92:95], v[144:147], v[188:191], v[92:95]\n\t" \
  "v_mfma_f32_16x16x32_bf16 v[88:91], v[144:147], v[184:187], v[88:91]\n\t" \
  "v_mfma_f32_16x16x32_bf16 v[84:87], v[144:147], v[180:183], v[84:87]\n\t" \
  "v_mfma_f32_16x16x32_bf16 v[80:83], v[144:147], v[176:179], v[80:83]\n\t" \
  "v_mfma_f32_16x16x32_bf16 v[76:79], v[144:147], v[172:175], v[76:79]\n\t" \
  "v_mfma_f32_16x16x32_bf16 v[72:75], v[144:147], v[168:171], v[72:75]\n\t" \
  "v_mfma_f32_16x16x32_bf16 v[68:71], v[144:147], v[164:167], v[68:71]\n\t" \
  "v_mfma_f32_16x16x32_bf16 v[64:67], v[144:147], v[160:163], v[64:67]\n\t" \
  "v_mfma_f32_16x16x32_bf16 v[92:95], v[148:151], v[184:187], v[92:95]\n\t" \
  "v_mfma_f32_16x16x32_bf16 v[88:91], v[148:151], v[180:183], v[88:91]\n\t" \
  "v_mfma_f32_16x16x32_bf16 v[84:87], v[148:151], v[176:179], v[84:87]\n\t" \
  "v_mfma_f32_16x16x32_bf16 v[80:83], v[148:151], v[172:175], v[80:83]\n\t" \
  "v_mfma_f32_16x16x32_bf16 v[76:79], v[148:151], v[168:171], v[76:79]\n\t" \
  "v_mfma_f32_16x16x32_bf16 v[72:75], v[148:151], v[164:167], v[72:75]\n\t" \
  "v_mfma_f32_16x16x32_bf16 v[68:71], v[148:151], v[160:163], v[68:71]\n\t" \
  "v_mfma_f32_16x16x32_bf16 v[92:95], v[152:155], v[180:183], v[92:95]\n\t" \
  "v_mfma_f32_16x16x32_bf16 v[88:91], v[152:155], v[176:179], v[88:91]\n\t" \
  "v_mfma_f32_16x16x32_bf16 v[84:87], v[152:155], v[172:175], v[84:87]\n\t" \
  "v_mfma_f32_16x16x32_bf16 v[80:83], v[152:155], v[168:171], v[80:83]\n\t" \
  "v_mfma_f32_16x16x32_bf16 v[76:79], v[152:155], v[164:167], v[76:79]\n\t" \
  "v_mfma_f32_16x16x32_bf16 v[72:75], v[152:155], v[160:163], v[72:75]\n\t" \
  "v_mfma_f32_16x16x32_bf16 v[92:95], v[156:159], v[176:179], v[92:95]\n\t" \
  "v_mfma_f32_16x16x32_bf16 v[88:91], v[156:159], v[172:175], v[88:91]\n\t" \
  "v_mfma_f32_16x16x32_bf16 v[84:87], v[156:159], v[168:171], v[84:87]\n\t" \
  "v_mfma_f32_16x16x32_bf16 v[80:83], v[156:159], v[164:167], v[80:83]\n\t" \
  "v_mfma_f32_16x16x32_bf16 v[76:79], v[156:159], v[160:163], v[76:79]\n\t" \
  "s_nop 7\n\t" \
  ""
#define CONV_MAIN_CLOB "v144","v145","v146","v147","v148","v149","v150","v151","v152","v153","v154","v155","v156","v157","v158","v159","v160","v161","v162","v163","v164","v165","v166","v167","v168","v169","v170","v171","v172","v173","v174","v175","v176","v177","v178","v179","v180","v181","v182","v183","v184","v185","v186","v187","v188","v189","v190","v191"

struct G1Ld { unsigned x[4], w[4]; };
DI G1Ld g1_lanes(int lane) {
  const int r8 = lane >> 3, sl = lane & 7;
  G1Ld v;
#pragma unroll
  for (int p = 0; p < 4; ++p) {
    v.x[p] = (r8 + 8 * p) * 1280 + ((sl ^ (r8 >> 1) ^ (4 * (p & 1))) << 4) - 1024 * p;
    v.w[p] = (r8 + 8 * p) * 3072 + ((sl ^ (r8 & 2) ^ (4 * (p & 1))) << 4) - 1024 * p;
  }
  return v;
}
template <int NT> DI void g1_issue(v4u rx, v4u rg, v4u ru, const G1Ld& v, int lw, int s, unsigned lb, int row0, int ch) {
  const unsigned xso = (row0 + lw * 16 * NT) * 1280 + s * 128, wso = ch * 128 + (lw * 32 + s * 64) * 3072;
  dma4<0>(rg, lb + XB + lw * 4096, v.w[0], v.w[1], v.w[2], v.w[3], wso);
  dma4<0>(ru, lb + XB + 8192 + lw * 4096, v.w[0], v.w[1], v.w[2], v.w[3], wso);
#pragma unroll
  for (int p = 0; p < NT / 2; ++p) dma4<0>(rx, lb + (lw * 2 * NT + 4 * p) * 1024, v.x[0], v.x[1], v.x[2], v.x[3], xso + p * 32 * 1280);
}
DI unsigned nxt(unsigned so) { return so == 0 ? 2 * STAGE : so - STAGE; }
DI unsigned rem_up(unsigned so) { return so == 0 ? LX : so == STAGE ? LX + 8192 : L_U; }
DI void g1_issue_rem(v4u rx, v4u rg, v4u ru, const G1Ld& v, int lw, int s, unsigned lb, int ch, int ch2) {
  g1_issue<4>(rx, rg, ru, v, lw, s, lb, 1920, ch);
  const unsigned wso = ch2 * 128 + (lw * 32 + s * 64) * 3072;
  dma4<0>(rg, lb + 16384 + lw * 4096, v.w[0], v.w[1], v.w[2], v.w[3], wso);
  dma4<0>(ru, rem_up(lb) + lw * 4096, v.w[0], v.w[1], v.w[2], v.w[3], wso);
}
template <int NP, int S0, class Issue> DI void g1_loader(int rot, unsigned so, Issue issue) {
#pragma unroll 1
  for (int s = S0; s < 10; ++s) {
    if (s >= S0 + 2) wait_vm<NP>();
    if (s >= 2) BAR(s - 2);
    issue(s + rot >= 10 ? s + rot - 10 : s + rot, so);
    so = nxt(so);
  }
  wait_vm<NP>(); BAR(8);
}

DI void colsum(rsrc_t rw, unsigned off, int k, int lane) {
  const int c = lane & 15, g = lane >> 4, q = (lane >> 2) & 3, p = lane & 3;
  const bf16x8 ones = {0x3f80, 0x3f80, 0x3f80, 0x3f80, 0x3f80, 0x3f80, 0x3f80, 0x3f80};
  f32x4 r[2];
#pragma unroll
  for (int a = 0; a < 2; ++a) {
    const unsigned ad = XB + (k >> 1) * 8192 + (8 * g + q) * 128 + (((4 * (k & 1) + 2 * a + (p >> 1)) ^ (q & 2) ^ ((g & 1) << 2)) << 4) + 8 * (p & 1);
    r[a] = mfma(trd(ad + 4096, ad + 4608), ones, mfma(trd(ad, ad + 512), ones, f32x4{0.f, 0.f, 0.f, 0.f}));
  }
  f32x4 v;
#pragma unroll
  for (int e = 0; e < 4; ++e) v[e] = c ? r[1][e] : r[0][e];
  if (c < 2) st128<SC1>(__builtin_bit_cast(v4u, v), rw, off + k * 128 + (16 * c + 4 * g) * 4);
}

struct G1Rd { unsigned aA[2], aB[2]; };
template <int NT> DI void g1_setup(int wid, int lane, G1Rd& r, f32x4 (&acc)[4][NT]) {
  const int wa = wid & 1, wt = wid >> 1;
  const int c = lane & 15, g = lane >> 4, q = (lane >> 2) & 3, p = lane & 3;
  unsigned (&aA)[2] = r.aA; unsigned (&aB)[2] = r.aB;
#pragma unroll
  for (int a = 0; a < 2; ++a) aA[a] = XB + (8 * g + q) * 128 + (((4 * wa + 2 * a + (p >> 1)) ^ (q & 2) ^ ((g & 1) << 2)) << 4) + 8 * (p & 1);
#pragma unroll
  for (int ks = 0; ks < 2; ++ks) aB[ks] = (16 * NT * wt + c) * 128 + (((4 * ks + g) ^ (c >> 1)) << 4);
#pragma unroll
  for (int a = 0; a < 4; ++a)
#pragma unroll
    for (int t = 0; t < NT; ++t) { acc[a][t] = f32x4{0.f, 0.f, 0.f, 0.f}; if (NT != 6) asm volatile("" : "+v"(acc[a][t])); }
  asm volatile("" : "+v"(aA[0]), "+v"(aA[1]), "+v"(aB[0]), "+v"(aB[1]));
}
template <int NT> struct G1Ep { f32x4 cs[4], st[NT]; };
template <int NT> DI void g1_ep_read(int wid, int lane, unsigned st_off, unsigned cs_off, G1Ep<NT>& e) {
  const int wa = wid & 1, wt = wid >> 1, c = lane & 15, g = lane >> 4;
#pragma unroll
  for (int a = 0; a < 4; ++a) e.cs[a] = *(LAS f32x4*)lds(cs_off + (a >> 1) * 256 + (32 * wa + 16 * (a & 1) + 4 * g) * 4);
#pragma unroll
  for (int t = 0; t < NT; ++t) e.st[t] = *(LAS f32x4*)lds(st_off + (16 * NT * wt + 16 * t + c) * 16);
}
DI void g1_main_rem(const G1Rd& r, unsigned db, f32x4 (&acc4)[4][4], f32x4 (&acc2)[4][2]) {
  const unsigned (&aA)[2] = r.aA; const unsigned (&aB)[2] = r.aB;
  bf16x8 fa[2][8], fb[2][6];
  auto rd = [&](int buf, int ks, unsigned o) {
    const unsigned ou = rem_up(o) - XB;
#pragma unroll
    for (int a = 0; a < 4; ++a) { unsigned ad = aA[a & 1] + o + (a >> 1) * 8192 + ks * 4096; fa[buf][a] = trd(ad, ad + 512); }
#pragma unroll
    for (int t = 0; t < 4; ++t) fb[buf][t] = rd128(aB[ks] + o + t * 2048);
#pragma unroll
    for (int a = 0; a < 4; ++a) { unsigned ad = aA[a & 1] + (a < 2 ? o - 8192 : ou) + ks * 4096; fa[buf][4 + a] = trd(ad, ad + 512); }
#pragma unroll
    for (int t = 0; t < 2; ++t) fb[buf][4 + t] = rd128(aB[ks] + o + db + t * 2048);
  };
  auto mm = [&](int buf) {
#pragma unroll
    for (int t = 0; t < 4; ++t)
#pragma unroll
      for (int a = 0; a < 4; ++a) acc4[a][t] = mfma(fa[buf][a], fb[buf][t], acc4[a][t]);
#pragma unroll
    for (int t = 0; t < 2; ++t)
#pragma unroll
      for (int a = 0; a < 4; ++a) acc2[a][t] = mfma(fa[buf][4 + a], fb[buf][4 + t], acc2[a][t]);
  };
  auto ilv = [&]() {
#pragma unroll
    for (int i = 0; i < 22; ++i) { __builtin_amdgcn_sched_group_barrier(0x008, 1, 0); __builtin_amdgcn_sched_group_barrier(0x100, 1, 0); }
    __builtin_amdgcn_sched_group_barrier(0x008, 2, 0);
    __builtin_amdgcn_sched_barrier(0);
  };
  unsigned so = 0;
  BAR(0);
  STAMP(2); RSTAMP(31);
  rd(0, 0, so);
#pragma unroll 1
  for (int s = 0; s < 9; ++s) {
    rd(1, 1, so); mm(0); ilv();
    so = nxt(so);
    wait_lg();
    BAR(s + 1);
    __builtin_amdgcn_sched_barrier(0);
    rd(0, 0, so); mm(1); ilv();
  }
  rd(1, 1, so); mm(0); ilv();
  mm(1);
}
struct G1Acc { f32x16 a[6]; unsigned csa, sta; };
DI void g1_setup6(int wid, int lane, G1Acc& z) {
#pragma unroll
  for (int t = 0; t < 6; ++t) z.a[t] = f32x16{};
  z.csa = L_CS + (32 * (wid & 1) + 4 * (lane >> 4)) * 4; z.sta = L_ST + (96 * (wid >> 1) + (lane & 15)) * 16;
  asm volatile("" : "+{v[64:79]}"(z.a[0]), "+{v[80:95]}"(z.a[1]), "+{v[96:111]}"(z.a[2]), "+{v[112:127]}"(z.a[3]), "+{v[128:143]}"(z.a[4]),
               "+{v[144:159]}"(z.a[5]), "+v"(z.csa), "+v"(z.sta));
}
DI void g1_main6(const G1Rd& r, G1Acc& z, f32x4 (&acc)[4][6], G1Ep<6>& ep) {
  f32x16 (&a)[6] = z.a; f32x16 cs, s0; f32x8 s1; int cnt;
  const unsigned csa = z.csa, sta = z.sta;
  asm volatile(G1_MAIN_ASM
               : "+{v[64:79]}"(a[0]), "+{v[80:95]}"(a[1]), "+{v[96:111]}"(a[2]), "+{v[112:127]}"(a[3]), "+{v[128:143]}"(a[4]), "+{v[144:159]}"(a[5]),
                 "=&{v[160:175]}"(cs), "=&{v[176:191]}"(s0), "=&{v[192:199]}"(s1), [cnt] "=&s"(cnt)
               : [a0] "v"(r.aA[0]), [a1] "v"(r.aA[1]), [a2] "v"(r.aB[0]), [a3] "v"(r.aB[1]), [cs] "v"(csa), [st] "v"(sta)
               : "memory", "scc", G1_MAIN_CLOB G1_STAMP_CLOB);
#pragma unroll
  for (int t = 0; t < 6; ++t)
#pragma unroll
    for (int i = 0; i < 16; ++i) acc[i >> 2][t][i & 3] = a[t][i];
#pragma unroll
  for (int i = 0; i < 16; ++i) { ep.cs[i >> 2][i & 3] = cs[i]; ep.st[i >> 2][i & 3] = s0[i]; }
#pragma unroll
  for (int i = 0; i < 8; ++i) ep.st[4 + (i >> 2)][i & 3] = s1[i];
}
template <int NT, bool LAST> DI void g1_epilogue(rsrc_t rw, int wid, int lane, int t16_0, int ch, const G1Ep<NT>& ep, const f32x4 (&acc)[4][NT]) {
  const int wa = wid & 1, wt = wid >> 1;
  const f32x4 (&cs)[4] = ep.cs; const f32x4 (&st)[NT] = ep.st;
#pragma unroll
  for (int t = 0; t < NT; ++t) {
    const float nm = st[t][0], qq = st[t][1], rl = st[t][2];
    v4u o;
#pragma unroll
    for (int a = 0; a < 2; ++a) {
      f32x4 tg = cs[a] * nm + acc[a][t], tu = cs[2 + a] * nm + acc[2 + a][t];
      f32x4 ar = tg * rl, e;
#pragma unroll
      for (int r = 0; r < 4; ++r) e[r] = __builtin_amdgcn_exp2f(ar[r]);
      f32x4 dn = e * qq + qq, pr = tg * tu;
#pragma unroll
      for (int r = 0; r < 4; ++r) dn[r] = __builtin_amdgcn_rcpf(dn[r]);
      pr *= dn;
      if (LAST && t == NT - 1 && a == 1) { asm volatile("" : "+v"(pr)); lds_set(L_MISC + 48 + wid * 4); }
      o[2 * a] = pk(pr[0], pr[1]); o[2 * a + 1] = pk(pr[2], pr[3]);
    }
    st128<SC1>(o, rw, lane * 16 + WS_H + ((t16_0 + NT * wt + t) * 48 + 2 * ch + wa) * 1024);
  }
}

template <int NM> DI void kgen(const unsigned (&w)[6], float dt, int lane, unsigned tb, unsigned done, f32x4 (&acc)[2][2]) {
  constexpr int RS = NM * 256 + 32, KS = NM * 4;
  float lr[NM], tr[NM]; f32x2 W[NM];
#pragma unroll
  for (int m = 0; m < NM; ++m) {
    float v[6];
#pragma unroll
    for (int i = 0; i < 6; ++i) v[i] = m == 0 ? blo(w[i]) : bhi(w[i]);
    lr[m] = dt * v[0] * 1.44269504f; tr[m] = dt * v[1] * 0.15915494f;
    const float br = dt * v[2], bi = dt * v[3];
    W[m] = f32x2{v[4] * br - v[5] * bi, -(v[4] * bi + v[5] * br)};
  }
  asm volatile("" : "+v"(lr[0]), "+v"(W[0]));
  STAMP(13);
  unsigned wa = tb + lane * 4 * NM, fa = tb + (lane & 15) * RS + (lane >> 4) * 16;
  asm volatile("" : "+v"(wa), "+v"(fa));
  auto put = [&](int row, const f32x2* z) {
    if (NM == 2) *(LAS v2u*)lds(wa + row * RS) = v2u{pk2(z[0]), pk2(z[NM - 1])};
    else *(LAS unsigned*)lds(wa + row * RS) = pk2(z[0]);
  };
  f32x2 c[8][NM], hi[NM], t[NM], c8[NM];
#pragma unroll
  for (int m = 0; m < NM; ++m) {
    c[0][m] = f32x2{1.f, 0.f}; c[1][m] = cpow(lr[m], tr[m], 8.f);
    c[2][m] = cmul(c[1][m], c[1][m]); c[3][m] = cmul(c[2][m], c[1][m]); c[4][m] = cmul(c[2][m], c[2][m]);
    c[5][m] = cmul(c[4][m], c[1][m]); c[6][m] = cmul(c[4][m], c[2][m]); c[7][m] = cmul(c[4][m], c[3][m]);
    c8[m] = cmul(c[4][m], c[4][m]); hi[m] = c8[m];
  }
  bf16x8 fb[2][KS];
#pragma unroll
  for (int j = 0; j < 8; ++j) put(j, c[j]);
#pragma unroll
  for (int a = 1; a < 4; ++a) {
    if (a == 2) {
#pragma unroll
      for (int s = 0; s < KS; ++s) fb[0][s] = rd128(fa + s * 64);
    }
    put((8 * a) & 15, hi);
#pragma unroll
    for (int j = 1; j < 8; ++j) {
#pragma unroll
      for (int m = 0; m < NM; ++m) t[m] = cmul(hi[m], c[j][m]);
      put((8 * a + j) & 15, t);
    }
    if (a < 3) {
#pragma unroll
      for (int m = 0; m < NM; ++m) hi[m] = cmul(hi[m], c8[m]);
    }
  }
#pragma unroll
  for (int s = 0; s < KS; ++s) fb[1][s] = rd128(fa + s * 64);
  STAMP(14);
  if (NM == 2 && TRIG == 0) lds_set(done);
  f32x2 z1[NM], zb[NM], r0[NM], r4[NM];
#pragma unroll
  for (int m = 0; m < NM; ++m) { z1[m] = cpow(lr[m], -tr[m], 1.f); zb[m] = cpow(lr[m], -tr[m], 256.f); t[m] = W[m]; }
#pragma unroll
  for (int b = 0; b < 8; ++b) {
    put(b, t);
    if ((b & 1) == 0) {
      f32x2 u[NM];
#pragma unroll
      for (int m = 0; m < NM; ++m) { u[m] = cmul(t[m], zb[m]); if (b == 0) r0[m] = u[m]; if (b == 4) r4[m] = u[m]; }
      put(8 + b / 2, u);
    }
    if (b < 7) {
#pragma unroll
      for (int m = 0; m < NM; ++m) t[m] = cmul(t[m], z1[m]);
    }
  }
  if (NM == 2 && TRIG == 1) lds_set(done);
#pragma unroll
  for (int i = 0; i < 2; ++i) {
#pragma unroll
    for (int m = 0; m < NM; ++m) { r0[m] = cmul(r0[m], zb[m]); r4[m] = cmul(r4[m], zb[m]); }
    put(12 + 2 * i, r0); put(13 + 2 * i, r4);
  }
  bf16x8 fr[2][KS];
#pragma unroll
  for (int s = 0; s < KS; ++s) fr[0][s] = rd128(fa + s * 64);
#pragma unroll
  for (int i = 0; i < 5; ++i) {
#pragma unroll
    for (int m = 0; m < NM; ++m) r0[m] = cmul(r0[m], zb[m]);
    put(i, r0);
  }
  if (NM == 2 && TRIG == 2) lds_set(done);
#pragma unroll
  for (int s = 0; s < KS; ++s) fr[1][s] = rd128(fa + s * 64);
  __builtin_amdgcn_sched_barrier(0);
  STAMP(15);
#pragma unroll
  for (int i = 0; i < 4; ++i) acc[i >> 1][i & 1] = f32x4{0.f, 0.f, 0.f, 0.f};
#pragma unroll
  for (int rt = 0; rt < 2; ++rt)
#pragma unroll
    for (int s = 0; s < KS; ++s) { acc[rt][0] = mfma(fr[rt][s], fb[0][s], acc[rt][0]); acc[rt][1] = mfma(fr[rt][s], fb[1][s], acc[rt][1]); }
}
struct LnOut { s16x4 tr[2][10]; v2u st; };
DI void ln8(rsrc_t rx, int tb, int lane, unsigned tile, LnOut& out) {
  const int r4 = lane >> 4, i = lane & 15;
  v4u raw[2][5];
#pragma unroll
  for (int h = 0; h < 2; ++h)
#pragma unroll
    for (int k = 0; k < 5; ++k) raw[h][k] = __builtin_amdgcn_raw_buffer_load_b128(rx, r4 * 1280 + i * 16 + k * 256, (tb * 8 + h * 4) * 1280, 0);
  const unsigned ta = tile + ((lane >> 2) & 3) * LN_RS + (16 * r4 + 4 * (lane & 3)) * 2;
#pragma unroll
  for (int h = 0; h < 2; ++h) {
    if (h == 0) { asm volatile("" : "+v"(raw[0][4])); STAMP(13); asm volatile("" : "+v"(raw[1][4])); STAMP(15); } else STAMP(14);
    float s1 = 0.f, s2 = 0.f, s3 = 0.f, s4 = 0.f;
    const bf16x2 one2 = __builtin_bit_cast(bf16x2, 0x3f803f80u);
#pragma unroll
    for (int k = 0; k < 10; ++k) {
      const unsigned w0 = raw[h][k >> 1][2 * (k & 1)], w1 = raw[h][k >> 1][2 * (k & 1) + 1];
      const bf16x2 p0 = __builtin_bit_cast(bf16x2, w0), p1 = __builtin_bit_cast(bf16x2, w1);
      s1 = __builtin_amdgcn_fdot2_f32_bf16(p0, one2, s1, false); s2 = __builtin_amdgcn_fdot2_f32_bf16(p0, p0, s2, false);
      s3 = __builtin_amdgcn_fdot2_f32_bf16(p1, one2, s3, false); s4 = __builtin_amdgcn_fdot2_f32_bf16(p1, p1, s4, false);
    }
    const float mean = rowsum16(s1 + s3) * (1.f / D);
    const float var = rbf(fmaxf(rowsum16(s2 + s4) * (1.f / D) - mean * mean, 0.f));
    const float m = rbf(mean), den = rbf(sqrtf(rbf(var + 1e-5f)));
    const float ri = __builtin_amdgcn_rcpf(den), nb = -m * ri;
    if (h == 0 || (i & 1)) out.st = v2u{__float_as_uint(m), __float_as_uint(den)};
#pragma unroll
    for (int k = 0; k < 5; ++k) {
      v4u o;
#pragma unroll
      for (int e = 0; e < 4; ++e) { const unsigned w = raw[h][k][e]; o[e] = pk2(f32x2{blo(w), bhi(w)} * ri + nb); }
      *(LAS v4u*)lds(tile + r4 * LN_RS + i * 16 + k * 256) = o;
    }
    cfence();
#pragma unroll
    for (int it = 0; it < 10; ++it) out.tr[h][it] = __builtin_amdgcn_ds_read_tr16_b64_v4i16((LAS s16x4*)lds(ta + it * 128));
  }
}
DI void ln8_store(rsrc_t rw, int tb, int lane, const LnOut& in) {
  if ((lane & 15) < 2) __builtin_amdgcn_raw_buffer_store_b64(in.st, rw, ((lane & 1) * 4 + (lane >> 4)) * 8, WS_STATS + tb * 128, SC1);
#pragma unroll
  for (int it = 0; it < 10; ++it) {
    bf16x8 o = __builtin_shufflevector(in.tr[0][it], in.tr[1][it], 0, 1, 2, 3, 4, 5, 6, 7);
    st128<SC1>(__builtin_bit_cast(v4u, o), rw, lane * 16 + it * 1024 + WS_U8 + tb * 10240);
  }
}

DI unsigned prm(unsigned hi, unsigned lo, unsigned sel) { return __builtin_amdgcn_perm(hi, lo, sel); }
DI unsigned colo(int col) { return (col & 15) * 16 + ((col >> 4) & 1) * 8; }
template <int S> DI v4u kchunk(unsigned raw, int lane) {
  const int i = lane & 31, up = lane >> 5;
  if (S == 0) {
    const v2u a0 = *(LAS v2u*)lds(raw + colo(i) + 512 * up);
    const v2u a1 = *(LAS v2u*)lds(raw + (up ? (i == 31 ? 768 : 512 + colo(i + 1)) : 256 + colo(i)));
    const unsigned s0 = up ? 0x03020504u : 0x05040706u, s1 = up ? 0x01000100u : 0x05040706u, s2 = up ? 0x03020504u : 0x05040706u,
                   s3 = up ? 0x01000100u : 0x01000302u;
    return v4u{prm(up ? a1.x : a1.y, a0.y, s0), prm(a1.x, a0.y, s1), prm(a0.y, a0.x, s2), prm(0, a0.x, s3)};
  } else {
    const unsigned b0 = *(LAS unsigned*)lds(raw + 768 + colo(i) + 4 * up);
    const unsigned b1 = *(LAS unsigned*)lds(raw + (lane == 63 ? 1024 : 768 + colo(i + 1) + 4 * ((lane + 1) >> 5)));
    return v4u{prm(0, b1, 0x01000100), prm(0, b0, 0x03020302), prm(0, b0, 0x01000302), prm(0, b0, 0x01000100)};
  }
}
DI unsigned vmov(unsigned x) { unsigned r; asm("v_mov_b32 %0, %1" : "=v"(r) : "v"(x)); return r; }
struct HWin { v8u w, e, o0, o1; };
DI void hwin(v4u x, v4u y, HWin& h) {
  h.w = __builtin_shufflevector(x, y, 0, 1, 2, 3, 4, 5, 6, 7);
  asm volatile("" : "+v"(h.w));
#pragma unroll
  for (int k = 0; k < 6; ++k) {
    h.e[k] = vmov(h.w[k + 1]);
    h.o0[k] = __builtin_amdgcn_alignbit(h.w[k + 1], h.w[k], 16);
    h.o1[k] = __builtin_amdgcn_alignbit(h.w[k + 2], h.w[k + 1], 16);
    asm volatile("" : "+v"(h.o1[k]));
  }
  asm volatile("" : "+v"(h.e), "+v"(h.o0), "+v"(h.o1));
}
DI void hwrite(const HWin& h, unsigned a) {
  *(LAS v4u*)lds(a + 7 * H_CS) = __builtin_shufflevector(h.w, h.w, 0, 1, 2, 3);
  *(LAS v4u*)lds(a + 3 * H_CS) = __builtin_shufflevector(h.w, h.w, 2, 3, 4, 5);
  *(LAS v4u*)lds(a + 5 * H_CS) = __builtin_shufflevector(h.e, h.e, 0, 1, 2, 3);
  *(LAS v4u*)lds(a + 1 * H_CS) = __builtin_shufflevector(h.e, h.e, 2, 3, 4, 5);
  *(LAS v4u*)lds(a + 6 * H_CS) = __builtin_shufflevector(h.o0, h.o0, 0, 1, 2, 3);
  *(LAS v4u*)lds(a + 2 * H_CS) = __builtin_shufflevector(h.o0, h.o0, 2, 3, 4, 5);
  *(LAS v4u*)lds(a + 4 * H_CS) = __builtin_shufflevector(h.o1, h.o1, 0, 1, 2, 3);
  *(LAS v4u*)lds(a + 0 * H_CS) = __builtin_shufflevector(h.o1, h.o1, 2, 3, 4, 5);
}
struct ConvPre { v4u x0, x1; unsigned kk, k3; bf16x8 kt[4], b9, b7; };
DI void conv_masks(int lane, ConvPre& p) {
#pragma unroll
  for (int jj = 0; jj < 8; ++jj) {
    const int dl = 8 * (lane >> 4) + jj - 8 - (lane & 15);
    p.b9[jj] = (dl >= -4 && dl <= 4) ? 0x3f80 : 0; p.b7[jj] = (dl >= -3 && dl <= 3) ? 0x3f80 : 0;
  }
  asm volatile("" : "+v"(p.b9), "+v"(p.b7));
}
DI void conv_pre(unsigned raw, int lane, ConvPre& p) {
  p.x0 = kchunk<0>(raw, lane); p.x1 = kchunk<1>(raw, lane);
  p.kk = *(LAS uint16_t*)lds(raw + 1024);
  const int n0 = 128 - 2 * lane, n1 = n0 - 1;
  const unsigned c0 = *(LAS uint16_t*)lds(raw + 1024 + (n0 == 128 ? 256 : colo(n0) + 2 * (n0 >> 5)));
  const unsigned c1 = *(LAS uint16_t*)lds(raw + 1024 + colo(n1) + 2 * (n1 >> 5));
  p.k3 = c0 | (c1 << 16);
  asm volatile("" : "+v"(p.x0), "+v"(p.x1), "+v"(p.kk), "+v"(p.k3));
}
DI void conv_image(ConvPre& p, unsigned hb, unsigned k3, int lane) {
  HWin h[3];
  v4u y0, y1, cl;
#pragma unroll
  for (int k = 0; k < 4; ++k) {
    y0[k] = __builtin_amdgcn_update_dpp(0u, p.x0[k], 0x138, 0xf, 0xf, false);
    y1[k] = __builtin_amdgcn_update_dpp((unsigned)__builtin_amdgcn_readlane(p.x0[k], 63), p.x1[k], 0x138, 0xf, 0xf, false);
    cl[k] = __builtin_amdgcn_readlane(p.x1[k], 63);
  }
  hwin(p.x0, y0, h[0]);
  hwin(p.x1, y1, h[1]);
  const unsigned kk = p.kk, k2 = kk | (kk << 16), m0 = lane == 0 ? ~0u : 0u, m1 = lane == 1 ? ~0u : 0u;
  hwin(v4u{0, (kk << 16) & m0, k2 & m0, k2 & m0},
       v4u{cl[0] & m0, (cl[1] & m0) | ((kk << 16) & m1), (cl[2] & m0) | (k2 & m1), (cl[3] & m0) | (k2 & m1)}, h[2]);
  unsigned ka = k3 + (254 - 2 * (lane & 15) + 8 * (lane >> 4)) * 2 - 448, a0 = hb + (1 + lane) * 16, a2 = hb + (lane < 3 ? 129 + lane : lane == 3 ? 0 : 132) * 16;
  unsigned kw = k3 + lane * 4;
  asm volatile("" : "+v"(ka), "+v"(a0), "+v"(a2), "+v"(kw));
  STAMP(3);
  *(LAS unsigned*)lds(kw) = p.k3;
  if (lane < 16) *(LAS unsigned*)lds(kw + 256) = 0;
#pragma unroll
  for (int f = 0; f < 4; ++f) {
    v4u t;
#pragma unroll
    for (int e = 0; e < 4; ++e) t[e] = *(LAS unsigned*)lds(ka + (3 - f) * 64 + e * 4);
    p.kt[f] = __builtin_bit_cast(bf16x8, t);
  }
  hwrite(h[0], a0);
  hwrite(h[1], a0 + 1024);
  if (lane < 5) hwrite(h[2], a2);
}

DI void conv(rsrc_t rw, int cw, int d, int lane, const ConvPre& p, unsigned go) {
  const unsigned hb = L_H + cw * H_SZ, ub = L_U + cw * U_SZ;
  const int c = lane & 15, g = lane >> 4;
  unsigned aa = hb + (c & 7) * H_CS + (3 + (c >> 3) - g) * 16, ab = ub + 32 + 32 * c + 16 * g;
  f32x4 acc[8] = {};
  v16u fa, fb0, fb1;
  asm volatile(CONV_FIRST_ASM : "=&{v[96:111]}"(fa), "=&{v[112:127]}"(fb0), "=&{v[128:143]}"(fb1) : [aa] "v"(aa), [ab] "v"(ab) : "memory");
  {
    const bf16x8 fx = rd128(aa + 2048);
    bf16x8 ue[4];
#pragma unroll
    for (int f = 0; f < 4; ++f) ue[f] = rd128(ab + 432 + f * 512);
#pragma unroll 1
    for (int spin = 0; spin < 64 * SPIN_MAX && *(volatile LAS unsigned*)lds(go) == 0; ++spin) {}
    asm volatile("s_waitcnt lgkmcnt(0)" : "+{v[96:111]}"(fa), "+{v[112:127]}"(fb0), "+{v[128:143]}"(fb1)::"memory");
    STAMP(5);
    __builtin_amdgcn_s_setprio(CPRIO);
#pragma unroll
    for (int f = 0; f < 4; ++f) {
      const bf16x8 uo = __builtin_bit_cast(bf16x8, v4u{fb0[4 * f], fb0[4 * f + 1], fb0[4 * f + 2], fb0[4 * f + 3]});
      acc[4 + f] = mfma(fx, uo, acc[4 + f]);
      const f32x4 ve = mfma(ue[f], p.b9, f32x4{0.f, 0.f, 0.f, 0.f}), vo = mfma(uo, p.b7, f32x4{0.f, 0.f, 0.f, 0.f});
      const bf16x8 vf = __builtin_bit_cast(bf16x8, v4u{pk(ve[0], vo[0]), pk(ve[1], vo[1]), pk(ve[2], vo[2]), pk(ve[3], vo[3])});
#pragma unroll
      for (int n = 4 + f; n < 8; ++n) acc[n] = mfma(vf, p.kt[n - f - 4], acc[n]);
    }
  }
  STAMP(8);
  f32x16 c0, c1;
#pragma unroll
  for (int i = 0; i < 16; ++i) { c0[i] = acc[i >> 2][i & 3]; c1[i] = acc[4 + (i >> 2)][i & 3]; }
  asm volatile("s_nop 7\n\t" CONV_MAIN_ASM
               : "+{v[64:79]}"(c0), "+{v[80:95]}"(c1), "+{v[96:111]}"(fa), "+{v[112:127]}"(fb0), "+{v[128:143]}"(fb1)
               : [aa] "v"(aa), [ab] "v"(ab) : "memory", CONV_MAIN_CLOB);
#pragma unroll
  for (int i = 0; i < 16; ++i) { acc[i >> 2][i & 3] = c0[i]; acc[4 + (i >> 2)][i & 3] = c1[i]; }
#pragma unroll
  for (int n = 0; n < 8; n += 2) {
    const auto s0 = __builtin_amdgcn_permlane16_swap(pk(acc[n][0], acc[n][1]), pk(acc[n + 1][0], acc[n + 1][1]), false, false);
    const auto s1 = __builtin_amdgcn_permlane16_swap(pk(acc[n][2], acc[n][3]), pk(acc[n + 1][2], acc[n + 1][3]), false, false);
    st128<SC1>(v4u{s0[0], s1[0], s0[1], s1[1]}, rw, WS_AT + d * 4096 + 32 * c + 16 * (g >> 1) + 512 * (n + (g & 1)));
  }
}
template <int D0, int ND> DI void finish(rsrc_t ro, int w, int lane, unsigned oo, const f32x4 (&own)[ND], const v2u (&xr)[ND]) {
  f32x4 p[3][ND]; v2u a[ND];
#pragma unroll
  for (int k = 1; k < 4; ++k)
#pragma unroll
    for (int i = 0; i < ND; ++i) p[k - 1][i] = *(LAS f32x4*)lds(((w + k) & 3) * 20480 + lane * 16 + (3 - k) * 5120 + (D0 + i) * 1024);
  const unsigned at = L_AT + (4 * (lane >> 4) + ((lane >> 2) & 3)) * 128 + (16 * w + 4 * (lane & 3)) * 2;
#pragma unroll
  for (int i = 0; i < ND; ++i) a[i] = __builtin_bit_cast(v2u, __builtin_amdgcn_ds_read_tr16_b64_v4i16((LAS s16x4*)lds(at + (D0 + i) * 2048)));
#pragma unroll
  for (int i = 0; i < ND; ++i) {
    const f32x4 r = (own[i] + f32x4{blo(xr[i].x), bhi(xr[i].x), blo(xr[i].y), bhi(xr[i].y)}) + (p[0][i] + p[1][i]) +
                    (p[2][i] + f32x4{blo(a[i].x), bhi(a[i].x), blo(a[i].y), bhi(a[i].y)});
    __builtin_amdgcn_raw_buffer_store_b64(v2u{pk(r[0], r[1]), pk(r[2], r[3])}, ro, oo + (D0 + i) * 32, 0, 0);
  }
}
#endif

__global__ __launch_bounds__(512, 2) void fused_s4_ffn(
    const uint16_t* __restrict__ x, const uint16_t* __restrict__ Are, const uint16_t* __restrict__ Aim, const uint16_t* __restrict__ Bre,
    const uint16_t* __restrict__ Bim, const uint16_t* __restrict__ Cre, const uint16_t* __restrict__ Cim, const uint16_t* __restrict__ logdt,
    const uint16_t* __restrict__ dskip, const uint16_t* __restrict__ wg, const uint16_t* __restrict__ wu, const uint16_t* __restrict__ wd,
    uint16_t* __restrict__ out, void* ws) {
#if __HIP_DEVICE_COMPILE__
  const int wid = __builtin_amdgcn_readfirstlane(threadIdx.x >> 6), lane = threadIdx.x & 63;
  const int b = blockIdx.x, xcd = b & 7, j = b >> 3;
  const bool normal = j < 30;
  const int rl = j % 5, ti = 5 * (xcd & 1) + rl;
  const int mm_ = 2 * (xcd >> 1) + (xcd & 1), par = j - 30;
  const int ch = normal ? 6 * (xcd >> 1) + j / 5 : 3 * mm_ + 2 * par;
  const int ch2 = 3 * mm_ + 1;
  const int nch = normal ? (j < 20 ? 3 : 2) : 0;
  const int d0 = 80 * xcd + (j < 20 ? 3 * j : 2 * j + 20);
  const int t64 = 8 * (xcd >> 1) + (j >> 2), dj = 4 * (xcd & 1) + (j & 3);
  const int rot = XROT ? (ti + (5 * (j / 5) + 1) / 3) % 10 : ti;
  const int rot2 = ROT2 == 6 ? 6 * ((j >> 1) & 1) : ROT2 * (j & 3);
  auto kst = [&](int s) { return s + rot2 >= 12 ? s + rot2 - 12 : s + rot2; };
  auto pbase = [](int w) -> unsigned { return w * 20480; };
  const rsrc_t rw = mkr(ws, WS_BYTES), rx = mkr(x, L * D * 2);
  auto finish_hi = [&]() {
    const int w = wid - 4;
    const unsigned oo = ((4 * t64 + w) * 16 + (lane & 15)) * 1280 + (80 * dj + 4 * (lane >> 4)) * 2;
    v2u xr[2];
#pragma unroll
    for (int i = 0; i < 2; ++i) xr[i] = __builtin_amdgcn_raw_buffer_load_b64(rx, oo + (3 + i) * 32, 0, 0);
    wait_vm<0>();
    BAR(11);
    const f32x4 own[2] = {*(LAS f32x4*)lds(pbase(w) + lane * 16 + 15360), *(LAS f32x4*)lds(pbase(w) + lane * 16 + 16384)};
    finish<3, 2>(mkr(out, L * D * 2), w, lane, oo, own, xr);
  };
  STAMP(0);
  if (wid == 6) {
    u64 pc; asm volatile("s_getpc_b64 %0" : "=s"(pc));
    dma<0>(srd((const void*)pc, CODE_BYTES), L_DUMMY, lane * 16, j * 1024);
    dma<0>(srd((const void*)pc, CODE_BYTES), L_DUMMY, lane * 16, 32768 + j * 1024);
  }
  if (wid == 7) { if (lane < 7) *(LAS v4u*)lds(L_MISC + 16 + lane * 16) = v4u{0, 0, 0, 0}; wait_lg(); }
  asm volatile("" ::"s"(x), "s"(Are), "s"(Aim), "s"(Bre), "s"(Bim), "s"(Cre), "s"(Cim), "s"(logdt), "s"(dskip), "s"(wg), "s"(wu), "s"(wd), "s"(out), "s"(ws));
  unsigned pw[6], ldw_ = 0, dsw_ = 0;
  const int kd = d0 + (wid & 3);
  if ((wid & 3) < nch) {
    const uint16_t* const par6[6] = {Are, Aim, Bre, Bim, Cre, Cim};
    const unsigned vo = wid >= 4 ? lane * 4 : 256 + (lane >> 1) * 4;
#pragma unroll
    for (int i = 0; i < 6; ++i) pw[i] = __builtin_amdgcn_raw_buffer_load_b32(mkr(par6[i], D * NS * 2), vo, kd * NS * 2, NT_);
    ldw_ = ((const __attribute__((address_space(4))) unsigned*)logdt)[kd >> 1];
    dsw_ = ((const __attribute__((address_space(4))) unsigned*)dskip)[kd >> 1];
  }
  ConvPre pre;
  if (wid >= 4) conv_masks(lane, pre);
  G1Ld gl = g1_lanes(lane);
  asm volatile("" : "+v"(gl.x[0]), "+v"(gl.x[1]), "+v"(gl.x[2]), "+v"(gl.x[3]), "+v"(gl.w[0]), "+v"(gl.w[1]), "+v"(gl.w[2]), "+v"(gl.w[3]));
  cfence(); bar(); cfence();
  STAMP(11);
  const float dt = rbf(expf((kd & 1) ? bhi(ldw_) : blo(ldw_)));
  const v4u sg = srd(wg, D * F * 2), su = srd(wu, D * F * 2), sx = srd(x, L * D * 2), sw = srd(ws, WS_BYTES);

  if (wid < 4) {
    if (wid < nch) {
      f32x4 ka[2][2];
#pragma unroll
      for (int i = 0; i < 6; ++i) pw[i] = (lane & 1) ? pw[i] >> 16 : pw[i];
      kgen<1>(pw, dt, lane, L_KG + wid * KG_SZ + 16 * 544, 0, ka);
#pragma unroll
      for (int i = 0; i < 4; ++i) *(LAS f32x4*)lds(L_KG + wid * KG_SZ + 16 * 544 + i * 1024 + lane * 16) = ka[i >> 1][i & 1];
      wait_lg();
      lds_set(L_MISC + 16 + wid * 4);
      STAMP(12);
    } else if (wid == 3 && normal) {
      const v4u sd = srd(wd, F * D * 2);
      const unsigned vd = (lane >> 3) * 1280 + (lane & 7) * 16;
#pragma unroll
      for (int p = 0; p < 2; ++p) dma4<NT_>(sd, L_WDU + p * 4096, vd, vd + 9216, vd + 18432, vd + 27648, (ch * 64 + p * 32) * 1280 + ti * 128);
      const unsigned vt = (lane >> 3) * 3072 + (lane & 7) * 16;
      v4u sink = {0, 0, 0, 0};
#pragma unroll
      for (int p = 0; p < 8; ++p) {
        touch(sink, sg, vt, ch * 128 + (rot * 64 + p * 8) * 3072);
        touch(sink, su, vt, ch * 128 + (rot * 64 + p * 8) * 3072);
      }
      wait_vm<16>();
      const unsigned a0 = L_WDU + (4 * (lane >> 4) + ((lane >> 2) & 3)) * 128 + 8 * (lane & 3);
#pragma unroll
      for (int p = 0; p < 8; ++p) {
        const unsigned a = a0 + (p >> 2) * 4096 + (p & 3) * 32;
        st128<SC1>(__builtin_bit_cast(v4u, trd(a, a + 2048)), rw, lane * 16 + WS_WDT + ((2 * ch + (p >> 2)) * 40 + 4 * ti + (p & 3)) * 1024);
      }
      STAMP(12);
      asm volatile("s_waitcnt vmcnt(8)" : "+v"(sink)::"memory");
    }
    G1Rd rdr;
    if (normal) {
      f32x4 acc[4][6];
      G1Acc z;
      g1_setup<6>(wid, lane, rdr, acc);
      g1_setup6(wid, lane, z);
      if (wid < 2) {
        lds_wait(L_MISC + 68); lds_wait(L_MISC + 72);
        if (nch == 3) lds_wait(L_MISC + 76);
        STAMP(10);
        g1_issue<6>(sx, sg, su, gl, wid, rot, 0, 192 * ti, ch);
        lds_set(L_MISC + 40 + wid * 4);
        wait_vm<0>();
      } else if (wid == 2) {
        lds_wait(L_MISC + 40); lds_wait(L_MISC + 44);
        STAMP(10);
#pragma unroll 1
        for (int h = 0; h < 2; ++h) g1_issue<6>(sx, sg, su, gl, h, rot == 9 ? 0 : rot + 1, 2 * STAGE, 192 * ti, ch);
      }
      STAMP(1);
      G1Ep<6> ep;
      g1_main6(rdr, z, acc, ep);
      lds_set(L_MISC + 112 + wid * 4);
      STAMP(3);
      g1_epilogue<6, true>(rw, wid, lane, 12 * ti, ch, ep, acc);
    } else {
      f32x4 acc4[4][4], acc2[4][2];
      G1Ep<4> ep4; G1Ep<2> ep2;
      g1_setup<2>(wid, lane, rdr, acc2);
      g1_setup<4>(wid, lane, rdr, acc4);
      g1_main_rem(rdr, (64 * par - 32 * (wid >> 1)) * 128, acc4, acc2);
      lds_set(L_MISC + 112 + wid * 4);
      lds_wait(L_MISC + 96);
      STAMP(3);
      g1_ep_read<4>(wid, lane, L_ST, L_CS, ep4);
      g1_ep_read<2>(wid, lane, L_ST + par * 1024, L_CS + 512, ep2);
      g1_epilogue<4, false>(rw, wid, lane, 120, ch, ep4, acc4);
      g1_epilogue<2, true>(rw, wid, lane, 120 + 4 * par, ch2, ep2, acc2);
    }
    STAMP(4); RSTAMP(32);
    const v2u tag = tag_of();

    const unsigned wso = WS_WDT + (12 * wid * 40 + 5 * dj) * 1024, hso = WS_H + (4 * t64 * 48 + 12 * wid) * 1024;
    const unsigned vo0 = lane * 16, vo1 = lane * 16 + 4096, pw = pbase(wid) + lane * 16;
    v4u wr[5][5], hr[3][4];
#pragma unroll
    for (int i = 0; i < 25; ++i) asm volatile("" : "=v"(wr[i / 5][i % 5]));
#pragma unroll
    for (int i = 0; i < 12; ++i) asm volatile("" : "=v"(hr[i / 4][i % 4]));
    auto ld1 = [&](v4u& dst, unsigned vo, unsigned so, int imm) {
      if (imm == 0) asm volatile("buffer_load_dwordx4 %0, %1, %2, %3 offen sc1" : "+v"(dst) : "v"(vo), "s"(sw), "s"(so) : "memory");
      if (imm == 1) asm volatile("buffer_load_dwordx4 %0, %1, %2, %3 offen offset:1024 sc1" : "+v"(dst) : "v"(vo), "s"(sw), "s"(so) : "memory");
      if (imm == 2) asm volatile("buffer_load_dwordx4 %0, %1, %2, %3 offen offset:2048 sc1" : "+v"(dst) : "v"(vo), "s"(sw), "s"(so) : "memory");
      if (imm == 3) asm volatile("buffer_load_dwordx4 %0, %1, %2, %3 offen offset:3072 sc1" : "+v"(dst) : "v"(vo), "s"(sw), "s"(so) : "memory");
    };
    auto ldw = [&](int s, int dd) { ld1(wr[s % 5][dd], dd < 4 ? vo0 : vo1, wso + kst(s) * 40960, dd & 3); };
    unsigned ht[4];
#pragma unroll
    for (int t = 0; t < 4; ++t) ht[t] = vo0 + ((wid + 1 + t) & 3) * 49152;
    auto ldh = [&](int s, int t) { ld1(hr[s % 3][t], ht[t], hso + kst(s) * 1024, 0); };
#pragma unroll
    for (int i = 0; i < NEARLY; ++i) ldw(i / 5, i % 5);
    wait_vm<NEARLY>();
    STAMP(5); RSTAMP(33);
    if (lane == 0) put_flag(rw, WS_F1 + ((normal ? ti * 24 + ch : 240 + 2 * mm_ + par) * 4 + wid) * 8, tag);
#pragma unroll
    for (int i = NEARLY; i < 25; ++i) ldw(i / 5, i % 5);
    if (STGB < 4) {
      lds_wait4(L_MISC + 112);
      if (wid < 4 - STGB) {
#pragma unroll
        for (int s = 0; s < NSTG; ++s) dma5(sw, pbase(wid) + s * 5120, vo0, wso + kst(12 - NSTG + s) * 40960);
      }
    }
    lds_set(L_MISC + 80 + wid * 4);
    __builtin_amdgcn_s_waitcnt(0x0F70);
    cfence();
#pragma unroll 1
    for (int spin = 0; spin < 64 * SPIN_MAX && *(volatile LAS unsigned*)lds(L_MISC + 32) == 0; ++spin) {}
    cfence();
    STAMP(6);
#pragma unroll
    for (int i = 0; i < 12; ++i) {
      ldh(i / 4, i % 4);
      if (HGAP && (i == 3 || i == 7)) __builtin_amdgcn_s_sleep(HGAP);
    }
    f32x4 acc[4][5];
    const int c = lane & 15, g = lane >> 4;
    const unsigned oo = ((4 * t64 + wid) * 16 + c) * 1280 + (80 * dj + 4 * g) * 2;
    v2u xr[3];
    auto mm1 = [&](int s, int t, int dd) {
      if (s == 0) asm volatile("v_mfma_f32_16x16x32_bf16 %0, %1, %2, 0" : "=&v"(acc[t][dd]) : "v"(wr[0][dd]), "v"(hr[0][t]));
      else asm volatile("v_mfma_f32_16x16x32_bf16 %0, %1, %2, %0" : "+v"(acc[t][dd]) : "v"(wr[s % 5][dd]), "v"(hr[s % 3][t]));
    };
#pragma unroll
    for (int s = 0; s < 10; ++s) {
      if (s == 12 - NSTG) wait_lg();
      asm volatile("s_waitcnt vmcnt(%0)" ::"i"(g2_wait(s)) : "memory");
#pragma unroll
      for (int t = 0; t < 4; ++t) {
#pragma unroll
        for (int dd = 0; dd < 5; ++dd) {
          mm1(s, t, dd);
          if (t == 3 && s >= 7 - NSTG && s < 7)
            asm volatile("ds_read_b128 %0, %1 offset:%2" : "+v"(wr[s % 5][dd]) : "v"(pw), "i"((s - 7 + NSTG) * 5120 + dd * 1024) : "memory");
        }
        if (s + 3 < 12) ldh(s + 3, t);
        if (g2_piece(s, t) >= 0) ldw(5 + g2_piece(s, t) / 5, g2_piece(s, t) % 5);
      }
      if (s == G2SIG && wid == 0) lds_set(L_MISC + 36);
    }
#pragma unroll
    for (int dd = 0; dd < 3; ++dd) asm volatile("buffer_load_dwordx2 %0, %1, %2, 0 offen offset:%3" : "=&v"(xr[dd]) : "v"(oo), "s"(sx), "i"(dd * 32) : "memory");
#pragma unroll
    for (int t = 0; t < 4; ++t) {
      asm volatile("s_waitcnt vmcnt(%0)" ::"i"(6 - t) : "memory");
#pragma unroll
      for (int i = 0; i < 10; ++i) {
        mm1(10 + i / 5, t, i % 5);
        if (t > 0 && i >= 3 && i < 8)
          asm volatile("ds_write_b128 %0, %1 offset:%2" ::"v"(pw), "v"(acc[t - 1][i - 3]), "i"((t - 1) * 5120 + (i - 3) * 1024) : "memory");
      }
    }
    asm volatile("s_nop 7" : "+v"(acc[3][0]), "+v"(acc[3][1]), "+v"(acc[3][2]), "+v"(acc[3][3]), "+v"(acc[3][4]));
    *(LAS f32x4*)lds(pw + 15360) = acc[3][3];
    *(LAS f32x4*)lds(pw + 16384) = acc[3][4];
    STAMP(7);
    asm volatile("s_waitcnt vmcnt(0) lgkmcnt(0)" : "+v"(xr[0]), "+v"(xr[1]), "+v"(xr[2])::"memory");
    BAR(11);
    STAMP(8);
    const f32x4 own[3] = {acc[3][0], acc[3][1], acc[3][2]};
    finish<0, 3>(mkr(out, L * D * 2), wid, lane, oo, own, xr);
    STAMP(9);
  } else if (wid < 7) {
    const int cw = wid - 4;
    v4u sink = {0, 0, 0, 0};
    auto warm_wd = [&]() {
#pragma unroll
      for (int i = 0; i < 3; ++i) {
        const unsigned so = WS_WDT + ((6 * (j >> 2) + 3 * cw + i) * 40 + 5 * dj) * 1024;
        touch4(sink, sw, lane * 16, so); touch(sink, sw, lane * 16, so + 4096);
      }
      wait_vm<15>();
    };
    if (normal) {
      LnOut lo2;
      const bool ln2 = cw == 2 && (j == 20 || j == 21);
      if (cw < nch) {
        f32x4 ka[2][2];
        kgen<2>(pw, dt, lane, L_KG + cw * KG_SZ, L_MISC + 68 + cw * 4, ka);
        STAMP(12);
        lds_wait(L_MISC + 16 + cw * 4);
#pragma unroll
        for (int i = 0; i < 4; ++i) ka[i >> 1][i & 1] += *(LAS f32x4*)lds(L_KG + cw * KG_SZ + 16 * 544 + i * 1024 + lane * 16);
        if (lane == 0) ka[0][0][0] += (kd & 1) ? bhi(dsw_) : blo(dsw_);
#pragma unroll
        for (int rt = 0; rt < 2; ++rt)
          *(LAS v4u*)lds(L_RAW + cw * 2048 + rt * 1024 + lane * 16) =
              v4u{pk(ka[rt][0][0], ka[rt][0][1]), pk(ka[rt][0][2], ka[rt][0][3]), pk(ka[rt][1][0], ka[rt][1][1]), pk(ka[rt][1][2], ka[rt][1][3])};
        STAMP(2);
        conv_pre(L_RAW + cw * 2048, lane, pre);
      } else if (ln2) {
        ln8(rx, 240 + 2 * xcd + j - 20, lane, L_KG + 2 * KG_SZ, lo2);
      }
      wait_lg();
      STAMP(1);
      if (cw < 2) {
        g1_loader<20, 2>(rot, STAGE, [&](int s, unsigned so) { g1_issue<6>(sx, sg, su, gl, cw, s, so, 192 * ti, ch); });
        warm_wd();
      } else {
        BAR(0);
        colsum(rw, WS_CSP + (ch * 10 + ti) * 512, 0, lane); colsum(rw, WS_CSP + (ch * 10 + ti) * 512, 1, lane);
        if (LNB == 0 && ln2) ln8_store(rw, 240 + 2 * xcd + j - 20, lane, lo2);
        BAR(1);
        if (LNB == 1 && ln2) ln8_store(rw, 240 + 2 * xcd + j - 20, lane, lo2);
        if (LNB == 1) BAR(2);
        wait_vm<0>();
        if (LNB == 0) BAR(2);
#pragma unroll 1
        for (int k = 3; k < 9; ++k) BAR(k);
      }
      BAR(9);
      if (cw < nch) conv_image(pre, L_H + cw * H_SZ, L_K3 + cw * 320, lane);
    } else if (cw < 2) {
      g1_loader<24, 0>(5 * (xcd & 1), 0, [&](int s, unsigned so) { g1_issue_rem(sx, sg, su, gl, cw, s, so, ch, ch2); });
      wait_vm<0>(); BAR(9);
      warm_wd();
    } else {
#pragma unroll 1
      for (int s = 0; s < 10; ++s) bar();
    }
    asm volatile("s_waitcnt vmcnt(0)" : "+v"(sink)::"memory");
    STAMP(4);
    if (cw < nch) {
      lds_wait(L_MISC + 64);
      conv(rw, cw, d0 + cw, lane, pre, L_MISC + 48 + cw * 4);
      STAMP(6);
      lds_set(L_MISC + 100 + cw * 4);
      wait_vm<0>();
      if (lane == 0) put_flag(rw, WS_F2 + (d0 + cw) * 8, tag_of());
    }
    STAMP(7);
    finish_hi();
  } else {
    const v2u tag = epoch_tag(rw);
    const int tb = 120 * (xcd & 1) + 30 * (xcd >> 1) + j;
    LnOut lo;
    if (normal) ln8(rx, tb, lane, L_LN, lo);
    if (lane == 0) *(LAS v4u*)lds(L_MISC) = v4u{tag.x, tag.y, 0, 0};
    wait_lg();
    STAMP(1);
    BAR(0);
    if (normal) {
      colsum(rw, WS_CSP + (ch * 10 + ti) * 512, 2, lane); colsum(rw, WS_CSP + (ch * 10 + ti) * 512, 3, lane);
      if (LNB == 0) ln8_store(rw, tb, lane, lo);
    }
    BAR(1);
    if (LNB == 1 && normal) ln8_store(rw, tb, lane, lo);
    BAR(2);
    if (LNB == 1) BAR(3);
    wait_vm<0>();
    if (lane == 0) put_flag(rw, WS_F0 + b * 8, tag);
    if (LNB == 0) BAR(3);
    BAR(4); BAR(5);
    if (!normal) { bar(); bar(); }
    Poll4 p0;
    poll4_issue(rw, WS_F0, 256, lane, p0);
    BAR(6);
    poll4_finish(rw, WS_F0, 256, tag, lane, p0);
    STAMP(2);
    if (b == 0 && lane == 0) put_flag(rw, WS_EPOCH, tag);
    const int row0 = normal ? 192 * ti : 1920;
    v2u st[3], cp[2][10];
#pragma unroll
    for (int k = 0; k < 3; ++k) { const int r = row0 + lane + 64 * k; st[k] = __builtin_amdgcn_raw_buffer_load_b64(rw, (r >> 3) * 128 + (r & 7) * 8, WS_STATS, SC1); }
#pragma unroll
    for (int k = 0; k < 10; ++k) cp[0][k] = __builtin_amdgcn_raw_buffer_load_b64(rw, lane * 8 + k * 512, WS_CSP + ch * 5120, SC1);
    if (!normal) {
#pragma unroll
      for (int k = 0; k < 10; ++k) cp[1][k] = __builtin_amdgcn_raw_buffer_load_b64(rw, lane * 8 + k * 512, WS_CSP + ch2 * 5120, SC1);
    }
    cfence();
    BAR(7);
    f32x4 so[3];
#pragma unroll
    for (int k = 0; k < 3; ++k) {
      const float m = __uint_as_float(st[k].x), den = __uint_as_float(st[k].y);
      so[k] = f32x4{-m, den * den, -1.44269504f * __builtin_amdgcn_rcpf(den), 0.f};
    }
    f32x2 co[2];
#pragma unroll
    for (int h = 0; h < 2; ++h) {
      float c0 = 0.f, c1 = 0.f;
      if (h == 0 || !normal) {
#pragma unroll
        for (int k = 0; k < 10; ++k) { c0 += __uint_as_float(cp[h][k].x); asm volatile("" : "+v"(c0)); c1 += __uint_as_float(cp[h][k].y); }
      }
      co[h] = f32x2{c0, c1};
    }
    asm volatile("" : "+v"(so[0]), "+v"(so[1]), "+v"(so[2]), "+v"(co[0]), "+v"(co[1]));
    if (normal) { BAR(8); __builtin_amdgcn_s_sleep(2); }
#pragma unroll
    for (int k = 0; k < 3; ++k) *(LAS f32x4*)lds(L_ST + (lane + 64 * k) * 16) = so[k];
#pragma unroll
    for (int h = 0; h < 2; ++h) *(LAS f32x2*)lds(L_CS + h * 512 + lane * 8) = co[h];
    v4u ur[16];
    auto ldu = [&](int p) { ur[p] = __builtin_amdgcn_raw_buffer_load_b128(rw, (lane >> 2) * 10240 + (lane & 3) * 16, WS_U8 + p * 163840 + d0 * 16, SC1); };
    if ((lane & 3) < nch) {
#pragma unroll
      for (int p = 0; p < NU1; ++p) ldu(p);
    }
    wait_lg();
    if (normal) BAR(9); else lds_set(L_MISC + 96);
    if ((lane & 3) < nch) {
#pragma unroll
      for (int p = NU1; p < 16; ++p) ldu(p);
    }
    if (lane < 32) {
#pragma unroll
      for (int k = 0; k < 3; ++k) *(LAS v4u*)lds(L_U + k * U_SZ + lane * 16) = v4u{0, 0, 0, 0};
    }
    if ((lane & 3) < nch) {
#pragma unroll
      for (int p = 0; p < NU1; ++p) *(LAS v4u*)lds(L_U + (lane & 3) * U_SZ + 512 + p * 256 + (lane >> 2) * 16) = ur[p];
    }
    if ((lane & 3) < nch) {
#pragma unroll
      for (int p = NU1; p < 16; ++p) *(LAS v4u*)lds(L_U + (lane & 3) * U_SZ + 512 + p * 256 + (lane >> 2) * 16) = ur[p];
    }
    wait_lg();
    lds_set(L_MISC + 64);
    STAMP(4);
    lds_wait4(L_MISC + 112);
#pragma unroll 1
    for (int w = 3; w >= 4 - STGB; --w) {
#pragma unroll
      for (int s = 0; s < NSTG; ++s) {
        dma5(sw, pbase(w) + s * 5120, lane * 16, WS_WDT + ((12 * w + kst(12 - NSTG + s)) * 40 + 5 * dj) * 1024);
        if (PACE) __builtin_amdgcn_s_sleep(PACE);
      }
    }
    STAMP(3);
    wait_vm<0>();
    lds_wait4(L_MISC + 80);
    STAMP(8);
    poll<2>(rw, WS_F1 + (t64 < 30 ? (t64 / 3) * 768 : 7680), t64 < 30 ? 96 : 64, tag, lane);
    lds_set(L_MISC + 32);
    STAMP(5); RSTAMP(34);
    lds_wait(L_MISC + 36);
    poll<2>(rw, WS_F2 + dj * 640, 80, tag, lane);
#pragma unroll 1
    for (int k = 0; k < nch; ++k) lds_wait(L_MISC + 100 + k * 4);
    STAMP(6);
    const unsigned va = (lane >> 3) * 4096 + (lane & 7) * 16, sa = WS_AT + 80 * dj * 4096 + t64 * 128;
    dma4<SC1>(sw, L_AT, va, va + 31744, va + 63488, va + 95232, sa);
    dma4<SC1>(sw, L_AT + 4096, va, va + 31744, va + 63488, va + 95232, sa + 131072);
    dma<SC1>(sw, L_AT + 8192, va, sa + 262144); dma<SC1>(sw, L_AT + 9216, va, sa + 294912);
    STAMP(7);
    finish_hi();
  }
  DUMP();
#endif
}

extern "C" void kernel_launch(void* const* d_in, const int* in_sizes, int n_in, void* d_out, int out_size, void* d_ws, size_t ws_size,
                              hipStream_t stream) {
  (void)hipFuncSetAttribute((const void*)fused_s4_ffn, hipFuncAttributeMaxDynamicSharedMemorySize, LDS_BYTES);
  fused_s4_ffn<<<256, 512, LDS_BYTES, stream>>>(
      (const uint16_t*)d_in[0], (const uint16_t*)d_in[1], (const uint16_t*)d_in[2], (const uint16_t*)d_in[3], (const uint16_t*)d_in[4],
      (const uint16_t*)d_in[5], (const uint16_t*)d_in[6], (const uint16_t*)d_in[7], (const uint16_t*)d_in[8], (const uint16_t*)d_in[10],
      (const uint16_t*)d_in[11], (const uint16_t*)d_in[12], (uint16_t*)d_out, d_ws);
}
```

```cpp
#define BENCH
#define PKIND 6
#define PW 0
#define PS 0
#include <hip/hip_runtime.h>
#include <stdint.h>

typedef short bf16x8 __attribute__((ext_vector_type(8)));
typedef short s16x4 __attribute__((ext_vector_type(4)));
typedef float f32x4 __attribute__((ext_vector_type(4)));
typedef float f32x2 __attribute__((ext_vector_type(2)));
typedef __bf16 bf16x2 __attribute__((ext_vector_type(2)));
typedef unsigned v4u __attribute__((ext_vector_type(4)));
typedef unsigned v2u __attribute__((ext_vector_type(2)));
typedef unsigned v8u __attribute__((ext_vector_type(8)));
typedef unsigned v16u __attribute__((ext_vector_type(16)));
typedef float f32x8 __attribute__((ext_vector_type(8)));
typedef float f32x16 __attribute__((ext_vector_type(16)));
typedef unsigned long long u64;
#define LAS __attribute__((address_space(3)))
#define DI __device__ __forceinline__
typedef __amdgpu_buffer_rsrc_t rsrc_t;

constexpr int L = 2048, D = 640, NS = 192, F = 1536;
constexpr int SC1 = 16, NT_ = 2;

constexpr unsigned WS_EPOCH = 0, WS_F0 = 1024, WS_F3 = 4096, WS_F2 = 8192, WS_STATS = 16384, WS_F1 = 49152, WS_CSP = 65536, WS_U8 = 262144,
                   WS_WDT = 3u << 20, WS_H = 5u << 20, WS_AT = 12u << 20, WS_TM = 15u << 20, WS_BYTES = 16u << 20;

constexpr unsigned STAGE = 40960, XB = 24576;
constexpr unsigned LX = 3 * STAGE;
constexpr unsigned L_LN = LX, LN_RS = 1296, L_AT = LX;
constexpr unsigned L_RAW = LX + 10240;
constexpr unsigned L_ST = L_RAW + 3 * 2048;
constexpr unsigned L_CS = L_ST + 3072;
constexpr unsigned L_MISC = L_CS + 1024;
constexpr unsigned L_U = L_MISC + 128, U_SZ = 4608;
constexpr unsigned L_K3 = L_U + 3 * U_SZ;
constexpr unsigned L_END = L_K3 + 960;
constexpr unsigned L_WDU = L_U, L_DUMMY = L_U + 8192;
constexpr unsigned L_KG = STAGE, KG_SZ = 16 * (544 + 288);
constexpr unsigned L_H = 2 * STAGE, H_CS = 2112, H_SZ = 8 * H_CS;
constexpr unsigned LDS_BYTES = 163840;
static_assert(L_END <= LDS_BYTES && L_H + 3 * H_SZ + 16 <= L_RAW, "lds");
#define G2AUXW SC1
#define G2AUXH SC1
#define ROT2 0
#define G2SIG 3
#define XROT 0
#define TA 20
#define TB 20
#define LNB 0
#define PDELAY 3
#define CPRIO 2
#define PWAIT 6
#define NEARLY 8
#define NSTG 4
#define GPRIO 0
#define STGB 0
#define PACE 0
#define TRIG 1
#define NU1 0
#define HGAP 2
#define WPS 4
constexpr int SPIN_MAX = 20000;
constexpr unsigned CODE_BYTES = 58368;

constexpr int G2_NW = 5 * (7 - NSTG), G2_W0 = 1;
constexpr int g2_piece(int s, int t) { const int p = WPS * (s - G2_W0) + t; return s >= G2_W0 && t < WPS && p < G2_NW ? p : -1; }
constexpr int g2_wait(int step) {
  int n = 0, need = 0;
  for (int i = 0; i < 12; ++i) { ++n; if (i / 4 == step) need = n; }
  for (int s = 0; s < step; ++s)
    for (int t = 0; t < 4; ++t) {
      if (s + 3 < 12) { ++n; if (s + 3 == step) need = n; }
      const int p = g2_piece(s, t);
      if (p >= 0) { ++n; if (5 + p / 5 == step) need = n; }
    }
  return n - need;
}
static_assert(NSTG == 4 && G2_W0 + (G2_NW - 1) / WPS <= 5, "streamed weights must be issued two steps ahead, into slots already drained");

#if __HIP_DEVICE_COMPILE__
extern __shared__ __attribute__((aligned(16))) char smem_[];
DI LAS char* lds(unsigned o) { return (LAS char*)smem_ + o; }
DI rsrc_t mkr(const void* p, unsigned bytes) { return __builtin_amdgcn_make_buffer_rsrc((void*)p, 0, bytes, 0x00020000); }
DI unsigned pk(float a, float b) { f32x2 f = {a, b}; return __builtin_bit_cast(unsigned, __builtin_convertvector(f, bf16x2)); }
DI unsigned pk2(f32x2 f) { return __builtin_bit_cast(unsigned, __builtin_convertvector(f, bf16x2)); }
DI float blo(unsigned v) { return __uint_as_float(v << 16); }
DI float bhi(unsigned v) { return __uint_as_float(v & 0xffff0000u); }
DI float rbf(float f) { return blo(pk(f, f)); }
DI void bar() { __builtin_amdgcn_s_barrier(); }
DI void cfence() { asm volatile("" ::: "memory"); }
template <int N> DI void wait_vm() { asm volatile("s_waitcnt vmcnt(%0)" ::"i"(N) : "memory"); }
DI void wait_lg() { asm volatile("s_waitcnt lgkmcnt(0)" ::: "memory"); }
DI bf16x8 trd(unsigned a0, unsigned a1) {
  s16x4 lo = __builtin_amdgcn_ds_read_tr16_b64_v4i16((LAS s16x4*)lds(a0));
  s16x4 hi = __builtin_amdgcn_ds_read_tr16_b64_v4i16((LAS s16x4*)lds(a1));
  return __builtin_shufflevector(lo, hi, 0, 1, 2, 3, 4, 5, 6, 7);
}
DI bf16x8 rd128(unsigned a) { return *(LAS bf16x8*)lds(a); }
DI f32x4 mfma(bf16x8 a, bf16x8 b, f32x4 c) { return __builtin_amdgcn_mfma_f32_16x16x32_bf16(a, b, c, 0, 0, 0); }
DI v4u srd(const void* p, unsigned bytes) { const u64 a = (u64)p; return v4u{(unsigned)a, (unsigned)(a >> 32) & 0xffffu, bytes, 0x00020000u}; }
template <int AUX> DI void dma(v4u r, unsigned ldsoff, unsigned voff, unsigned soff) {
  if (AUX == 0) asm volatile("s_mov_b32 m0, %0\n\ts_nop 0\n\tbuffer_load_dwordx4 %1, %2, %3 offen lds" ::"s"(ldsoff), "v"(voff), "s"(r), "s"(soff) : "memory");
  if (AUX == NT_) asm volatile("s_mov_b32 m0, %0\n\ts_nop 0\n\tbuffer_load_dwordx4 %1, %2, %3 offen nt lds" ::"s"(ldsoff), "v"(voff), "s"(r), "s"(soff) : "memory");
  if (AUX == SC1) asm volatile("s_mov_b32 m0, %0\n\ts_nop 0\n\tbuffer_load_dwordx4 %1, %2, %3 offen sc1 lds" ::"s"(ldsoff), "v"(voff), "s"(r), "s"(soff) : "memory");
}
#define DMA4_(POL) asm volatile("s_mov_b32 m0, %0\n\ts_nop 0\n\tbuffer_load_dwordx4 %1, %5, %6 offen " POL "lds\n\t" \
    "buffer_load_dwordx4 %2, %5, %6 offen offset:1024 " POL "lds\n\tbuffer_load_dwordx4 %3, %5, %6 offen offset:2048 " POL "lds\n\t" \
    "buffer_load_dwordx4 %4, %5, %6 offen offset:3072 " POL "lds" ::"s"(ldsoff), "v"(v0), "v"(v1), "v"(v2), "v"(v3), "s"(r), "s"(soff) : "memory")
template <int AUX> DI void dma4(v4u r, unsigned ldsoff, unsigned v0, unsigned v1, unsigned v2, unsigned v3, unsigned soff) {
  if (AUX == 0) DMA4_("");
  if (AUX == NT_) DMA4_("nt ");
  if (AUX == SC1) DMA4_("sc1 ");
}
template <int AUX> DI void st128(v4u v, rsrc_t r, unsigned off) { __builtin_amdgcn_raw_buffer_store_b128(v, r, off, 0, AUX); }
DI void touch(v4u& sink, v4u r, unsigned vo, unsigned so) {
  asm volatile("buffer_load_dwordx4 %0, %1, %2, %3 offen sc1" : "+v"(sink) : "v"(vo), "s"(r), "s"(so) : "memory");
}
DI void touch4(v4u& sink, v4u r, unsigned vo, unsigned so) {
  asm volatile("buffer_load_dwordx4 %0, %1, %2, %3 offen sc1\n\tbuffer_load_dwordx4 %0, %1, %2, %3 offen offset:1024 sc1\n\t"
               "buffer_load_dwordx4 %0, %1, %2, %3 offen offset:2048 sc1\n\tbuffer_load_dwordx4 %0, %1, %2, %3 offen offset:3072 sc1"
               : "+v"(sink) : "v"(vo), "s"(r), "s"(so) : "memory");
}
DI void dma5(v4u r, unsigned ldsoff, unsigned vo, unsigned so) { dma4<SC1>(r, ldsoff, vo, vo, vo, vo, so); dma<SC1>(r, ldsoff + 4096, vo, so + 4096); }
DI float rowsum16(float v) {
  v += __builtin_bit_cast(float, __builtin_amdgcn_update_dpp(0, __builtin_bit_cast(int, v), 0x128, 0xf, 0xf, false));
  v += __builtin_bit_cast(float, __builtin_amdgcn_update_dpp(0, __builtin_bit_cast(int, v), 0x124, 0xf, 0xf, false));
  v += __builtin_bit_cast(float, __builtin_amdgcn_update_dpp(0, __builtin_bit_cast(int, v), 0x122, 0xf, 0xf, false));
  v += __builtin_bit_cast(float, __builtin_amdgcn_update_dpp(0, __builtin_bit_cast(int, v), 0x121, 0xf, 0xf, false));
  return v;
}
DI f32x2 cmul(f32x2 a, f32x2 b) {
  f32x2 t = a * b.xx, r;
  asm("v_pk_fma_f32 %0, %1, %2, %3 op_sel:[1,1,0] op_sel_hi:[0,1,1] neg_lo:[0,1,0]" : "=v"(r) : "v"(a), "v"(b), "v"(t));
  return r;
}
DI f32x2 cpow(float lr, float tr, float e) {
  float m = __builtin_amdgcn_exp2f(lr * e), th = __builtin_amdgcn_fractf(tr * e);
  return f32x2{m * __builtin_amdgcn_cosf(th), m * __builtin_amdgcn_sinf(th)};
}
DI v2u tag_of(void) { return *(LAS v2u*)lds(L_MISC); }
DI v2u epoch_tag(rsrc_t rw) {
  const v2u e = __builtin_amdgcn_raw_buffer_load_b64(rw, WS_EPOCH, 0, SC1);
  const u64 t = (((u64)e.y << 32) | e.x) + 0x9E3779B97F4A7C15ull;
  return v2u{(unsigned)t, (unsigned)(t >> 32)};
}
DI void put_flag(rsrc_t rw, unsigned off, v2u tag) { __builtin_amdgcn_raw_buffer_store_b64(tag, rw, off, 0, SC1); }
template <int R> DI void poll(rsrc_t rw, unsigned off, int n, v2u tag, int lane) {
#pragma unroll 1
  for (int spin = 0; spin < SPIN_MAX; ++spin) {
    bool ok = true;
#pragma unroll
    for (int r = 0; r < R; ++r) {
      int i = lane + 64 * r; i = i < n ? i : n - 1;
      v2u f = __builtin_amdgcn_raw_buffer_load_b64(rw, off + i * 8, 0, SC1);
      ok = ok && f.x == tag.x && f.y == tag.y;
    }
    if (__all(ok)) break;
    cfence();
  }
  cfence();
}
struct Poll4 { v2u f[4]; };
DI void poll4_issue(rsrc_t rw, unsigned off, int n, int lane, Poll4& p) {
#pragma unroll
  for (int r = 0; r < 4; ++r) { int i = lane + 64 * r; i = i < n ? i : n - 1; p.f[r] = __builtin_amdgcn_raw_buffer_load_b64(rw, off + i * 8, 0, SC1); }
  cfence();
}
DI void poll4_finish(rsrc_t rw, unsigned off, int n, v2u tag, int lane, const Poll4& p) {
  bool ok = true;
#pragma unroll
  for (int r = 0; r < 4; ++r) ok = ok && p.f[r].x == tag.x && p.f[r].y == tag.y;
  if (!__all(ok)) poll<4>(rw, off, n, tag, lane);
  cfence();
}
DI void lds_set(unsigned o) { cfence(); *(volatile LAS unsigned*)lds(o) = 1; }
DI void lds_wait(unsigned o) {
#pragma unroll 1
  for (int spin = 0; spin < 16 * SPIN_MAX && *(volatile LAS unsigned*)lds(o) == 0; ++spin) __builtin_amdgcn_s_sleep(1);
  cfence();
}
DI void lds_wait4(unsigned o) {
#pragma unroll 1
  for (int spin = 0; spin < 16 * SPIN_MAX; ++spin) {
    const v4u f = *(volatile LAS v4u*)lds(o);
    if (f[0] & f[1] & f[2] & f[3]) break;
    __builtin_amdgcn_s_sleep(1);
  }
  cfence();
}

DI void stamp_(int i) {
  u64 c; asm volatile("s_memtime %0\n\ts_waitcnt lgkmcnt(0)" : "=s"(c) :: "memory");
  asm volatile("s_mov_b32 m0, %1\n\tv_writelane_b32 v255, %0, m0" :: "s"((unsigned)c), "s"(i) : "memory", "v255");
}
DI void rstamp_(int i) {
  u64 c; asm volatile("s_memrealtime %0\n\ts_waitcnt lgkmcnt(0)" : "=s"(c) :: "memory");
  asm volatile("s_mov_b32 m0, %1\n\tv_writelane_b32 v255, %0, m0" :: "s"((unsigned)c), "s"(i) : "memory", "v255");
}
#define RSTAMP(i) rstamp_(i)
#define STAMP(i) stamp_(i)
#define BAR(i) bar()
#define DUMP() do { u64 t_ = __builtin_amdgcn_s_memrealtime(); unsigned sv_; asm volatile("v_mov_b32 %0, v255" : "=v"(sv_)); \
    unsigned* p_ = (unsigned*)((char*)ws + WS_TM + (blockIdx.x * 8 + wid) * 256); \
    if (lane < 40) p_[lane] = sv_; if (lane == 0) { ((u64*)p_)[24] = T0_; ((u64*)p_)[25] = t_; } } while (0)

#define G1_STAMP "s_memtime s[96:97]\n\ts_waitcnt lgkmcnt(0)\n\tv_writelane_b32 v255, s96, 2\n\ts_memrealtime s[96:97]\n\ts_waitcnt lgkmcnt(0)\n\tv_writelane_b32 v255, s96, 31\n\t"
#define G1_STAMP_CLOB , "s96", "s97", "v255"
#define G1_MAIN_ASM \
  "v_mov_b32 v240, %[a0]\n\t" \
  "v_mov_b32 v241, %[a1]\n\t" \
  "v_mov_b32 v242, %[a2]\n\t" \
  "v_mov_b32 v243, %[a3]\n\t" \
  "v_add_u32 v244, 81920, %[a0]\n\t" \
  "v_add_u32 v245, 81920, %[a1]\n\t" \
  "v_add_u32 v246, 81920, %[a2]\n\t" \
  "v_add_u32 v247, 81920, %[a3]\n\t" \
  "v_add_u32 v248, 40960, %[a0]\n\t" \
  "v_add_u32 v249, 40960, %[a1]\n\t" \
  "v_add_u32 v250, 40960, %[a2]\n\t" \
  "v_add_u32 v251, 40960, %[a3]\n\t" \
  "s_mov_b32 %[cnt], 3\n\t" \
  "s_barrier\n\t" \
  G1_STAMP \
  "ds_read_b64_tr_b16 v[160:161], v240 offset:0\n\t" \
  "ds_read_b64_tr_b16 v[162:163], v240 offset:512\n\t" \
  "ds_read_b64_tr_b16 v[164:165], v241 offset:0\n\t" \
  "ds_read_b64_tr_b16 v[166:167], v241 offset:512\n\t" \
  "ds_read_b64_tr_b16 v[168:169], v240 offset:8192\n\t" \
  "ds_read_b64_tr_b16 v[170:171], v240 offset:8704\n\t" \
  "ds_read_b64_tr_b16 v[172:173], v241 offset:8192\n\t" \
  "ds_read_b64_tr_b16 v[174:175], v241 offset:8704\n\t" \
  "ds_read_b128 v[176:179], v242 offset:0\n\t" \
  "ds_read_b128 v[180:183], v242 offset:2048\n\t" \
  "ds_read_b128 v[184:187], v242 offset:4096\n\t" \
  "ds_read_b128 v[188:191], v242 offset:6144\n\t" \
  "ds_read_b128 v[192:195], v242 offset:8192\n\t" \
  "ds_read_b128 v[196:199], v242 offset:10240\n\t" \
  "s_waitcnt lgkmcnt(0)\n\t" \
  ".Lg1_%=:\n\t" \
  "v_mfma_f32_16x16x32_bf16 v[64:67], v[160:163], v[176:179], v[64:67]\n\t" \
  "ds_read_b64_tr_b16 v[200:201], v240 offset:4096\n\t" \
  "v_mfma_f32_16x16x32_bf16 v[68:71], v[164:167], v[176:179], v[68:71]\n\t" \
  "ds_read_b64_tr_b16 v[202:203], v240 offset:4608\n\t" \
  "v_mfma_f32_16x16x32_bf16 v[72:75], v[168:171], v[176:179], v[72:75]\n\t" \
  "ds_read_b64_tr_b16 v[204:205], v241 offset:4096\n\t" \
  "v_mfma_f32_16x16x32_bf16 v[76:79], v[172:175], v[176:179], v[76:79]\n\t" \
  "ds_read_b64_tr_b16 v[206:207], v241 offset:4608\n\t" \
  "v_mfma_f32_16x16x32_bf16 v[80:83], v[160:163], v[180:183], v[80:83]\n\t" \
  "ds_read_b64_tr_b16 v[208:209], v240 offset:12288\n\t" \
  "v_mfma_f32_16x16x32_bf16 v[84:87], v[164:167], v[180:183], v[84:87]\n\t" \
  "ds_read_b64_tr_b16 v[210:211], v240 offset:12800\n\t" \
  "v_mfma_f32_16x16x32_bf16 v[88:91], v[168:171], v[180:183], v[88:91]\n\t" \
  "ds_read_b64_tr_b16 v[212:213], v241 offset:12288\n\t" \
  "v_mfma_f32_16x16x32_bf16 v[92:95], v[172:175], v[180:183], v[92:95]\n\t" \
  "ds_read_b64_tr_b16 v[214:215], v241 offset:12800\n\t" \
  "v_mfma_f32_16x16x32_bf16 v[96:99], v[160:163], v[184:187], v[96:99]\n\t" \
  "ds_read_b128 v[216:219], v243 offset:0\n\t" \
  "v_mfma_f32_16x16x32_bf16 v[100:103], v[164:167], v[184:187], v[100:103]\n\t" \
  "ds_read_b128 v[220:223], v243 offset:2048\n\t" \
  "v_mfma_f32_16x16x32_bf16 v[104:107], v[168:171], v[184:187], v[104:107]\n\t" \
  "ds_read_b128 v[224:227], v243 offset:4096\n\t" \
  "v_mfma_f32_16x16x32_bf16 v[108:111], v[172:175], v[184:187], v[108:111]\n\t" \
  "ds_read_b128 v[228:231], v243 offset:6144\n\t" \
  "v_mfma_f32_16x16x32_bf16 v[112:115], v[160:163], v[188:191], v[112:115]\n\t" \
  "ds_read_b128 v[232:235], v243 offset:8192\n\t" \
  "v_mfma_f32_16x16x32_bf16 v[116:119], v[164:167], v[188:191], v[116:119]\n\t" \
  "ds_read_b128 v[236:239], v243 offset:10240\n\t" \
  "v_mfma_f32_16x16x32_bf16 v[120:123], v[168:171], v[188:191], v[120:123]\n\t" \
  "v_mfma_f32_16x16x32_bf16 v[124:127], v[172:175], v[188:191], v[124:127]\n\t" \
  "v_mfma_f32_16x16x32_bf16 v[128:131], v[160:163], v[192:195], v[128:131]\n\t" \
  "v_mfma_f32_16x16x32_bf16 v[132:135], v[164:167], v[192:195], v[132:135]\n\t" \
  "v_mfma_f32_16x16x32_bf16 v[136:139], v[168:171], v[192:195], v[136:139]\n\t" \
  "v_mfma_f32_16x16x32_bf16 v[140:143], v[172:175], v[192:195], v[140:143]\n\t" \
  "v_mfma_f32_16x16x32_bf16 v[144:147], v[160:163], v[196:199], v[144:147]\n\t" \
  "v_mfma_f32_16x16x32_bf16 v[148:151], v[164:167], v[196:199], v[148:151]\n\t" \
  "v_mfma_f32_16x16x32_bf16 v[152:155], v[168:171], v[196:199], v[152:155]\n\t" \
  "v_mfma_f32_16x16x32_bf16 v[156:159], v[172:175], v[196:199], v[156:159]\n\t" \
  "s_waitcnt vmcnt(0) lgkmcnt(0)\n\t" \
  "s_barrier\n\t" \
  "v_mfma_f32_16x16x32_bf16 v[64:67], v[200:203], v[216:219], v[64:67]\n\t" \
  "ds_read_b64_tr_b16 v[160:161], v244 offset:0\n\t" \
  "v_mfma_f32_16x16x32_bf16 v[68:71], v[204:207], v[216:219], v[68:71]\n\t" \
  "ds_read_b64_tr_b16 v[162:163], v244 offset:512\n\t" \
  "v_mfma_f32_16x16x32_bf16 v[72:75], v[208:211], v[216:219], v[72:75]\n\t" \
  "ds_read_b64_tr_b16 v[164:165], v245 offset:0\n\t" \
  "v_mfma_f32_16x16x32_bf16 v[76:79], v[212:215], v[216:219], v[76:79]\n\t" \
  "ds_read_b64_tr_b16 v[166:167], v245 offset:512\n\t" \
  "v_mfma_f32_16x16x32_bf16 v[80:83], v[200:203], v[220:223], v[80:83]\n\t" \
  "ds_read_b64_tr_b16 v[168:169], v244 offset:8192\n\t" \
  "v_mfma_f32_16x16x32_bf16 v[84:87], v[204:207], v[220:223], v[84:87]\n\t" \
  "ds_read_b64_tr_b16 v[170:171], v244 offset:8704\n\t" \
  "v_mfma_f32_16x16x32_bf16 v[88:91], v[208:211], v[220:223], v[88:91]\n\t" \
  "ds_read_b64_tr_b16 v[172:173], v245 offset:8192\n\t" \
  "v_mfma_f32_16x16x32_bf16 v[92:95], v[212:215], v[220:223], v[92:95]\n\t" \
  "ds_read_b64_tr_b16 v[174:175], v245 offset:8704\n\t" \
  "v_mfma_f32_16x16x32_bf16 v[96:99], v[200:203], v[224:227], v[96:99]\n\t" \
  "ds_read_b128 v[176:179], v246 offset:0\n\t" \
  "v_mfma_f32_16x16x32_bf16 v[100:103], v[204:207], v[224:227], v[100:103]\n\t" \
  "ds_read_b128 v[180:183], v246 offset:2048\n\t" \
  "v_mfma_f32_16x16x32_bf16 v[104:107], v[208:211], v[224:227], v[104:107]\n\t" \
  "ds_read_b128 v[184:187], v246 offset:4096\n\t" \
  "v_mfma_f32_16x16x32_bf16 v[108:111], v[212:215], v[224:227], v[108:111]\n\t" \
  "ds_read_b128 v[188:191], v246 offset:6144\n\t" \
  "v_mfma_f32_16x16x32_bf16 v[112:115], v[200:203], v[228:231], v[112:115]\n\t" \
  "ds_read_b128 v[192:195], v246 offset:8192\n\t" \
  "v_mfma_f32_16x16x32_bf16 v[116:119], v[204:207], v[228:231], v[116:119]\n\t" \
  "ds_read_b128 v[196:199], v246 offset:10240\n\t" \
  "v_mfma_f32_16x16x32_bf16 v[120:123], v[208:211], v[228:231], v[120:123]\n\t" \
  "v_mfma_f32_16x16x32_bf16 v[124:127], v[212:215], v[228:231], v[124:127]\n\t" \
  "v_mfma_f32_16x16x32_bf16 v[128:131], v[200:203], v[232:235], v[128:131]\n\t" \
  "v_mfma_f32_16x16x32_bf16 v[132:135], v[204:207], v[232:235], v[132:135]\n\t" \
  "v_mfma_f32_16x16x32_bf16 v[136:139], v[208:211], v[232:235], v[136:139]\n\t" \
  "v_mfma_f32_16x16x32_bf16 v[140:143], v[212:215], v[232:235], v[140:143]\n\t" \
  "v_mfma_f32_16x16x32_bf16 v[144:147], v[200:203], v[236:239], v[144:147]\n\t" \
  "v_mfma_f32_16x16x32_bf16 v[148:151], v[204:207], v[236:239], v[148:151]\n\t" \
  "v_mfma_f32_16x16x32_bf16 v[152:155], v[208:211], v[236:239], v[152:155]\n\t" \
  "v_mfma_f32_16x16x32_bf16 v[156:159], v[212:215], v[236:239], v[156:159]\n\t" \
  "s_waitcnt lgkmcnt(0)\n\t" \
  "v_mfma_f32_16x16x32_bf16 v[64:67], v[160:163], v[176:179], v[64:67]\n\t" \
  "ds_read_b64_tr_b16 v[200:201], v244 offset:4096\n\t" \
  "v_mfma_f32_16x16x32_bf16 v[68:71], v[164:167], v[176:179], v[68:71]\n\t" \
  "ds_read_b64_tr_b16 v[202:203], v244 offset:4608\n\t" \
  "v_mfma_f32_16x16x32_bf16 v[72:75], v[168:171], v[176:179], v[72:75]\n\t" \
  "ds_read_b64_tr_b16 v[204:205], v245 offset:4096\n\t" \
  "v_mfma_f32_16x16x32_bf16 v[76:79], v[172:175], v[176:179], v[76:79]\n\t" \
  "ds_read_b64_tr_b16 v[206:207], v245 offset:4608\n\t" \
  "v_mfma_f32_16x16x32_bf16 v[80:83], v[160:163], v[180:183], v[80:83]\n\t" \
  "ds_read_b64_tr_b16 v[208:209], v244 offset:12288\n\t" \
  "v_mfma_f32_16x16x32_bf16 v[84:87], v[164:167], v[180:183], v[84:87]\n\t" \
  "ds_read_b64_tr_b16 v[210:211], v244 offset:12800\n\t" \
  "v_mfma_f32_16x16x32_bf16 v[88:91], v[168:171], v[180:183], v[88:91]\n\t" \
  "ds_read_b64_tr_b16 v[212:213], v245 offset:12288\n\t" \
  "v_mfma_f32_16x16x32_bf16 v[92:95], v[172:175], v[180:183], v[92:95]\n\t" \
  "ds_read_b64_tr_b16 v[214:215], v245 offset:12800\n\t" \
  "v_mfma_f32_16x16x32_bf16 v[96:99], v[160:163], v[184:187], v[96:99]\n\t" \
  "ds_read_b128 v[216:219], v247 offset:0\n\t" \
  "v_mfma_f32_16x16x32_bf16 v[100:103], v[164:167], v[184:187], v[100:103]\n\t" \
  "ds_read_b128 v[220:223], v247 offset:2048\n\t" \
  "v_mfma_f32_16x16x32_bf16 v[104:107], v[168:171], v[184:187], v[104:107]\n\t" \
  "ds_read_b128 v[224:227], v247 offset:4096\n\t" \
  "v_mfma_f32_16x16x32_bf16 v[108:111], v[172:175], v[184:187], v[108:111]\n\t" \
  "ds_read_b128 v[228:231], v247 offset:6144\n\t" \
  "v_mfma_f32_16x16x32_bf16 v[112:115], v[160:163], v[188:191], v[112:115]\n\t" \
  "ds_read_b128 v[232:235], v247 offset:8192\n\t" \
  "v_mfma_f32_16x16x32_bf16 v[116:119], v[164:167], v[188:191], v[116:119]\n\t" \
  "ds_read_b128 v[236:239], v247 offset:10240\n\t" \
  "v_mfma_f32_16x16x32_bf16 v[120:123], v[168:171], v[188:191], v[120:123]\n\t" \
  "v_mfma_f32_16x16x32_bf16 v[124:127], v[172:175], v[188:191], v[124:127]\n\t" \
  "v_mfma_f32_16x16x32_bf16 v[128:131], v[160:163], v[192:195], v[128:131]\n\t" \
  "v_mfma_f32_16x16x32_bf16 v[132:135], v[164:167], v[192:195], v[132:135]\n\t" \
  "v_mfma_f32_16x16x32_bf16 v[136:139], v[168:171], v[192:195], v[136:139]\n\t" \
  "v_mfma_f32_16x16x32_bf16 v[140:143], v[172:175], v[192:195], v[140:143]\n\t" \
  "v_mfma_f32_16x16x32_bf16 v[144:147], v[160:163], v[196:199], v[144:147]\n\t" \
  "v_mfma_f32_16x16x32_bf16 v[148:151], v[164:167], v[196:199], v[148:151]\n\t" \
  "v_mfma_f32_16x16x32_bf16 v[152:155], v[168:171], v[196:199], v[152:155]\n\t" \
  "v_mfma_f32_16x16x32_bf16 v[156:159], v[172:175], v[196:199], v[156:159]\n\t" \
  "s_waitcnt vmcnt(0) lgkmcnt(0)\n\t" \
  "s_barrier\n\t" \
  "v_mfma_f32_16x16x32_bf16 v[64:67], v[200:203], v[216:219], v[64:67]\n\t" \
  "ds_read_b64_tr_b16 v[160:161], v248 offset:0\n\t" \
  "v_mfma_f32_16x16x32_bf16 v[68:71], v[204:207], v[216:219], v[68:71]\n\t" \
  "ds_read_b64_tr_b16 v[162:163], v248 offset:512\n\t" \
  "v_mfma_f32_16x16x32_bf16 v[72:75], v[208:211], v[216:219], v[72:75]\n\t" \
  "ds_read_b64_tr_b16 v[164:165], v249 offset:0\n\t" \
  "v_mfma_f32_16x16x32_bf16 v[76:79], v[212:215], v[216:219], v[76:79]\n\t" \
  "ds_read_b64_tr_b16 v[166:167], v249 offset:512\n\t" \
  "v_mfma_f32_16x16x32_bf16 v[80:83], v[200:203], v[220:223], v[80:83]\n\t" \
  "ds_read_b64_tr_b16 v[168:169], v248 offset:8192\n\t" \
  "v_mfma_f32_16x16x32_bf16 v[84:87], v[204:207], v[220:223], v[84:87]\n\t" \
  "ds_read_b64_tr_b16 v[170:171], v248 offset:8704\n\t" \
  "v_mfma_f32_16x16x32_bf16 v[88:91], v[208:211], v[220:223], v[88:91]\n\t" \
  "ds_read_b64_tr_b16 v[172:173], v249 offset:8192\n\t" \
  "v_mfma_f32_16x16x32_bf16 v[92:95], v[212:215], v[220:223], v[92:95]\n\t" \
  "ds_read_b64_tr_b16 v[174:175], v249 offset:8704\n\t" \
  "v_mfma_f32_16x16x32_bf16 v[96:99], v[200:203], v[224:227], v[96:99]\n\t" \
  "ds_read_b128 v[176:179], v250 offset:0\n\t" \
  "v_mfma_f32_16x16x32_bf16 v[100:103], v[204:207], v[224:227], v[100:103]\n\t" \
  "ds_read_b128 v[180:183], v250 offset:2048\n\t" \
  "v_mfma_f32_16x16x32_bf16 v[104:107], v[208:211], v[224:227], v[104:107]\n\t" \
  "ds_read_b128 v[184:187], v250 offset:4096\n\t" \
  "v_mfma_f32_16x16x32_bf16 v[108:111], v[212:215], v[224:227], v[108:111]\n\t" \
  "ds_read_b128 v[188:191], v250 offset:6144\n\t" \
  "v_mfma_f32_16x16x32_bf16 v[112:115], v[200:203], v[228:231], v[112:115]\n\t" \
  "ds_read_b128 v[192:195], v250 offset:8192\n\t" \
  "v_mfma_f32_16x16x32_bf16 v[116:119], v[204:207], v[228:231], v[116:119]\n\t" \
  "ds_read_b128 v[196:199], v250 offset:10240\n\t" \
  "v_mfma_f32_16x16x32_bf16 v[120:123], v[208:211], v[228:231], v[120:123]\n\t" \
  "v_mfma_f32_16x16x32_bf16 v[124:127], v[212:215], v[228:231], v[124:127]\n\t" \
  "v_mfma_f32_16x16x32_bf16 v[128:131], v[200:203], v[232:235], v[128:131]\n\t" \
  "v_mfma_f32_16x16x32_bf16 v[132:135], v[204:207], v[232:235], v[132:135]\n\t" \
  "v_mfma_f32_16x16x32_bf16 v[136:139], v[208:211], v[232:235], v[136:139]\n\t" \
  "v_mfma_f32_16x16x32_bf16 v[140:143], v[212:215], v[232:235], v[140:143]\n\t" \
  "v_mfma_f32_16x16x32_bf16 v[144:147], v[200:203], v[236:239], v[144:147]\n\t" \
  "v_mfma_f32_16x16x32_bf16 v[148:151], v[204:207], v[236:239], v[148:151]\n\t" \
  "v_mfma_f32_16x16x32_bf16 v[152:155], v[208:211], v[236:239], v[152:155]\n\t" \
  "v_mfma_f32_16x16x32_bf16 v[156:159], v[212:215], v[236:239], v[156:159]\n\t" \
  "s_waitcnt lgkmcnt(0)\n\t" \
  "v_mfma_f32_16x16x32_bf16 v[64:67], v[160:163], v[176:179], v[64:67]\n\t" \
  "ds_read_b64_tr_b16 v[200:201], v248 offset:4096\n\t" \
  "v_mfma_f32_16x16x32_bf16 v[68:71], v[164:167], v[176:179], v[68:71]\n\t" \
  "ds_read_b64_tr_b16 v[202:203], v248 offset:4608\n\t" \
  "v_mfma_f32_16x16x32_bf16 v[72:75], v[168:171], v[176:179], v[72:75]\n\t" \
  "ds_read_b64_tr_b16 v[204:205], v249 offset:4096\n\t" \
  "v_mfma_f32_16x16x32_bf16 v[76:79], v[172:175], v[176:179], v[76:79]\n\t" \
  "ds_read_b64_tr_b16 v[206:207], v249 offset:4608\n\t" \
  "v_mfma_f32_16x16x32_bf16 v[80:83], v[160:163], v[180:183], v[80:83]\n\t" \
  "ds_read_b64_tr_b16 v[208:209], v248 offset:12288\n\t" \
  "v_mfma_f32_16x16x32_bf16 v[84:87], v[164:167], v[180:183], v[84:87]\n\t" \
  "ds_read_b64_tr_b16 v[210:211], v248 offset:12800\n\t" \
  "v_mfma_f32_16x16x32_bf16 v[88:91], v[168:171], v[180:183], v[88:91]\n\t" \
  "ds_read_b64_tr_b16 v[212:213], v249 offset:12288\n\t" \
  "v_mfma_f32_16x16x32_bf16 v[92:95], v[172:175], v[180:183], v[92:95]\n\t" \
  "ds_read_b64_tr_b16 v[214:215], v249 offset:12800\n\t" \
  "v_mfma_f32_16x16x32_bf16 v[96:99], v[160:163], v[184:187], v[96:99]\n\t" \
  "ds_read_b128 v[216:219], v251 offset:0\n\t" \
  "v_mfma_f32_16x16x32_bf16 v[100:103], v[164:167], v[184:187], v[100:103]\n\t" \
  "ds_read_b128 v[220:223], v251 offset:2048\n\t" \
  "v_mfma_f32_16x16x32_bf16 v[104:107], v[168:171], v[184:187], v[104:107]\n\t" \
  "ds_read_b128 v[224:227], v251 offset:4096\n\t" \
  "v_mfma_f32_16x16x32_bf16 v[108:111], v[172:175], v[184:187], v[108:111]\n\t" \
  "ds_read_b128 v[228:231], v251 offset:6144\n\t" \
  "v_mfma_f32_16x16x32_bf16 v[112:115], v[160:163], v[188:191], v[112:115]\n\t" \
  "ds_read_b128 v[232:235], v251 offset:8192\n\t" \
  "v_mfma_f32_16x16x32_bf16 v[116:119], v[164:167], v[188:191], v[116:119]\n\t" \
  "ds_read_b128 v[236:239], v251 offset:10240\n\t" \
  "v_mfma_f32_16x16x32_bf16 v[120:123], v[168:171], v[188:191], v[120:123]\n\t" \
  "v_mfma_f32_16x16x32_bf16 v[124:127], v[172:175], v[188:191], v[124:127]\n\t" \
  "v_mfma_f32_16x16x32_bf16 v[128:131], v[160:163], v[192:195], v[128:131]\n\t" \
  "v_mfma_f32_16x16x32_bf16 v[132:135], v[164:167], v[192:195], v[132:135]\n\t" \
  "v_mfma_f32_16x16x32_bf16 v[136:139], v[168:171], v[192:195], v[136:139]\n\t" \
  "v_mfma_f32_16x16x32_bf16 v[140:143], v[172:175], v[192:195], v[140:143]\n\t" \
  "v_mfma_f32_16x16x32_bf16 v[144:147], v[160:163], v[196:199], v[144:147]\n\t" \
  "v_mfma_f32_16x16x32_bf16 v[148:151], v[164:167], v[196:199], v[148:151]\n\t" \
  "v_mfma_f32_16x16x32_bf16 v[152:155], v[168:171], v[196:199], v[152:155]\n\t" \
  "v_mfma_f32_16x16x32_bf16 v[156:159], v[172:175], v[196:199], v[156:159]\n\t" \
  "s_waitcnt vmcnt(0) lgkmcnt(0)\n\t" \
  "s_barrier\n\t" \
  "v_mfma_f32_16x16x32_bf16 v[64:67], v[200:203], v[216:219], v[64:67]\n\t" \
  "ds_read_b64_tr_b16 v[160:161], v240 offset:0\n\t" \
  "v_mfma_f32_16x16x32_bf16 v[68:71], v[204:207], v[216:219], v[68:71]\n\t" \
  "ds_read_b64_tr_b16 v[162:163], v240 offset:512\n\t" \
  "v_mfma_f32_16x16x32_bf16 v[72:75], v[208:211], v[216:219], v[72:75]\n\t" \
  "ds_read_b64_tr_b16 v[164:165], v241 offset:0\n\t" \
  "v_mfma_f32_16x16x32_bf16 v[76:79], v[212:215], v[216:219], v[76:79]\n\t" \
  "ds_read_b64_tr_b16 v[166:167], v241 offset:512\n\t" \
  "v_mfma_f32_16x16x32_bf16 v[80:83], v[200:203], v[220:223], v[80:83]\n\t" \
  "ds_read_b64_tr_b16 v[168:169], v240 offset:8192\n\t" \
  "v_mfma_f32_16x16x32_bf16 v[84:87], v[204:207], v[220:223], v[84:87]\n\t" \
  "ds_read_b64_tr_b16 v[170:171], v240 offset:8704\n\t" \
  "v_mfma_f32_16x16x32_bf16 v[88:91], v[208:211], v[220:223], v[88:91]\n\t" \
  "ds_read_b64_tr_b16 v[172:173], v241 offset:8192\n\t" \
  "v_mfma_f32_16x16x32_bf16 v[92:95], v[212:215], v[220:223], v[92:95]\n\t" \
  "ds_read_b64_tr_b16 v[174:175], v241 offset:8704\n\t" \
  "v_mfma_f32_16x16x32_bf16 v[96:99], v[200:203], v[224:227], v[96:99]\n\t" \
  "ds_read_b128 v[176:179], v242 offset:0\n\t" \
  "v_mfma_f32_16x16x32_bf16 v[100:103], v[204:207], v[224:227], v[100:103]\n\t" \
  "ds_read_b128 v[180:183], v242 offset:2048\n\t" \
  "v_mfma_f32_16x16x32_bf16 v[104:107], v[208:211], v[224:227], v[104:107]\n\t" \
  "ds_read_b128 v[184:187], v242 offset:4096\n\t" \
  "v_mfma_f32_16x16x32_bf16 v[108:111], v[212:215], v[224:227], v[108:111]\n\t" \
  "ds_read_b128 v[188:191], v242 offset:6144\n\t" \
  "v_mfma_f32_16x16x32_bf16 v[112:115], v[200:203], v[228:231], v[112:115]\n\t" \
  "ds_read_b128 v[192:195], v242 offset:8192\n\t" \
  "v_mfma_f32_16x16x32_bf16 v[116:119], v[204:207], v[228:231], v[116:119]\n\t" \
  "ds_read_b128 v[196:199], v242 offset:10240\n\t" \
  "v_mfma_f32_16x16x32_bf16 v[120:123], v[208:211], v[228:231], v[120:123]\n\t" \
  "v_mfma_f32_16x16x32_bf16 v[124:127], v[212:215], v[228:231], v[124:127]\n\t" \
  "v_mfma_f32_16x16x32_bf16 v[128:131], v[200:203], v[232:235], v[128:131]\n\t" \
  "v_mfma_f32_16x16x32_bf16 v[132:135], v[204:207], v[232:235], v[132:135]\n\t" \
  "v_mfma_f32_16x16x32_bf16 v[136:139], v[208:211], v[232:235], v[136:139]\n\t" \
  "v_mfma_f32_16x16x32_bf16 v[140:143], v[212:215], v[232:235], v[140:143]\n\t" \
  "v_mfma_f32_16x16x32_bf16 v[144:147], v[200:203], v[236:239], v[144:147]\n\t" \
  "v_mfma_f32_16x16x32_bf16 v[148:151], v[204:207], v[236:239], v[148:151]\n\t" \
  "v_mfma_f32_16x16x32_bf16 v[152:155], v[208:211], v[236:239], v[152:155]\n\t" \
  "v_mfma_f32_16x16x32_bf16 v[156:159], v[212:215], v[236:239], v[156:159]\n\t" \
  "s_sub_u32 %[cnt], %[cnt], 1\n\t" \
  "s_cmp_lg_u32 %[cnt], 0\n\t" \
  "s_waitcnt lgkmcnt(0)\n\t" \
  "s_cbranch_scc1 .Lg1_%=\n\t" \
  "v_mfma_f32_16x16x32_bf16 v[64:67], v[160:163], v[176:179], v[64:67]\n\t" \
  "ds_read_b64_tr_b16 v[200:201], v240 offset:4096\n\t" \
  "v_mfma_f32_16x16x32_bf16 v[68:71], v[164:167], v[176:179], v[68:71]\n\t" \
  "ds_read_b64_tr_b16 v[202:203], v240 offset:4608\n\t" \
  "v_mfma_f32_16x16x32_bf16 v[72:75], v[168:171], v[176:179], v[72:75]\n\t" \
  "ds_read_b64_tr_b16 v[204:205], v241 offset:4096\n\t" \
  "v_mfma_f32_16x16x32_bf16 v[76:79], v[172:175], v[176:179], v[76:79]\n\t" \
  "ds_read_b64_tr_b16 v[206:207], v241 offset:4608\n\t" \
  "v_mfma_f32_16x16x32_bf16 v[80:83], v[160:163], v[180:183], v[80:83]\n\t" \
  "ds_read_b64_tr_b16 v[208:209], v240 offset:12288\n\t" \
  "v_mfma_f32_16x16x32_bf16 v[84:87], v[164:167], v[180:183], v[84:87]\n\t" \
  "ds_read_b64_tr_b16 v[210:211], v240 offset:12800\n\t" \
  "v_mfma_f32_16x16x32_bf16 v[88:91], v[168:171], v[180:183], v[88:91]\n\t" \
  "ds_read_b64_tr_b16 v[212:213], v241 offset:12288\n\t" \
  "v_mfma_f32_16x16x32_bf16 v[92:95], v[172:175], v[180:183], v[92:95]\n\t" \
  "ds_read_b64_tr_b16 v[214:215], v241 offset:12800\n\t" \
  "v_mfma_f32_16x16x32_bf16 v[96:99], v[160:163], v[184:187], v[96:99]\n\t" \
  "ds_read_b128 v[216:219], v243 offset:0\n\t" \
  "v_mfma_f32_16x16x32_bf16 v[100:103], v[164:167], v[184:187], v[100:103]\n\t" \
  "ds_read_b128 v[220:223], v243 offset:2048\n\t" \
  "v_mfma_f32_16x16x32_bf16 v[104:107], v[168:171], v[184:187], v[104:107]\n\t" \
  "ds_read_b128 v[224:227], v243 offset:4096\n\t" \
  "v_mfma_f32_16x16x32_bf16 v[108:111], v[172:175], v[184:187], v[108:111]\n\t" \
  "ds_read_b128 v[228:231], v243 offset:6144\n\t" \
  "v_mfma_f32_16x16x32_bf16 v[112:115], v[160:163], v[188:191], v[112:115]\n\t" \
  "ds_read_b128 v[232:235], v243 offset:8192\n\t" \
  "v_mfma_f32_16x16x32_bf16 v[116:119], v[164:167], v[188:191], v[116:119]\n\t" \
  "ds_read_b128 v[236:239], v243 offset:10240\n\t" \
  "v_mfma_f32_16x16x32_bf16 v[120:123], v[168:171], v[188:191], v[120:123]\n\t" \
  "v_mfma_f32_16x16x32_bf16 v[124:127], v[172:175], v[188:191], v[124:127]\n\t" \
  "v_mfma_f32_16x16x32_bf16 v[128:131], v[160:163], v[192:195], v[128:131]\n\t" \
  "v_mfma_f32_16x16x32_bf16 v[132:135], v[164:167], v[192:195], v[132:135]\n\t" \
  "v_mfma_f32_16x16x32_bf16 v[136:139], v[168:171], v[192:195], v[136:139]\n\t" \
  "v_mfma_f32_16x16x32_bf16 v[140:143], v[172:175], v[192:195], v[140:143]\n\t" \
  "v_mfma_f32_16x16x32_bf16 v[144:147], v[160:163], v[196:199], v[144:147]\n\t" \
  "v_mfma_f32_16x16x32_bf16 v[148:151], v[164:167], v[196:199], v[148:151]\n\t" \
  "v_mfma_f32_16x16x32_bf16 v[152:155], v[168:171], v[196:199], v[152:155]\n\t" \
  "v_mfma_f32_16x16x32_bf16 v[156:159], v[172:175], v[196:199], v[156:159]\n\t" \
  "s_waitcnt lgkmcnt(0)\n\t" \
  "v_mfma_f32_16x16x32_bf16 v[64:67], v[200:203], v[216:219], v[64:67]\n\t" \
  "ds_read_b128 v[160:163], %[cs] offset:0\n\t" \
  "v_mfma_f32_16x16x32_bf16 v[68:71], v[204:207], v[216:219], v[68:71]\n\t" \
  "ds_read_b128 v[164:167], %[cs] offset:64\n\t" \
  "v_mfma_f32_16x16x32_bf16 v[72:75], v[208:211], v[216:219], v[72:75]\n\t" \
  "ds_read_b128 v[168:171], %[cs] offset:256\n\t" \
  "v_mfma_f32_16x16x32_bf16 v[76:79], v[212:215], v[216:219], v[76:79]\n\t" \
  "ds_read_b128 v[172:175], %[cs] offset:320\n\t" \
  "v_mfma_f32_16x16x32_bf16 v[80:83], v[200:203], v[220:223], v[80:83]\n\t" \
  "ds_read_b128 v[176:179], %[st] offset:0\n\t" \
  "v_mfma_f32_16x16x32_bf16 v[84:87], v[204:207], v[220:223], v[84:87]\n\t" \
  "ds_read_b128 v[180:183], %[st] offset:256\n\t" \
  "v_mfma_f32_16x16x32_bf16 v[88:91], v[208:211], v[220:223], v[88:91]\n\t" \
  "ds_read_b128 v[184:187], %[st] offset:512\n\t" \
  "v_mfma_f32_16x16x32_bf16 v[92:95], v[212:215], v[220:223], v[92:95]\n\t" \
  "ds_read_b128 v[188:191], %[st] offset:768\n\t" \
  "v_mfma_f32_16x16x32_bf16 v[96:99], v[200:203], v[224:227], v[96:99]\n\t" \
  "ds_read_b128 v[192:195], %[st] offset:1024\n\t" \
  "v_mfma_f32_16x16x32_bf16 v[100:103], v[204:207], v[224:227], v[100:103]\n\t" \
  "ds_read_b128 v[196:199], %[st] offset:1280\n\t" \
  "v_mfma_f32_16x16x32_bf16 v[104:107], v[208:211], v[224:227], v[104:107]\n\t" \
  "v_mfma_f32_16x16x32_bf16 v[108:111], v[212:215], v[224:227], v[108:111]\n\t" \
  "v_mfma_f32_16x16x32_bf16 v[112:115], v[200:203], v[228:231], v[112:115]\n\t" \
  "v_mfma_f32_16x16x32_bf16 v[116:119], v[204:207], v[228:231], v[116:119]\n\t" \
  "v_mfma_f32_16x16x32_bf16 v[120:123], v[208:211], v[228:231], v[120:123]\n\t" \
  "v_mfma_f32_16x16x32_bf16 v[124:127], v[212:215], v[228:231], v[124:127]\n\t" \
  "v_mfma_f32_16x16x32_bf16 v[128:131], v[200:203], v[232:235], v[128:131]\n\t" \
  "v_mfma_f32_16x16x32_bf16 v[132:135], v[204:207], v[232:235], v[132:135]\n\t" \
  "v_mfma_f32_16x16x32_bf16 v[136:139], v[208:211], v[232:235], v[136:139]\n\t" \
  "v_mfma_f32_16x16x32_bf16 v[140:143], v[212:215], v[232:235], v[140:143]\n\t" \
  "v_mfma_f32_16x16x32_bf16 v[144:147], v[200:203], v[236:239], v[144:147]\n\t" \
  "v_mfma_f32_16x16x32_bf16 v[148:151], v[204:207], v[236:239], v[148:151]\n\t" \
  "v_mfma_f32_16x16x32_bf16 v[152:155], v[208:211], v[236:239], v[152:155]\n\t" \
  "v_mfma_f32_16x16x32_bf16 v[156:159], v[212:215], v[236:239], v[156:159]\n\t" \
  "s_nop 7\n\t" \
  "s_waitcnt lgkmcnt(0)\n\t" \
  ""
#define G1_MAIN_CLOB "v200","v201","v202","v203","v204","v205","v206","v207","v208","v209","v210","v211","v212","v213","v214","v215","v216","v217","v218","v219","v220","v221","v222","v223","v224","v225","v226","v227","v228","v229","v230","v231","v232","v233","v234","v235","v236","v237","v238","v239","v240","v241","v242","v243","v244","v245","v246","v247","v248","v249","v250","v251"
#define CONV_FIRST_ASM \
  "ds_read_b128 v[96:99], %[aa] offset:0\n\t" \
  "ds_read_b128 v[100:103], %[aa] offset:512\n\t" \
  "ds_read_b128 v[104:107], %[aa] offset:1024\n\t" \
  "ds_read_b128 v[108:111], %[aa] offset:1536\n\t" \
  "ds_read_b128 v[140:143], %[ab] offset:4032\n\t" \
  "ds_read_b128 v[136:139], %[ab] offset:3520\n\t" \
  "ds_read_b128 v[132:135], %[ab] offset:3008\n\t" \
  "ds_read_b128 v[128:131], %[ab] offset:2496\n\t" \
  "ds_read_b128 v[124:127], %[ab] offset:1984\n\t" \
  "ds_read_b128 v[120:123], %[ab] offset:1472\n\t" \
  "ds_read_b128 v[116:119], %[ab] offset:960\n\t" \
  "ds_read_b128 v[112:115], %[ab] offset:448\n\t" \
  ""
#define CONV_MAIN_ASM \
  "v_mfma_f32_16x16x32_bf16 v[92:95], v[96:99], v[140:143], v[92:95]\n\t" \
  "ds_read_b128 v[144:147], %[aa] offset:64\n\t" \
  "v_mfma_f32_16x16x32_bf16 v[88:91], v[96:99], v[136:139], v[88:91]\n\t" \
  "ds_read_b128 v[148:151], %[aa] offset:576\n\t" \
  "v_mfma_f32_16x16x32_bf16 v[84:87], v[96:99], v[132:135], v[84:87]\n\t" \
  "ds_read_b128 v[152:155], %[aa] offset:1088\n\t" \
  "v_mfma_f32_16x16x32_bf16 v[80:83], v[96:99], v[128:131], v[80:83]\n\t" \
  "ds_read_b128 v[156:159], %[aa] offset:1600\n\t" \
  "v_mfma_f32_16x16x32_bf16 v[76:79], v[96:99], v[124:127], v[76:79]\n\t" \
  "ds_read_b128 v[188:191], %[ab] offset:3968\n\t" \
  "v_mfma_f32_16x16x32_bf16 v[72:75], v[96:99], v[120:123], v[72:75]\n\t" \
  "ds_read_b128 v[184:187], %[ab] offset:3456\n\t" \
  "v_mfma_f32_16x16x32_bf16 v[68:71], v[96:99], v[116:119], v[68:71]\n\t" \
  "ds_read_b128 v[180:183], %[ab] offset:2944\n\t" \
  "v_mfma_f32_16x16x32_bf16 v[64:67], v[96:99], v[112:115], v[64:67]\n\t" \
  "ds_read_b128 v[176:179], %[ab] offset:2432\n\t" \
  "v_mfma_f32_16x16x32_bf16 v[92:95], v[100:103], v[136:139], v[92:95]\n\t" \
  "ds_read_b128 v[172:175], %[ab] offset:1920\n\t" \
  "v_mfma_f32_16x16x32_bf16 v[88:91], v[100:103], v[132:135], v[88:91]\n\t" \
  "ds_read_b128 v[168:171], %[ab] offset:1408\n\t" \
  "v_mfma_f32_16x16x32_bf16 v[84:87], v[100:103], v[128:131], v[84:87]\n\t" \
  "ds_read_b128 v[164:167], %[ab] offset:896\n\t" \
  "v_mfma_f32_16x16x32_bf16 v[80:83], v[100:103], v[124:127], v[80:83]\n\t" \
  "ds_read_b128 v[160:163], %[ab] offset:384\n\t" \
  "v_mfma_f32_16x16x32_bf16 v[76:79], v[100:103], v[120:123], v[76:79]\n\t" \
  "v_mfma_f32_16x16x32_bf16 v[72:75], v[100:103], v[116:119], v[72:75]\n\t" \
  "v_mfma_f32_16x16x32_bf16 v[68:71], v[100:103], v[112:115], v[68:71]\n\t" \
  "v_mfma_f32_16x16x32_bf16 v[92:95], v[104:107], v[132:135], v[92:95]\n\t" \
  "v_mfma_f32_16x16x32_bf16 v[88:91], v[104:107], v[128:131], v[88:91]\n\t" \
  "v_mfma_f32_16x16x32_bf16 v[84:87], v[104:107], v[124:127], v[84:87]\n\t" \
  "v_mfma_f32_16x16x32_bf16 v[80:83], v[104:107], v[120:123], v[80:83]\n\t" \
  "v_mfma_f32_16x16x32_bf16 v[76:79], v[104:107], v[116:119], v[76:79]\n\t" \
  "v_mfma_f32_16x16x32_bf16 v[72:75], v[104:107], v[112:115], v[72:75]\n\t" \
  "v_mfma_f32_16x16x32_bf16 v[92:95], v[108:111], v[128:131], v[92:95]\n\t" \
  "v_mfma_f32_16x16x32_bf16 v[88:91], v[108:111], v[124:127], v[88:91]\n\t" \
  "v_mfma_f32_16x16x32_bf16 v[84:87], v[108:111], v[120:123], v[84:87]\n\t" \
  "v_mfma_f32_16x16x32_bf16 v[80:83], v[108:111], v[116:119], v[80:83]\n\t" \
  "v_mfma_f32_16x16x32_bf16 v[76:79], v[108:111], v[112:115], v[76:79]\n\t" \
  "s_waitcnt lgkmcnt(0)\n\t" \
  "v_mfma_f32_16x16x32_bf16 v[92:95], v[144:147], v[188:191], v[92:95]\n\t" \
  "ds_read_b128 v[96:99], %[aa] offset:128\n\t" \
  "v_mfma_f32_16x16x32_bf16 v[88:91], v[144:147], v[184:187], v[88:91]\n\t" \
  "ds_read_b128 v[100:103], %[aa] offset:640\n\t" \
  "v_mfma_f32_16x16x32_bf16 v[84:87], v[144:147], v[180:183], v[84:87]\n\t" \
  "ds_read_b128 v[104:107], %[aa] offset:1152\n\t" \
  "v_mfma_f32_16x16x32_bf16 v[80:83], v[144:147], v[176:179], v[80:83]\n\t" \
  "ds_read_b128 v[108:111], %[aa] offset:1664\n\t" \
  "v_mfma_f32_16x16x32_bf16 v[76:79], v[144:147], v[172:175], v[76:79]\n\t" \
  "ds_read_b128 v[140:143], %[ab] offset:3904\n\t" \
  "v_mfma_f32_16x16x32_bf16 v[72:75], v[144:147], v[168:171], v[72:75]\n\t" \
  "ds_read_b128 v[136:139], %[ab] offset:3392\n\t" \
  "v_mfma_f32_16x16x32_bf16 v[68:71], v[144:147], v[164:167], v[68:71]\n\t" \
  "ds_read_b128 v[132:135], %[ab] offset:2880\n\t" \
  "v_mfma_f32_16x16x32_bf16 v[64:67], v[144:147], v[160:163], v[64:67]\n\t" \
  "ds_read_b128 v[128:131], %[ab] offset:2368\n\t" \
  "v_mfma_f32_16x16x32_bf16 v[92:95], v[148:151], v[184:187], v[92:95]\n\t" \
  "ds_read_b128 v[124:127], %[ab] offset:1856\n\t" \
  "v_mfma_f32_16x16x32_bf16 v[88:91], v[148:151], v[180:183], v[88:91]\n\t" \
  "ds_read_b128 v[120:123], %[ab] offset:1344\n\t" \
  "v_mfma_f32_16x16x32_bf16 v[84:87], v[148:151], v[176:179], v[84:87]\n\t" \
  "ds_read_b128 v[116:119], %[ab] offset:832\n\t" \
  "v_mfma_f32_16x16x32_bf16 v[80:83], v[148:151], v[172:175], v[80:83]\n\t" \
  "ds_read_b128 v[112:115], %[ab] offset:320\n\t" \
  "v_mfma_f32_16x16x32_bf16 v[76:79], v[148:151], v[168:171], v[76:79]\n\t" \
  "v_mfma_f32_16x16x32_bf16 v[72:75], v[148:151], v[164:167], v[72:75]\n\t" \
  "v_mfma_f32_16x16x32_bf16 v[68:71], v[148:151], v[160:163], v[68:71]\n\t" \
  "v_mfma_f32_16x16x32_bf16 v[92:95], v[152:155], v[180:183], v[92:95]\n\t" \
  "v_mfma_f32_16x16x32_bf16 v[88:91], v[152:155], v[176:179], v[88:91]\n\t" \
  "v_mfma_f32_16x16x32_bf16 v[84:87], v[152:155], v[172:175], v[84:87]\n\t" \
  "v_mfma_f32_16x16x32_bf16 v[80:83], v[152:155], v[168:171], v[80:83]\n\t" \
  "v_mfma_f32_16x16x32_bf16 v[76:79], v[152:155], v[164:167], v[76:79]\n\t" \
  "v_mfma_f32_16x16x32_bf16 v[72:75], v[152:155], v[160:163], v[72:75]\n\t" \
  "v_mfma_f32_16x16x32_bf16 v[92:95], v[156:159], v[176:179], v[92:95]\n\t" \
  "v_mfma_f32_16x16x32_bf16 v[88:91], v[156:159], v[172:175], v[88:91]\n\t" \
  "v_mfma_f32_16x16x32_bf16 v[84:87], v[156:159], v[168:171], v[84:87]\n\t" \
  "v_mfma_f32_16x16x32_bf16 v[80:83], v[156:159], v[164:167], v[80:83]\n\t" \
  "v_mfma_f32_16x16x32_bf16 v[76:79], v[156:159], v[160:163], v[76:79]\n\t" \
  "s_waitcnt lgkmcnt(0)\n\t" \
  "v_mfma_f32_16x16x32_bf16 v[92:95], v[96:99], v[140:143], v[92:95]\n\t" \
  "ds_read_b128 v[144:147], %[aa] offset:192\n\t" \
  "v_mfma_f32_16x16x32_bf16 v[88:91], v[96:99], v[136:139], v[88:91]\n\t" \
  "ds_read_b128 v[148:151], %[aa] offset:704\n\t" \
  "v_mfma_f32_16x16x32_bf16 v[84:87], v[96:99], v[132:135], v[84:87]\n\t" \
  "ds_read_b128 v[152:155], %[aa] offset:1216\n\t" \
  "v_mfma_f32_16x16x32_bf16 v[80:83], v[96:99], v[128:131], v[80:83]\n\t" \
  "ds_read_b128 v[156:159], %[aa] offset:1728\n\t" \
  "v_mfma_f32_16x16x32_bf16 v[76:79], v[96:99], v[124:127], v[76:79]\n\t" \
  "ds_read_b128 v[188:191], %[ab] offset:3840\n\t" \
  "v_mfma_f32_16x16x32_bf16 v[72:75], v[96:99], v[120:123], v[72:75]\n\t" \
  "ds_read_b128 v[184:187], %[ab] offset:3328\n\t" \
  "v_mfma_f32_16x16x32_bf16 v[68:71], v[96:99], v[116:119], v[68:71]\n\t" \
  "ds_read_b128 v[180:183], %[ab] offset:2816\n\t" \
  "v_mfma_f32_16x16x32_bf16 v[64:67], v[96:99], v[112:115], v[64:67]\n\t" \
  "ds_read_b128 v[176:179], %[ab] offset:2304\n\t" \
  "v_mfma_f32_16x16x32_bf16 v[92:95], v[100:103], v[136:139], v[92:95]\n\t" \
  "ds_read_b128 v[172:175], %[ab] offset:1792\n\t" \
  "v_mfma_f32_16x16x32_bf16 v[88:91], v[100:103], v[132:135], v[88:91]\n\t" \
  "ds_read_b128 v[168:171], %[ab] offset:1280\n\t" \
  "v_mfma_f32_16x16x32_bf16 v[84:87], v[100:103], v[128:131], v[84:87]\n\t" \
  "ds_read_b128 v[164:167], %[ab] offset:768\n\t" \
  "v_mfma_f32_16x16x32_bf16 v[80:83], v[100:103], v[124:127], v[80:83]\n\t" \
  "ds_read_b128 v[160:163], %[ab] offset:256\n\t" \
  "v_mfma_f32_16x16x32_bf16 v[76:79], v[100:103], v[120:123], v[76:79]\n\t" \
  "v_mfma_f32_16x16x32_bf16 v[72:75], v[100:103], v[116:119], v[72:75]\n\t" \
  "v_mfma_f32_16x16x32_bf16 v[68:71], v[100:103], v[112:115], v[68:71]\n\t" \
  "v_mfma_f32_16x16x32_bf16 v[92:95], v[104:107], v[132:135], v[92:95]\n\t" \
  "v_mfma_f32_16x16x32_bf16 v[88:91], v[104:107], v[128:131], v[88:91]\n\t" \
  "v_mfma_f32_16x16x32_bf16 v[84:87], v[104:107], v[124:127], v[84:87]\n\t" \
  "v_mfma_f32_16x16x32_bf16 v[80:83], v[104:107], v[120:123], v[80:83]\n\t" \
  "v_mfma_f32_16x16x32_bf16 v[76:79], v[104:107], v[116:119], v[76:79]\n\t" \
  "v_mfma_f32_16x16x32_bf16 v[72:75], v[104:107], v[112:115], v[72:75]\n\t" \
  "v_mfma_f32_16x16x32_bf16 v[92:95], v[108:111], v[128:131], v[92:95]\n\t" \
  "v_mfma_f32_16x16x32_bf16 v[88:91], v[108:111], v[124:127], v[88:91]\n\t" \
  "v_mfma_f32_16x16x32_bf16 v[84:87], v[108:111], v[120:123], v[84:87]\n\t" \
  "v_mfma_f32_16x16x32_bf16 v[80:83], v[108:111], v[116:119], v[80:83]\n\t" \
  "v_mfma_f32_16x16x32_bf16 v[76:79], v[108:111], v[112:115], v[76:79]\n\t" \
  "s_waitcnt lgkmcnt(0)\n\t" \
  "v_mfma_f32_16x16x32_bf16 v[92:95], v[144:147], v[188:191], v[92:95]\n\t" \
  "ds_read_b128 v[96:99], %[aa] offset:256\n\t" \
  "v_mfma_f32_16x16x32_bf16 v[88:91], v[144:147], v[184:187], v[88:91]\n\t" \
  "ds_read_b128 v[100:103], %[aa] offset:768\n\t" \
  "v_mfma_f32_16x16x32_bf16 v[84:87], v[144:147], v[180:183], v[84:87]\n\t" \
  "ds_read_b128 v[104:107], %[aa] offset:1280\n\t" \
  "v_mfma_f32_16x16x32_bf16 v[80:83], v[144:147], v[176:179], v[80:83]\n\t" \
  "ds_read_b128 v[108:111], %[aa] offset:1792\n\t" \
  "v_mfma_f32_16x16x32_bf16 v[76:79], v[144:147], v[172:175], v[76:79]\n\t" \
  "ds_read_b128 v[140:143], %[ab] offset:3776\n\t" \
  "v_mfma_f32_16x16x32_bf16 v[72:75], v[144:147], v[168:171], v[72:75]\n\t" \
  "ds_read_b128 v[136:139], %[ab] offset:3264\n\t" \
  "v_mfma_f32_16x16x32_bf16 v[68:71], v[144:147], v[164:167], v[68:71]\n\t" \
  "ds_read_b128 v[132:135], %[ab] offset:2752\n\t" \
  "v_mfma_f32_16x16x32_bf16 v[64:67], v[144:147], v[160:163], v[64:67]\n\t" \
  "ds_read_b128 v[128:131], %[ab] offset:2240\n\t" \
  "v_mfma_f32_16x16x32_bf16 v[92:95], v[148:151], v[184:187], v[92:95]\n\t" \
  "ds_read_b128 v[124:127], %[ab] offset:1728\n\t" \
  "v_mfma_f32_16x16x32_bf16 v[88:91], v[148:151], v[180:183], v[88:91]\n\t" \
  "ds_read_b128 v[120:123], %[ab] offset:1216\n\t" \
  "v_mfma_f32_16x16x32_bf16 v[84:87], v[148:151], v[176:179], v[84:87]\n\t" \
  "ds_read_b128 v[116:119], %[ab] offset:704\n\t" \
  "v_mfma_f32_16x16x32_bf16 v[80:83], v[148:151], v[172:175], v[80:83]\n\t" \
  "ds_read_b128 v[112:115], %[ab] offset:192\n\t" \
  "v_mfma_f32_16x16x32_bf16 v[76:79], v[148:151], v[168:171], v[76:79]\n\t" \
  "v_mfma_f32_16x16x32_bf16 v[72:75], v[148:151], v[164:167], v[72:75]\n\t" \
  "v_mfma_f32_16x16x32_bf16 v[68:71], v[148:151], v[160:163], v[68:71]\n\t" \
  "v_mfma_f32_16x16x32_bf16 v[92:95], v[152:155], v[180:183], v[92:95]\n\t" \
  "v_mfma_f32_16x16x32_bf16 v[88:91], v[152:155], v[176:179], v[88:91]\n\t" \
  "v_mfma_f32_16x16x32_bf16 v[84:87], v[152:155], v[172:175], v[84:87]\n\t" \
  "v_mfma_f32_16x16x32_bf16 v[80:83], v[152:155], v[168:171], v[80:83]\n\t" \
  "v_mfma_f32_16x16x32_bf16 v[76:79], v[152:155], v[164:167], v[76:79]\n\t" \
  "v_mfma_f32_16x16x32_bf16 v[72:75], v[152:155], v[160:163], v[72:75]\n\t" \
  "v_mfma_f32_16x16x32_bf16 v[92:95], v[156:159], v[176:179], v[92:95]\n\t" \
  "v_mfma_f32_16x16x32_bf16 v[88:91], v[156:159], v[172:175], v[88:91]\n\t" \
  "v_mfma_f32_16x16x32_bf16 v[84:87], v[156:159], v[168:171], v[84:87]\n\t" \
  "v_mfma_f32_16x16x32_bf16 v[80:83], v[156:159], v[164:167], v[80:83]\n\t" \
  "v_mfma_f32_16x16x32_bf16 v[76:79], v[156:159], v[160:163], v[76:79]\n\t" \
  "s_waitcnt lgkmcnt(0)\n\t" \
  "v_mfma_f32_16x16x32_bf16 v[92:95], v[96:99], v[140:143], v[92:95]\n\t" \
  "ds_read_b128 v[144:147], %[aa] offset:320\n\t" \
  "v_mfma_f32_16x16x32_bf16 v[88:91], v[96:99], v[136:139], v[88:91]\n\t" \
  "ds_read_b128 v[148:151], %[aa] offset:832\n\t" \
  "v_mfma_f32_16x16x32_bf16 v[84:87], v[96:99], v[132:135], v[84:87]\n\t" \
  "ds_read_b128 v[152:155], %[aa] offset:1344\n\t" \
  "v_mfma_f32_16x16x32_bf16 v[80:83], v[96:99], v[128:131], v[80:83]\n\t" \
  "ds_read_b128 v[156:159], %[aa] offset:1856\n\t" \
  "v_mfma_f32_16x16x32_bf16 v[76:79], v[96:99], v[124:127], v[76:79]\n\t" \
  "ds_read_b128 v[188:191], %[ab] offset:3712\n\t" \
  "v_mfma_f32_16x16x32_bf16 v[72:75], v[96:99], v[120:123], v[72:75]\n\t" \
  "ds_read_b128 v[184:187], %[ab] offset:3200\n\t" \
  "v_mfma_f32_16x16x32_bf16 v[68:71], v[96:99], v[116:119], v[68:71]\n\t" \
  "ds_read_b128 v[180:183], %[ab] offset:2688\n\t" \
  "v_mfma_f32_16x16x32_bf16 v[64:67], v[96:99], v[112:115], v[64:67]\n\t" \
  "ds_read_b128 v[176:179], %[ab] offset:2176\n\t" \
  "v_mfma_f32_16x16x32_bf16 v[92:95], v[100:103], v[136:139], v[92:95]\n\t" \
  "ds_read_b128 v[172:175], %[ab] offset:1664\n\t" \
  "v_mfma_f32_16x16x32_bf16 v[88:91], v[100:103], v[132:135], v[88:91]\n\t" \
  "ds_read_b128 v[168:171], %[ab] offset:1152\n\t" \
  "v_mfma_f32_16x16x32_bf16 v[84:87], v[100:103], v[128:131], v[84:87]\n\t" \
  "ds_read_b128 v[164:167], %[ab] offset:640\n\t" \
  "v_mfma_f32_16x16x32_bf16 v[80:83], v[100:103], v[124:127], v[80:83]\n\t" \
  "ds_read_b128 v[160:163], %[ab] offset:128\n\t" \
  "v_mfma_f32_16x16x32_bf16 v[76:79], v[100:103], v[120:123], v[76:79]\n\t" \
  "v_mfma_f32_16x16x32_bf16 v[72:75], v[100:103], v[116:119], v[72:75]\n\t" \
  "v_mfma_f32_16x16x32_bf16 v[68:71], v[100:103], v[112:115], v[68:71]\n\t" \
  "v_mfma_f32_16x16x32_bf16 v[92:95], v[104:107], v[132:135], v[92:95]\n\t" \
  "v_mfma_f32_16x16x32_bf16 v[88:91], v[104:107], v[128:131], v[88:91]\n\t" \
  "v_mfma_f32_16x16x32_bf16 v[84:87], v[104:107], v[124:127], v[84:87]\n\t" \
  "v_mfma_f32_16x16x32_bf16 v[80:83], v[104:107], v[120:123], v[80:83]\n\t" \
  "v_mfma_f32_16x16x32_bf16 v[76:79], v[104:107], v[116:119], v[76:79]\n\t" \
  "v_mfma_f32_16x16x32_bf16 v[72:75], v[104:107], v[112:115], v[72:75]\n\t" \
  "v_mfma_f32_16x16x32_bf16 v[92:95], v[108:111], v[128:131], v[92:95]\n\t" \
  "v_mfma_f32_16x16x32_bf16 v[88:91], v[108:111], v[124:127], v[88:91]\n\t" \
  "v_mfma_f32_16x16x32_bf16 v[84:87], v[108:111], v[120:123], v[84:87]\n\t" \
  "v_mfma_f32_16x16x32_bf16 v[80:83], v[108:111], v[116:119], v[80:83]\n\t" \
  "v_mfma_f32_16x16x32_bf16 v[76:79], v[108:111], v[112:115], v[76:79]\n\t" \
  "s_waitcnt lgkmcnt(0)\n\t" \
  "v_mfma_f32_16x16x32_bf16 v[92:95], v[144:147], v[188:191], v[92:95]\n\t" \
  "ds_read_b128 v[96:99], %[aa] offset:384\n\t" \
  "v_mfma_f32_16x16x32_bf16 v[88:91], v[144:147], v[184:187], v[88:91]\n\t" \
  "ds_read_b128 v[100:103], %[aa] offset:896\n\t" \
  "v_mfma_f32_16x16x32_bf16 v[84:87], v[144:147], v[180:183], v[84:87]\n\t" \
  "ds_read_b128 v[104:107], %[aa] offset:1408\n\t" \
  "v_mfma_f32_16x16x32_bf16 v[80:83], v[144:147], v[176:179], v[80:83]\n\t" \
  "ds_read_b128 v[108:111], %[aa] offset:1920\n\t" \
  "v_mfma_f32_16x16x32_bf16 v[76:79], v[144:147], v[172:175], v[76:79]\n\t" \
  "ds_read_b128 v[140:143], %[ab] offset:3648\n\t" \
  "v_mfma_f32_16x16x32_bf16 v[72:75], v[144:147], v[168:171], v[72:75]\n\t" \
  "ds_read_b128 v[136:139], %[ab] offset:3136\n\t" \
  "v_mfma_f32_16x16x32_bf16 v[68:71], v[144:147], v[164:167], v[68:71]\n\t" \
  "ds_read_b128 v[132:135], %[ab] offset:2624\n\t" \
  "v_mfma_f32_16x16x32_bf16 v[64:67], v[144:147], v[160:163], v[64:67]\n\t" \
  "ds_read_b128 v[128:131], %[ab] offset:2112\n\t" \
  "v_mfma_f32_16x16x32_bf16 v[92:95], v[148:151], v[184:187], v[92:95]\n\t" \
  "ds_read_b128 v[124:127], %[ab] offset:1600\n\t" \
  "v_mfma_f32_16x16x32_bf16 v[88:91], v[148:151], v[180:183], v[88:91]\n\t" \
  "ds_read_b128 v[120:123], %[ab] offset:1088\n\t" \
  "v_mfma_f32_16x16x32_bf16 v[84:87], v[148:151], v[176:179], v[84:87]\n\t" \
  "ds_read_b128 v[116:119], %[ab] offset:576\n\t" \
  "v_mfma_f32_16x16x32_bf16 v[80:83], v[148:151], v[172:175], v[80:83]\n\t" \
  "ds_read_b128 v[112:115], %[ab] offset:64\n\t" \
  "v_mfma_f32_16x16x32_bf16 v[76:79], v[148:151], v[168:171], v[76:79]\n\t" \
  "v_mfma_f32_16x16x32_bf16 v[72:75], v[148:151], v[164:167], v[72:75]\n\t" \
  "v_mfma_f32_16x16x32_bf16 v[68:71], v[148:151], v[160:163], v[68:71]\n\t" \
  "v_mfma_f32_16x16x32_bf16 v[92:95], v[152:155], v[180:183], v[92:95]\n\t" \
  "v_mfma_f32_16x16x32_bf16 v[88:91], v[152:155], v[176:179], v[88:91]\n\t" \
  "v_mfma_f32_16x16x32_bf16 v[84:87], v[152:155], v[172:175], v[84:87]\n\t" \
  "v_mfma_f32_16x16x32_bf16 v[80:83], v[152:155], v[168:171], v[80:83]\n\t" \
  "v_mfma_f32_16x16x32_bf16 v[76:79], v[152:155], v[164:167], v[76:79]\n\t" \
  "v_mfma_f32_16x16x32_bf16 v[72:75], v[152:155], v[160:163], v[72:75]\n\t" \
  "v_mfma_f32_16x16x32_bf16 v[92:95], v[156:159], v[176:179], v[92:95]\n\t" \
  "v_mfma_f32_16x16x32_bf16 v[88:91], v[156:159], v[172:175], v[88:91]\n\t" \
  "v_mfma_f32_16x16x32_bf16 v[84:87], v[156:159], v[168:171], v[84:87]\n\t" \
  "v_mfma_f32_16x16x32_bf16 v[80:83], v[156:159], v[164:167], v[80:83]\n\t" \
  "v_mfma_f32_16x16x32_bf16 v[76:79], v[156:159], v[160:163], v[76:79]\n\t" \
  "s_waitcnt lgkmcnt(0)\n\t" \
  "v_mfma_f32_16x16x32_bf16 v[92:95], v[96:99], v[140:143], v[92:95]\n\t" \
  "ds_read_b128 v[144:147], %[aa] offset:448\n\t" \
  "v_mfma_f32_16x16x32_bf16 v[88:91], v[96:99], v[136:139], v[88:91]\n\t" \
  "ds_read_b128 v[148:151], %[aa] offset:960\n\t" \
  "v_mfma_f32_16x16x32_bf16 v[84:87], v[96:99], v[132:135], v[84:87]\n\t" \
  "ds_read_b128 v[152:155], %[aa] offset:1472\n\t" \
  "v_mfma_f32_16x16x32_bf16 v[80:83], v[96:99], v[128:131], v[80:83]\n\t" \
  "ds_read_b128 v[156:159], %[aa] offset:1984\n\t" \
  "v_mfma_f32_16x16x32_bf16 v[76:79], v[96:99], v[124:127], v[76:79]\n\t" \
  "ds_read_b128 v[188:191], %[ab] offset:3584\n\t" \
  "v_mfma_f32_16x16x32_bf16 v[72:75], v[96:99], v[120:123], v[72:75]\n\t" \
  "ds_read_b128 v[184:187], %[ab] offset:3072\n\t" \
  "v_mfma_f32_16x16x32_bf16 v[68:71], v[96:99], v[116:119], v[68:71]\n\t" \
  "ds_read_b128 v[180:183], %[ab] offset:2560\n\t" \
  "v_mfma_f32_16x16x32_bf16 v[64:67], v[96:99], v[112:115], v[64:67]\n\t" \
  "ds_read_b128 v[176:179], %[ab] offset:2048\n\t" \
  "v_mfma_f32_16x16x32_bf16 v[92:95], v[100:103], v[136:139], v[92:95]\n\t" \
  "ds_read_b128 v[172:175], %[ab] offset:1536\n\t" \
  "v_mfma_f32_16x16x32_bf16 v[88:91], v[100:103], v[132:135], v[88:91]\n\t" \
  "ds_read_b128 v[168:171], %[ab] offset:1024\n\t" \
  "v_mfma_f32_16x16x32_bf16 v[84:87], v[100:103], v[128:131], v[84:87]\n\t" \
  "ds_read_b128 v[164:167], %[ab] offset:512\n\t" \
  "v_mfma_f32_16x16x32_bf16 v[80:83], v[100:103], v[124:127], v[80:83]\n\t" \
  "ds_read_b128 v[160:163], %[ab] offset:0\n\t" \
  "v_mfma_f32_16x16x32_bf16 v[76:79], v[100:103], v[120:123], v[76:79]\n\t" \
  "v_mfma_f32_16x16x32_bf16 v[72:75], v[100:103], v[116:119], v[72:75]\n\t" \
  "v_mfma_f32_16x16x32_bf16 v[68:71], v[100:103], v[112:115], v[68:71]\n\t" \
  "v_mfma_f32_16x16x32_bf16 v[92:95], v[104:107], v[132:135], v[92:95]\n\t" \
  "v_mfma_f32_16x16x32_bf16 v[88:91], v[104:107], v[128:131], v[88:91]\n\t" \
  "v_mfma_f32_16x16x32_bf16 v[84:87], v[104:107], v[124:127], v[84:87]\n\t" \
  "v_mfma_f32_16x16x32_bf16 v[80:83], v[104:107], v[120:123], v[80:83]\n\t" \
  "v_mfma_f32_16x16x32_bf16 v[76:79], v[104:107], v[116:119], v[76:79]\n\t" \
  "v_mfma_f32_16x16x32_bf16 v[72:75], v[104:107], v[112:115], v[72:75]\n\t" \
  "v_mfma_f32_16x16x32_bf16 v[92:95], v[108:111], v[128:131], v[92:95]\n\t" \
  "v_mfma_f32_16x16x32_bf16 v[88:91], v[108:111], v[124:127], v[88:91]\n\t" \
  "v_mfma_f32_16x16x32_bf16 v[84:87], v[108:111], v[120:123], v[84:87]\n\t" \
  "v_mfma_f32_16x16x32_bf16 v[80:83], v[108:111], v[116:119], v[80:83]\n\t" \
  "v_mfma_f32_16x16x32_bf16 v[76:79], v[108:111], v[112:115], v[76:79]\n\t" \
  "s_waitcnt lgkmcnt(0)\n\t" \
  "v_mfma_f32_16x16x32_bf16 v[92:95], v[144:147], v[188:191], v[92:95]\n\t" \
  "v_mfma_f32_16x16x32_bf16 v[88:91], v[144:147], v[184:187], v[88:91]\n\t" \
  "v_mfma_f32_16x16x32_bf16 v[84:87], v[144:147], v[180:183], v[84:87]\n\t" \
  "v_mfma_f32_16x16x32_bf16 v[80:83], v[144:147], v[176:179], v[80:83]\n\t" \
  "v_mfma_f32_16x16x32_bf16 v[76:79], v[144:147], v[172:175], v[76:79]\n\t" \
  "v_mfma_f32_16x16x32_bf16 v[72:75], v[144:147], v[168:171], v[72:75]\n\t" \
  "v_mfma_f32_16x16x32_bf16 v[68:71], v[144:147], v[164:167], v[68:71]\n\t" \
  "v_mfma_f32_16x16x32_bf16 v[64:67], v[144:147], v[160:163], v[64:67]\n\t" \
  "v_mfma_f32_16x16x32_bf16 v[92:95], v[148:151], v[184:187], v[92:95]\n\t" \
  "v_mfma_f32_16x16x32_bf16 v[88:91], v[148:151], v[180:183], v[88:91]\n\t" \
  "v_mfma_f32_16x16x32_bf16 v[84:87], v[148:151], v[176:179], v[84:87]\n\t" \
  "v_mfma_f32_16x16x32_bf16 v[80:83], v[148:151], v[172:175], v[80:83]\n\t" \
  "v_mfma_f32_16x16x32_bf16 v[76:79], v[148:151], v[168:171], v[76:79]\n\t" \
  "v_mfma_f32_16x16x32_bf16 v[72:75], v[148:151], v[164:167], v[72:75]\n\t" \
  "v_mfma_f32_16x16x32_bf16 v[68:71], v[148:151], v[160:163], v[68:71]\n\t" \
  "v_mfma_f32_16x16x32_bf16 v[92:95], v[152:155], v[180:183], v[92:95]\n\t" \
  "v_mfma_f32_16x16x32_bf16 v[88:91], v[152:155], v[176:179], v[88:91]\n\t" \
  "v_mfma_f32_16x16x32_bf16 v[84:87], v[152:155], v[172:175], v[84:87]\n\t" \
  "v_mfma_f32_16x16x32_bf16 v[80:83], v[152:155], v[168:171], v[80:83]\n\t" \
  "v_mfma_f32_16x16x32_bf16 v[76:79], v[152:155], v[164:167], v[76:79]\n\t" \
  "v_mfma_f32_16x16x32_bf16 v[72:75], v[152:155], v[160:163], v[72:75]\n\t" \
  "v_mfma_f32_16x16x32_bf16 v[92:95], v[156:159], v[176:179], v[92:95]\n\t" \
  "v_mfma_f32_16x16x32_bf16 v[88:91], v[156:159], v[172:175], v[88:91]\n\t" \
  "v_mfma_f32_16x16x32_bf16 v[84:87], v[156:159], v[168:171], v[84:87]\n\t" \
  "v_mfma_f32_16x16x32_bf16 v[80:83], v[156:159], v[164:167], v[80:83]\n\t" \
  "v_mfma_f32_16x16x32_bf16 v[76:79], v[156:159], v[160:163], v[76:79]\n\t" \
  "s_nop 7\n\t" \
  ""
#define CONV_MAIN_CLOB "v144","v145","v146","v147","v148","v149","v150","v151","v152","v153","v154","v155","v156","v157","v158","v159","v160","v161","v162","v163","v164","v165","v166","v167","v168","v169","v170","v171","v172","v173","v174","v175","v176","v177","v178","v179","v180","v181","v182","v183","v184","v185","v186","v187","v188","v189","v190","v191"

struct G1Ld { unsigned x[4], w[4]; };
DI G1Ld g1_lanes(int lane) {
  const int r8 = lane >> 3, sl = lane & 7;
  G1Ld v;
#pragma unroll
  for (int p = 0; p < 4; ++p) {
    v.x[p] = (r8 + 8 * p) * 1280 + ((sl ^ (r8 >> 1) ^ (4 * (p & 1))) << 4) - 1024 * p;
    v.w[p] = (r8 + 8 * p) * 3072 + ((sl ^ (r8 & 2) ^ (4 * (p & 1))) << 4) - 1024 * p;
  }
  return v;
}
template <int NT> DI void g1_issue(v4u rx, v4u rg, v4u ru, const G1Ld& v, int lw, int s, unsigned lb, int row0, int ch) {
  const unsigned xso = (row0 + lw * 16 * NT) * 1280 + s * 128, wso = ch * 128 + (lw * 32 + s * 64) * 3072;
  dma4<0>(rg, lb + XB + lw * 4096, v.w[0], v.w[1], v.w[2], v.w[3], wso);
  dma4<0>(ru, lb + XB + 8192 + lw * 4096, v.w[0], v.w[1], v.w[2], v.w[3], wso);
#pragma unroll
  for (int p = 0; p < NT / 2; ++p) dma4<0>(rx, lb + (lw * 2 * NT + 4 * p) * 1024, v.x[0], v.x[1], v.x[2], v.x[3], xso + p * 32 * 1280);
}
DI unsigned nxt(unsigned so) { return so == 0 ? 2 * STAGE : so - STAGE; }
DI unsigned rem_up(unsigned so) { return so == 0 ? LX : so == STAGE ? LX + 8192 : L_U; }
DI void g1_issue_rem(v4u rx, v4u rg, v4u ru, const G1Ld& v, int lw, int s, unsigned lb, int ch, int ch2) {
  g1_issue<4>(rx, rg, ru, v, lw, s, lb, 1920, ch);
  const unsigned wso = ch2 * 128 + (lw * 32 + s * 64) * 3072;
  dma4<0>(rg, lb + 16384 + lw * 4096, v.w[0], v.w[1], v.w[2], v.w[3], wso);
  dma4<0>(ru, rem_up(lb) + lw * 4096, v.w[0], v.w[1], v.w[2], v.w[3], wso);
}
template <int NP, int S0, class Issue> DI void g1_loader(int rot, unsigned so, Issue issue) {
#pragma unroll 1
  for (int s = S0; s < 10; ++s) {
    if (s >= S0 + 2) wait_vm<NP>();
    if (s >= 2) BAR(s - 2);
    issue(s + rot >= 10 ? s + rot - 10 : s + rot, so);
    so = nxt(so);
  }
  wait_vm<NP>(); BAR(8);
}

DI void colsum(rsrc_t rw, unsigned off, int k, int lane) {
  const int c = lane & 15, g = lane >> 4, q = (lane >> 2) & 3, p = lane & 3;
  const bf16x8 ones = {0x3f80, 0x3f80, 0x3f80, 0x3f80, 0x3f80, 0x3f80, 0x3f80, 0x3f80};
  f32x4 r[2];
#pragma unroll
  for (int a = 0; a < 2; ++a) {
    const unsigned ad = XB + (k >> 1) * 8192 + (8 * g + q) * 128 + (((4 * (k & 1) + 2 * a + (p >> 1)) ^ (q & 2) ^ ((g & 1) << 2)) << 4) + 8 * (p & 1);
    r[a] = mfma(trd(ad + 4096, ad + 4608), ones, mfma(trd(ad, ad + 512), ones, f32x4{0.f, 0.f, 0.f, 0.f}));
  }
  f32x4 v;
#pragma unroll
  for (int e = 0; e < 4; ++e) v[e] = c ? r[1][e] : r[0][e];
  if (c < 2) st128<SC1>(__builtin_bit_cast(v4u, v), rw, off + k * 128 + (16 * c + 4 * g) * 4);
}

struct G1Rd { unsigned aA[2], aB[2]; };
template <int NT> DI void g1_setup(int wid, int lane, G1Rd& r, f32x4 (&acc)[4][NT]) {
  const int wa = wid & 1, wt = wid >> 1;
  const int c = lane & 15, g = lane >> 4, q = (lane >> 2) & 3, p = lane & 3;
  unsigned (&aA)[2] = r.aA; unsigned (&aB)[2] = r.aB;
#pragma unroll
  for (int a = 0; a < 2; ++a) aA[a] = XB + (8 * g + q) * 128 + (((4 * wa + 2 * a + (p >> 1)) ^ (q & 2) ^ ((g & 1) << 2)) << 4) + 8 * (p & 1);
#pragma unroll
  for (int ks = 0; ks < 2; ++ks) aB[ks] = (16 * NT * wt + c) * 128 + (((4 * ks + g) ^ (c >> 1)) << 4);
#pragma unroll
  for (int a = 0; a < 4; ++a)
#pragma unroll
    for (int t = 0; t < NT; ++t) { acc[a][t] = f32x4{0.f, 0.f, 0.f, 0.f}; if (NT != 6) asm volatile("" : "+v"(acc[a][t])); }
  asm volatile("" : "+v"(aA[0]), "+v"(aA[1]), "+v"(aB[0]), "+v"(aB[1]));
}
template <int NT> struct G1Ep { f32x4 cs[4], st[NT]; };
template <int NT> DI void g1_ep_read(int wid, int lane, unsigned st_off, unsigned cs_off, G1Ep<NT>& e) {
  const int wa = wid & 1, wt = wid >> 1, c = lane & 15, g = lane >> 4;
#pragma unroll
  for (int a = 0; a < 4; ++a) e.cs[a] = *(LAS f32x4*)lds(cs_off + (a >> 1) * 256 + (32 * wa + 16 * (a & 1) + 4 * g) * 4);
#pragma unroll
  for (int t = 0; t < NT; ++t) e.st[t] = *(LAS f32x4*)lds(st_off + (16 * NT * wt + 16 * t + c) * 16);
}
DI void g1_main_rem(const G1Rd& r, unsigned db, f32x4 (&acc4)[4][4], f32x4 (&acc2)[4][2]) {
  const unsigned (&aA)[2] = r.aA; const unsigned (&aB)[2] = r.aB;
  bf16x8 fa[2][8], fb[2][6];
  auto rd = [&](int buf, int ks, unsigned o) {
    const unsigned ou = rem_up(o) - XB;
#pragma unroll
    for (int a = 0; a < 4; ++a) { unsigned ad = aA[a & 1] + o + (a >> 1) * 8192 + ks * 4096; fa[buf][a] = trd(ad, ad + 512); }
#pragma unroll
    for (int t = 0; t < 4; ++t) fb[buf][t] = rd128(aB[ks] + o + t * 2048);
#pragma unroll
    for (int a = 0; a < 4; ++a) { unsigned ad = aA[a & 1] + (a < 2 ? o - 8192 : ou) + ks * 4096; fa[buf][4 + a] = trd(ad, ad + 512); }
#pragma unroll
    for (int t = 0; t < 2; ++t) fb[buf][4 + t] = rd128(aB[ks] + o + db + t * 2048);
  };
  auto mm = [&](int buf) {
#pragma unroll
    for (int t = 0; t < 4; ++t)
#pragma unroll
      for (int a = 0; a < 4; ++a) acc4[a][t] = mfma(fa[buf][a], fb[buf][t], acc4[a][t]);
#pragma unroll
    for (int t = 0; t < 2; ++t)
#pragma unroll
      for (int a = 0; a < 4; ++a) acc2[a][t] = mfma(fa[buf][4 + a], fb[buf][4 + t], acc2[a][t]);
  };
  auto ilv = [&]() {
#pragma unroll
    for (int i = 0; i < 22; ++i) { __builtin_amdgcn_sched_group_barrier(0x008, 1, 0); __builtin_amdgcn_sched_group_barrier(0x100, 1, 0); }
    __builtin_amdgcn_sched_group_barrier(0x008, 2, 0);
    __builtin_amdgcn_sched_barrier(0);
  };
  unsigned so = 0;
  BAR(0);
  STAMP(2); RSTAMP(31);
  rd(0, 0, so);
#pragma unroll 1
  for (int s = 0; s < 9; ++s) {
    rd(1, 1, so); mm(0); ilv();
    so = nxt(so);
    wait_lg();
    BAR(s + 1);
    __builtin_amdgcn_sched_barrier(0);
    rd(0, 0, so); mm(1); ilv();
  }
  rd(1, 1, so); mm(0); ilv();
  mm(1);
}
struct G1Acc { f32x16 a[6]; unsigned csa, sta; };
DI void g1_setup6(int wid, int lane, G1Acc& z) {
#pragma unroll
  for (int t = 0; t < 6; ++t) z.a[t] = f32x16{};
  z.csa = L_CS + (32 * (wid & 1) + 4 * (lane >> 4)) * 4; z.sta = L_ST + (96 * (wid >> 1) + (lane & 15)) * 16;
  asm volatile("" : "+{v[64:79]}"(z.a[0]), "+{v[80:95]}"(z.a[1]), "+{v[96:111]}"(z.a[2]), "+{v[112:127]}"(z.a[3]), "+{v[128:143]}"(z.a[4]),
               "+{v[144:159]}"(z.a[5]), "+v"(z.csa), "+v"(z.sta));
}
DI void g1_main6(const G1Rd& r, G1Acc& z, f32x4 (&acc)[4][6], G1Ep<6>& ep) {
  f32x16 (&a)[6] = z.a; f32x16 cs, s0; f32x8 s1; int cnt;
  const unsigned csa = z.csa, sta = z.sta;
  asm volatile(G1_MAIN_ASM
               : "+{v[64:79]}"(a[0]), "+{v[80:95]}"(a[1]), "+{v[96:111]}"(a[2]), "+{v[112:127]}"(a[3]), "+{v[128:143]}"(a[4]), "+{v[144:159]}"(a[5]),
                 "=&{v[160:175]}"(cs), "=&{v[176:191]}"(s0), "=&{v[192:199]}"(s1), [cnt] "=&s"(cnt)
               : [a0] "v"(r.aA[0]), [a1] "v"(r.aA[1]), [a2] "v"(r.aB[0]), [a3] "v"(r.aB[1]), [cs] "v"(csa), [st] "v"(sta)
               : "memory", "scc", G1_MAIN_CLOB G1_STAMP_CLOB);
#pragma unroll
  for (int t = 0; t < 6; ++t)
#pragma unroll
    for (int i = 0; i < 16; ++i) acc[i >> 2][t][i & 3] = a[t][i];
#pragma unroll
  for (int i = 0; i < 16; ++i) { ep.cs[i >> 2][i & 3] = cs[i]; ep.st[i >> 2][i & 3] = s0[i]; }
#pragma unroll
  for (int i = 0; i < 8; ++i) ep.st[4 + (i >> 2)][i & 3] = s1[i];
}
template <int NT, bool LAST> DI void g1_epilogue(rsrc_t rw, int wid, int lane, int t16_0, int ch, const G1Ep<NT>& ep, const f32x4 (&acc)[4][NT]) {
  const int wa = wid & 1, wt = wid >> 1;
  const f32x4 (&cs)[4] = ep.cs; const f32x4 (&st)[NT] = ep.st;
#pragma unroll
  for (int t = 0; t < NT; ++t) {
    const float nm = st[t][0], qq = st[t][1], rl = st[t][2];
    v4u o;
#pragma unroll
    for (int a = 0; a < 2; ++a) {
      f32x4 tg = cs[a] * nm + acc[a][t], tu = cs[2 + a] * nm + acc[2 + a][t];
      f32x4 ar = tg * rl, e;
#pragma unroll
      for (int r = 0; r < 4; ++r) e[r] = __builtin_amdgcn_exp2f(ar[r]);
      f32x4 dn = e * qq + qq, pr = tg * tu;
#pragma unroll
      for (int r = 0; r < 4; ++r) dn[r] = __builtin_amdgcn_rcpf(dn[r]);
      pr *= dn;
      if (LAST && t == NT - 1 && a == 1) { asm volatile("" : "+v"(pr)); lds_set(L_MISC + 48 + wid * 4); }
      o[2 * a] = pk(pr[0], pr[1]); o[2 * a + 1] = pk(pr[2], pr[3]);
    }
    st128<SC1>(o, rw, lane * 16 + WS_H + ((t16_0 + NT * wt + t) * 48 + 2 * ch + wa) * 1024);
  }
}

template <int NM> DI void kgen(const unsigned (&w)[6], float dt, int lane, unsigned tb, unsigned done, f32x4 (&acc)[2][2]) {
  constexpr int RS = NM * 256 + 32, KS = NM * 4;
  float lr[NM], tr[NM]; f32x2 W[NM];
#pragma unroll
  for (int m = 0; m < NM; ++m) {
    float v[6];
#pragma unroll
    for (int i = 0; i < 6; ++i) v[i] = m == 0 ? blo(w[i]) : bhi(w[i]);
    lr[m] = dt * v[0] * 1.44269504f; tr[m] = dt * v[1] * 0.15915494f;
    const float br = dt * v[2], bi = dt * v[3];
    W[m] = f32x2{v[4] * br - v[5] * bi, -(v[4] * bi + v[5] * br)};
  }
  asm volatile("" : "+v"(lr[0]), "+v"(W[0]));
  STAMP(13);
  unsigned wa = tb + lane * 4 * NM, fa = tb + (lane & 15) * RS + (lane >> 4) * 16;
  asm volatile("" : "+v"(wa), "+v"(fa));
  auto put = [&](int row, const f32x2* z) {
    if (NM == 2) *(LAS v2u*)lds(wa + row * RS) = v2u{pk2(z[0]), pk2(z[NM - 1])};
    else *(LAS unsigned*)lds(wa + row * RS) = pk2(z[0]);
  };
  f32x2 c[8][NM], hi[NM], t[NM], c8[NM];
#pragma unroll
  for (int m = 0; m < NM; ++m) {
    c[0][m] = f32x2{1.f, 0.f}; c[1][m] = cpow(lr[m], tr[m], 8.f);
    c[2][m] = cmul(c[1][m], c[1][m]); c[3][m] = cmul(c[2][m], c[1][m]); c[4][m] = cmul(c[2][m], c[2][m]);
    c[5][m] = cmul(c[4][m], c[1][m]); c[6][m] = cmul(c[4][m], c[2][m]); c[7][m] = cmul(c[4][m], c[3][m]);
    c8[m] = cmul(c[4][m], c[4][m]); hi[m] = c8[m];
  }
  bf16x8 fb[2][KS];
#pragma unroll
  for (int j = 0; j < 8; ++j) put(j, c[j]);
#pragma unroll
  for (int a = 1; a < 4; ++a) {
    if (a == 2) {
#pragma unroll
      for (int s = 0; s < KS; ++s) fb[0][s] = rd128(fa + s * 64);
    }
    put((8 * a) & 15, hi);
#pragma unroll
    for (int j = 1; j < 8; ++j) {
#pragma unroll
      for (int m = 0; m < NM; ++m) t[m] = cmul(hi[m], c[j][m]);
      put((8 * a + j) & 15, t);
    }
    if (a < 3) {
#pragma unroll
      for (int m = 0; m < NM; ++m) hi[m] = cmul(hi[m], c8[m]);
    }
  }
#pragma unroll
  for (int s = 0; s < KS; ++s) fb[1][s] = rd128(fa + s * 64);
  STAMP(14);
  if (NM == 2 && TRIG == 0) lds_set(done);
  f32x2 z1[NM], zb[NM], r0[NM], r4[NM];
#pragma unroll
  for (int m = 0; m < NM; ++m) { z1[m] = cpow(lr[m], -tr[m], 1.f); zb[m] = cpow(lr[m], -tr[m], 256.f); t[m] = W[m]; }
#pragma unroll
  for (int b = 0; b < 8; ++b) {
    put(b, t);
    if ((b & 1) == 0) {
      f32x2 u[NM];
#pragma unroll
      for (int m = 0; m < NM; ++m) { u[m] = cmul(t[m], zb[m]); if (b == 0) r0[m] = u[m]; if (b == 4) r4[m] = u[m]; }
      put(8 + b / 2, u);
    }
    if (b < 7) {
#pragma unroll
      for (int m = 0; m < NM; ++m) t[m] = cmul(t[m], z1[m]);
    }
  }
  if (NM == 2 && TRIG == 1) lds_set(done);
#pragma unroll
  for (int i = 0; i < 2; ++i) {
#pragma unroll
    for (int m = 0; m < NM; ++m) { r0[m] = cmul(r0[m], zb[m]); r4[m] = cmul(r4[m], zb[m]); }
    put(12 + 2 * i, r0); put(13 + 2 * i, r4);
  }
  bf16x8 fr[2][KS];
#pragma unroll
  for (int s = 0; s < KS; ++s) fr[0][s] = rd128(fa + s * 64);
#pragma unroll
  for (int i = 0; i < 5; ++i) {
#pragma unroll
    for (int m = 0; m < NM; ++m) r0[m] = cmul(r0[m], zb[m]);
    put(i, r0);
  }
  if (NM == 2 && TRIG == 2) lds_set(done);
#pragma unroll
  for (int s = 0; s < KS; ++s) fr[1][s] = rd128(fa + s * 64);
  __builtin_amdgcn_sched_barrier(0);
  STAMP(15);
#pragma unroll
  for (int i = 0; i < 4; ++i) acc[i >> 1][i & 1] = f32x4{0.f, 0.f, 0.f, 0.f};
#pragma unroll
  for (int rt = 0; rt < 2; ++rt)
#pragma unroll
    for (int s = 0; s < KS; ++s) { acc[rt][0] = mfma(fr[rt][s], fb[0][s], acc[rt][0]); acc[rt][1] = mfma(fr[rt][s], fb[1][s], acc[rt][1]); }
}
struct LnOut { s16x4 tr[2][10]; v2u st; };
DI void ln8(rsrc_t rx, int tb, int lane, unsigned tile, LnOut& out) {
  const int r4 = lane >> 4, i = lane & 15;
  v4u raw[2][5];
#pragma unroll
  for (int h = 0; h < 2; ++h)
#pragma unroll
    for (int k = 0; k < 5; ++k) raw[h][k] = __builtin_amdgcn_raw_buffer_load_b128(rx, r4 * 1280 + i * 16 + k * 256, (tb * 8 + h * 4) * 1280, 0);
  const unsigned ta = tile + ((lane >> 2) & 3) * LN_RS + (16 * r4 + 4 * (lane & 3)) * 2;
#pragma unroll
  for (int h = 0; h < 2; ++h) {
    if (h == 0) { asm volatile("" : "+v"(raw[0][4])); STAMP(13); asm volatile("" : "+v"(raw[1][4])); STAMP(15); } else STAMP(14);
    float s1 = 0.f, s2 = 0.f, s3 = 0.f, s4 = 0.f;
    const bf16x2 one2 = __builtin_bit_cast(bf16x2, 0x3f803f80u);
#pragma unroll
    for (int k = 0; k < 10; ++k) {
      const unsigned w0 = raw[h][k >> 1][2 * (k & 1)], w1 = raw[h][k >> 1][2 * (k & 1) + 1];
      const bf16x2 p0 = __builtin_bit_cast(bf16x2, w0), p1 = __builtin_bit_cast(bf16x2, w1);
      s1 = __builtin_amdgcn_fdot2_f32_bf16(p0, one2, s1, false); s2 = __builtin_amdgcn_fdot2_f32_bf16(p0, p0, s2, false);
      s3 = __builtin_amdgcn_fdot2_f32_bf16(p1, one2, s3, false); s4 = __builtin_amdgcn_fdot2_f32_bf16(p1, p1, s4, false);
    }
    const float mean = rowsum16(s1 + s3) * (1.f / D);
    const float var = rbf(fmaxf(rowsum16(s2 + s4) * (1.f / D) - mean * mean, 0.f));
    const float m = rbf(mean), den = rbf(sqrtf(rbf(var + 1e-5f)));
    const float ri = __builtin_amdgcn_rcpf(den), nb = -m * ri;
    if (h == 0 || (i & 1)) out.st = v2u{__float_as_uint(m), __float_as_uint(den)};
#pragma unroll
    for (int k = 0; k < 5; ++k) {
      v4u o;
#pragma unroll
      for (int e = 0; e < 4; ++e) { const unsigned w = raw[h][k][e]; o[e] = pk2(f32x2{blo(w), bhi(w)} * ri + nb); }
      *(LAS v4u*)lds(tile + r4 * LN_RS + i * 16 + k * 256) = o;
    }
    cfence();
#pragma unroll
    for (int it = 0; it < 10; ++it) out.tr[h][it] = __builtin_amdgcn_ds_read_tr16_b64_v4i16((LAS s16x4*)lds(ta + it * 128));
  }
}
DI void ln8_store(rsrc_t rw, int tb, int lane, const LnOut& in) {
  if ((lane & 15) < 2) __builtin_amdgcn_raw_buffer_store_b64(in.st, rw, ((lane & 1) * 4 + (lane >> 4)) * 8, WS_STATS + tb * 128, SC1);
#pragma unroll
  for (int it = 0; it < 10; ++it) {
    bf16x8 o = __builtin_shufflevector(in.tr[0][it], in.tr[1][it], 0, 1, 2, 3, 4, 5, 6, 7);
    st128<SC1>(__builtin_bit_cast(v4u, o), rw, lane * 16 + it * 1024 + WS_U8 + tb * 10240);
  }
}

DI unsigned prm(unsigned hi, unsigned lo, unsigned sel) { return __builtin_amdgcn_perm(hi, lo, sel); }
DI unsigned colo(int col) { return (col & 15) * 16 + ((col >> 4) & 1) * 8; }
template <int S> DI v4u kchunk(unsigned raw, int lane) {
  const int i = lane & 31, up = lane >> 5;
  if (S == 0) {
    const v2u a0 = *(LAS v2u*)lds(raw + colo(i) + 512 * up);
    const v2u a1 = *(LAS v2u*)lds(raw + (up ? (i == 31 ? 768 : 512 + colo(i + 1)) : 256 + colo(i)));
    const unsigned s0 = up ? 0x03020504u : 0x05040706u, s1 = up ? 0x01000100u : 0x05040706u, s2 = up ? 0x03020504u : 0x05040706u,
                   s3 = up ? 0x01000100u : 0x01000302u;
    return v4u{prm(up ? a1.x : a1.y, a0.y, s0), prm(a1.x, a0.y, s1), prm(a0.y, a0.x, s2), prm(0, a0.x, s3)};
  } else {
    const unsigned b0 = *(LAS unsigned*)lds(raw + 768 + colo(i) + 4 * up);
    const unsigned b1 = *(LAS unsigned*)lds(raw + (lane == 63 ? 1024 : 768 + colo(i + 1) + 4 * ((lane + 1) >> 5)));
    return v4u{prm(0, b1, 0x01000100), prm(0, b0, 0x03020302), prm(0, b0, 0x01000302), prm(0, b0, 0x01000100)};
  }
}
DI unsigned vmov(unsigned x) { unsigned r; asm("v_mov_b32 %0, %1" : "=v"(r) : "v"(x)); return r; }
struct HWin { v8u w, e, o0, o1; };
DI void hwin(v4u x, v4u y, HWin& h) {
  h.w = __builtin_shufflevector(x, y, 0, 1, 2, 3, 4, 5, 6, 7);
  asm volatile("" : "+v"(h.w));
#pragma unroll
  for (int k = 0; k < 6; ++k) {
    h.e[k] = vmov(h.w[k + 1]);
    h.o0[k] = __builtin_amdgcn_alignbit(h.w[k + 1], h.w[k], 16);
    h.o1[k] = __builtin_amdgcn_alignbit(h.w[k + 2], h.w[k + 1], 16);
    asm volatile("" : "+v"(h.o1[k]));
  }
  asm volatile("" : "+v"(h.e), "+v"(h.o0), "+v"(h.o1));
}
DI void hwrite(const HWin& h, unsigned a) {
  *(LAS v4u*)lds(a + 7 * H_CS) = __builtin_shufflevector(h.w, h.w, 0, 1, 2, 3);
  *(LAS v4u*)lds(a + 3 * H_CS) = __builtin_shufflevector(h.w, h.w, 2, 3, 4, 5);
  *(LAS v4u*)lds(a + 5 * H_CS) = __builtin_shufflevector(h.e, h.e, 0, 1, 2, 3);
  *(LAS v4u*)lds(a + 1 * H_CS) = __builtin_shufflevector(h.e, h.e, 2, 3, 4, 5);
  *(LAS v4u*)lds(a + 6 * H_CS) = __builtin_shufflevector(h.o0, h.o0, 0, 1, 2, 3);
  *(LAS v4u*)lds(a + 2 * H_CS) = __builtin_shufflevector(h.o0, h.o0, 2, 3, 4, 5);
  *(LAS v4u*)lds(a + 4 * H_CS) = __builtin_shufflevector(h.o1, h.o1, 0, 1, 2, 3);
  *(LAS v4u*)lds(a + 0 * H_CS) = __builtin_shufflevector(h.o1, h.o1, 2, 3, 4, 5);
}
struct ConvPre { v4u x0, x1; unsigned kk, k3; bf16x8 kt[4], b9, b7; };
DI void conv_masks(int lane, ConvPre& p) {
#pragma unroll
  for (int jj = 0; jj < 8; ++jj) {
    const int dl = 8 * (lane >> 4) + jj - 8 - (lane & 15);
    p.b9[jj] = (dl >= -4 && dl <= 4) ? 0x3f80 : 0; p.b7[jj] = (dl >= -3 && dl <= 3) ? 0x3f80 : 0;
  }
  asm volatile("" : "+v"(p.b9), "+v"(p.b7));
}
DI void conv_pre(unsigned raw, int lane, ConvPre& p) {
  p.x0 = kchunk<0>(raw, lane); p.x1 = kchunk<1>(raw, lane);
  p.kk = *(LAS uint16_t*)lds(raw + 1024);
  const int n0 = 128 - 2 * lane, n1 = n0 - 1;
  const unsigned c0 = *(LAS uint16_t*)lds(raw + 1024 + (n0 == 128 ? 256 : colo(n0) + 2 * (n0 >> 5)));
  const unsigned c1 = *(LAS uint16_t*)lds(raw + 1024 + colo(n1) + 2 * (n1 >> 5));
  p.k3 = c0 | (c1 << 16);
  asm volatile("" : "+v"(p.x0), "+v"(p.x1), "+v"(p.kk), "+v"(p.k3));
}
DI void conv_image(ConvPre& p, unsigned hb, unsigned k3, int lane) {
  HWin h[3];
  v4u y0, y1, cl;
#pragma unroll
  for (int k = 0; k < 4; ++k) {
    y0[k] = __builtin_amdgcn_update_dpp(0u, p.x0[k], 0x138, 0xf, 0xf, false);
    y1[k] = __builtin_amdgcn_update_dpp((unsigned)__builtin_amdgcn_readlane(p.x0[k], 63), p.x1[k], 0x138, 0xf, 0xf, false);
    cl[k] = __builtin_amdgcn_readlane(p.x1[k], 63);
  }
  hwin(p.x0, y0, h[0]);
  hwin(p.x1, y1, h[1]);
  const unsigned kk = p.kk, k2 = kk | (kk << 16), m0 = lane == 0 ? ~0u : 0u, m1 = lane == 1 ? ~0u : 0u;
  hwin(v4u{0, (kk << 16) & m0, k2 & m0, k2 & m0},
       v4u{cl[0] & m0, (cl[1] & m0) | ((kk << 16) & m1), (cl[2] & m0) | (k2 & m1), (cl[3] & m0) | (k2 & m1)}, h[2]);
  unsigned ka = k3 + (254 - 2 * (lane & 15) + 8 * (lane >> 4)) * 2 - 448, a0 = hb + (1 + lane) * 16, a2 = hb + (lane < 3 ? 129 + lane : lane == 3 ? 0 : 132) * 16;
  unsigned kw = k3 + lane * 4;
  asm volatile("" : "+v"(ka), "+v"(a0), "+v"(a2), "+v"(kw));
  STAMP(3);
  *(LAS unsigned*)lds(kw) = p.k3;
  if (lane < 16) *(LAS unsigned*)lds(kw + 256) = 0;
#pragma unroll
  for (int f = 0; f < 4; ++f) {
    v4u t;
#pragma unroll
    for (int e = 0; e < 4; ++e) t[e] = *(LAS unsigned*)lds(ka + (3 - f) * 64 + e * 4);
    p.kt[f] = __builtin_bit_cast(bf16x8, t);
  }
  hwrite(h[0], a0);
  hwrite(h[1], a0 + 1024);
  if (lane < 5) hwrite(h[2], a2);
}

DI void conv(rsrc_t rw, int cw, int d, int lane, const ConvPre& p, unsigned go) {
  const unsigned hb = L_H + cw * H_SZ, ub = L_U + cw * U_SZ;
  const int c = lane & 15, g = lane >> 4;
  unsigned aa = hb + (c & 7) * H_CS + (3 + (c >> 3) - g) * 16, ab = ub + 32 + 32 * c + 16 * g;
  f32x4 acc[8] = {};
  v16u fa, fb0, fb1;
  asm volatile(CONV_FIRST_ASM : "=&{v[96:111]}"(fa), "=&{v[112:127]}"(fb0), "=&{v[128:143]}"(fb1) : [aa] "v"(aa), [ab] "v"(ab) : "memory");
  {
    const bf16x8 fx = rd128(aa + 2048);
    bf16x8 ue[4];
#pragma unroll
    for (int f = 0; f < 4; ++f) ue[f] = rd128(ab + 432 + f * 512);
#pragma unroll 1
    for (int spin = 0; spin < 64 * SPIN_MAX && *(volatile LAS unsigned*)lds(go) == 0; ++spin) {}
    asm volatile("s_waitcnt lgkmcnt(0)" : "+{v[96:111]}"(fa), "+{v[112:127]}"(fb0), "+{v[128:143]}"(fb1)::"memory");
    STAMP(5);
    __builtin_amdgcn_s_setprio(CPRIO);
#pragma unroll
    for (int f = 0; f < 4; ++f) {
      const bf16x8 uo = __builtin_bit_cast(bf16x8, v4u{fb0[4 * f], fb0[4 * f + 1], fb0[4 * f + 2], fb0[4 * f + 3]});
      acc[4 + f] = mfma(fx, uo, acc[4 + f]);
      const f32x4 ve = mfma(ue[f], p.b9, f32x4{0.f, 0.f, 0.f, 0.f}), vo = mfma(uo, p.b7, f32x4{0.f, 0.f, 0.f, 0.f});
      const bf16x8 vf = __builtin_bit_cast(bf16x8, v4u{pk(ve[0], vo[0]), pk(ve[1], vo[1]), pk(ve[2], vo[2]), pk(ve[3], vo[3])});
#pragma unroll
      for (int n = 4 + f; n < 8; ++n) acc[n] = mfma(vf, p.kt[n - f - 4], acc[n]);
    }
  }
  STAMP(8);
  f32x16 c0, c1;
#pragma unroll
  for (int i = 0; i < 16; ++i) { c0[i] = acc[i >> 2][i & 3]; c1[i] = acc[4 + (i >> 2)][i & 3]; }
  asm volatile("s_nop 7\n\t" CONV_MAIN_ASM
               : "+{v[64:79]}"(c0), "+{v[80:95]}"(c1), "+{v[96:111]}"(fa), "+{v[112:127]}"(fb0), "+{v[128:143]}"(fb1)
               : [aa] "v"(aa), [ab] "v"(ab) : "memory", CONV_MAIN_CLOB);
#pragma unroll
  for (int i = 0; i < 16; ++i) { acc[i >> 2][i & 3] = c0[i]; acc[4 + (i >> 2)][i & 3] = c1[i]; }
#pragma unroll
  for (int n = 0; n < 8; n += 2) {
    const auto s0 = __builtin_amdgcn_permlane16_swap(pk(acc[n][0], acc[n][1]), pk(acc[n + 1][0], acc[n + 1][1]), false, false);
    const auto s1 = __builtin_amdgcn_permlane16_swap(pk(acc[n][2], acc[n][3]), pk(acc[n + 1][2], acc[n + 1][3]), false, false);
    st128<SC1>(v4u{s0[0], s1[0], s0[1], s1[1]}, rw, WS_AT + d * 4096 + 32 * c + 16 * (g >> 1) + 512 * (n + (g & 1)));
  }
}
template <int D0, int ND> DI void finish(rsrc_t ro, int w, int lane, unsigned oo, const f32x4 (&own)[ND], const v2u (&xr)[ND]) {
  f32x4 p[3][ND]; v2u a[ND];
#pragma unroll
  for (int k = 1; k < 4; ++k)
#pragma unroll
    for (int i = 0; i < ND; ++i) p[k - 1][i] = *(LAS f32x4*)lds(((w + k) & 3) * 20480 + lane * 16 + (3 - k) * 5120 + (D0 + i) * 1024);
  const unsigned at = L_AT + (4 * (lane >> 4) + ((lane >> 2) & 3)) * 128 + (16 * w + 4 * (lane & 3)) * 2;
#pragma unroll
  for (int i = 0; i < ND; ++i) a[i] = __builtin_bit_cast(v2u, __builtin_amdgcn_ds_read_tr16_b64_v4i16((LAS s16x4*)lds(at + (D0 + i) * 2048)));
#pragma unroll
  for (int i = 0; i < ND; ++i) {
    const f32x4 r = (own[i] + f32x4{blo(xr[i].x), bhi(xr[i].x), blo(xr[i].y), bhi(xr[i].y)}) + (p[0][i] + p[1][i]) +
                    (p[2][i] + f32x4{blo(a[i].x), bhi(a[i].x), blo(a[i].y), bhi(a[i].y)});
    __builtin_amdgcn_raw_buffer_store_b64(v2u{pk(r[0], r[1]), pk(r[2], r[3])}, ro, oo + (D0 + i) * 32, 0, 0);
  }
}
#endif

__global__ __launch_bounds__(512, 2) void fused_s4_ffn(
    const uint16_t* __restrict__ x, const uint16_t* __restrict__ Are, const uint16_t* __restrict__ Aim, const uint16_t* __restrict__ Bre,
    const uint16_t* __restrict__ Bim, const uint16_t* __restrict__ Cre, const uint16_t* __restrict__ Cim, const uint16_t* __restrict__ logdt,
    const uint16_t* __restrict__ dskip, const uint16_t* __restrict__ wg, const uint16_t* __restrict__ wu, const uint16_t* __restrict__ wd,
    uint16_t* __restrict__ out, void* ws) {
#if __HIP_DEVICE_COMPILE__
  const int wid = __builtin_amdgcn_readfirstlane(threadIdx.x >> 6), lane = threadIdx.x & 63;
  const int b = blockIdx.x, xcd = b & 7, j = b >> 3;
  const bool normal = j < 30;
  const int rl = j % 5, ti = 5 * (xcd & 1) + rl;
  const int mm_ = 2 * (xcd >> 1) + (xcd & 1), par = j - 30;
  const int ch = normal ? 6 * (xcd >> 1) + j / 5 : 3 * mm_ + 2 * par;
  const int ch2 = 3 * mm_ + 1;
  const int nch = normal ? (j < 20 ? 3 : 2) : 0;
  const int d0 = 80 * xcd + (j < 20 ? 3 * j : 2 * j + 20);
  const int t64 = 8 * (xcd >> 1) + (j >> 2), dj = 4 * (xcd & 1) + (j & 3);
  const int rot = XROT ? (ti + (5 * (j / 5) + 1) / 3) % 10 : ti;
  const int rot2 = ROT2 == 6 ? 6 * ((j >> 1) & 1) : ROT2 * (j & 3);
  auto kst = [&](int s) { return s + rot2 >= 12 ? s + rot2 - 12 : s + rot2; };
  auto pbase = [](int w) -> unsigned { return w * 20480; };
  const rsrc_t rw = mkr(ws, WS_BYTES), rx = mkr(x, L * D * 2);
  auto finish_hi = [&]() {
    const int w = wid - 4;
    const unsigned oo = ((4 * t64 + w) * 16 + (lane & 15)) * 1280 + (80 * dj + 4 * (lane >> 4)) * 2;
    v2u xr[2];
#pragma unroll
    for (int i = 0; i < 2; ++i) xr[i] = __builtin_amdgcn_raw_buffer_load_b64(rx, oo + (3 + i) * 32, 0, 0);
    wait_vm<0>();
    BAR(11);
    const f32x4 own[2] = {*(LAS f32x4*)lds(pbase(w) + lane * 16 + 15360), *(LAS f32x4*)lds(pbase(w) + lane * 16 + 16384)};
    finish<3, 2>(mkr(out, L * D * 2), w, lane, oo, own, xr);
  };
  const u64 T0_ = __builtin_amdgcn_s_memrealtime();
  STAMP(0);
  if (wid == 6) {
    u64 pc; asm volatile("s_getpc_b64 %0" : "=s"(pc));
    dma<0>(srd((const void*)pc, CODE_BYTES), L_DUMMY, lane * 16, j * 1024);
    dma<0>(srd((const void*)pc, CODE_BYTES), L_DUMMY, lane * 16, 32768 + j * 1024);
  }
  if (wid == 7) { if (lane < 7) *(LAS v4u*)lds(L_MISC + 16 + lane * 16) = v4u{0, 0, 0, 0}; wait_lg(); }
  asm volatile("" ::"s"(x), "s"(Are), "s"(Aim), "s"(Bre), "s"(Bim), "s"(Cre), "s"(Cim), "s"(logdt), "s"(dskip), "s"(wg), "s"(wu), "s"(wd), "s"(out), "s"(ws));
  unsigned pw[6], ldw_ = 0, dsw_ = 0;
  const int kd = d0 + (wid & 3);
  if ((wid & 3) < nch) {
    const uint16_t* const par6[6] = {Are, Aim, Bre, Bim, Cre, Cim};
    const unsigned vo = wid >= 4 ? lane * 4 : 256 + (lane >> 1) * 4;
#pragma unroll
    for (int i = 0; i < 6; ++i) pw[i] = __builtin_amdgcn_raw_buffer_load_b32(mkr(par6[i], D * NS * 2), vo, kd * NS * 2, NT_);
    ldw_ = ((const __attribute__((address_space(4))) unsigned*)logdt)[kd >> 1];
    dsw_ = ((const __attribute__((address_space(4))) unsigned*)dskip)[kd >> 1];
  }
  ConvPre pre;
  if (wid >= 4) conv_masks(lane, pre);
  G1Ld gl = g1_lanes(lane);
  asm volatile("" : "+v"(gl.x[0]), "+v"(gl.x[1]), "+v"(gl.x[2]), "+v"(gl.x[3]), "+v"(gl.w[0]), "+v"(gl.w[1]), "+v"(gl.w[2]), "+v"(gl.w[3]));
  cfence(); bar(); cfence();
  STAMP(11);
  const float dt = rbf(expf((kd & 1) ? bhi(ldw_) : blo(ldw_)));
  const v4u sg = srd(wg, D * F * 2), su = srd(wu, D * F * 2), sx = srd(x, L * D * 2), sw = srd(ws, WS_BYTES);

  if (wid < 4) {
    if (wid < nch) {
      f32x4 ka[2][2];
#pragma unroll
      for (int i = 0; i < 6; ++i) pw[i] = (lane & 1) ? pw[i] >> 16 : pw[i];
      kgen<1>(pw, dt, lane, L_KG + wid * KG_SZ + 16 * 544, 0, ka);
#pragma unroll
      for (int i = 0; i < 4; ++i) *(LAS f32x4*)lds(L_KG + wid * KG_SZ + 16 * 544 + i * 1024 + lane * 16) = ka[i >> 1][i & 1];
      wait_lg();
      lds_set(L_MISC + 16 + wid * 4);
      STAMP(12);
    } else if (wid == 3 && normal) {
      const v4u sd = srd(wd, F * D * 2);
      const unsigned vd = (lane >> 3) * 1280 + (lane & 7) * 16;
#pragma unroll
      for (int p = 0; p < 2; ++p) dma4<NT_>(sd, L_WDU + p * 4096, vd, vd + 9216, vd + 18432, vd + 27648, (ch * 64 + p * 32) * 1280 + ti * 128);
      const unsigned vt = (lane >> 3) * 3072 + (lane & 7) * 16;
      v4u sink = {0, 0, 0, 0};
#pragma unroll
      for (int p = 0; p < 8; ++p) {
        touch(sink, sg, vt, ch * 128 + (rot * 64 + p * 8) * 3072);
        touch(sink, su, vt, ch * 128 + (rot * 64 + p * 8) * 3072);
      }
      wait_vm<16>();
      const unsigned a0 = L_WDU + (4 * (lane >> 4) + ((lane >> 2) & 3)) * 128 + 8 * (lane & 3);
#pragma unroll
      for (int p = 0; p < 8; ++p) {
        const unsigned a = a0 + (p >> 2) * 4096 + (p & 3) * 32;
        st128<SC1>(__builtin_bit_cast(v4u, trd(a, a + 2048)), rw, lane * 16 + WS_WDT + ((2 * ch + (p >> 2)) * 40 + 4 * ti + (p & 3)) * 1024);
      }
      STAMP(12);
      asm volatile("s_waitcnt vmcnt(8)" : "+v"(sink)::"memory");
    }
    G1Rd rdr;
    if (normal) {
      f32x4 acc[4][6];
      G1Acc z;
      g1_setup<6>(wid, lane, rdr, acc);
      g1_setup6(wid, lane, z);
      if (wid < 2) {
        lds_wait(L_MISC + 68); lds_wait(L_MISC + 72);
        if (nch == 3) lds_wait(L_MISC + 76);
        STAMP(10);
        g1_issue<6>(sx, sg, su, gl, wid, rot, 0, 192 * ti, ch);
        lds_set(L_MISC + 40 + wid * 4);
        wait_vm<0>();
      } else if (wid == 2) {
        lds_wait(L_MISC + 40); lds_wait(L_MISC + 44);
        STAMP(10);
#pragma unroll 1
        for (int h = 0; h < 2; ++h) g1_issue<6>(sx, sg, su, gl, h, rot == 9 ? 0 : rot + 1, 2 * STAGE, 192 * ti, ch);
      }
      STAMP(1);
      G1Ep<6> ep;
      g1_main6(rdr, z, acc, ep);
      lds_set(L_MISC + 112 + wid * 4);
      STAMP(3);
      g1_epilogue<6, true>(rw, wid, lane, 12 * ti, ch, ep, acc);
    } else {
      f32x4 acc4[4][4], acc2[4][2];
      G1Ep<4> ep4; G1Ep<2> ep2;
      g1_setup<2>(wid, lane, rdr, acc2);
      g1_setup<4>(wid, lane, rdr, acc4);
      g1_main_rem(rdr, (64 * par - 32 * (wid >> 1)) * 128, acc4, acc2);
      lds_set(L_MISC + 112 + wid * 4);
      lds_wait(L_MISC + 96);
      STAMP(3);
      g1_ep_read<4>(wid, lane, L_ST, L_CS, ep4);
      g1_ep_read<2>(wid, lane, L_ST + par * 1024, L_CS + 512, ep2);
      g1_epilogue<4, false>(rw, wid, lane, 120, ch, ep4, acc4);
      g1_epilogue<2, true>(rw, wid, lane, 120 + 4 * par, ch2, ep2, acc2);
    }
    STAMP(4); RSTAMP(32);
    const v2u tag = tag_of();

    const unsigned wso = WS_WDT + (12 * wid * 40 + 5 * dj) * 1024, hso = WS_H + (4 * t64 * 48 + 12 * wid) * 1024;
    const unsigned vo0 = lane * 16, vo1 = lane * 16 + 4096, pw = pbase(wid) + lane * 16;
    v4u wr[5][5], hr[3][4];
#pragma unroll
    for (int i = 0; i < 25; ++i) asm volatile("" : "=v"(wr[i / 5][i % 5]));
#pragma unroll
    for (int i = 0; i < 12; ++i) asm volatile("" : "=v"(hr[i / 4][i % 4]));
    auto ld1 = [&](v4u& dst, unsigned vo, unsigned so, int imm) {
      if (imm == 0) asm volatile("buffer_load_dwordx4 %0, %1, %2, %3 offen sc1" : "+v"(dst) : "v"(vo), "s"(sw), "s"(so) : "memory");
      if (imm == 1) asm volatile("buffer_load_dwordx4 %0, %1, %2, %3 offen offset:1024 sc1" : "+v"(dst) : "v"(vo), "s"(sw), "s"(so) : "memory");
      if (imm == 2) asm volatile("buffer_load_dwordx4 %0, %1, %2, %3 offen offset:2048 sc1" : "+v"(dst) : "v"(vo), "s"(sw), "s"(so) : "memory");
      if (imm == 3) asm volatile("buffer_load_dwordx4 %0, %1, %2, %3 offen offset:3072 sc1" : "+v"(dst) : "v"(vo), "s"(sw), "s"(so) : "memory");
    };
    auto ldw = [&](int s, int dd) { ld1(wr[s % 5][dd], dd < 4 ? vo0 : vo1, wso + kst(s) * 40960, dd & 3); };
    unsigned ht[4];
#pragma unroll
    for (int t = 0; t < 4; ++t) ht[t] = vo0 + ((wid + 1 + t) & 3) * 49152;
    auto ldh = [&](int s, int t) { ld1(hr[s % 3][t], ht[t], hso + kst(s) * 1024, 0); };
#pragma unroll
    for (int i = 0; i < NEARLY; ++i) ldw(i / 5, i % 5);
    wait_vm<NEARLY>();
    STAMP(5); RSTAMP(33);
    if (lane == 0) put_flag(rw, WS_F1 + ((normal ? ti * 24 + ch : 240 + 2 * mm_ + par) * 4 + wid) * 8, tag);
#pragma unroll
    for (int i = NEARLY; i < 25; ++i) ldw(i / 5, i % 5);
    if (STGB < 4) {
      lds_wait4(L_MISC + 112);
      if (wid < 4 - STGB) {
#pragma unroll
        for (int s = 0; s < NSTG; ++s) dma5(sw, pbase(wid) + s * 5120, vo0, wso + kst(12 - NSTG + s) * 40960);
      }
    }
    lds_set(L_MISC + 80 + wid * 4);
    __builtin_amdgcn_s_waitcnt(0x0F70);
    cfence();
#pragma unroll 1
    for (int spin = 0; spin < 64 * SPIN_MAX && *(volatile LAS unsigned*)lds(L_MISC + 32) == 0; ++spin) {}
    cfence();
    STAMP(6);
#pragma unroll
    for (int i = 0; i < 12; ++i) {
      ldh(i / 4, i % 4);
      if (HGAP && (i == 3 || i == 7)) __builtin_amdgcn_s_sleep(HGAP);
    }
    f32x4 acc[4][5];
    const int c = lane & 15, g = lane >> 4;
    const unsigned oo = ((4 * t64 + wid) * 16 + c) * 1280 + (80 * dj + 4 * g) * 2;
    v2u xr[3];
    auto mm1 = [&](int s, int t, int dd) {
      if (s == 0) asm volatile("v_mfma_f32_16x16x32_bf16 %0, %1, %2, 0" : "=&v"(acc[t][dd]) : "v"(wr[0][dd]), "v"(hr[0][t]));
      else asm volatile("v_mfma_f32_16x16x32_bf16 %0, %1, %2, %0" : "+v"(acc[t][dd]) : "v"(wr[s % 5][dd]), "v"(hr[s % 3][t]));
    };
#pragma unroll
    for (int s = 0; s < 10; ++s) {
      if (s == 12 - NSTG) wait_lg();
      asm volatile("s_waitcnt vmcnt(%0)" ::"i"(g2_wait(s)) : "memory");
#pragma unroll
      for (int t = 0; t < 4; ++t) {
#pragma unroll
        for (int dd = 0; dd < 5; ++dd) {
          mm1(s, t, dd);
          if (t == 3 && s >= 7 - NSTG && s < 7)
            asm volatile("ds_read_b128 %0, %1 offset:%2" : "+v"(wr[s % 5][dd]) : "v"(pw), "i"((s - 7 + NSTG) * 5120 + dd * 1024) : "memory");
        }
        if (s + 3 < 12) ldh(s + 3, t);
        if (g2_piece(s, t) >= 0) ldw(5 + g2_piece(s, t) / 5, g2_piece(s, t) % 5);
      }
      if (s == G2SIG && wid == 0) lds_set(L_MISC + 36);
    }
#pragma unroll
    for (int dd = 0; dd < 3; ++dd) asm volatile("buffer_load_dwordx2 %0, %1, %2, 0 offen offset:%3" : "=&v"(xr[dd]) : "v"(oo), "s"(sx), "i"(dd * 32) : "memory");
#pragma unroll
    for (int t = 0; t < 4; ++t) {
      asm volatile("s_waitcnt vmcnt(%0)" ::"i"(6 - t) : "memory");
#pragma unroll
      for (int i = 0; i < 10; ++i) {
        mm1(10 + i / 5, t, i % 5);
        if (t > 0 && i >= 3 && i < 8)
          asm volatile("ds_write_b128 %0, %1 offset:%2" ::"v"(pw), "v"(acc[t - 1][i - 3]), "i"((t - 1) * 5120 + (i - 3) * 1024) : "memory");
      }
    }
    asm volatile("s_nop 7" : "+v"(acc[3][0]), "+v"(acc[3][1]), "+v"(acc[3][2]), "+v"(acc[3][3]), "+v"(acc[3][4]));
    *(LAS f32x4*)lds(pw + 15360) = acc[3][3];
    *(LAS f32x4*)lds(pw + 16384) = acc[3][4];
    STAMP(7);
    asm volatile("s_waitcnt vmcnt(0) lgkmcnt(0)" : "+v"(xr[0]), "+v"(xr[1]), "+v"(xr[2])::"memory");
    BAR(11);
    STAMP(8);
    const f32x4 own[3] = {acc[3][0], acc[3][1], acc[3][2]};
    finish<0, 3>(mkr(out, L * D * 2), wid, lane, oo, own, xr);
    STAMP(9);
  } else if (wid < 7) {
    const int cw = wid - 4;
    v4u sink = {0, 0, 0, 0};
    auto warm_wd = [&]() {
#pragma unroll
      for (int i = 0; i < 3; ++i) {
        const unsigned so = WS_WDT + ((6 * (j >> 2) + 3 * cw + i) * 40 + 5 * dj) * 1024;
        touch4(sink, sw, lane * 16, so); touch(sink, sw, lane * 16, so + 4096);
      }
      wait_vm<15>();
    };
    if (normal) {
      LnOut lo2;
      const bool ln2 = cw == 2 && (j == 20 || j == 21);
      if (cw < nch) {
        f32x4 ka[2][2];
        kgen<2>(pw, dt, lane, L_KG + cw * KG_SZ, L_MISC + 68 + cw * 4, ka);
        STAMP(12);
        lds_wait(L_MISC + 16 + cw * 4);
#pragma unroll
        for (int i = 0; i < 4; ++i) ka[i >> 1][i & 1] += *(LAS f32x4*)lds(L_KG + cw * KG_SZ + 16 * 544 + i * 1024 + lane * 16);
        if (lane == 0) ka[0][0][0] += (kd & 1) ? bhi(dsw_) : blo(dsw_);
#pragma unroll
        for (int rt = 0; rt < 2; ++rt)
          *(LAS v4u*)lds(L_RAW + cw * 2048 + rt * 1024 + lane * 16) =
              v4u{pk(ka[rt][0][0], ka[rt][0][1]), pk(ka[rt][0][2], ka[rt][0][3]), pk(ka[rt][1][0], ka[rt][1][1]), pk(ka[rt][1][2], ka[rt][1][3])};
        STAMP(2);
        conv_pre(L_RAW + cw * 2048, lane, pre);
      } else if (ln2) {
        ln8(rx, 240 + 2 * xcd + j - 20, lane, L_KG + 2 * KG_SZ, lo2);
      }
      wait_lg();
      STAMP(1);
      if (cw < 2) {
        g1_loader<20, 2>(rot, STAGE, [&](int s, unsigned so) { g1_issue<6>(sx, sg, su, gl, cw, s, so, 192 * ti, ch); });
        warm_wd();
      } else {
        BAR(0);
        colsum(rw, WS_CSP + (ch * 10 + ti) * 512, 0, lane); colsum(rw, WS_CSP + (ch * 10 + ti) * 512, 1, lane);
        if (LNB == 0 && ln2) ln8_store(rw, 240 + 2 * xcd + j - 20, lane, lo2);
        BAR(1);
        if (LNB == 1 && ln2) ln8_store(rw, 240 + 2 * xcd + j - 20, lane, lo2);
        if (LNB == 1) BAR(2);
        wait_vm<0>();
        if (LNB == 0) BAR(2);
#pragma unroll 1
        for (int k = 3; k < 9; ++k) BAR(k);
      }
      BAR(9);
      if (cw < nch) conv_image(pre, L_H + cw * H_SZ, L_K3 + cw * 320, lane);
    } else if (cw < 2) {
      g1_loader<24, 0>(5 * (xcd & 1), 0, [&](int s, unsigned so) { g1_issue_rem(sx, sg, su, gl, cw, s, so, ch, ch2); });
      wait_vm<0>(); BAR(9);
      warm_wd();
    } else {
#pragma unroll 1
      for (int s = 0; s < 10; ++s) bar();
    }
    asm volatile("s_waitcnt vmcnt(0)" : "+v"(sink)::"memory");
    STAMP(4);
    if (cw < nch) {
      lds_wait(L_MISC + 64);
      conv(rw, cw, d0 + cw, lane, pre, L_MISC + 48 + cw * 4);
      STAMP(6);
      lds_set(L_MISC + 100 + cw * 4);
      wait_vm<0>();
      if (lane == 0) put_flag(rw, WS_F2 + (d0 + cw) * 8, tag_of());
    }
    STAMP(7);
    finish_hi();
  } else {
    const v2u tag = epoch_tag(rw);
    const int tb = 120 * (xcd & 1) + 30 * (xcd >> 1) + j;
    LnOut lo;
    if (normal) ln8(rx, tb, lane, L_LN, lo);
    if (lane == 0) *(LAS v4u*)lds(L_MISC) = v4u{tag.x, tag.y, 0, 0};
    wait_lg();
    STAMP(1);
    BAR(0);
    if (normal) {
      colsum(rw, WS_CSP + (ch * 10 + ti) * 512, 2, lane); colsum(rw, WS_CSP + (ch * 10 + ti) * 512, 3, lane);
      if (LNB == 0) ln8_store(rw, tb, lane, lo);
    }
    BAR(1);
    if (LNB == 1 && normal) ln8_store(rw, tb, lane, lo);
    BAR(2);
    if (LNB == 1) BAR(3);
    wait_vm<0>();
    if (lane == 0) put_flag(rw, WS_F0 + b * 8, tag);
    if (LNB == 0) BAR(3);
    BAR(4); BAR(5);
    if (!normal) { bar(); bar(); }
    Poll4 p0;
    poll4_issue(rw, WS_F0, 256, lane, p0);
    BAR(6);
    poll4_finish(rw, WS_F0, 256, tag, lane, p0);
    STAMP(2);
    if (b == 0 && lane == 0) put_flag(rw, WS_EPOCH, tag);
    const int row0 = normal ? 192 * ti : 1920;
    v2u st[3], cp[2][10];
#pragma unroll
    for (int k = 0; k < 3; ++k) { const int r = row0 + lane + 64 * k; st[k] = __builtin_amdgcn_raw_buffer_load_b64(rw, (r >> 3) * 128 + (r & 7) * 8, WS_STATS, SC1); }
#pragma unroll
    for (int k = 0; k < 10; ++k) cp[0][k] = __builtin_amdgcn_raw_buffer_load_b64(rw, lane * 8 + k * 512, WS_CSP + ch * 5120, SC1);
    if (!normal) {
#pragma unroll
      for (int k = 0; k < 10; ++k) cp[1][k] = __builtin_amdgcn_raw_buffer_load_b64(rw, lane * 8 + k * 512, WS_CSP + ch2 * 5120, SC1);
    }
    cfence();
    BAR(7);
    f32x4 so[3];
#pragma unroll
    for (int k = 0; k < 3; ++k) {
      const float m = __uint_as_float(st[k].x), den = __uint_as_float(st[k].y);
      so[k] = f32x4{-m, den * den, -1.44269504f * __builtin_amdgcn_rcpf(den), 0.f};
    }
    f32x2 co[2];
#pragma unroll
    for (int h = 0; h < 2; ++h) {
      float c0 = 0.f, c1 = 0.f;
      if (h == 0 || !normal) {
#pragma unroll
        for (int k = 0; k < 10; ++k) { c0 += __uint_as_float(cp[h][k].x); asm volatile("" : "+v"(c0)); c1 += __uint_as_float(cp[h][k].y); }
      }
      co[h] = f32x2{c0, c1};
    }
    asm volatile("" : "+v"(so[0]), "+v"(so[1]), "+v"(so[2]), "+v"(co[0]), "+v"(co[1]));
    if (normal) { BAR(8); __builtin_amdgcn_s_sleep(2); }
#pragma unroll
    for (int k = 0; k < 3; ++k) *(LAS f32x4*)lds(L_ST + (lane + 64 * k) * 16) = so[k];
#pragma unroll
    for (int h = 0; h < 2; ++h) *(LAS f32x2*)lds(L_CS + h * 512 + lane * 8) = co[h];
    v4u ur[16];
    auto ldu = [&](int p) { ur[p] = __builtin_amdgcn_raw_buffer_load_b128(rw, (lane >> 2) * 10240 + (lane & 3) * 16, WS_U8 + p * 163840 + d0 * 16, SC1); };
    if ((lane & 3) < nch) {
#pragma unroll
      for (int p = 0; p < NU1; ++p) ldu(p);
    }
    wait_lg();
    if (normal) BAR(9); else lds_set(L_MISC + 96);
    if ((lane & 3) < nch) {
#pragma unroll
      for (int p = NU1; p < 16; ++p) ldu(p);
    }
    if (lane < 32) {
#pragma unroll
      for (int k = 0; k < 3; ++k) *(LAS v4u*)lds(L_U + k * U_SZ + lane * 16) = v4u{0, 0, 0, 0};
    }
    if ((lane & 3) < nch) {
#pragma unroll
      for (int p = 0; p < NU1; ++p) *(LAS v4u*)lds(L_U + (lane & 3) * U_SZ + 512 + p * 256 + (lane >> 2) * 16) = ur[p];
    }
    if ((lane & 3) < nch) {
#pragma unroll
      for (int p = NU1; p < 16; ++p) *(LAS v4u*)lds(L_U + (lane & 3) * U_SZ + 512 + p * 256 + (lane >> 2) * 16) = ur[p];
    }
    wait_lg();
    lds_set(L_MISC + 64);
    STAMP(4);
    lds_wait4(L_MISC + 112);
#pragma unroll 1
    for (int w = 3; w >= 4 - STGB; --w) {
#pragma unroll
      for (int s = 0; s < NSTG; ++s) {
        dma5(sw, pbase(w) + s * 5120, lane * 16, WS_WDT + ((12 * w + kst(12 - NSTG + s)) * 40 + 5 * dj) * 1024);
        if (PACE) __builtin_amdgcn_s_sleep(PACE);
      }
    }
    STAMP(3);
    wait_vm<0>();
    lds_wait4(L_MISC + 80);
    STAMP(8);
    poll<2>(rw, WS_F1 + (t64 < 30 ? (t64 / 3) * 768 : 7680), t64 < 30 ? 96 : 64, tag, lane);
    lds_set(L_MISC + 32);
    STAMP(5); RSTAMP(34);
    lds_wait(L_MISC + 36);
    poll<2>(rw, WS_F2 + dj * 640, 80, tag, lane);
#pragma unroll 1
    for (int k = 0; k < nch; ++k) lds_wait(L_MISC + 100 + k * 4);
    STAMP(6);
    const unsigned va = (lane >> 3) * 4096 + (lane & 7) * 16, sa = WS_AT + 80 * dj * 4096 + t64 * 128;
    dma4<SC1>(sw, L_AT, va, va + 31744, va + 63488, va + 95232, sa);
    dma4<SC1>(sw, L_AT + 4096, va, va + 31744, va + 63488, va + 95232, sa + 131072);
    dma<SC1>(sw, L_AT + 8192, va, sa + 262144); dma<SC1>(sw, L_AT + 9216, va, sa + 294912);
    STAMP(7);
    finish_hi();
  }
  DUMP();
#endif
}

__global__ __launch_bounds__(64) void probeSpin(const unsigned* t) {
#if __HIP_DEVICE_COMPILE__
  const unsigned long long tstart = __builtin_amdgcn_s_memrealtime();
  if (PKIND == 4) return;
  unsigned long long lo = ~0ull, hiS = 0, hiE = 0, sum = 0;
  for (int b = 0; b < 256; ++b) {
    const unsigned* p = t + (b * 8) * 64;
    unsigned long long s0 = *(const unsigned long long*)(p + 48), e0 = *(const unsigned long long*)(p + 50);
    for (int w = 1; w < 8; ++w) { unsigned long long e1 = *(const unsigned long long*)(p + w * 64 + 50); e0 = e1 > e0 ? e1 : e0; }
    lo = s0 < lo ? s0 : lo; hiS = s0 > hiS ? s0 : hiS; hiE = e0 > hiE ? e0 : hiE;
    if (b < 240) sum += (unsigned)(t[(b * 8 + PW) * 64 + PS] - p[0]);
  }
  unsigned long long rs = 0, es = 0;
  for (int b = 0; b < 240; ++b) { rs += (unsigned)(t[(b * 8 + PW) * 64 + PS] - (unsigned)lo); es += *(const unsigned long long*)(t + b * 8 * 64 + 50) - lo; }
  unsigned long long ticks = PKIND == 7 ? rs * 100 / 240 : PKIND == 8 ? es * 100 / 240 : PKIND == 0 ? (hiE - lo) * 100 : PKIND == 1 ? (hiS - lo) * 100 : PKIND == 2 ? sum / 240 * 10 : PKIND == 3 ? (tstart - hiE) * 100 : PKIND == 5 ? (lo - *(const unsigned long long*)(t + 200000)) * 100 : 0;
  if (ticks > 1000000) ticks = 1000000;
  unsigned long long t0 = __builtin_amdgcn_s_memrealtime();
  while (__builtin_amdgcn_s_memrealtime() - t0 < ticks) __builtin_amdgcn_s_sleep(8);
#endif
}

extern "C" void kernel_launch(void* const* d_in, const int* in_sizes, int n_in, void* d_out, int out_size, void* d_ws, size_t ws_size,
                              hipStream_t stream) {
  (void)hipFuncSetAttribute((const void*)fused_s4_ffn, hipFuncAttributeMaxDynamicSharedMemorySize, LDS_BYTES);
  fused_s4_ffn<<<256, 512, LDS_BYTES, stream>>>(
      (const uint16_t*)d_in[0], (const uint16_t*)d_in[1], (const uint16_t*)d_in[2], (const uint16_t*)d_in[3], (const uint16_t*)d_in[4],
      (const uint16_t*)d_in[5], (const uint16_t*)d_in[6], (const uint16_t*)d_in[7], (const uint16_t*)d_in[8], (const uint16_t*)d_in[10],
      (const uint16_t*)d_in[11], (const uint16_t*)d_in[12], (uint16_t*)d_out, d_ws);
  probeSpin<<<1, 64, 0, stream>>>((const unsigned*)((char*)d_ws + WS_TM));
}
```

```cpp
#include <hip/hip_runtime.h>
#include <stdint.h>

typedef short bf16x8 __attribute__((ext_vector_type(8)));
typedef short s16x4 __attribute__((ext_vector_type(4)));
typedef float f32x4 __attribute__((ext_vector_type(4)));
typedef float f32x2 __attribute__((ext_vector_type(2)));
typedef __bf16 bf16x2 __attribute__((ext_vector_type(2)));
typedef unsigned v4u __attribute__((ext_vector_type(4)));
typedef unsigned v2u __attribute__((ext_vector_type(2)));
typedef unsigned v8u __attribute__((ext_vector_type(8)));
typedef unsigned v16u __attribute__((ext_vector_type(16)));
typedef float f32x8 __attribute__((ext_vector_type(8)));
typedef float f32x16 __attribute__((ext_vector_type(16)));
typedef unsigned long long u64;
#define LAS __attribute__((address_space(3)))
#define DI __device__ __forceinline__
typedef __amdgpu_buffer_rsrc_t rsrc_t;

constexpr int L = 2048, D = 640, NS = 192, F = 1536;
constexpr int SC1 = 16, NT_ = 2;

constexpr unsigned WS_EPOCH = 0, WS_F0 = 1024, WS_F3 = 4096, WS_F2 = 8192, WS_STATS = 16384, WS_F1 = 49152, WS_CSP = 65536, WS_U8 = 262144,
                   WS_WDT = 3u << 20, WS_H = 5u << 20, WS_AT = 12u << 20, WS_TM = 15u << 20, WS_BYTES = 16u << 20;

constexpr unsigned STAGE = 40960, XB = 24576;
constexpr unsigned LX = 3 * STAGE;
constexpr unsigned L_LN = LX, LN_RS = 1296, L_AT = LX;
constexpr unsigned L_RAW = LX + 10240;
constexpr unsigned L_ST = L_RAW + 3 * 2048;
constexpr unsigned L_CS = L_ST + 3072;
constexpr unsigned L_MISC = L_CS + 1024;
constexpr unsigned L_U = L_MISC + 128, U_SZ = 4608;
constexpr unsigned L_K3 = L_U + 3 * U_SZ;
constexpr unsigned L_END = L_K3 + 960;
constexpr unsigned L_WDU = L_U, L_DUMMY = L_U + 8192;
constexpr unsigned L_KG = STAGE, KG_SZ = 16 * (544 + 288);
constexpr unsigned L_H = 2 * STAGE, H_CS = 2112, H_SZ = 8 * H_CS;
constexpr unsigned LDS_BYTES = 163840;
static_assert(L_END <= LDS_BYTES && L_H + 3 * H_SZ + 16 <= L_RAW, "lds");
#define G2AUXW SC1
#define G2AUXH SC1
#define ROT2 0
#define G2SIG 3
#define XROT 0
#define TA 20
#define TB 20
#define LNB 0
#define PDELAY 3
#define CPRIO 2
#define PWAIT 6
#define NEARLY 8
#define NSTG 4
#define GPRIO 0
#define STGB 4
#define PACE 0
#define TRIG 1
#define NU1 0
#define HGAP 2
#define WPS 4
#define W3SLEEP 14
#define WDFIRST 1
#define CSW6 1
#define PEARLY 0
constexpr int SPIN_MAX = 20000;
constexpr unsigned CODE_BYTES = 55808;

constexpr int G2_NW = 5 * (7 - NSTG), G2_W0 = 1;
constexpr int g2_piece(int s, int t) { const int p = WPS * (s - G2_W0) + t; return s >= G2_W0 && t < WPS && p < G2_NW ? p : -1; }
constexpr int g2_wait(int step) {
  int n = 0, need = 0;
  for (int i = 0; i < 12; ++i) { ++n; if (i / 4 == step) need = n; }
  for (int s = 0; s < step; ++s)
    for (int t = 0; t < 4; ++t) {
      if (s + 3 < 12) { ++n; if (s + 3 == step) need = n; }
      const int p = g2_piece(s, t);
      if (p >= 0) { ++n; if (5 + p / 5 == step) need = n; }
    }
  return n - need;
}
static_assert(NSTG == 4 && G2_W0 + (G2_NW - 1) / WPS <= 5, "streamed weights must be issued two steps ahead, into slots already drained");

#if __HIP_DEVICE_COMPILE__
extern __shared__ __attribute__((aligned(16))) char smem_[];
DI LAS char* lds(unsigned o) { return (LAS char*)smem_ + o; }
DI rsrc_t mkr(const void* p, unsigned bytes) { return __builtin_amdgcn_make_buffer_rsrc((void*)p, 0, bytes, 0x00020000); }
DI unsigned pk(float a, float b) { f32x2 f = {a, b}; return __builtin_bit_cast(unsigned, __builtin_convertvector(f, bf16x2)); }
DI unsigned pk2(f32x2 f) { return __builtin_bit_cast(unsigned, __builtin_convertvector(f, bf16x2)); }
DI float blo(unsigned v) { return __uint_as_float(v << 16); }
DI float bhi(unsigned v) { return __uint_as_float(v & 0xffff0000u); }
DI float rbf(float f) { return blo(pk(f, f)); }
DI void bar() { __builtin_amdgcn_s_barrier(); }
DI void cfence() { asm volatile("" ::: "memory"); }
template <int N> DI void wait_vm() { asm volatile("s_waitcnt vmcnt(%0)" ::"i"(N) : "memory"); }
DI void wait_lg() { asm volatile("s_waitcnt lgkmcnt(0)" ::: "memory"); }
DI bf16x8 trd(unsigned a0, unsigned a1) {
  s16x4 lo = __builtin_amdgcn_ds_read_tr16_b64_v4i16((LAS s16x4*)lds(a0));
  s16x4 hi = __builtin_amdgcn_ds_read_tr16_b64_v4i16((LAS s16x4*)lds(a1));
  return __builtin_shufflevector(lo, hi, 0, 1, 2, 3, 4, 5, 6, 7);
}
DI bf16x8 rd128(unsigned a) { return *(LAS bf16x8*)lds(a); }
DI f32x4 mfma(bf16x8 a, bf16x8 b, f32x4 c) { return __builtin_amdgcn_mfma_f32_16x16x32_bf16(a, b, c, 0, 0, 0); }
DI v4u srd(const void* p, unsigned bytes) { const u64 a = (u64)p; return v4u{(unsigned)a, (unsigned)(a >> 32) & 0xffffu, bytes, 0x00020000u}; }
template <int AUX> DI void dma(v4u r, unsigned ldsoff, unsigned voff, unsigned soff) {
  if (AUX == 0) asm volatile("s_mov_b32 m0, %0\n\ts_nop 0\n\tbuffer_load_dwordx4 %1, %2, %3 offen lds" ::"s"(ldsoff), "v"(voff), "s"(r), "s"(soff) : "memory");
  if (AUX == NT_) asm volatile("s_mov_b32 m0, %0\n\ts_nop 0\n\tbuffer_load_dwordx4 %1, %2, %3 offen nt lds" ::"s"(ldsoff), "v"(voff), "s"(r), "s"(soff) : "memory");
  if (AUX == SC1) asm volatile("s_mov_b32 m0, %0\n\ts_nop 0\n\tbuffer_load_dwordx4 %1, %2, %3 offen sc1 lds" ::"s"(ldsoff), "v"(voff), "s"(r), "s"(soff) : "memory");
}
#define DMA4_(POL) asm volatile("s_mov_b32 m0, %0\n\ts_nop 0\n\tbuffer_load_dwordx4 %1, %5, %6 offen " POL "lds\n\t" \
    "buffer_load_dwordx4 %2, %5, %6 offen offset:1024 " POL "lds\n\tbuffer_load_dwordx4 %3, %5, %6 offen offset:2048 " POL "lds\n\t" \
    "buffer_load_dwordx4 %4, %5, %6 offen offset:3072 " POL "lds" ::"s"(ldsoff), "v"(v0), "v"(v1), "v"(v2), "v"(v3), "s"(r), "s"(soff) : "memory")
template <int AUX> DI void dma4(v4u r, unsigned ldsoff, unsigned v0, unsigned v1, unsigned v2, unsigned v3, unsigned soff) {
  if (AUX == 0) DMA4_("");
  if (AUX == NT_) DMA4_("nt ");
  if (AUX == SC1) DMA4_("sc1 ");
}
template <int AUX> DI void st128(v4u v, rsrc_t r, unsigned off) { __builtin_amdgcn_raw_buffer_store_b128(v, r, off, 0, AUX); }
DI void touch(v4u& sink, v4u r, unsigned vo, unsigned so) {
  asm volatile("buffer_load_dwordx4 %0, %1, %2, %3 offen sc1" : "+v"(sink) : "v"(vo), "s"(r), "s"(so) : "memory");
}
DI void touch4(v4u& sink, v4u r, unsigned vo, unsigned so) {
  asm volatile("buffer_load_dwordx4 %0, %1, %2, %3 offen sc1\n\tbuffer_load_dwordx4 %0, %1, %2, %3 offen offset:1024 sc1\n\t"
               "buffer_load_dwordx4 %0, %1, %2, %3 offen offset:2048 sc1\n\tbuffer_load_dwordx4 %0, %1, %2, %3 offen offset:3072 sc1"
               : "+v"(sink) : "v"(vo), "s"(r), "s"(so) : "memory");
}
DI void dma5(v4u r, unsigned ldsoff, unsigned vo, unsigned so) { dma4<SC1>(r, ldsoff, vo, vo, vo, vo, so); dma<SC1>(r, ldsoff + 4096, vo, so + 4096); }
DI unsigned vmov(unsigned x) { unsigned r; asm("v_mov_b32 %0, %1" : "=v"(r) : "v"(x)); return r; }
DI float vmul(float a, float b) { float r; asm("v_mul_f32 %0, %1, %2" : "=v"(r) : "v"(a), "v"(b)); return r; }
DI float rowsum16(float v) {
  v += __builtin_bit_cast(float, __builtin_amdgcn_update_dpp(0, __builtin_bit_cast(int, v), 0x128, 0xf, 0xf, false));
  v += __builtin_bit_cast(float, __builtin_amdgcn_update_dpp(0, __builtin_bit_cast(int, v), 0x124, 0xf, 0xf, false));
  v += __builtin_bit_cast(float, __builtin_amdgcn_update_dpp(0, __builtin_bit_cast(int, v), 0x122, 0xf, 0xf, false));
  v += __builtin_bit_cast(float, __builtin_amdgcn_update_dpp(0, __builtin_bit_cast(int, v), 0x121, 0xf, 0xf, false));
  return v;
}
DI f32x2 cmul(f32x2 a, f32x2 b) {
  f32x2 r;
  asm("v_pk_mul_f32 %0, %1, %2 op_sel_hi:[1,0]\n\tv_pk_fma_f32 %0, %1, %2, %0 op_sel:[1,1,0] op_sel_hi:[0,1,1] neg_lo:[0,1,0]" : "=&v"(r) : "v"(a), "v"(b));
  return r;
}
DI f32x2 cpow(float lr, float tr, float e) {
  float m = __builtin_amdgcn_exp2f(lr * e), th = __builtin_amdgcn_fractf(tr * e);
  return f32x2{m * __builtin_amdgcn_cosf(th), m * __builtin_amdgcn_sinf(th)};
}
DI v2u tag_of(void) { return *(LAS v2u*)lds(L_MISC); }
DI v2u epoch_tag(rsrc_t rw) {
  const v2u e = __builtin_amdgcn_raw_buffer_load_b64(rw, WS_EPOCH, 0, SC1);
  const u64 t = (((u64)e.y << 32) | e.x) + 0x9E3779B97F4A7C15ull;
  return v2u{(unsigned)t, (unsigned)(t >> 32)};
}
DI void put_flag(rsrc_t rw, unsigned off, v2u tag) { __builtin_amdgcn_raw_buffer_store_b64(tag, rw, off, 0, SC1); }
template <int R> DI void poll(rsrc_t rw, unsigned off, int n, v2u tag, int lane) {
#pragma unroll 1
  for (int spin = 0; spin < SPIN_MAX; ++spin) {
    bool ok = true;
#pragma unroll
    for (int r = 0; r < R; ++r) {
      int i = lane + 64 * r; i = i < n ? i : n - 1;
      v2u f = __builtin_amdgcn_raw_buffer_load_b64(rw, off + i * 8, 0, SC1);
      ok = ok && f.x == tag.x && f.y == tag.y;
    }
    if (__all(ok)) break;
    cfence();
  }
  cfence();
}
struct Poll4 { v2u f[4]; };
DI void poll4_issue(rsrc_t rw, unsigned off, int n, int lane, Poll4& p) {
#pragma unroll
  for (int r = 0; r < 4; ++r) { int i = lane + 64 * r; i = i < n ? i : n - 1; p.f[r] = __builtin_amdgcn_raw_buffer_load_b64(rw, off + i * 8, 0, SC1); }
  cfence();
}
DI void poll4_finish(rsrc_t rw, unsigned off, int n, v2u tag, int lane, const Poll4& p) {
  bool ok = true;
#pragma unroll
  for (int r = 0; r < 4; ++r) ok = ok && p.f[r].x == tag.x && p.f[r].y == tag.y;
  if (!__all(ok)) poll<4>(rw, off, n, tag, lane);
  cfence();
}
DI void lds_set(unsigned o) { cfence(); *(volatile LAS unsigned*)lds(o) = 1; }
DI void lds_wait(unsigned o) {
#pragma unroll 1
  for (int spin = 0; spin < 16 * SPIN_MAX && *(volatile LAS unsigned*)lds(o) == 0; ++spin) __builtin_amdgcn_s_sleep(1);
  cfence();
}
DI void lds_wait4(unsigned o) {
#pragma unroll 1
  for (int spin = 0; spin < 16 * SPIN_MAX; ++spin) {
    const v4u f = *(volatile LAS v4u*)lds(o);
    if (f[0] & f[1] & f[2] & f[3]) break;
    __builtin_amdgcn_s_sleep(1);
  }
  cfence();
}

#define RSTAMP(i)
#define STAMP(i)
#define BAR(i) bar()
#define DUMP()

#define G1_STAMP ""
#define G1_STAMP_CLOB
#define G1_MAIN_ASM \
  "v_mov_b32 v240, %[a0]\n\t" \
  "v_mov_b32 v241, %[a1]\n\t" \
  "v_mov_b32 v242, %[a2]\n\t" \
  "v_mov_b32 v243, %[a3]\n\t" \
  "v_add_u32 v244, 81920, %[a0]\n\t" \
  "v_add_u32 v245, 81920, %[a1]\n\t" \
  "v_add_u32 v246, 81920, %[a2]\n\t" \
  "v_add_u32 v247, 81920, %[a3]\n\t" \
  "v_add_u32 v248, 40960, %[a0]\n\t" \
  "v_add_u32 v249, 40960, %[a1]\n\t" \
  "v_add_u32 v250, 40960, %[a2]\n\t" \
  "v_add_u32 v251, 40960, %[a3]\n\t" \
  "s_mov_b32 %[cnt], 3\n\t" \
  "s_barrier\n\t" \
  G1_STAMP \
  "ds_read_b64_tr_b16 v[160:161], v240 offset:0\n\t" \
  "ds_read_b64_tr_b16 v[162:163], v240 offset:512\n\t" \
  "ds_read_b64_tr_b16 v[164:165], v241 offset:0\n\t" \
  "ds_read_b64_tr_b16 v[166:167], v241 offset:512\n\t" \
  "ds_read_b64_tr_b16 v[168:169], v240 offset:8192\n\t" \
  "ds_read_b64_tr_b16 v[170:171], v240 offset:8704\n\t" \
  "ds_read_b64_tr_b16 v[172:173], v241 offset:8192\n\t" \
  "ds_read_b64_tr_b16 v[174:175], v241 offset:8704\n\t" \
  "ds_read_b128 v[176:179], v242 offset:0\n\t" \
  "ds_read_b128 v[180:183], v242 offset:2048\n\t" \
  "ds_read_b128 v[184:187], v242 offset:4096\n\t" \
  "ds_read_b128 v[188:191], v242 offset:6144\n\t" \
  "ds_read_b128 v[192:195], v242 offset:8192\n\t" \
  "ds_read_b128 v[196:199], v242 offset:10240\n\t" \
  "s_waitcnt lgkmcnt(0)\n\t" \
  ".Lg1_%=:\n\t" \
  "v_mfma_f32_16x16x32_bf16 v[64:67], v[160:163], v[176:179], v[64:67]\n\t" \
  "ds_read_b64_tr_b16 v[200:201], v240 offset:4096\n\t" \
  "v_mfma_f32_16x16x32_bf16 v[68:71], v[164:167], v[176:179], v[68:71]\n\t" \
  "ds_read_b64_tr_b16 v[202:203], v240 offset:4608\n\t" \
  "v_mfma_f32_16x16x32_bf16 v[72:75], v[168:171], v[176:179], v[72:75]\n\t" \
  "ds_read_b64_tr_b16 v[204:205], v241 offset:4096\n\t" \
  "v_mfma_f32_16x16x32_bf16 v[76:79], v[172:175], v[176:179], v[76:79]\n\t" \
  "ds_read_b64_tr_b16 v[206:207], v241 offset:4608\n\t" \
  "v_mfma_f32_16x16x32_bf16 v[80:83], v[160:163], v[180:183], v[80:83]\n\t" \
  "ds_read_b64_tr_b16 v[208:209], v240 offset:12288\n\t" \
  "v_mfma_f32_16x16x32_bf16 v[84:87], v[164:167], v[180:183], v[84:87]\n\t" \
  "ds_read_b64_tr_b16 v[210:211], v240 offset:12800\n\t" \
  "v_mfma_f32_16x16x32_bf16 v[88:91], v[168:171], v[180:183], v[88:91]\n\t" \
  "ds_read_b64_tr_b16 v[212:213], v241 offset:12288\n\t" \
  "v_mfma_f32_16x16x32_bf16 v[92:95], v[172:175], v[180:183], v[92:95]\n\t" \
  "ds_read_b64_tr_b16 v[214:215], v241 offset:12800\n\t" \
  "v_mfma_f32_16x16x32_bf16 v[96:99], v[160:163], v[184:187], v[96:99]\n\t" \
  "ds_read_b128 v[216:219], v243 offset:0\n\t" \
  "v_mfma_f32_16x16x32_bf16 v[100:103], v[164:167], v[184:187], v[100:103]\n\t" \
  "ds_read_b128 v[220:223], v243 offset:2048\n\t" \
  "v_mfma_f32_16x16x32_bf16 v[104:107], v[168:171], v[184:187], v[104:107]\n\t" \
  "ds_read_b128 v[224:227], v243 offset:4096\n\t" \
  "v_mfma_f32_16x16x32_bf16 v[108:111], v[172:175], v[184:187], v[108:111]\n\t" \
  "ds_read_b128 v[228:231], v243 offset:6144\n\t" \
  "v_mfma_f32_16x16x32_bf16 v[112:115], v[160:163], v[188:191], v[112:115]\n\t" \
  "ds_read_b128 v[232:235], v243 offset:8192\n\t" \
  "v_mfma_f32_16x16x32_bf16 v[116:119], v[164:167], v[188:191], v[116:119]\n\t" \
  "ds_read_b128 v[236:239], v243 offset:10240\n\t" \
  "v_mfma_f32_16x16x32_bf16 v[120:123], v[168:171], v[188:191], v[120:123]\n\t" \
  "v_mfma_f32_16x16x32_bf16 v[124:127], v[172:175], v[188:191], v[124:127]\n\t" \
  "v_mfma_f32_16x16x32_bf16 v[128:131], v[160:163], v[192:195], v[128:131]\n\t" \
  "v_mfma_f32_16x16x32_bf16 v[132:135], v[164:167], v[192:195], v[132:135]\n\t" \
  "v_mfma_f32_16x16x32_bf16 v[136:139], v[168:171], v[192:195], v[136:139]\n\t" \
  "v_mfma_f32_16x16x32_bf16 v[140:143], v[172:175], v[192:195], v[140:143]\n\t" \
  "v_mfma_f32_16x16x32_bf16 v[144:147], v[160:163], v[196:199], v[144:147]\n\t" \
  "v_mfma_f32_16x16x32_bf16 v[148:151], v[164:167], v[196:199], v[148:151]\n\t" \
  "v_mfma_f32_16x16x32_bf16 v[152:155], v[168:171], v[196:199], v[152:155]\n\t" \
  "v_mfma_f32_16x16x32_bf16 v[156:159], v[172:175], v[196:199], v[156:159]\n\t" \
  "s_waitcnt vmcnt(0) lgkmcnt(0)\n\t" \
  "s_barrier\n\t" \
  "v_mfma_f32_16x16x32_bf16 v[64:67], v[200:203], v[216:219], v[64:67]\n\t" \
  "ds_read_b64_tr_b16 v[160:161], v244 offset:0\n\t" \
  "v_mfma_f32_16x16x32_bf16 v[68:71], v[204:207], v[216:219], v[68:71]\n\t" \
  "ds_read_b64_tr_b16 v[162:163], v244 offset:512\n\t" \
  "v_mfma_f32_16x16x32_bf16 v[72:75], v[208:211], v[216:219], v[72:75]\n\t" \
  "ds_read_b64_tr_b16 v[164:165], v245 offset:0\n\t" \
  "v_mfma_f32_16x16x32_bf16 v[76:79], v[212:215], v[216:219], v[76:79]\n\t" \
  "ds_read_b64_tr_b16 v[166:167], v245 offset:512\n\t" \
  "v_mfma_f32_16x16x32_bf16 v[80:83], v[200:203], v[220:223], v[80:83]\n\t" \
  "ds_read_b64_tr_b16 v[168:169], v244 offset:8192\n\t" \
  "v_mfma_f32_16x16x32_bf16 v[84:87], v[204:207], v[220:223], v[84:87]\n\t" \
  "ds_read_b64_tr_b16 v[170:171], v244 offset:8704\n\t" \
  "v_mfma_f32_16x16x32_bf16 v[88:91], v[208:211], v[220:223], v[88:91]\n\t" \
  "ds_read_b64_tr_b16 v[172:173], v245 offset:8192\n\t" \
  "v_mfma_f32_16x16x32_bf16 v[92:95], v[212:215], v[220:223], v[92:95]\n\t" \
  "ds_read_b64_tr_b16 v[174:175], v245 offset:8704\n\t" \
  "v_mfma_f32_16x16x32_bf16 v[96:99], v[200:203], v[224:227], v[96:99]\n\t" \
  "ds_read_b128 v[176:179], v246 offset:0\n\t" \
  "v_mfma_f32_16x16x32_bf16 v[100:103], v[204:207], v[224:227], v[100:103]\n\t" \
  "ds_read_b128 v[180:183], v246 offset:2048\n\t" \
  "v_mfma_f32_16x16x32_bf16 v[104:107], v[208:211], v[224:227], v[104:107]\n\t" \
  "ds_read_b128 v[184:187], v246 offset:4096\n\t" \
  "v_mfma_f32_16x16x32_bf16 v[108:111], v[212:215], v[224:227], v[108:111]\n\t" \
  "ds_read_b128 v[188:191], v246 offset:6144\n\t" \
  "v_mfma_f32_16x16x32_bf16 v[112:115], v[200:203], v[228:231], v[112:115]\n\t" \
  "ds_read_b128 v[192:195], v246 offset:8192\n\t" \
  "v_mfma_f32_16x16x32_bf16 v[116:119], v[204:207], v[228:231], v[116:119]\n\t" \
  "ds_read_b128 v[196:199], v246 offset:10240\n\t" \
  "v_mfma_f32_16x16x32_bf16 v[120:123], v[208:211], v[228:231], v[120:123]\n\t" \
  "v_mfma_f32_16x16x32_bf16 v[124:127], v[212:215], v[228:231], v[124:127]\n\t" \
  "v_mfma_f32_16x16x32_bf16 v[128:131], v[200:203], v[232:235], v[128:131]\n\t" \
  "v_mfma_f32_16x16x32_bf16 v[132:135], v[204:207], v[232:235], v[132:135]\n\t" \
  "v_mfma_f32_16x16x32_bf16 v[136:139], v[208:211], v[232:235], v[136:139]\n\t" \
  "v_mfma_f32_16x16x32_bf16 v[140:143], v[212:215], v[232:235], v[140:143]\n\t" \
  "v_mfma_f32_16x16x32_bf16 v[144:147], v[200:203], v[236:239], v[144:147]\n\t" \
  "v_mfma_f32_16x16x32_bf16 v[148:151], v[204:207], v[236:239], v[148:151]\n\t" \
  "v_mfma_f32_16x16x32_bf16 v[152:155], v[208:211], v[236:239], v[152:155]\n\t" \
  "v_mfma_f32_16x16x32_bf16 v[156:159], v[212:215], v[236:239], v[156:159]\n\t" \
  "s_waitcnt lgkmcnt(0)\n\t" \
  "v_mfma_f32_16x16x32_bf16 v[64:67], v[160:163], v[176:179], v[64:67]\n\t" \
  "ds_read_b64_tr_b16 v[200:201], v244 offset:4096\n\t" \
  "v_mfma_f32_16x16x32_bf16 v[68:71], v[164:167], v[176:179], v[68:71]\n\t" \
  "ds_read_b64_tr_b16 v[202:203], v244 offset:4608\n\t" \
  "v_mfma_f32_16x16x32_bf16 v[72:75], v[168:171], v[176:179], v[72:75]\n\t" \
  "ds_read_b64_tr_b16 v[204:205], v245 offset:4096\n\t" \
  "v_mfma_f32_16x16x32_bf16 v[76:79], v[172:175], v[176:179], v[76:79]\n\t" \
  "ds_read_b64_tr_b16 v[206:207], v245 offset:4608\n\t" \
  "v_mfma_f32_16x16x32_bf16 v[80:83], v[160:163], v[180:183], v[80:83]\n\t" \
  "ds_read_b64_tr_b16 v[208:209], v244 offset:12288\n\t" \
  "v_mfma_f32_16x16x32_bf16 v[84:87], v[164:167], v[180:183], v[84:87]\n\t" \
  "ds_read_b64_tr_b16 v[210:211], v244 offset:12800\n\t" \
  "v_mfma_f32_16x16x32_bf16 v[88:91], v[168:171], v[180:183], v[88:91]\n\t" \
  "ds_read_b64_tr_b16 v[212:213], v245 offset:12288\n\t" \
  "v_mfma_f32_16x16x32_bf16 v[92:95], v[172:175], v[180:183], v[92:95]\n\t" \
  "ds_read_b64_tr_b16 v[214:215], v245 offset:12800\n\t" \
  "v_mfma_f32_16x16x32_bf16 v[96:99], v[160:163], v[184:187], v[96:99]\n\t" \
  "ds_read_b128 v[216:219], v247 offset:0\n\t" \
  "v_mfma_f32_16x16x32_bf16 v[100:103], v[164:167], v[184:187], v[100:103]\n\t" \
  "ds_read_b128 v[220:223], v247 offset:2048\n\t" \
  "v_mfma_f32_16x16x32_bf16 v[104:107], v[168:171], v[184:187], v[104:107]\n\t" \
  "ds_read_b128 v[224:227], v247 offset:4096\n\t" \
  "v_mfma_f32_16x16x32_bf16 v[108:111], v[172:175], v[184:187], v[108:111]\n\t" \
  "ds_read_b128 v[228:231], v247 offset:6144\n\t" \
  "v_mfma_f32_16x16x32_bf16 v[112:115], v[160:163], v[188:191], v[112:115]\n\t" \
  "ds_read_b128 v[232:235], v247 offset:8192\n\t" \
  "v_mfma_f32_16x16x32_bf16 v[116:119], v[164:167], v[188:191], v[116:119]\n\t" \
  "ds_read_b128 v[236:239], v247 offset:10240\n\t" \
  "v_mfma_f32_16x16x32_bf16 v[120:123], v[168:171], v[188:191], v[120:123]\n\t" \
  "v_mfma_f32_16x16x32_bf16 v[124:127], v[172:175], v[188:191], v[124:127]\n\t" \
  "v_mfma_f32_16x16x32_bf16 v[128:131], v[160:163], v[192:195], v[128:131]\n\t" \
  "v_mfma_f32_16x16x32_bf16 v[132:135], v[164:167], v[192:195], v[132:135]\n\t" \
  "v_mfma_f32_16x16x32_bf16 v[136:139], v[168:171], v[192:195], v[136:139]\n\t" \
  "v_mfma_f32_16x16x32_bf16 v[140:143], v[172:175], v[192:195], v[140:143]\n\t" \
  "v_mfma_f32_16x16x32_bf16 v[144:147], v[160:163], v[196:199], v[144:147]\n\t" \
  "v_mfma_f32_16x16x32_bf16 v[148:151], v[164:167], v[196:199], v[148:151]\n\t" \
  "v_mfma_f32_16x16x32_bf16 v[152:155], v[168:171], v[196:199], v[152:155]\n\t" \
  "v_mfma_f32_16x16x32_bf16 v[156:159], v[172:175], v[196:199], v[156:159]\n\t" \
  "s_waitcnt vmcnt(0) lgkmcnt(0)\n\t" \
  "s_barrier\n\t" \
  "v_mfma_f32_16x16x32_bf16 v[64:67], v[200:203], v[216:219], v[64:67]\n\t" \
  "ds_read_b64_tr_b16 v[160:161], v248 offset:0\n\t" \
  "v_mfma_f32_16x16x32_bf16 v[68:71], v[204:207], v[216:219], v[68:71]\n\t" \
  "ds_read_b64_tr_b16 v[162:163], v248 offset:512\n\t" \
  "v_mfma_f32_16x16x32_bf16 v[72:75], v[208:211], v[216:219], v[72:75]\n\t" \
  "ds_read_b64_tr_b16 v[164:165], v249 offset:0\n\t" \
  "v_mfma_f32_16x16x32_bf16 v[76:79], v[212:215], v[216:219], v[76:79]\n\t" \
  "ds_read_b64_tr_b16 v[166:167], v249 offset:512\n\t" \
  "v_mfma_f32_16x16x32_bf16 v[80:83], v[200:203], v[220:223], v[80:83]\n\t" \
  "ds_read_b64_tr_b16 v[168:169], v248 offset:8192\n\t" \
  "v_mfma_f32_16x16x32_bf16 v[84:87], v[204:207], v[220:223], v[84:87]\n\t" \
  "ds_read_b64_tr_b16 v[170:171], v248 offset:8704\n\t" \
  "v_mfma_f32_16x16x32_bf16 v[88:91], v[208:211], v[220:223], v[88:91]\n\t" \
  "ds_read_b64_tr_b16 v[172:173], v249 offset:8192\n\t" \
  "v_mfma_f32_16x16x32_bf16 v[92:95], v[212:215], v[220:223], v[92:95]\n\t" \
  "ds_read_b64_tr_b16 v[174:175], v249 offset:8704\n\t" \
  "v_mfma_f32_16x16x32_bf16 v[96:99], v[200:203], v[224:227], v[96:99]\n\t" \
  "ds_read_b128 v[176:179], v250 offset:0\n\t" \
  "v_mfma_f32_16x16x32_bf16 v[100:103], v[204:207], v[224:227], v[100:103]\n\t" \
  "ds_read_b128 v[180:183], v250 offset:2048\n\t" \
  "v_mfma_f32_16x16x32_bf16 v[104:107], v[208:211], v[224:227], v[104:107]\n\t" \
  "ds_read_b128 v[184:187], v250 offset:4096\n\t" \
  "v_mfma_f32_16x16x32_bf16 v[108:111], v[212:215], v[224:227], v[108:111]\n\t" \
  "ds_read_b128 v[188:191], v250 offset:6144\n\t" \
  "v_mfma_f32_16x16x32_bf16 v[112:115], v[200:203], v[228:231], v[112:115]\n\t" \
  "ds_read_b128 v[192:195], v250 offset:8192\n\t" \
  "v_mfma_f32_16x16x32_bf16 v[116:119], v[204:207], v[228:231], v[116:119]\n\t" \
  "ds_read_b128 v[196:199], v250 offset:10240\n\t" \
  "v_mfma_f32_16x16x32_bf16 v[120:123], v[208:211], v[228:231], v[120:123]\n\t" \
  "v_mfma_f32_16x16x32_bf16 v[124:127], v[212:215], v[228:231], v[124:127]\n\t" \
  "v_mfma_f32_16x16x32_bf16 v[128:131], v[200:203], v[232:235], v[128:131]\n\t" \
  "v_mfma_f32_16x16x32_bf16 v[132:135], v[204:207], v[232:235], v[132:135]\n\t" \
  "v_mfma_f32_16x16x32_bf16 v[136:139], v[208:211], v[232:235], v[136:139]\n\t" \
  "v_mfma_f32_16x16x32_bf16 v[140:143], v[212:215], v[232:235], v[140:143]\n\t" \
  "v_mfma_f32_16x16x32_bf16 v[144:147], v[200:203], v[236:239], v[144:147]\n\t" \
  "v_mfma_f32_16x16x32_bf16 v[148:151], v[204:207], v[236:239], v[148:151]\n\t" \
  "v_mfma_f32_16x16x32_bf16 v[152:155], v[208:211], v[236:239], v[152:155]\n\t" \
  "v_mfma_f32_16x16x32_bf16 v[156:159], v[212:215], v[236:239], v[156:159]\n\t" \
  "s_waitcnt lgkmcnt(0)\n\t" \
  "v_mfma_f32_16x16x32_bf16 v[64:67], v[160:163], v[176:179], v[64:67]\n\t" \
  "ds_read_b64_tr_b16 v[200:201], v248 offset:4096\n\t" \
  "v_mfma_f32_16x16x32_bf16 v[68:71], v[164:167], v[176:179], v[68:71]\n\t" \
  "ds_read_b64_tr_b16 v[202:203], v248 offset:4608\n\t" \
  "v_mfma_f32_16x16x32_bf16 v[72:75], v[168:171], v[176:179], v[72:75]\n\t" \
  "ds_read_b64_tr_b16 v[204:205], v249 offset:4096\n\t" \
  "v_mfma_f32_16x16x32_bf16 v[76:79], v[172:175], v[176:179], v[76:79]\n\t" \
  "ds_read_b64_tr_b16 v[206:207], v249 offset:4608\n\t" \
  "v_mfma_f32_16x16x32_bf16 v[80:83], v[160:163], v[180:183], v[80:83]\n\t" \
  "ds_read_b64_tr_b16 v[208:209], v248 offset:12288\n\t" \
  "v_mfma_f32_16x16x32_bf16 v[84:87], v[164:167], v[180:183], v[84:87]\n\t" \
  "ds_read_b64_tr_b16 v[210:211], v248 offset:12800\n\t" \
  "v_mfma_f32_16x16x32_bf16 v[88:91], v[168:171], v[180:183], v[88:91]\n\t" \
  "ds_read_b64_tr_b16 v[212:213], v249 offset:12288\n\t" \
  "v_mfma_f32_16x16x32_bf16 v[92:95], v[172:175], v[180:183], v[92:95]\n\t" \
  "ds_read_b64_tr_b16 v[214:215], v249 offset:12800\n\t" \
  "v_mfma_f32_16x16x32_bf16 v[96:99], v[160:163], v[184:187], v[96:99]\n\t" \
  "ds_read_b128 v[216:219], v251 offset:0\n\t" \
  "v_mfma_f32_16x16x32_bf16 v[100:103], v[164:167], v[184:187], v[100:103]\n\t" \
  "ds_read_b128 v[220:223], v251 offset:2048\n\t" \
  "v_mfma_f32_16x16x32_bf16 v[104:107], v[168:171], v[184:187], v[104:107]\n\t" \
  "ds_read_b128 v[224:227], v251 offset:4096\n\t" \
  "v_mfma_f32_16x16x32_bf16 v[108:111], v[172:175], v[184:187], v[108:111]\n\t" \
  "ds_read_b128 v[228:231], v251 offset:6144\n\t" \
  "v_mfma_f32_16x16x32_bf16 v[112:115], v[160:163], v[188:191], v[112:115]\n\t" \
  "ds_read_b128 v[232:235], v251 offset:8192\n\t" \
  "v_mfma_f32_16x16x32_bf16 v[116:119], v[164:167], v[188:191], v[116:119]\n\t" \
  "ds_read_b128 v[236:239], v251 offset:10240\n\t" \
  "v_mfma_f32_16x16x32_bf16 v[120:123], v[168:171], v[188:191], v[120:123]\n\t" \
  "v_mfma_f32_16x16x32_bf16 v[124:127], v[172:175], v[188:191], v[124:127]\n\t" \
  "v_mfma_f32_16x16x32_bf16 v[128:131], v[160:163], v[192:195], v[128:131]\n\t" \
  "v_mfma_f32_16x16x32_bf16 v[132:135], v[164:167], v[192:195], v[132:135]\n\t" \
  "v_mfma_f32_16x16x32_bf16 v[136:139], v[168:171], v[192:195], v[136:139]\n\t" \
  "v_mfma_f32_16x16x32_bf16 v[140:143], v[172:175], v[192:195], v[140:143]\n\t" \
  "v_mfma_f32_16x16x32_bf16 v[144:147], v[160:163], v[196:199], v[144:147]\n\t" \
  "v_mfma_f32_16x16x32_bf16 v[148:151], v[164:167], v[196:199], v[148:151]\n\t" \
  "v_mfma_f32_16x16x32_bf16 v[152:155], v[168:171], v[196:199], v[152:155]\n\t" \
  "v_mfma_f32_16x16x32_bf16 v[156:159], v[172:175], v[196:199], v[156:159]\n\t" \
  "s_waitcnt vmcnt(0) lgkmcnt(0)\n\t" \
  "s_barrier\n\t" \
  "v_mfma_f32_16x16x32_bf16 v[64:67], v[200:203], v[216:219], v[64:67]\n\t" \
  "ds_read_b64_tr_b16 v[160:161], v240 offset:0\n\t" \
  "v_mfma_f32_16x16x32_bf16 v[68:71], v[204:207], v[216:219], v[68:71]\n\t" \
  "ds_read_b64_tr_b16 v[162:163], v240 offset:512\n\t" \
  "v_mfma_f32_16x16x32_bf16 v[72:75], v[208:211], v[216:219], v[72:75]\n\t" \
  "ds_read_b64_tr_b16 v[164:165], v241 offset:0\n\t" \
  "v_mfma_f32_16x16x32_bf16 v[76:79], v[212:215], v[216:219], v[76:79]\n\t" \
  "ds_read_b64_tr_b16 v[166:167], v241 offset:512\n\t" \
  "v_mfma_f32_16x16x32_bf16 v[80:83], v[200:203], v[220:223], v[80:83]\n\t" \
  "ds_read_b64_tr_b16 v[168:169], v240 offset:8192\n\t" \
  "v_mfma_f32_16x16x32_bf16 v[84:87], v[204:207], v[220:223], v[84:87]\n\t" \
  "ds_read_b64_tr_b16 v[170:171], v240 offset:8704\n\t" \
  "v_mfma_f32_16x16x32_bf16 v[88:91], v[208:211], v[220:223], v[88:91]\n\t" \
  "ds_read_b64_tr_b16 v[172:173], v241 offset:8192\n\t" \
  "v_mfma_f32_16x16x32_bf16 v[92:95], v[212:215], v[220:223], v[92:95]\n\t" \
  "ds_read_b64_tr_b16 v[174:175], v241 offset:8704\n\t" \
  "v_mfma_f32_16x16x32_bf16 v[96:99], v[200:203], v[224:227], v[96:99]\n\t" \
  "ds_read_b128 v[176:179], v242 offset:0\n\t" \
  "v_mfma_f32_16x16x32_bf16 v[100:103], v[204:207], v[224:227], v[100:103]\n\t" \
  "ds_read_b128 v[180:183], v242 offset:2048\n\t" \
  "v_mfma_f32_16x16x32_bf16 v[104:107], v[208:211], v[224:227], v[104:107]\n\t" \
  "ds_read_b128 v[184:187], v242 offset:4096\n\t" \
  "v_mfma_f32_16x16x32_bf16 v[108:111], v[212:215], v[224:227], v[108:111]\n\t" \
  "ds_read_b128 v[188:191], v242 offset:6144\n\t" \
  "v_mfma_f32_16x16x32_bf16 v[112:115], v[200:203], v[228:231], v[112:115]\n\t" \
  "ds_read_b128 v[192:195], v242 offset:8192\n\t" \
  "v_mfma_f32_16x16x32_bf16 v[116:119], v[204:207], v[228:231], v[116:119]\n\t" \
  "ds_read_b128 v[196:199], v242 offset:10240\n\t" \
  "v_mfma_f32_16x16x32_bf16 v[120:123], v[208:211], v[228:231], v[120:123]\n\t" \
  "v_mfma_f32_16x16x32_bf16 v[124:127], v[212:215], v[228:231], v[124:127]\n\t" \
  "v_mfma_f32_16x16x32_bf16 v[128:131], v[200:203], v[232:235], v[128:131]\n\t" \
  "v_mfma_f32_16x16x32_bf16 v[132:135], v[204:207], v[232:235], v[132:135]\n\t" \
  "v_mfma_f32_16x16x32_bf16 v[136:139], v[208:211], v[232:235], v[136:139]\n\t" \
  "v_mfma_f32_16x16x32_bf16 v[140:143], v[212:215], v[232:235], v[140:143]\n\t" \
  "v_mfma_f32_16x16x32_bf16 v[144:147], v[200:203], v[236:239], v[144:147]\n\t" \
  "v_mfma_f32_16x16x32_bf16 v[148:151], v[204:207], v[236:239], v[148:151]\n\t" \
  "v_mfma_f32_16x16x32_bf16 v[152:155], v[208:211], v[236:239], v[152:155]\n\t" \
  "v_mfma_f32_16x16x32_bf16 v[156:159], v[212:215], v[236:239], v[156:159]\n\t" \
  "s_sub_u32 %[cnt], %[cnt], 1\n\t" \
  "s_cmp_lg_u32 %[cnt], 0\n\t" \
  "s_waitcnt lgkmcnt(0)\n\t" \
  "s_cbranch_scc1 .Lg1_%=\n\t" \
  "v_mfma_f32_16x16x32_bf16 v[64:67], v[160:163], v[176:179], v[64:67]\n\t" \
  "ds_read_b64_tr_b16 v[200:201], v240 offset:4096\n\t" \
  "v_mfma_f32_16x16x32_bf16 v[68:71], v[164:167], v[176:179], v[68:71]\n\t" \
  "ds_read_b64_tr_b16 v[202:203], v240 offset:4608\n\t" \
  "v_mfma_f32_16x16x32_bf16 v[72:75], v[168:171], v[176:179], v[72:75]\n\t" \
  "ds_read_b64_tr_b16 v[204:205], v241 offset:4096\n\t" \
  "v_mfma_f32_16x16x32_bf16 v[76:79], v[172:175], v[176:179], v[76:79]\n\t" \
  "ds_read_b64_tr_b16 v[206:207], v241 offset:4608\n\t" \
  "v_mfma_f32_16x16x32_bf16 v[80:83], v[160:163], v[180:183], v[80:83]\n\t" \
  "ds_read_b64_tr_b16 v[208:209], v240 offset:12288\n\t" \
  "v_mfma_f32_16x16x32_bf16 v[84:87], v[164:167], v[180:183], v[84:87]\n\t" \
  "ds_read_b64_tr_b16 v[210:211], v240 offset:12800\n\t" \
  "v_mfma_f32_16x16x32_bf16 v[88:91], v[168:171], v[180:183], v[88:91]\n\t" \
  "ds_read_b64_tr_b16 v[212:213], v241 offset:12288\n\t" \
  "v_mfma_f32_16x16x32_bf16 v[92:95], v[172:175], v[180:183], v[92:95]\n\t" \
  "ds_read_b64_tr_b16 v[214:215], v241 offset:12800\n\t" \
  "v_mfma_f32_16x16x32_bf16 v[96:99], v[160:163], v[184:187], v[96:99]\n\t" \
  "ds_read_b128 v[216:219], v243 offset:0\n\t" \
  "v_mfma_f32_16x16x32_bf16 v[100:103], v[164:167], v[184:187], v[100:103]\n\t" \
  "ds_read_b128 v[220:223], v243 offset:2048\n\t" \
  "v_mfma_f32_16x16x32_bf16 v[104:107], v[168:171], v[184:187], v[104:107]\n\t" \
  "ds_read_b128 v[224:227], v243 offset:4096\n\t" \
  "v_mfma_f32_16x16x32_bf16 v[108:111], v[172:175], v[184:187], v[108:111]\n\t" \
  "ds_read_b128 v[228:231], v243 offset:6144\n\t" \
  "v_mfma_f32_16x16x32_bf16 v[112:115], v[160:163], v[188:191], v[112:115]\n\t" \
  "ds_read_b128 v[232:235], v243 offset:8192\n\t" \
  "v_mfma_f32_16x16x32_bf16 v[116:119], v[164:167], v[188:191], v[116:119]\n\t" \
  "ds_read_b128 v[236:239], v243 offset:10240\n\t" \
  "v_mfma_f32_16x16x32_bf16 v[120:123], v[168:171], v[188:191], v[120:123]\n\t" \
  "v_mfma_f32_16x16x32_bf16 v[124:127], v[172:175], v[188:191], v[124:127]\n\t" \
  "v_mfma_f32_16x16x32_bf16 v[128:131], v[160:163], v[192:195], v[128:131]\n\t" \
  "v_mfma_f32_16x16x32_bf16 v[132:135], v[164:167], v[192:195], v[132:135]\n\t" \
  "v_mfma_f32_16x16x32_bf16 v[136:139], v[168:171], v[192:195], v[136:139]\n\t" \
  "v_mfma_f32_16x16x32_bf16 v[140:143], v[172:175], v[192:195], v[140:143]\n\t" \
  "v_mfma_f32_16x16x32_bf16 v[144:147], v[160:163], v[196:199], v[144:147]\n\t" \
  "v_mfma_f32_16x16x32_bf16 v[148:151], v[164:167], v[196:199], v[148:151]\n\t" \
  "v_mfma_f32_16x16x32_bf16 v[152:155], v[168:171], v[196:199], v[152:155]\n\t" \
  "v_mfma_f32_16x16x32_bf16 v[156:159], v[172:175], v[196:199], v[156:159]\n\t" \
  "s_waitcnt lgkmcnt(0)\n\t" \
  "v_mfma_f32_16x16x32_bf16 v[64:67], v[200:203], v[216:219], v[64:67]\n\t" \
  "ds_read_b128 v[160:163], %[cs] offset:0\n\t" \
  "v_mfma_f32_16x16x32_bf16 v[68:71], v[204:207], v[216:219], v[68:71]\n\t" \
  "ds_read_b128 v[164:167], %[cs] offset:64\n\t" \
  "v_mfma_f32_16x16x32_bf16 v[72:75], v[208:211], v[216:219], v[72:75]\n\t" \
  "ds_read_b128 v[168:171], %[cs] offset:256\n\t" \
  "v_mfma_f32_16x16x32_bf16 v[76:79], v[212:215], v[216:219], v[76:79]\n\t" \
  "ds_read_b128 v[172:175], %[cs] offset:320\n\t" \
  "v_mfma_f32_16x16x32_bf16 v[80:83], v[200:203], v[220:223], v[80:83]\n\t" \
  "ds_read_b128 v[176:179], %[st] offset:0\n\t" \
  "v_mfma_f32_16x16x32_bf16 v[84:87], v[204:207], v[220:223], v[84:87]\n\t" \
  "ds_read_b128 v[180:183], %[st] offset:256\n\t" \
  "v_mfma_f32_16x16x32_bf16 v[88:91], v[208:211], v[220:223], v[88:91]\n\t" \
  "ds_read_b128 v[184:187], %[st] offset:512\n\t" \
  "v_mfma_f32_16x16x32_bf16 v[92:95], v[212:215], v[220:223], v[92:95]\n\t" \
  "ds_read_b128 v[188:191], %[st] offset:768\n\t" \
  "v_mfma_f32_16x16x32_bf16 v[96:99], v[200:203], v[224:227], v[96:99]\n\t" \
  "ds_read_b128 v[192:195], %[st] offset:1024\n\t" \
  "v_mfma_f32_16x16x32_bf16 v[100:103], v[204:207], v[224:227], v[100:103]\n\t" \
  "ds_read_b128 v[196:199], %[st] offset:1280\n\t" \
  "v_mfma_f32_16x16x32_bf16 v[104:107], v[208:211], v[224:227], v[104:107]\n\t" \
  "v_mfma_f32_16x16x32_bf16 v[108:111], v[212:215], v[224:227], v[108:111]\n\t" \
  "v_mfma_f32_16x16x32_bf16 v[112:115], v[200:203], v[228:231], v[112:115]\n\t" \
  "v_mfma_f32_16x16x32_bf16 v[116:119], v[204:207], v[228:231], v[116:119]\n\t" \
  "v_mfma_f32_16x16x32_bf16 v[120:123], v[208:211], v[228:231], v[120:123]\n\t" \
  "v_mfma_f32_16x16x32_bf16 v[124:127], v[212:215], v[228:231], v[124:127]\n\t" \
  "v_mfma_f32_16x16x32_bf16 v[128:131], v[200:203], v[232:235], v[128:131]\n\t" \
  "v_mfma_f32_16x16x32_bf16 v[132:135], v[204:207], v[232:235], v[132:135]\n\t" \
  "v_mfma_f32_16x16x32_bf16 v[136:139], v[208:211], v[232:235], v[136:139]\n\t" \
  "v_mfma_f32_16x16x32_bf16 v[140:143], v[212:215], v[232:235], v[140:143]\n\t" \
  "v_mfma_f32_16x16x32_bf16 v[144:147], v[200:203], v[236:239], v[144:147]\n\t" \
  "v_mfma_f32_16x16x32_bf16 v[148:151], v[204:207], v[236:239], v[148:151]\n\t" \
  "v_mfma_f32_16x16x32_bf16 v[152:155], v[208:211], v[236:239], v[152:155]\n\t" \
  "v_mfma_f32_16x16x32_bf16 v[156:159], v[212:215], v[236:239], v[156:159]\n\t" \
  "s_nop 7\n\t" \
  "s_waitcnt lgkmcnt(0)\n\t" \
  ""
#define G1_MAIN_CLOB "v200","v201","v202","v203","v204","v205","v206","v207","v208","v209","v210","v211","v212","v213","v214","v215","v216","v217","v218","v219","v220","v221","v222","v223","v224","v225","v226","v227","v228","v229","v230","v231","v232","v233","v234","v235","v236","v237","v238","v239","v240","v241","v242","v243","v244","v245","v246","v247","v248","v249","v250","v251"
#define CONV_FIRST_ASM \
  "ds_read_b128 v[96:99], %[aa] offset:0\n\t" \
  "ds_read_b128 v[100:103], %[aa] offset:512\n\t" \
  "ds_read_b128 v[104:107], %[aa] offset:1024\n\t" \
  "ds_read_b128 v[108:111], %[aa] offset:1536\n\t" \
  "ds_read_b128 v[140:143], %[ab] offset:4032\n\t" \
  "ds_read_b128 v[136:139], %[ab] offset:3520\n\t" \
  "ds_read_b128 v[132:135], %[ab] offset:3008\n\t" \
  "ds_read_b128 v[128:131], %[ab] offset:2496\n\t" \
  "ds_read_b128 v[124:127], %[ab] offset:1984\n\t" \
  "ds_read_b128 v[120:123], %[ab] offset:1472\n\t" \
  "ds_read_b128 v[116:119], %[ab] offset:960\n\t" \
  "ds_read_b128 v[112:115], %[ab] offset:448\n\t" \
  ""
#define CONV_MAIN_ASM \
  "v_mfma_f32_16x16x32_bf16 v[92:95], v[96:99], v[140:143], v[92:95]\n\t" \
  "ds_read_b128 v[144:147], %[aa] offset:64\n\t" \
  "v_mfma_f32_16x16x32_bf16 v[88:91], v[96:99], v[136:139], v[88:91]\n\t" \
  "ds_read_b128 v[148:151], %[aa] offset:576\n\t" \
  "v_mfma_f32_16x16x32_bf16 v[84:87], v[96:99], v[132:135], v[84:87]\n\t" \
  "ds_read_b128 v[152:155], %[aa] offset:1088\n\t" \
  "v_mfma_f32_16x16x32_bf16 v[80:83], v[96:99], v[128:131], v[80:83]\n\t" \
  "ds_read_b128 v[156:159], %[aa] offset:1600\n\t" \
  "v_mfma_f32_16x16x32_bf16 v[76:79], v[96:99], v[124:127], v[76:79]\n\t" \
  "ds_read_b128 v[188:191], %[ab] offset:3968\n\t" \
  "v_mfma_f32_16x16x32_bf16 v[72:75], v[96:99], v[120:123], v[72:75]\n\t" \
  "ds_read_b128 v[184:187], %[ab] offset:3456\n\t" \
  "v_mfma_f32_16x16x32_bf16 v[68:71], v[96:99], v[116:119], v[68:71]\n\t" \
  "ds_read_b128 v[180:183], %[ab] offset:2944\n\t" \
  "v_mfma_f32_16x16x32_bf16 v[64:67], v[96:99], v[112:115], v[64:67]\n\t" \
  "ds_read_b128 v[176:179], %[ab] offset:2432\n\t" \
  "v_mfma_f32_16x16x32_bf16 v[92:95], v[100:103], v[136:139], v[92:95]\n\t" \
  "ds_read_b128 v[172:175], %[ab] offset:1920\n\t" \
  "v_mfma_f32_16x16x32_bf16 v[88:91], v[100:103], v[132:135], v[88:91]\n\t" \
  "ds_read_b128 v[168:171], %[ab] offset:1408\n\t" \
  "v_mfma_f32_16x16x32_bf16 v[84:87], v[100:103], v[128:131], v[84:87]\n\t" \
  "ds_read_b128 v[164:167], %[ab] offset:896\n\t" \
  "v_mfma_f32_16x16x32_bf16 v[80:83], v[100:103], v[124:127], v[80:83]\n\t" \
  "ds_read_b128 v[160:163], %[ab] offset:384\n\t" \
  "v_mfma_f32_16x16x32_bf16 v[76:79], v[100:103], v[120:123], v[76:79]\n\t" \
  "v_mfma_f32_16x16x32_bf16 v[72:75], v[100:103], v[116:119], v[72:75]\n\t" \
  "v_mfma_f32_16x16x32_bf16 v[68:71], v[100:103], v[112:115], v[68:71]\n\t" \
  "v_mfma_f32_16x16x32_bf16 v[92:95], v[104:107], v[132:135], v[92:95]\n\t" \
  "v_mfma_f32_16x16x32_bf16 v[88:91], v[104:107], v[128:131], v[88:91]\n\t" \
  "v_mfma_f32_16x16x32_bf16 v[84:87], v[104:107], v[124:127], v[84:87]\n\t" \
  "v_mfma_f32_16x16x32_bf16 v[80:83], v[104:107], v[120:123], v[80:83]\n\t" \
  "v_mfma_f32_16x16x32_bf16 v[76:79], v[104:107], v[116:119], v[76:79]\n\t" \
  "v_mfma_f32_16x16x32_bf16 v[72:75], v[104:107], v[112:115], v[72:75]\n\t" \
  "v_mfma_f32_16x16x32_bf16 v[92:95], v[108:111], v[128:131], v[92:95]\n\t" \
  "v_mfma_f32_16x16x32_bf16 v[88:91], v[108:111], v[124:127], v[88:91]\n\t" \
  "v_mfma_f32_16x16x32_bf16 v[84:87], v[108:111], v[120:123], v[84:87]\n\t" \
  "v_mfma_f32_16x16x32_bf16 v[80:83], v[108:111], v[116:119], v[80:83]\n\t" \
  "v_mfma_f32_16x16x32_bf16 v[76:79], v[108:111], v[112:115], v[76:79]\n\t" \
  "s_waitcnt lgkmcnt(0)\n\t" \
  "v_mfma_f32_16x16x32_bf16 v[92:95], v[144:147], v[188:191], v[92:95]\n\t" \
  "ds_read_b128 v[96:99], %[aa] offset:128\n\t" \
  "v_mfma_f32_16x16x32_bf16 v[88:91], v[144:147], v[184:187], v[88:91]\n\t" \
  "ds_read_b128 v[100:103], %[aa] offset:640\n\t" \
  "v_mfma_f32_16x16x32_bf16 v[84:87], v[144:147], v[180:183], v[84:87]\n\t" \
  "ds_read_b128 v[104:107], %[aa] offset:1152\n\t" \
  "v_mfma_f32_16x16x32_bf16 v[80:83], v[144:147], v[176:179], v[80:83]\n\t" \
  "ds_read_b128 v[108:111], %[aa] offset:1664\n\t" \
  "v_mfma_f32_16x16x32_bf16 v[76:79], v[144:147], v[172:175], v[76:79]\n\t" \
  "ds_read_b128 v[140:143], %[ab] offset:3904\n\t" \
  "v_mfma_f32_16x16x32_bf16 v[72:75], v[144:147], v[168:171], v[72:75]\n\t" \
  "ds_read_b128 v[136:139], %[ab] offset:3392\n\t" \
  "v_mfma_f32_16x16x32_bf16 v[68:71], v[144:147], v[164:167], v[68:71]\n\t" \
  "ds_read_b128 v[132:135], %[ab] offset:2880\n\t" \
  "v_mfma_f32_16x16x32_bf16 v[64:67], v[144:147], v[160:163], v[64:67]\n\t" \
  "ds_read_b128 v[128:131], %[ab] offset:2368\n\t" \
  "v_mfma_f32_16x16x32_bf16 v[92:95], v[148:151], v[184:187], v[92:95]\n\t" \
  "ds_read_b128 v[124:127], %[ab] offset:1856\n\t" \
  "v_mfma_f32_16x16x32_bf16 v[88:91], v[148:151], v[180:183], v[88:91]\n\t" \
  "ds_read_b128 v[120:123], %[ab] offset:1344\n\t" \
  "v_mfma_f32_16x16x32_bf16 v[84:87], v[148:151], v[176:179], v[84:87]\n\t" \
  "ds_read_b128 v[116:119], %[ab] offset:832\n\t" \
  "v_mfma_f32_16x16x32_bf16 v[80:83], v[148:151], v[172:175], v[80:83]\n\t" \
  "ds_read_b128 v[112:115], %[ab] offset:320\n\t" \
  "v_mfma_f32_16x16x32_bf16 v[76:79], v[148:151], v[168:171], v[76:79]\n\t" \
  "v_mfma_f32_16x16x32_bf16 v[72:75], v[148:151], v[164:167], v[72:75]\n\t" \
  "v_mfma_f32_16x16x32_bf16 v[68:71], v[148:151], v[160:163], v[68:71]\n\t" \
  "v_mfma_f32_16x16x32_bf16 v[92:95], v[152:155], v[180:183], v[92:95]\n\t" \
  "v_mfma_f32_16x16x32_bf16 v[88:91], v[152:155], v[176:179], v[88:91]\n\t" \
  "v_mfma_f32_16x16x32_bf16 v[84:87], v[152:155], v[172:175], v[84:87]\n\t" \
  "v_mfma_f32_16x16x32_bf16 v[80:83], v[152:155], v[168:171], v[80:83]\n\t" \
  "v_mfma_f32_16x16x32_bf16 v[76:79], v[152:155], v[164:167], v[76:79]\n\t" \
  "v_mfma_f32_16x16x32_bf16 v[72:75], v[152:155], v[160:163], v[72:75]\n\t" \
  "v_mfma_f32_16x16x32_bf16 v[92:95], v[156:159], v[176:179], v[92:95]\n\t" \
  "v_mfma_f32_16x16x32_bf16 v[88:91], v[156:159], v[172:175], v[88:91]\n\t" \
  "v_mfma_f32_16x16x32_bf16 v[84:87], v[156:159], v[168:171], v[84:87]\n\t" \
  "v_mfma_f32_16x16x32_bf16 v[80:83], v[156:159], v[164:167], v[80:83]\n\t" \
  "v_mfma_f32_16x16x32_bf16 v[76:79], v[156:159], v[160:163], v[76:79]\n\t" \
  "s_waitcnt lgkmcnt(0)\n\t" \
  "v_mfma_f32_16x16x32_bf16 v[92:95], v[96:99], v[140:143], v[92:95]\n\t" \
  "ds_read_b128 v[144:147], %[aa] offset:192\n\t" \
  "v_mfma_f32_16x16x32_bf16 v[88:91], v[96:99], v[136:139], v[88:91]\n\t" \
  "ds_read_b128 v[148:151], %[aa] offset:704\n\t" \
  "v_mfma_f32_16x16x32_bf16 v[84:87], v[96:99], v[132:135], v[84:87]\n\t" \
  "ds_read_b128 v[152:155], %[aa] offset:1216\n\t" \
  "v_mfma_f32_16x16x32_bf16 v[80:83], v[96:99], v[128:131], v[80:83]\n\t" \
  "ds_read_b128 v[156:159], %[aa] offset:1728\n\t" \
  "v_mfma_f32_16x16x32_bf16 v[76:79], v[96:99], v[124:127], v[76:79]\n\t" \
  "ds_read_b128 v[188:191], %[ab] offset:3840\n\t" \
  "v_mfma_f32_16x16x32_bf16 v[72:75], v[96:99], v[120:123], v[72:75]\n\t" \
  "ds_read_b128 v[184:187], %[ab] offset:3328\n\t" \
  "v_mfma_f32_16x16x32_bf16 v[68:71], v[96:99], v[116:119], v[68:71]\n\t" \
  "ds_read_b128 v[180:183], %[ab] offset:2816\n\t" \
  "v_mfma_f32_16x16x32_bf16 v[64:67], v[96:99], v[112:115], v[64:67]\n\t" \
  "ds_read_b128 v[176:179], %[ab] offset:2304\n\t" \
  "v_mfma_f32_16x16x32_bf16 v[92:95], v[100:103], v[136:139], v[92:95]\n\t" \
  "ds_read_b128 v[172:175], %[ab] offset:1792\n\t" \
  "v_mfma_f32_16x16x32_bf16 v[88:91], v[100:103], v[132:135], v[88:91]\n\t" \
  "ds_read_b128 v[168:171], %[ab] offset:1280\n\t" \
  "v_mfma_f32_16x16x32_bf16 v[84:87], v[100:103], v[128:131], v[84:87]\n\t" \
  "ds_read_b128 v[164:167], %[ab] offset:768\n\t" \
  "v_mfma_f32_16x16x32_bf16 v[80:83], v[100:103], v[124:127], v[80:83]\n\t" \
  "ds_read_b128 v[160:163], %[ab] offset:256\n\t" \
  "v_mfma_f32_16x16x32_bf16 v[76:79], v[100:103], v[120:123], v[76:79]\n\t" \
  "v_mfma_f32_16x16x32_bf16 v[72:75], v[100:103], v[116:119], v[72:75]\n\t" \
  "v_mfma_f32_16x16x32_bf16 v[68:71], v[100:103], v[112:115], v[68:71]\n\t" \
  "v_mfma_f32_16x16x32_bf16 v[92:95], v[104:107], v[132:135], v[92:95]\n\t" \
  "v_mfma_f32_16x16x32_bf16 v[88:91], v[104:107], v[128:131], v[88:91]\n\t" \
  "v_mfma_f32_16x16x32_bf16 v[84:87], v[104:107], v[124:127], v[84:87]\n\t" \
  "v_mfma_f32_16x16x32_bf16 v[80:83], v[104:107], v[120:123], v[80:83]\n\t" \
  "v_mfma_f32_16x16x32_bf16 v[76:79], v[104:107], v[116:119], v[76:79]\n\t" \
  "v_mfma_f32_16x16x32_bf16 v[72:75], v[104:107], v[112:115], v[72:75]\n\t" \
  "v_mfma_f32_16x16x32_bf16 v[92:95], v[108:111], v[128:131], v[92:95]\n\t" \
  "v_mfma_f32_16x16x32_bf16 v[88:91], v[108:111], v[124:127], v[88:91]\n\t" \
  "v_mfma_f32_16x16x32_bf16 v[84:87], v[108:111], v[120:123], v[84:87]\n\t" \
  "v_mfma_f32_16x16x32_bf16 v[80:83], v[108:111], v[116:119], v[80:83]\n\t" \
  "v_mfma_f32_16x16x32_bf16 v[76:79], v[108:111], v[112:115], v[76:79]\n\t" \
  "s_waitcnt lgkmcnt(0)\n\t" \
  "v_mfma_f32_16x16x32_bf16 v[92:95], v[144:147], v[188:191], v[92:95]\n\t" \
  "ds_read_b128 v[96:99], %[aa] offset:256\n\t" \
  "v_mfma_f32_16x16x32_bf16 v[88:91], v[144:147], v[184:187], v[88:91]\n\t" \
  "ds_read_b128 v[100:103], %[aa] offset:768\n\t" \
  "v_mfma_f32_16x16x32_bf16 v[84:87], v[144:147], v[180:183], v[84:87]\n\t" \
  "ds_read_b128 v[104:107], %[aa] offset:1280\n\t" \
  "v_mfma_f32_16x16x32_bf16 v[80:83], v[144:147], v[176:179], v[80:83]\n\t" \
  "ds_read_b128 v[108:111], %[aa] offset:1792\n\t" \
  "v_mfma_f32_16x16x32_bf16 v[76:79], v[144:147], v[172:175], v[76:79]\n\t" \
  "ds_read_b128 v[140:143], %[ab] offset:3776\n\t" \
  "v_mfma_f32_16x16x32_bf16 v[72:75], v[144:147], v[168:171], v[72:75]\n\t" \
  "ds_read_b128 v[136:139], %[ab] offset:3264\n\t" \
  "v_mfma_f32_16x16x32_bf16 v[68:71], v[144:147], v[164:167], v[68:71]\n\t" \
  "ds_read_b128 v[132:135], %[ab] offset:2752\n\t" \
  "v_mfma_f32_16x16x32_bf16 v[64:67], v[144:147], v[160:163], v[64:67]\n\t" \
  "ds_read_b128 v[128:131], %[ab] offset:2240\n\t" \
  "v_mfma_f32_16x16x32_bf16 v[92:95], v[148:151], v[184:187], v[92:95]\n\t" \
  "ds_read_b128 v[124:127], %[ab] offset:1728\n\t" \
  "v_mfma_f32_16x16x32_bf16 v[88:91], v[148:151], v[180:183], v[88:91]\n\t" \
  "ds_read_b128 v[120:123], %[ab] offset:1216\n\t" \
  "v_mfma_f32_16x16x32_bf16 v[84:87], v[148:151], v[176:179], v[84:87]\n\t" \
  "ds_read_b128 v[116:119], %[ab] offset:704\n\t" \
  "v_mfma_f32_16x16x32_bf16 v[80:83], v[148:151], v[172:175], v[80:83]\n\t" \
  "ds_read_b128 v[112:115], %[ab] offset:192\n\t" \
  "v_mfma_f32_16x16x32_bf16 v[76:79], v[148:151], v[168:171], v[76:79]\n\t" \
  "v_mfma_f32_16x16x32_bf16 v[72:75], v[148:151], v[164:167], v[72:75]\n\t" \
  "v_mfma_f32_16x16x32_bf16 v[68:71], v[148:151], v[160:163], v[68:71]\n\t" \
  "v_mfma_f32_16x16x32_bf16 v[92:95], v[152:155], v[180:183], v[92:95]\n\t" \
  "v_mfma_f32_16x16x32_bf16 v[88:91], v[152:155], v[176:179], v[88:91]\n\t" \
  "v_mfma_f32_16x16x32_bf16 v[84:87], v[152:155], v[172:175], v[84:87]\n\t" \
  "v_mfma_f32_16x16x32_bf16 v[80:83], v[152:155], v[168:171], v[80:83]\n\t" \
  "v_mfma_f32_16x16x32_bf16 v[76:79], v[152:155], v[164:167], v[76:79]\n\t" \
  "v_mfma_f32_16x16x32_bf16 v[72:75], v[152:155], v[160:163], v[72:75]\n\t" \
  "v_mfma_f32_16x16x32_bf16 v[92:95], v[156:159], v[176:179], v[92:95]\n\t" \
  "v_mfma_f32_16x16x32_bf16 v[88:91], v[156:159], v[172:175], v[88:91]\n\t" \
  "v_mfma_f32_16x16x32_bf16 v[84:87], v[156:159], v[168:171], v[84:87]\n\t" \
  "v_mfma_f32_16x16x32_bf16 v[80:83], v[156:159], v[164:167], v[80:83]\n\t" \
  "v_mfma_f32_16x16x32_bf16 v[76:79], v[156:159], v[160:163], v[76:79]\n\t" \
  "s_waitcnt lgkmcnt(0)\n\t" \
  "v_mfma_f32_16x16x32_bf16 v[92:95], v[96:99], v[140:143], v[92:95]\n\t" \
  "ds_read_b128 v[144:147], %[aa] offset:320\n\t" \
  "v_mfma_f32_16x16x32_bf16 v[88:91], v[96:99], v[136:139], v[88:91]\n\t" \
  "ds_read_b128 v[148:151], %[aa] offset:832\n\t" \
  "v_mfma_f32_16x16x32_bf16 v[84:87], v[96:99], v[132:135], v[84:87]\n\t" \
  "ds_read_b128 v[152:155], %[aa] offset:1344\n\t" \
  "v_mfma_f32_16x16x32_bf16 v[80:83], v[96:99], v[128:131], v[80:83]\n\t" \
  "ds_read_b128 v[156:159], %[aa] offset:1856\n\t" \
  "v_mfma_f32_16x16x32_bf16 v[76:79], v[96:99], v[124:127], v[76:79]\n\t" \
  "ds_read_b128 v[188:191], %[ab] offset:3712\n\t" \
  "v_mfma_f32_16x16x32_bf16 v[72:75], v[96:99], v[120:123], v[72:75]\n\t" \
  "ds_read_b128 v[184:187], %[ab] offset:3200\n\t" \
  "v_mfma_f32_16x16x32_bf16 v[68:71], v[96:99], v[116:119], v[68:71]\n\t" \
  "ds_read_b128 v[180:183], %[ab] offset:2688\n\t" \
  "v_mfma_f32_16x16x32_bf16 v[64:67], v[96:99], v[112:115], v[64:67]\n\t" \
  "ds_read_b128 v[176:179], %[ab] offset:2176\n\t" \
  "v_mfma_f32_16x16x32_bf16 v[92:95], v[100:103], v[136:139], v[92:95]\n\t" \
  "ds_read_b128 v[172:175], %[ab] offset:1664\n\t" \
  "v_mfma_f32_16x16x32_bf16 v[88:91], v[100:103], v[132:135], v[88:91]\n\t" \
  "ds_read_b128 v[168:171], %[ab] offset:1152\n\t" \
  "v_mfma_f32_16x16x32_bf16 v[84:87], v[100:103], v[128:131], v[84:87]\n\t" \
  "ds_read_b128 v[164:167], %[ab] offset:640\n\t" \
  "v_mfma_f32_16x16x32_bf16 v[80:83], v[100:103], v[124:127], v[80:83]\n\t" \
  "ds_read_b128 v[160:163], %[ab] offset:128\n\t" \
  "v_mfma_f32_16x16x32_bf16 v[76:79], v[100:103], v[120:123], v[76:79]\n\t" \
  "v_mfma_f32_16x16x32_bf16 v[72:75], v[100:103], v[116:119], v[72:75]\n\t" \
  "v_mfma_f32_16x16x32_bf16 v[68:71], v[100:103], v[112:115], v[68:71]\n\t" \
  "v_mfma_f32_16x16x32_bf16 v[92:95], v[104:107], v[132:135], v[92:95]\n\t" \
  "v_mfma_f32_16x16x32_bf16 v[88:91], v[104:107], v[128:131], v[88:91]\n\t" \
  "v_mfma_f32_16x16x32_bf16 v[84:87], v[104:107], v[124:127], v[84:87]\n\t" \
  "v_mfma_f32_16x16x32_bf16 v[80:83], v[104:107], v[120:123], v[80:83]\n\t" \
  "v_mfma_f32_16x16x32_bf16 v[76:79], v[104:107], v[116:119], v[76:79]\n\t" \
  "v_mfma_f32_16x16x32_bf16 v[72:75], v[104:107], v[112:115], v[72:75]\n\t" \
  "v_mfma_f32_16x16x32_bf16 v[92:95], v[108:111], v[128:131], v[92:95]\n\t" \
  "v_mfma_f32_16x16x32_bf16 v[88:91], v[108:111], v[124:127], v[88:91]\n\t" \
  "v_mfma_f32_16x16x32_bf16 v[84:87], v[108:111], v[120:123], v[84:87]\n\t" \
  "v_mfma_f32_16x16x32_bf16 v[80:83], v[108:111], v[116:119], v[80:83]\n\t" \
  "v_mfma_f32_16x16x32_bf16 v[76:79], v[108:111], v[112:115], v[76:79]\n\t" \
  "s_waitcnt lgkmcnt(0)\n\t" \
  "v_mfma_f32_16x16x32_bf16 v[92:95], v[144:147], v[188:191], v[92:95]\n\t" \
  "ds_read_b128 v[96:99], %[aa] offset:384\n\t" \
  "v_mfma_f32_16x16x32_bf16 v[88:91], v[144:147], v[184:187], v[88:91]\n\t" \
  "ds_read_b128 v[100:103], %[aa] offset:896\n\t" \
  "v_mfma_f32_16x16x32_bf16 v[84:87], v[144:147], v[180:183], v[84:87]\n\t" \
  "ds_read_b128 v[104:107], %[aa] offset:1408\n\t" \
  "v_mfma_f32_16x16x32_bf16 v[80:83], v[144:147], v[176:179], v[80:83]\n\t" \
  "ds_read_b128 v[108:111], %[aa] offset:1920\n\t" \
  "v_mfma_f32_16x16x32_bf16 v[76:79], v[144:147], v[172:175], v[76:79]\n\t" \
  "ds_read_b128 v[140:143], %[ab] offset:3648\n\t" \
  "v_mfma_f32_16x16x32_bf16 v[72:75], v[144:147], v[168:171], v[72:75]\n\t" \
  "ds_read_b128 v[136:139], %[ab] offset:3136\n\t" \
  "v_mfma_f32_16x16x32_bf16 v[68:71], v[144:147], v[164:167], v[68:71]\n\t" \
  "ds_read_b128 v[132:135], %[ab] offset:2624\n\t" \
  "v_mfma_f32_16x16x32_bf16 v[64:67], v[144:147], v[160:163], v[64:67]\n\t" \
  "ds_read_b128 v[128:131], %[ab] offset:2112\n\t" \
  "v_mfma_f32_16x16x32_bf16 v[92:95], v[148:151], v[184:187], v[92:95]\n\t" \
  "ds_read_b128 v[124:127], %[ab] offset:1600\n\t" \
  "v_mfma_f32_16x16x32_bf16 v[88:91], v[148:151], v[180:183], v[88:91]\n\t" \
  "ds_read_b128 v[120:123], %[ab] offset:1088\n\t" \
  "v_mfma_f32_16x16x32_bf16 v[84:87], v[148:151], v[176:179], v[84:87]\n\t" \
  "ds_read_b128 v[116:119], %[ab] offset:576\n\t" \
  "v_mfma_f32_16x16x32_bf16 v[80:83], v[148:151], v[172:175], v[80:83]\n\t" \
  "ds_read_b128 v[112:115], %[ab] offset:64\n\t" \
  "v_mfma_f32_16x16x32_bf16 v[76:79], v[148:151], v[168:171], v[76:79]\n\t" \
  "v_mfma_f32_16x16x32_bf16 v[72:75], v[148:151], v[164:167], v[72:75]\n\t" \
  "v_mfma_f32_16x16x32_bf16 v[68:71], v[148:151], v[160:163], v[68:71]\n\t" \
  "v_mfma_f32_16x16x32_bf16 v[92:95], v[152:155], v[180:183], v[92:95]\n\t" \
  "v_mfma_f32_16x16x32_bf16 v[88:91], v[152:155], v[176:179], v[88:91]\n\t" \
  "v_mfma_f32_16x16x32_bf16 v[84:87], v[152:155], v[172:175], v[84:87]\n\t" \
  "v_mfma_f32_16x16x32_bf16 v[80:83], v[152:155], v[168:171], v[80:83]\n\t" \
  "v_mfma_f32_16x16x32_bf16 v[76:79], v[152:155], v[164:167], v[76:79]\n\t" \
  "v_mfma_f32_16x16x32_bf16 v[72:75], v[152:155], v[160:163], v[72:75]\n\t" \
  "v_mfma_f32_16x16x32_bf16 v[92:95], v[156:159], v[176:179], v[92:95]\n\t" \
  "v_mfma_f32_16x16x32_bf16 v[88:91], v[156:159], v[172:175], v[88:91]\n\t" \
  "v_mfma_f32_16x16x32_bf16 v[84:87], v[156:159], v[168:171], v[84:87]\n\t" \
  "v_mfma_f32_16x16x32_bf16 v[80:83], v[156:159], v[164:167], v[80:83]\n\t" \
  "v_mfma_f32_16x16x32_bf16 v[76:79], v[156:159], v[160:163], v[76:79]\n\t" \
  "s_waitcnt lgkmcnt(0)\n\t" \
  "v_mfma_f32_16x16x32_bf16 v[92:95], v[96:99], v[140:143], v[92:95]\n\t" \
  "ds_read_b128 v[144:147], %[aa] offset:448\n\t" \
  "v_mfma_f32_16x16x32_bf16 v[88:91], v[96:99], v[136:139], v[88:91]\n\t" \
  "ds_read_b128 v[148:151], %[aa] offset:960\n\t" \
  "v_mfma_f32_16x16x32_bf16 v[84:87], v[96:99], v[132:135], v[84:87]\n\t" \
  "ds_read_b128 v[152:155], %[aa] offset:1472\n\t" \
  "v_mfma_f32_16x16x32_bf16 v[80:83], v[96:99], v[128:131], v[80:83]\n\t" \
  "ds_read_b128 v[156:159], %[aa] offset:1984\n\t" \
  "v_mfma_f32_16x16x32_bf16 v[76:79], v[96:99], v[124:127], v[76:79]\n\t" \
  "ds_read_b128 v[188:191], %[ab] offset:3584\n\t" \
  "v_mfma_f32_16x16x32_bf16 v[72:75], v[96:99], v[120:123], v[72:75]\n\t" \
  "ds_read_b128 v[184:187], %[ab] offset:3072\n\t" \
  "v_mfma_f32_16x16x32_bf16 v[68:71], v[96:99], v[116:119], v[68:71]\n\t" \
  "ds_read_b128 v[180:183], %[ab] offset:2560\n\t" \
  "v_mfma_f32_16x16x32_bf16 v[64:67], v[96:99], v[112:115], v[64:67]\n\t" \
  "ds_read_b128 v[176:179], %[ab] offset:2048\n\t" \
  "v_mfma_f32_16x16x32_bf16 v[92:95], v[100:103], v[136:139], v[92:95]\n\t" \
  "ds_read_b128 v[172:175], %[ab] offset:1536\n\t" \
  "v_mfma_f32_16x16x32_bf16 v[88:91], v[100:103], v[132:135], v[88:91]\n\t" \
  "ds_read_b128 v[168:171], %[ab] offset:1024\n\t" \
  "v_mfma_f32_16x16x32_bf16 v[84:87], v[100:103], v[128:131], v[84:87]\n\t" \
  "ds_read_b128 v[164:167], %[ab] offset:512\n\t" \
  "v_mfma_f32_16x16x32_bf16 v[80:83], v[100:103], v[124:127], v[80:83]\n\t" \
  "ds_read_b128 v[160:163], %[ab] offset:0\n\t" \
  "v_mfma_f32_16x16x32_bf16 v[76:79], v[100:103], v[120:123], v[76:79]\n\t" \
  "v_mfma_f32_16x16x32_bf16 v[72:75], v[100:103], v[116:119], v[72:75]\n\t" \
  "v_mfma_f32_16x16x32_bf16 v[68:71], v[100:103], v[112:115], v[68:71]\n\t" \
  "v_mfma_f32_16x16x32_bf16 v[92:95], v[104:107], v[132:135], v[92:95]\n\t" \
  "v_mfma_f32_16x16x32_bf16 v[88:91], v[104:107], v[128:131], v[88:91]\n\t" \
  "v_mfma_f32_16x16x32_bf16 v[84:87], v[104:107], v[124:127], v[84:87]\n\t" \
  "v_mfma_f32_16x16x32_bf16 v[80:83], v[104:107], v[120:123], v[80:83]\n\t" \
  "v_mfma_f32_16x16x32_bf16 v[76:79], v[104:107], v[116:119], v[76:79]\n\t" \
  "v_mfma_f32_16x16x32_bf16 v[72:75], v[104:107], v[112:115], v[72:75]\n\t" \
  "v_mfma_f32_16x16x32_bf16 v[92:95], v[108:111], v[128:131], v[92:95]\n\t" \
  "v_mfma_f32_16x16x32_bf16 v[88:91], v[108:111], v[124:127], v[88:91]\n\t" \
  "v_mfma_f32_16x16x32_bf16 v[84:87], v[108:111], v[120:123], v[84:87]\n\t" \
  "v_mfma_f32_16x16x32_bf16 v[80:83], v[108:111], v[116:119], v[80:83]\n\t" \
  "v_mfma_f32_16x16x32_bf16 v[76:79], v[108:111], v[112:115], v[76:79]\n\t" \
  "s_waitcnt lgkmcnt(0)\n\t" \
  "v_mfma_f32_16x16x32_bf16 v[92:95], v[144:147], v[188:191], v[92:95]\n\t" \
  "v_mfma_f32_16x16x32_bf16 v[88:91], v[144:147], v[184:187], v[88:91]\n\t" \
  "v_mfma_f32_16x16x32_bf16 v[84:87], v[144:147], v[180:183], v[84:87]\n\t" \
  "v_mfma_f32_16x16x32_bf16 v[80:83], v[144:147], v[176:179], v[80:83]\n\t" \
  "v_mfma_f32_16x16x32_bf16 v[76:79], v[144:147], v[172:175], v[76:79]\n\t" \
  "v_mfma_f32_16x16x32_bf16 v[72:75], v[144:147], v[168:171], v[72:75]\n\t" \
  "v_mfma_f32_16x16x32_bf16 v[68:71], v[144:147], v[164:167], v[68:71]\n\t" \
  "v_mfma_f32_16x16x32_bf16 v[64:67], v[144:147], v[160:163], v[64:67]\n\t" \
  "v_mfma_f32_16x16x32_bf16 v[92:95], v[148:151], v[184:187], v[92:95]\n\t" \
  "v_mfma_f32_16x16x32_bf16 v[88:91], v[148:151], v[180:183], v[88:91]\n\t" \
  "v_mfma_f32_16x16x32_bf16 v[84:87], v[148:151], v[176:179], v[84:87]\n\t" \
  "v_mfma_f32_16x16x32_bf16 v[80:83], v[148:151], v[172:175], v[80:83]\n\t" \
  "v_mfma_f32_16x16x32_bf16 v[76:79], v[148:151], v[168:171], v[76:79]\n\t" \
  "v_mfma_f32_16x16x32_bf16 v[72:75], v[148:151], v[164:167], v[72:75]\n\t" \
  "v_mfma_f32_16x16x32_bf16 v[68:71], v[148:151], v[160:163], v[68:71]\n\t" \
  "v_mfma_f32_16x16x32_bf16 v[92:95], v[152:155], v[180:183], v[92:95]\n\t" \
  "v_mfma_f32_16x16x32_bf16 v[88:91], v[152:155], v[176:179], v[88:91]\n\t" \
  "v_mfma_f32_16x16x32_bf16 v[84:87], v[152:155], v[172:175], v[84:87]\n\t" \
  "v_mfma_f32_16x16x32_bf16 v[80:83], v[152:155], v[168:171], v[80:83]\n\t" \
  "v_mfma_f32_16x16x32_bf16 v[76:79], v[152:155], v[164:167], v[76:79]\n\t" \
  "v_mfma_f32_16x16x32_bf16 v[72:75], v[152:155], v[160:163], v[72:75]\n\t" \
  "v_mfma_f32_16x16x32_bf16 v[92:95], v[156:159], v[176:179], v[92:95]\n\t" \
  "v_mfma_f32_16x16x32_bf16 v[88:91], v[156:159], v[172:175], v[88:91]\n\t" \
  "v_mfma_f32_16x16x32_bf16 v[84:87], v[156:159], v[168:171], v[84:87]\n\t" \
  "v_mfma_f32_16x16x32_bf16 v[80:83], v[156:159], v[164:167], v[80:83]\n\t" \
  "v_mfma_f32_16x16x32_bf16 v[76:79], v[156:159], v[160:163], v[76:79]\n\t" \
  "s_nop 7\n\t" \
  ""
#define CONV_MAIN_CLOB "v144","v145","v146","v147","v148","v149","v150","v151","v152","v153","v154","v155","v156","v157","v158","v159","v160","v161","v162","v163","v164","v165","v166","v167","v168","v169","v170","v171","v172","v173","v174","v175","v176","v177","v178","v179","v180","v181","v182","v183","v184","v185","v186","v187","v188","v189","v190","v191"

struct G1Ld { unsigned x[4], w[4]; };
DI G1Ld g1_lanes(int lane) {
  const int r8 = lane >> 3, sl = lane & 7;
  G1Ld v;
#pragma unroll
  for (int p = 0; p < 4; ++p) {
    v.x[p] = (r8 + 8 * p) * 1280 + ((sl ^ (r8 >> 1) ^ (4 * (p & 1))) << 4) - 1024 * p;
    v.w[p] = (r8 + 8 * p) * 3072 + ((sl ^ (r8 & 2) ^ (4 * (p & 1))) << 4) - 1024 * p;
  }
  return v;
}
template <int NT, int PART = 3> DI void g1_issue(v4u rx, v4u rg, v4u ru, const G1Ld& v, int lw, int s, unsigned lb, int row0, int ch) {
  const unsigned xso = (row0 + lw * 16 * NT) * 1280 + s * 128, wso = ch * 128 + (lw * 32 + s * 64) * 3072;
  if (PART & 1) {
    dma4<0>(rg, lb + XB + lw * 4096, v.w[0], v.w[1], v.w[2], v.w[3], wso);
    dma4<0>(ru, lb + XB + 8192 + lw * 4096, v.w[0], v.w[1], v.w[2], v.w[3], wso);
  }
#pragma unroll
  for (int p = 0; p < ((PART & 2) ? NT / 2 : 0); ++p) dma4<0>(rx, lb + (lw * 2 * NT + 4 * p) * 1024, v.x[0], v.x[1], v.x[2], v.x[3], xso + p * 32 * 1280);
}
DI unsigned nxt(unsigned so) { return so == 0 ? 2 * STAGE : so - STAGE; }
DI unsigned rem_up(unsigned so) { return so == 0 ? LX : so == STAGE ? LX + 8192 : L_U; }
DI void g1_issue_rem(v4u rx, v4u rg, v4u ru, const G1Ld& v, int lw, int s, unsigned lb, int ch, int ch2) {
  g1_issue<4>(rx, rg, ru, v, lw, s, lb, 1920, ch);
  const unsigned wso = ch2 * 128 + (lw * 32 + s * 64) * 3072;
  dma4<0>(rg, lb + 16384 + lw * 4096, v.w[0], v.w[1], v.w[2], v.w[3], wso);
  dma4<0>(ru, rem_up(lb) + lw * 4096, v.w[0], v.w[1], v.w[2], v.w[3], wso);
}
template <int NP, int S0, class Issue> DI void g1_loader(int rot, unsigned so, Issue issue) {
#pragma unroll 1
  for (int s = S0; s < 10; ++s) {
    if (s >= S0 + 2) wait_vm<NP>();
    if (S0 == 1 ? s != 2 : s >= 2) BAR(s == 1 ? 0 : s - 2);
    issue(s + rot >= 10 ? s + rot - 10 : s + rot, so);
    so = nxt(so);
  }
  wait_vm<NP>(); BAR(8);
}

DI void colsum(rsrc_t rw, unsigned off, int mat, int lane) {
  const int c = lane & 15, g = lane >> 4, q = (lane >> 2) & 3, p = lane & 3;
  const bf16x8 ones = __builtin_bit_cast(bf16x8, v4u{vmov(0x3f803f80u), vmov(0x3f803f80u), vmov(0x3f803f80u), vmov(0x3f803f80u)});
  bf16x8 f[4][2];
#pragma unroll
  for (int a = 0; a < 4; ++a) {
    const unsigned ad = XB + mat * 8192 + (8 * g + q) * 128 + (((2 * a + (p >> 1)) ^ (q & 2) ^ ((g & 1) << 2)) << 4) + 8 * (p & 1);
    f[a][0] = trd(ad, ad + 512); f[a][1] = trd(ad + 4096, ad + 4608);
  }
  __builtin_amdgcn_sched_barrier(0);
  f32x4 r[4];
#pragma unroll
  for (int a = 0; a < 4; ++a) r[a] = mfma(f[a][0], ones, f32x4{0.f, 0.f, 0.f, 0.f});
#pragma unroll
  for (int a = 0; a < 4; ++a) r[a] = mfma(f[a][1], ones, r[a]);
  f32x4 v;
#pragma unroll
  for (int e = 0; e < 4; ++e) v[e] = (c & 2) ? ((c & 1) ? r[3][e] : r[2][e]) : ((c & 1) ? r[1][e] : r[0][e]);
  if (c < 4) st128<SC1>(__builtin_bit_cast(v4u, v), rw, off + mat * 256 + (16 * c + 4 * g) * 4);
}

struct CsIn { v2u p[10]; };
DI void cs_load(rsrc_t rw, int ch, int lane, CsIn& c) {
#pragma unroll
  for (int k = 0; k < 10; ++k) c.p[k] = __builtin_amdgcn_raw_buffer_load_b64(rw, lane * 8 + k * 512, WS_CSP + ch * 5120, SC1);
  cfence();
}
DI void cs_put(const CsIn& c, unsigned off, int lane) {
  float c0 = 0.f, c1 = 0.f;
#pragma unroll
  for (int k = 0; k < 10; ++k) { c0 += __uint_as_float(c.p[k].x); asm volatile("" : "+v"(c0)); c1 += __uint_as_float(c.p[k].y); }
  *(LAS f32x2*)lds(off + lane * 8) = f32x2{c0, c1};
}

struct G1Rd { unsigned aA[2], aB[2]; };
template <int NT> DI void g1_setup(int wid, int lane, G1Rd& r, f32x4 (&acc)[4][NT]) {
  const int wa = wid & 1, wt = wid >> 1;
  const int c = lane & 15, g = lane >> 4, q = (lane >> 2) & 3, p = lane & 3;
  unsigned (&aA)[2] = r.aA; unsigned (&aB)[2] = r.aB;
#pragma unroll
  for (int a = 0; a < 2; ++a) aA[a] = XB + (8 * g + q) * 128 + (((4 * wa + 2 * a + (p >> 1)) ^ (q & 2) ^ ((g & 1) << 2)) << 4) + 8 * (p & 1);
#pragma unroll
  for (int ks = 0; ks < 2; ++ks) aB[ks] = (16 * NT * wt + c) * 128 + (((4 * ks + g) ^ (c >> 1)) << 4);
#pragma unroll
  for (int a = 0; a < 4; ++a)
#pragma unroll
    for (int t = 0; t < NT; ++t) acc[a][t] = f32x4{0.f, 0.f, 0.f, 0.f};
  asm volatile("" : "+v"(aA[0]), "+v"(aA[1]), "+v"(aB[0]), "+v"(aB[1]));
}
template <int NT> struct G1Ep { f32x4 cs[4], st[NT]; };
template <int NT> DI void g1_ep_read(int wid, int lane, unsigned st_off, unsigned cs_off, G1Ep<NT>& e) {
  const int wa = wid & 1, wt = wid >> 1, c = lane & 15, g = lane >> 4;
#pragma unroll
  for (int a = 0; a < 4; ++a) e.cs[a] = *(LAS f32x4*)lds(cs_off + (a >> 1) * 256 + (32 * wa + 16 * (a & 1) + 4 * g) * 4);
#pragma unroll
  for (int t = 0; t < NT; ++t) e.st[t] = *(LAS f32x4*)lds(st_off + (16 * NT * wt + 16 * t + c) * 16);
}
DI void g1_main_rem(const G1Rd& r, unsigned db, f32x4 (&acc4)[4][4], f32x4 (&acc2)[4][2]) {
  const unsigned (&aA)[2] = r.aA; const unsigned (&aB)[2] = r.aB;
  bf16x8 fa[2][8], fb[2][6];
  auto rd = [&](int buf, int ks, unsigned o) {
    const unsigned ou = rem_up(o) - XB;
#pragma unroll
    for (int a = 0; a < 4; ++a) { unsigned ad = aA[a & 1] + o + (a >> 1) * 8192 + ks * 4096; fa[buf][a] = trd(ad, ad + 512); }
#pragma unroll
    for (int t = 0; t < 4; ++t) fb[buf][t] = rd128(aB[ks] + o + t * 2048);
#pragma unroll
    for (int a = 0; a < 4; ++a) { unsigned ad = aA[a & 1] + (a < 2 ? o - 8192 : ou) + ks * 4096; fa[buf][4 + a] = trd(ad, ad + 512); }
#pragma unroll
    for (int t = 0; t < 2; ++t) fb[buf][4 + t] = rd128(aB[ks] + o + db + t * 2048);
  };
  auto mm = [&](int buf) {
#pragma unroll
    for (int t = 0; t < 4; ++t)
#pragma unroll
      for (int a = 0; a < 4; ++a) acc4[a][t] = mfma(fa[buf][a], fb[buf][t], acc4[a][t]);
#pragma unroll
    for (int t = 0; t < 2; ++t)
#pragma unroll
      for (int a = 0; a < 4; ++a) acc2[a][t] = mfma(fa[buf][4 + a], fb[buf][4 + t], acc2[a][t]);
  };
  auto ilv = [&]() {
#pragma unroll
    for (int i = 0; i < 22; ++i) { __builtin_amdgcn_sched_group_barrier(0x008, 1, 0); __builtin_amdgcn_sched_group_barrier(0x100, 1, 0); }
    __builtin_amdgcn_sched_group_barrier(0x008, 2, 0);
    __builtin_amdgcn_sched_barrier(0);
  };
  unsigned so = 0;
  BAR(0);
  STAMP(2); RSTAMP(31);
  rd(0, 0, so);
#pragma unroll 1
  for (int s = 0; s < 9; ++s) {
    rd(1, 1, so); mm(0); ilv();
    so = nxt(so);
    wait_lg();
    BAR(s + 1);
    __builtin_amdgcn_sched_barrier(0);
    rd(0, 0, so); mm(1); ilv();
  }
  rd(1, 1, so); mm(0); ilv();
  mm(1);
}
struct G1Acc { f32x16 a[6]; unsigned csa, sta; };
DI void g1_setup6(int wid, int lane, G1Acc& z) {
#pragma unroll
  for (int t = 0; t < 6; ++t) z.a[t] = f32x16{};
  z.csa = L_CS + (32 * (wid & 1) + 4 * (lane >> 4)) * 4; z.sta = L_ST + (96 * (wid >> 1) + (lane & 15)) * 16;
  asm volatile("" : "+{v[64:79]}"(z.a[0]), "+{v[80:95]}"(z.a[1]), "+{v[96:111]}"(z.a[2]), "+{v[112:127]}"(z.a[3]), "+{v[128:143]}"(z.a[4]),
               "+{v[144:159]}"(z.a[5]), "+v"(z.csa), "+v"(z.sta));
}
DI void g1_main6(const G1Rd& r, G1Acc& z, f32x4 (&acc)[4][6], G1Ep<6>& ep) {
  f32x16 (&a)[6] = z.a; f32x16 cs, s0; f32x8 s1; int cnt;
  const unsigned csa = z.csa, sta = z.sta;
  asm volatile(G1_MAIN_ASM
               : "+{v[64:79]}"(a[0]), "+{v[80:95]}"(a[1]), "+{v[96:111]}"(a[2]), "+{v[112:127]}"(a[3]), "+{v[128:143]}"(a[4]), "+{v[144:159]}"(a[5]),
                 "=&{v[160:175]}"(cs), "=&{v[176:191]}"(s0), "=&{v[192:199]}"(s1), [cnt] "=&s"(cnt)
               : [a0] "v"(r.aA[0]), [a1] "v"(r.aA[1]), [a2] "v"(r.aB[0]), [a3] "v"(r.aB[1]), [cs] "v"(csa), [st] "v"(sta)
               : "memory", "scc", G1_MAIN_CLOB G1_STAMP_CLOB);
#pragma unroll
  for (int t = 0; t < 6; ++t)
#pragma unroll
    for (int i = 0; i < 16; ++i) acc[i >> 2][t][i & 3] = a[t][i];
#pragma unroll
  for (int i = 0; i < 16; ++i) { ep.cs[i >> 2][i & 3] = cs[i]; ep.st[i >> 2][i & 3] = s0[i]; }
#pragma unroll
  for (int i = 0; i < 8; ++i) ep.st[4 + (i >> 2)][i & 3] = s1[i];
}
template <int NT, bool LAST> DI void g1_epilogue(rsrc_t rw, int wid, int lane, int t16_0, int ch, const G1Ep<NT>& ep, const f32x4 (&acc)[4][NT]) {
  const int wa = wid & 1, wt = wid >> 1;
  const f32x4 (&cs)[4] = ep.cs; const f32x4 (&st)[NT] = ep.st;
#pragma unroll
  for (int t = 0; t < NT; ++t) {
    const float nm = st[t][0], qq = st[t][1], rl = st[t][2];
    v4u o;
#pragma unroll
    for (int a = 0; a < 2; ++a) {
      f32x4 tg = cs[a] * nm + acc[a][t], tu = cs[2 + a] * nm + acc[2 + a][t];
      f32x4 ar = tg * rl, e;
#pragma unroll
      for (int r = 0; r < 4; ++r) e[r] = __builtin_amdgcn_exp2f(ar[r]);
      f32x4 dn = e * qq + qq, pr = tg * tu;
#pragma unroll
      for (int r = 0; r < 4; ++r) dn[r] = __builtin_amdgcn_rcpf(dn[r]);
      pr *= dn;
      if (LAST && t == NT - 1 && a == 1) { asm volatile("" : "+v"(pr)); lds_set(L_MISC + 48 + wid * 4); }
      o[2 * a] = pk(pr[0], pr[1]); o[2 * a + 1] = pk(pr[2], pr[3]);
    }
    st128<SC1>(o, rw, lane * 16 + WS_H + ((t16_0 + NT * wt + t) * 48 + 2 * ch + wa) * 1024);
  }
}

template <int NM, class Init> DI void kgen(const unsigned (&w)[6], float dt, int lane, unsigned tb, unsigned done, f32x4 (&acc)[2][2], Init init) {
  constexpr int RS = NM * 256 + 32, KS = NM * 4;
  float lr[NM], tr[NM]; f32x2 W[NM];
#pragma unroll
  for (int m = 0; m < NM; ++m) {
    float v[6];
#pragma unroll
    for (int i = 0; i < 6; ++i) v[i] = m == 0 ? blo(w[i]) : bhi(w[i]);
    lr[m] = dt * v[0] * 1.44269504f; tr[m] = dt * v[1] * 0.15915494f;
    const float br = dt * v[2], bi = dt * v[3];
    W[m] = f32x2{v[4] * br - v[5] * bi, -(v[4] * bi + v[5] * br)};
  }
  asm volatile("" : "+v"(lr[0]), "+v"(W[0]));
  STAMP(13);
  unsigned wa = tb + lane * 4 * NM, fa = tb + (lane & 15) * RS + (lane >> 4) * 16;
  asm volatile("" : "+v"(wa), "+v"(fa));
  unsigned wq[4] = {wa, wa + 4 * RS, wa + 8 * RS, wa + 12 * RS};
  if (NM == 1) asm volatile("" : "+v"(wq[1]), "+v"(wq[2]), "+v"(wq[3]));
  auto put = [&](int row, const f32x2* z) {
    if (NM == 2) *(LAS v2u*)lds(wa + row * RS) = v2u{pk2(z[0]), pk2(z[NM - 1])};
    else *(LAS unsigned*)lds(wq[row >> 2] + (row & 3) * RS) = pk2(z[0]);
  };
  f32x2 c[8][NM], hi[NM], t[NM], c8[NM];
#pragma unroll
  for (int m = 0; m < NM; ++m) {
    c[0][m] = f32x2{1.f, 0.f}; c[1][m] = cpow(lr[m], tr[m], 8.f);
    c[2][m] = cmul(c[1][m], c[1][m]); c[3][m] = cmul(c[2][m], c[1][m]); c[4][m] = cmul(c[2][m], c[2][m]);
    c[5][m] = cmul(c[4][m], c[1][m]); c[6][m] = cmul(c[4][m], c[2][m]); c[7][m] = cmul(c[4][m], c[3][m]);
    c8[m] = cmul(c[4][m], c[4][m]); hi[m] = c8[m];
  }
  bf16x8 fb[2][KS];
#pragma unroll
  for (int j = 0; j < 8; ++j) put(j, c[j]);
#pragma unroll
  for (int a = 1; a < 4; ++a) {
    if (a == 2) {
#pragma unroll
      for (int s = 0; s < KS; ++s) fb[0][s] = rd128(fa + s * 64);
    }
    put((8 * a) & 15, hi);
#pragma unroll
    for (int j = 1; j < 8; ++j) {
#pragma unroll
      for (int m = 0; m < NM; ++m) t[m] = cmul(hi[m], c[j][m]);
      put((8 * a + j) & 15, t);
    }
    if (a < 3) {
#pragma unroll
      for (int m = 0; m < NM; ++m) hi[m] = cmul(hi[m], c8[m]);
    }
  }
#pragma unroll
  for (int s = 0; s < KS; ++s) fb[1][s] = rd128(fa + s * 64);
  STAMP(14);
  if (NM == 2 && TRIG == 0) lds_set(done);
  f32x2 z1[NM], zb[NM], r0[NM], r4[NM];
#pragma unroll
  for (int m = 0; m < NM; ++m) { z1[m] = cpow(lr[m], -tr[m], 1.f); zb[m] = cpow(lr[m], -tr[m], 256.f); t[m] = W[m]; }
#pragma unroll
  for (int b = 0; b < 8; ++b) {
    put(b, t);
    if ((b & 1) == 0) {
      f32x2 u[NM];
#pragma unroll
      for (int m = 0; m < NM; ++m) { u[m] = cmul(t[m], zb[m]); if (b == 0) r0[m] = u[m]; if (b == 4) r4[m] = u[m]; }
      put(8 + b / 2, u);
    }
    if (b < 7) {
#pragma unroll
      for (int m = 0; m < NM; ++m) t[m] = cmul(t[m], z1[m]);
    }
  }
  if (NM == 2 && TRIG == 1) lds_set(done);
#pragma unroll
  for (int i = 0; i < 2; ++i) {
#pragma unroll
    for (int m = 0; m < NM; ++m) { r0[m] = cmul(r0[m], zb[m]); r4[m] = cmul(r4[m], zb[m]); }
    put(12 + 2 * i, r0); put(13 + 2 * i, r4);
  }
  init();
  bf16x8 fr[2][KS];
#pragma unroll
  for (int s = 0; s < KS; ++s) fr[0][s] = rd128(fa + s * 64);
#pragma unroll
  for (int i = 0; i < 5; ++i) {
#pragma unroll
    for (int m = 0; m < NM; ++m) r0[m] = cmul(r0[m], zb[m]);
    put(i, r0);
  }
  if (NM == 2 && TRIG == 2) lds_set(done);
#pragma unroll
  for (int s = 0; s < KS; ++s) fr[1][s] = rd128(fa + s * 64);
  __builtin_amdgcn_sched_barrier(0);
  STAMP(15);
#pragma unroll
  for (int rt = 0; rt < 2; ++rt)
#pragma unroll
    for (int s = 0; s < KS; ++s) { acc[rt][0] = mfma(fr[rt][s], fb[0][s], acc[rt][0]); acc[rt][1] = mfma(fr[rt][s], fb[1][s], acc[rt][1]); }
}
struct LnOut { s16x4 tr[2][10]; v2u st; };
DI void ln8(rsrc_t rx, int tb, int lane, unsigned tile, LnOut& out) {
  const int r4 = lane >> 4, i = lane & 15;
  v4u raw[2][5];
#pragma unroll
  for (int h = 0; h < 2; ++h)
#pragma unroll
    for (int k = 0; k < 5; ++k) raw[h][k] = __builtin_amdgcn_raw_buffer_load_b128(rx, r4 * 1280 + i * 16 + k * 256, (tb * 8 + h * 4) * 1280, 0);
  const unsigned ta = tile + ((lane >> 2) & 3) * LN_RS + (16 * r4 + 4 * (lane & 3)) * 2;
#pragma unroll
  for (int h = 0; h < 2; ++h) {
    if (h == 0) { asm volatile("" : "+v"(raw[0][4])); STAMP(13); asm volatile("" : "+v"(raw[1][4])); STAMP(15); } else STAMP(14);
    float s1 = 0.f, s2 = 0.f, s3 = 0.f, s4 = 0.f;
    const bf16x2 one2 = __builtin_bit_cast(bf16x2, 0x3f803f80u);
#pragma unroll
    for (int k = 0; k < 10; ++k) {
      const unsigned w0 = raw[h][k >> 1][2 * (k & 1)], w1 = raw[h][k >> 1][2 * (k & 1) + 1];
      const bf16x2 p0 = __builtin_bit_cast(bf16x2, w0), p1 = __builtin_bit_cast(bf16x2, w1);
      s1 = __builtin_amdgcn_fdot2_f32_bf16(p0, one2, s1, false); s2 = __builtin_amdgcn_fdot2_f32_bf16(p0, p0, s2, false);
      s3 = __builtin_amdgcn_fdot2_f32_bf16(p1, one2, s3, false); s4 = __builtin_amdgcn_fdot2_f32_bf16(p1, p1, s4, false);
    }
    const float mean = rowsum16(s1 + s3) * (1.f / D);
    const float var = rbf(fmaxf(rowsum16(s2 + s4) * (1.f / D) - mean * mean, 0.f));
    const float m = rbf(mean), den = rbf(sqrtf(rbf(var + 1e-5f)));
    const float ri = __builtin_amdgcn_rcpf(den), nb = -m * ri;
    if (h == 0 || (i & 1)) out.st = v2u{__float_as_uint(m), __float_as_uint(den)};
#pragma unroll
    for (int k = 0; k < 5; ++k) {
      v4u o;
#pragma unroll
      for (int e = 0; e < 4; ++e) { const unsigned w = raw[h][k][e]; o[e] = pk2(f32x2{blo(w), bhi(w)} * ri + nb); }
      *(LAS v4u*)lds(tile + r4 * LN_RS + i * 16 + k * 256) = o;
    }
    cfence();
#pragma unroll
    for (int it = 0; it < 10; ++it) out.tr[h][it] = __builtin_amdgcn_ds_read_tr16_b64_v4i16((LAS s16x4*)lds(ta + it * 128));
  }
}
DI void ln8_store(rsrc_t rw, int tb, int lane, const LnOut& in) {
  if ((lane & 15) < 2) __builtin_amdgcn_raw_buffer_store_b64(in.st, rw, ((lane & 1) * 4 + (lane >> 4)) * 8, WS_STATS + tb * 128, SC1);
#pragma unroll
  for (int it = 0; it < 10; ++it) {
    bf16x8 o = __builtin_shufflevector(in.tr[0][it], in.tr[1][it], 0, 1, 2, 3, 4, 5, 6, 7);
    st128<SC1>(__builtin_bit_cast(v4u, o), rw, lane * 16 + it * 1024 + WS_U8 + tb * 10240);
  }
}

DI unsigned prm(unsigned hi, unsigned lo, unsigned sel) { return __builtin_amdgcn_perm(hi, lo, sel); }
DI unsigned colo(int col) { return (col & 15) * 16 + ((col >> 4) & 1) * 8; }
template <int S> DI v4u kchunk(unsigned raw, int lane) {
  const int i = lane & 31, up = lane >> 5;
  if (S == 0) {
    const v2u a0 = *(LAS v2u*)lds(raw + colo(i) + 512 * up);
    const v2u a1 = *(LAS v2u*)lds(raw + (up ? (i == 31 ? 768 : 512 + colo(i + 1)) : 256 + colo(i)));
    const unsigned s0 = up ? 0x03020504u : 0x05040706u, s1 = up ? 0x01000100u : 0x05040706u, s2 = up ? 0x03020504u : 0x05040706u,
                   s3 = up ? 0x01000100u : 0x01000302u;
    return v4u{prm(up ? a1.x : a1.y, a0.y, s0), prm(a1.x, a0.y, s1), prm(a0.y, a0.x, s2), prm(0, a0.x, s3)};
  } else {
    const unsigned b0 = *(LAS unsigned*)lds(raw + 768 + colo(i) + 4 * up);
    const unsigned b1 = *(LAS unsigned*)lds(raw + (lane == 63 ? 1024 : 768 + colo(i + 1) + 4 * ((lane + 1) >> 5)));
    return v4u{prm(0, b1, 0x01000100), prm(0, b0, 0x03020302), prm(0, b0, 0x01000302), prm(0, b0, 0x01000100)};
  }
}
struct HWin { v8u w, e, o0, o1; };
DI void hwin(v4u x, v4u y, HWin& h) {
#pragma unroll
  for (int k = 0; k < 4; ++k) { h.w[k] = vmov(x[k]); h.w[4 + k] = y[k]; }
  asm volatile("" : "+v"(h.w));
#pragma unroll
  for (int k = 0; k < 6; ++k) {
    h.e[k] = vmov(h.w[k + 1]);
    h.o0[k] = __builtin_amdgcn_alignbit(h.w[k + 1], h.w[k], 16);
    asm("v_alignbit_b32 %0, %1, %2, 16" : "=v"(h.o1[k]) : "v"(h.w[k + 2]), "v"(h.w[k + 1]));
  }
  asm volatile("" : "+v"(h.e), "+v"(h.o0), "+v"(h.o1));
}
DI void hwrite(const HWin& h, unsigned a) {
  *(LAS v4u*)lds(a + 7 * H_CS) = __builtin_shufflevector(h.w, h.w, 0, 1, 2, 3);
  *(LAS v4u*)lds(a + 3 * H_CS) = __builtin_shufflevector(h.w, h.w, 2, 3, 4, 5);
  *(LAS v4u*)lds(a + 5 * H_CS) = __builtin_shufflevector(h.e, h.e, 0, 1, 2, 3);
  *(LAS v4u*)lds(a + 1 * H_CS) = __builtin_shufflevector(h.e, h.e, 2, 3, 4, 5);
  *(LAS v4u*)lds(a + 6 * H_CS) = __builtin_shufflevector(h.o0, h.o0, 0, 1, 2, 3);
  *(LAS v4u*)lds(a + 2 * H_CS) = __builtin_shufflevector(h.o0, h.o0, 2, 3, 4, 5);
  *(LAS v4u*)lds(a + 4 * H_CS) = __builtin_shufflevector(h.o1, h.o1, 0, 1, 2, 3);
  *(LAS v4u*)lds(a + 0 * H_CS) = __builtin_shufflevector(h.o1, h.o1, 2, 3, 4, 5);
}
struct ConvPre { v4u x0, x1; unsigned kk, k3; bf16x8 kt[4], b9, b7; };
DI void conv_masks(int lane, ConvPre& p) {
#pragma unroll
  for (int jj = 0; jj < 8; ++jj) {
    const int dl = 8 * (lane >> 4) + jj - 8 - (lane & 15);
    p.b9[jj] = (dl >= -4 && dl <= 4) ? 0x3f80 : 0; p.b7[jj] = (dl >= -3 && dl <= 3) ? 0x3f80 : 0;
  }
  asm volatile("" : "+v"(p.b9), "+v"(p.b7));
}
DI void conv_pre(unsigned raw, int lane, ConvPre& p) {
  p.x0 = kchunk<0>(raw, lane); p.x1 = kchunk<1>(raw, lane);
  p.kk = *(LAS uint16_t*)lds(raw + 1024);
  const int n0 = 128 - 2 * lane, n1 = n0 - 1;
  const unsigned c0 = *(LAS uint16_t*)lds(raw + 1024 + (n0 == 128 ? 256 : colo(n0) + 2 * (n0 >> 5)));
  const unsigned c1 = *(LAS uint16_t*)lds(raw + 1024 + colo(n1) + 2 * (n1 >> 5));
  p.k3 = c0 | (c1 << 16);
  asm volatile("" : "+v"(p.x0), "+v"(p.x1), "+v"(p.kk), "+v"(p.k3));
}
DI void conv_image(ConvPre& p, unsigned hb, unsigned k3, int lane) {
  HWin h[3];
  v4u y0, y1, cl;
#pragma unroll
  for (int k = 0; k < 4; ++k) {
    y0[k] = __builtin_amdgcn_update_dpp(0u, p.x0[k], 0x138, 0xf, 0xf, false);
    y1[k] = __builtin_amdgcn_update_dpp((unsigned)__builtin_amdgcn_readlane(p.x0[k], 63), p.x1[k], 0x138, 0xf, 0xf, false);
    cl[k] = __builtin_amdgcn_readlane(p.x1[k], 63);
  }
  hwin(p.x0, y0, h[0]);
  hwin(p.x1, y1, h[1]);
  const unsigned kk = p.kk, k2 = kk | (kk << 16), m0 = lane == 0 ? ~0u : 0u, m1 = lane == 1 ? ~0u : 0u;
  hwin(v4u{0, (kk << 16) & m0, k2 & m0, k2 & m0},
       v4u{cl[0] & m0, (cl[1] & m0) | ((kk << 16) & m1), (cl[2] & m0) | (k2 & m1), (cl[3] & m0) | (k2 & m1)}, h[2]);
  unsigned ka = k3 + (254 - 2 * (lane & 15) + 8 * (lane >> 4)) * 2 - 448, a0 = hb + (1 + lane) * 16, a2 = hb + (lane < 3 ? 129 + lane : lane == 3 ? 0 : 132) * 16;
  unsigned kw = k3 + lane * 4;
  asm volatile("" : "+v"(ka), "+v"(a0), "+v"(a2), "+v"(kw)::"memory");
  STAMP(3);
  *(LAS unsigned*)lds(kw) = p.k3;
  if (lane < 16) *(LAS unsigned*)lds(kw + 256) = 0;
#pragma unroll
  for (int f = 0; f < 4; ++f) {
    v4u t;
#pragma unroll
    for (int e = 0; e < 4; ++e) t[e] = *(LAS unsigned*)lds(ka + (3 - f) * 64 + e * 4);
    p.kt[f] = __builtin_bit_cast(bf16x8, t);
  }
  hwrite(h[0], a0);
  hwrite(h[1], a0 + 1024);
  if (lane < 5) hwrite(h[2], a2);
}

DI void conv(rsrc_t rw, int cw, int d, int lane, const ConvPre& p, unsigned go) {
  const unsigned hb = L_H + cw * H_SZ, ub = L_U + cw * U_SZ;
  const int c = lane & 15, g = lane >> 4;
  unsigned aa = hb + (c & 7) * H_CS + (3 + (c >> 3) - g) * 16, ab = ub + 32 + 32 * c + 16 * g;
  f32x4 acc[8] = {};
  v16u fa, fb0, fb1;
  asm volatile(CONV_FIRST_ASM : "=&{v[96:111]}"(fa), "=&{v[112:127]}"(fb0), "=&{v[128:143]}"(fb1) : [aa] "v"(aa), [ab] "v"(ab) : "memory");
  {
    const bf16x8 fx = rd128(aa + 2048);
    bf16x8 ue[4];
#pragma unroll
    for (int f = 0; f < 4; ++f) ue[f] = rd128(ab + 432 + f * 512);
#pragma unroll 1
    for (int spin = 0; spin < 64 * SPIN_MAX && *(volatile LAS unsigned*)lds(go) == 0; ++spin) {}
    asm volatile("s_waitcnt lgkmcnt(0)" : "+{v[96:111]}"(fa), "+{v[112:127]}"(fb0), "+{v[128:143]}"(fb1)::"memory");
    STAMP(5);
    __builtin_amdgcn_s_setprio(CPRIO);
#pragma unroll
    for (int f = 0; f < 4; ++f) {
      const bf16x8 uo = __builtin_bit_cast(bf16x8, v4u{fb0[4 * f], fb0[4 * f + 1], fb0[4 * f + 2], fb0[4 * f + 3]});
      acc[4 + f] = mfma(fx, uo, acc[4 + f]);
      const f32x4 ve = mfma(ue[f], p.b9, f32x4{0.f, 0.f, 0.f, 0.f}), vo = mfma(uo, p.b7, f32x4{0.f, 0.f, 0.f, 0.f});
      const bf16x8 vf = __builtin_bit_cast(bf16x8, v4u{pk(ve[0], vo[0]), pk(ve[1], vo[1]), pk(ve[2], vo[2]), pk(ve[3], vo[3])});
#pragma unroll
      for (int n = 4 + f; n < 8; ++n) acc[n] = mfma(vf, p.kt[n - f - 4], acc[n]);
    }
  }
  STAMP(8);
  f32x16 c0, c1;
#pragma unroll
  for (int i = 0; i < 16; ++i) { c0[i] = acc[i >> 2][i & 3]; c1[i] = acc[4 + (i >> 2)][i & 3]; }
  asm volatile("s_nop 7\n\t" CONV_MAIN_ASM
               : "+{v[64:79]}"(c0), "+{v[80:95]}"(c1), "+{v[96:111]}"(fa), "+{v[112:127]}"(fb0), "+{v[128:143]}"(fb1)
               : [aa] "v"(aa), [ab] "v"(ab) : "memory", CONV_MAIN_CLOB);
#pragma unroll
  for (int i = 0; i < 16; ++i) { acc[i >> 2][i & 3] = c0[i]; acc[4 + (i >> 2)][i & 3] = c1[i]; }
#pragma unroll
  for (int n = 0; n < 8; n += 2) {
    const auto s0 = __builtin_amdgcn_permlane16_swap(pk(acc[n][0], acc[n][1]), pk(acc[n + 1][0], acc[n + 1][1]), false, false);
    const auto s1 = __builtin_amdgcn_permlane16_swap(pk(acc[n][2], acc[n][3]), pk(acc[n + 1][2], acc[n + 1][3]), false, false);
    st128<SC1>(v4u{s0[0], s1[0], s0[1], s1[1]}, rw, WS_AT + d * 4096 + 32 * c + 16 * (g >> 1) + 512 * (n + (g & 1)));
  }
}
template <int D0, int ND> DI void finish(rsrc_t ro, int w, int lane, unsigned oo, const f32x4 (&own)[ND], const v2u (&xr)[ND]) {
  f32x4 p[3][ND]; v2u a[ND];
#pragma unroll
  for (int k = 1; k < 4; ++k)
#pragma unroll
    for (int i = 0; i < ND; ++i) p[k - 1][i] = *(LAS f32x4*)lds(((w + k) & 3) * 20480 + lane * 16 + (3 - k) * 5120 + (D0 + i) * 1024);
  const unsigned at = L_AT + (4 * (lane >> 4) + ((lane >> 2) & 3)) * 128 + (16 * w + 4 * (lane & 3)) * 2;
#pragma unroll
  for (int i = 0; i < ND; ++i) a[i] = __builtin_bit_cast(v2u, __builtin_amdgcn_ds_read_tr16_b64_v4i16((LAS s16x4*)lds(at + (D0 + i) * 2048)));
#pragma unroll
  for (int i = 0; i < ND; ++i) {
    const f32x4 r = (own[i] + f32x4{blo(xr[i].x), bhi(xr[i].x), blo(xr[i].y), bhi(xr[i].y)}) + (p[0][i] + p[1][i]) +
                    (p[2][i] + f32x4{blo(a[i].x), bhi(a[i].x), blo(a[i].y), bhi(a[i].y)});
    __builtin_amdgcn_raw_buffer_store_b64(v2u{pk(r[0], r[1]), pk(r[2], r[3])}, ro, oo + (D0 + i) * 32, 0, 0);
  }
}
#endif

__global__ __launch_bounds__(512, 2) void fused_s4_ffn(
    const uint16_t* __restrict__ x, const uint16_t* __restrict__ Are, const uint16_t* __restrict__ Aim, const uint16_t* __restrict__ Bre,
    const uint16_t* __restrict__ Bim, const uint16_t* __restrict__ Cre, const uint16_t* __restrict__ Cim, const uint16_t* __restrict__ logdt,
    const uint16_t* __restrict__ dskip, const uint16_t* __restrict__ wg, const uint16_t* __restrict__ wu, const uint16_t* __restrict__ wd,
    uint16_t* __restrict__ out, void* ws) {
#if __HIP_DEVICE_COMPILE__
  const int wid = __builtin_amdgcn_readfirstlane(threadIdx.x >> 6), lane = threadIdx.x & 63;
  const int b = blockIdx.x, xcd = b & 7, j = b >> 3;
  const bool normal = j < 30;
  const int rl = j % 5, ti = 5 * (xcd & 1) + rl;
  const int mm_ = 2 * (xcd >> 1) + (xcd & 1), par = j - 30;
  const int ch = normal ? 6 * (xcd >> 1) + j / 5 : 3 * mm_ + 2 * par;
  const int ch2 = 3 * mm_ + 1;
  const int nch = normal ? (j < 20 ? 3 : 2) : 0;
  const int d0 = 80 * xcd + (j < 20 ? 3 * j : 2 * j + 20);
  const int t64 = 8 * (xcd >> 1) + (j >> 2), dj = 4 * (xcd & 1) + (j & 3);
  const int rot = XROT ? (ti + (5 * (j / 5) + 1) / 3) % 10 : ti;
  const int rot2 = ROT2 == 6 ? 6 * ((j >> 1) & 1) : ROT2 * (j & 3);
  auto kst = [&](int s) { return s + rot2 >= 12 ? s + rot2 - 12 : s + rot2; };
  auto pbase = [](int w) -> unsigned { return w * 20480; };
  const rsrc_t rw = mkr(ws, WS_BYTES), rx = mkr(x, L * D * 2);
  auto finish_hi = [&]() {
    const int w = wid - 4;
    const unsigned oo = ((4 * t64 + w) * 16 + (lane & 15)) * 1280 + (80 * dj + 4 * (lane >> 4)) * 2;
    v2u xr[2];
#pragma unroll
    for (int i = 0; i < 2; ++i) xr[i] = __builtin_amdgcn_raw_buffer_load_b64(rx, oo + (3 + i) * 32, 0, 0);
    wait_vm<0>();
    BAR(11);
    const f32x4 own[2] = {*(LAS f32x4*)lds(pbase(w) + lane * 16 + 15360), *(LAS f32x4*)lds(pbase(w) + lane * 16 + 16384)};
    finish<3, 2>(mkr(out, L * D * 2), w, lane, oo, own, xr);
  };
  STAMP(0);
  if (wid == 6) {
    u64 pc; asm volatile("s_getpc_b64 %0" : "=s"(pc));
    dma<0>(srd((const void*)pc, CODE_BYTES), L_DUMMY, lane * 16, j * 1024);
    dma<0>(srd((const void*)pc, CODE_BYTES), L_DUMMY, lane * 16, 32768 + j * 1024);
  }
  if (wid == 7) { if (lane < 7) *(LAS v4u*)lds(L_MISC + 16 + lane * 16) = v4u{0, 0, 0, 0}; wait_lg(); }
  asm volatile("" ::"s"(x), "s"(Are), "s"(Aim), "s"(Bre), "s"(Bim), "s"(Cre), "s"(Cim), "s"(logdt), "s"(dskip), "s"(wg), "s"(wu), "s"(wd), "s"(out), "s"(ws));
  unsigned pw[6], ldw_ = 0, dsw_ = 0;
  const int kd = d0 + (wid & 3);
  if ((wid & 3) < nch) {
    const uint16_t* const par6[6] = {Are, Aim, Bre, Bim, Cre, Cim};
    const unsigned vo = wid >= 4 ? lane * 4 : 256 + (lane >> 1) * 4;
#pragma unroll
    for (int i = 0; i < 6; ++i) pw[i] = __builtin_amdgcn_raw_buffer_load_b32(mkr(par6[i], D * NS * 2), vo, kd * NS * 2, NT_);
    ldw_ = ((const __attribute__((address_space(4))) unsigned*)logdt)[kd >> 1];
    dsw_ = ((const __attribute__((address_space(4))) unsigned*)dskip)[kd >> 1];
  }
  ConvPre pre;
  if (wid >= 4) conv_masks(lane, pre);
  G1Ld gl = g1_lanes(lane);
  asm volatile("" : "+v"(gl.x[0]), "+v"(gl.x[1]), "+v"(gl.x[2]), "+v"(gl.x[3]), "+v"(gl.w[0]), "+v"(gl.w[1]), "+v"(gl.w[2]), "+v"(gl.w[3]));
  cfence(); bar(); cfence();
  STAMP(11);
  const float dt = rbf(expf((kd & 1) ? bhi(ldw_) : blo(ldw_)));
  const v4u sg = srd(wg, D * F * 2), su = srd(wu, D * F * 2), sx = srd(x, L * D * 2), sw = srd(ws, WS_BYTES);

  if (wid < 4) {
    if (wid < nch) {
      f32x4 ka[2][2];
#pragma unroll
      for (int i = 0; i < 6; ++i) pw[i] = (lane & 1) ? pw[i] >> 16 : pw[i];
      kgen<1>(pw, dt, lane, L_KG + wid * KG_SZ + 16 * 544, 0, ka, [&]() {
#pragma unroll
        for (int i = 0; i < 4; ++i) ka[i >> 1][i & 1] = f32x4{0.f, 0.f, 0.f, 0.f};
        if (lane == 0) ka[0][0][0] = (kd & 1) ? bhi(dsw_) : blo(dsw_);
      });
#pragma unroll
      for (int i = 0; i < 4; ++i) *(LAS f32x4*)lds(L_KG + wid * KG_SZ + 16 * 544 + i * 1024 + lane * 16) = ka[i >> 1][i & 1];
      wait_lg();
      lds_set(L_MISC + 16 + wid * 4);
      STAMP(12);
    } else if (wid == 3 && normal) {
      __builtin_amdgcn_s_sleep(W3SLEEP);
      const v4u sd = srd(wd, F * D * 2);
      const unsigned vd = (lane >> 3) * 1280 + (lane & 7) * 16;
      if (WDFIRST) {
#pragma unroll
        for (int p = 0; p < 2; ++p) dma4<NT_>(sd, L_WDU + p * 4096, vd, vd + 9216, vd + 18432, vd + 27648, (ch * 64 + p * 32) * 1280 + ti * 128);
      }
#pragma unroll
      for (int h = 0; h < 2; ++h) g1_issue<6, 1>(sx, sg, su, gl, h, rot, 0, 192 * ti, ch);
      if (!WDFIRST) {
#pragma unroll
        for (int p = 0; p < 2; ++p) dma4<NT_>(sd, L_WDU + p * 4096, vd, vd + 9216, vd + 18432, vd + 27648, (ch * 64 + p * 32) * 1280 + ti * 128);
      }
      wait_vm<WDFIRST ? 16 : 0>();
      const unsigned a0 = L_WDU + (4 * (lane >> 4) + ((lane >> 2) & 3)) * 128 + 8 * (lane & 3);
#pragma unroll
      for (int p = 0; p < 8; ++p) {
        const unsigned a = a0 + (p >> 2) * 4096 + (p & 3) * 32;
        st128<SC1>(__builtin_bit_cast(v4u, trd(a, a + 2048)), rw, lane * 16 + WS_WDT + ((2 * ch + (p >> 2)) * 40 + 4 * ti + (p & 3)) * 1024);
      }
      STAMP(12);
    }
    G1Rd rdr;
    if (normal) {
      f32x4 acc[4][6];
      G1Acc z;
      g1_setup<6>(wid, lane, rdr, acc);
      g1_setup6(wid, lane, z);
      if (wid < 2) {
        lds_wait(L_MISC + 68); lds_wait(L_MISC + 72);
        if (nch == 3) lds_wait(L_MISC + 76);
        STAMP(10);
        g1_issue<6, 2>(sx, sg, su, gl, wid, rot, 0, 192 * ti, ch);
        lds_set(L_MISC + 40 + wid * 4);
        wait_vm<0>();
      } else if (wid == 2) {
        lds_wait(L_MISC + 40); lds_wait(L_MISC + 44);
        STAMP(10);
#pragma unroll 1
        for (int h = 0; h < 2; ++h) g1_issue<6>(sx, sg, su, gl, h, rot == 9 ? 0 : rot + 1, 2 * STAGE, 192 * ti, ch);
      } else {
        wait_vm<8>();
      }
      STAMP(1);
      G1Ep<6> ep;
      g1_main6(rdr, z, acc, ep);
      lds_set(L_MISC + 112 + wid * 4);
      STAMP(3);
      g1_epilogue<6, true>(rw, wid, lane, 12 * ti, ch, ep, acc);
    } else {
      f32x4 acc4[4][4], acc2[4][2];
      G1Ep<4> ep4; G1Ep<2> ep2;
      g1_setup<2>(wid, lane, rdr, acc2);
      g1_setup<4>(wid, lane, rdr, acc4);
      g1_main_rem(rdr, (64 * par - 32 * (wid >> 1)) * 128, acc4, acc2);
      lds_set(L_MISC + 112 + wid * 4);
      STAMP(3);
      g1_ep_read<4>(wid, lane, L_ST, L_CS, ep4);
      g1_ep_read<2>(wid, lane, L_ST + par * 1024, L_CS + 512, ep2);
      g1_epilogue<4, false>(rw, wid, lane, 120, ch, ep4, acc4);
      g1_epilogue<2, true>(rw, wid, lane, 120 + 4 * par, ch2, ep2, acc2);
    }
    STAMP(4); RSTAMP(32);
    const v2u tag = tag_of();

    const unsigned wso = WS_WDT + (12 * wid * 40 + 5 * dj) * 1024, hso = WS_H + (4 * t64 * 48 + 12 * wid) * 1024;
    const unsigned vo0 = lane * 16, vo1 = lane * 16 + 4096, pw = pbase(wid) + lane * 16;
    v4u wr[5][5], hr[3][4];
#pragma unroll
    for (int i = 0; i < 25; ++i) asm volatile("" : "=v"(wr[i / 5][i % 5]));
#pragma unroll
    for (int i = 0; i < 12; ++i) asm volatile("" : "=v"(hr[i / 4][i % 4]));
    auto ld1 = [&](v4u& dst, unsigned vo, unsigned so, int imm) {
      if (imm == 0) asm volatile("buffer_load_dwordx4 %0, %1, %2, %3 offen sc1" : "+v"(dst) : "v"(vo), "s"(sw), "s"(so) : "memory");
      if (imm == 1) asm volatile("buffer_load_dwordx4 %0, %1, %2, %3 offen offset:1024 sc1" : "+v"(dst) : "v"(vo), "s"(sw), "s"(so) : "memory");
      if (imm == 2) asm volatile("buffer_load_dwordx4 %0, %1, %2, %3 offen offset:2048 sc1" : "+v"(dst) : "v"(vo), "s"(sw), "s"(so) : "memory");
      if (imm == 3) asm volatile("buffer_load_dwordx4 %0, %1, %2, %3 offen offset:3072 sc1" : "+v"(dst) : "v"(vo), "s"(sw), "s"(so) : "memory");
    };
    auto ldw = [&](int s, int dd) { ld1(wr[s % 5][dd], dd < 4 ? vo0 : vo1, wso + kst(s) * 40960, dd & 3); };
    unsigned ht[4];
#pragma unroll
    for (int t = 0; t < 4; ++t) ht[t] = vo0 + ((wid + 1 + t) & 3) * 49152;
    auto ldh = [&](int s, int t) { ld1(hr[s % 3][t], ht[t], hso + kst(s) * 1024, 0); };
#pragma unroll
    for (int i = 0; i < NEARLY; ++i) ldw(i / 5, i % 5);
    wait_vm<NEARLY>();
    STAMP(5); RSTAMP(33);
    if (lane == 0) put_flag(rw, WS_F1 + ((normal ? ti * 24 + ch : 240 + 2 * mm_ + par) * 4 + wid) * 8, tag);
#pragma unroll
    for (int i = NEARLY; i < 25; ++i) ldw(i / 5, i % 5);
    if (STGB < 4) {
      lds_wait4(L_MISC + 112);
      if (wid < 4 - STGB) {
#pragma unroll
        for (int s = 0; s < NSTG; ++s) dma5(sw, pbase(wid) + s * 5120, vo0, wso + kst(12 - NSTG + s) * 40960);
      }
    }
    lds_set(L_MISC + 80 + wid * 4);
    __builtin_amdgcn_s_waitcnt(0x0F70);
    cfence();
#pragma unroll 1
    for (int spin = 0; spin < 64 * SPIN_MAX && *(volatile LAS unsigned*)lds(L_MISC + 32) == 0; ++spin) {}
    cfence();
    STAMP(6);
#pragma unroll
    for (int i = 0; i < 12; ++i) {
      ldh(i / 4, i % 4);
      if (HGAP && (i == 3 || i == 7)) __builtin_amdgcn_s_sleep(HGAP);
    }
    f32x4 acc[4][5];
    const int c = lane & 15, g = lane >> 4;
    const unsigned oo = ((4 * t64 + wid) * 16 + c) * 1280 + (80 * dj + 4 * g) * 2;
    v2u xr[3];
    auto mm1 = [&](int s, int t, int dd) {
      if (s == 0) asm volatile("v_mfma_f32_16x16x32_bf16 %0, %1, %2, 0" : "=&v"(acc[t][dd]) : "v"(wr[0][dd]), "v"(hr[0][t]));
      else asm volatile("v_mfma_f32_16x16x32_bf16 %0, %1, %2, %0" : "+v"(acc[t][dd]) : "v"(wr[s % 5][dd]), "v"(hr[s % 3][t]));
    };
#pragma unroll
    for (int s = 0; s < 10; ++s) {
      if (s == 12 - NSTG) wait_lg();
      asm volatile("s_waitcnt vmcnt(%0)" ::"i"(g2_wait(s)) : "memory");
#pragma unroll
      for (int t = 0; t < 4; ++t) {
#pragma unroll
        for (int dd = 0; dd < 5; ++dd) {
          mm1(s, t, dd);
          if (t == 3 && s >= 7 - NSTG && s < 7)
            asm volatile("ds_read_b128 %0, %1 offset:%2" : "+v"(wr[s % 5][dd]) : "v"(pw), "i"((s - 7 + NSTG) * 5120 + dd * 1024) : "memory");
        }
        if (s + 3 < 12) ldh(s + 3, t);
        if (g2_piece(s, t) >= 0) ldw(5 + g2_piece(s, t) / 5, g2_piece(s, t) % 5);
      }
      if (s == G2SIG && wid == 0) lds_set(L_MISC + 36);
    }
#pragma unroll
    for (int dd = 0; dd < 3; ++dd) asm volatile("buffer_load_dwordx2 %0, %1, %2, 0 offen offset:%3" : "=&v"(xr[dd]) : "v"(oo), "s"(sx), "i"(dd * 32) : "memory");
#pragma unroll
    for (int t = 0; t < 4; ++t) {
      asm volatile("s_waitcnt vmcnt(%0)" ::"i"(6 - t) : "memory");
#pragma unroll
      for (int i = 0; i < 10; ++i) {
        mm1(10 + i / 5, t, i % 5);
        if (t > 0 && i >= 3 && i < 8)
          asm volatile("ds_write_b128 %0, %1 offset:%2" ::"v"(pw), "v"(acc[t - 1][i - 3]), "i"((t - 1) * 5120 + (i - 3) * 1024) : "memory");
      }
    }
    asm volatile("s_nop 7" : "+v"(acc[3][0]), "+v"(acc[3][1]), "+v"(acc[3][2]), "+v"(acc[3][3]), "+v"(acc[3][4]));
    *(LAS f32x4*)lds(pw + 15360) = acc[3][3];
    *(LAS f32x4*)lds(pw + 16384) = acc[3][4];
    STAMP(7);
    asm volatile("s_waitcnt vmcnt(0) lgkmcnt(0)" : "+v"(xr[0]), "+v"(xr[1]), "+v"(xr[2])::"memory");
    BAR(11);
    STAMP(8);
    const f32x4 own[3] = {acc[3][0], acc[3][1], acc[3][2]};
    finish<0, 3>(mkr(out, L * D * 2), wid, lane, oo, own, xr);
    STAMP(9);
    DUMP();
    return;
  }
  const bool ln2 = wid == 6 && (j == 20 || j == 21);
  const int tb = ln2 ? 240 + 2 * xcd + j - 20 : 120 * (xcd & 1) + 30 * (xcd >> 1) + j;
  LnOut lo;
  v2u tag7 = {0, 0};
  if (wid == 7) tag7 = epoch_tag(rw);
  if (ln2 || (wid == 7 && normal)) ln8(rx, tb, lane, ln2 ? L_KG + 2 * KG_SZ : L_LN, lo);
  if (wid < 7) {
    const int cw = wid - 4;
    v4u sink = {0, 0, 0, 0}, kraw[2] = {};
    auto gather_k = [&]() {
      if (cw < nch) {
#pragma unroll
        for (int rt = 0; rt < 2; ++rt) *(LAS v4u*)lds(L_RAW + cw * 2048 + rt * 1024 + lane * 16) = kraw[rt];
        conv_pre(L_RAW + cw * 2048, lane, pre);
      }
    };
    auto warm_wd = [&]() {
#pragma unroll
      for (int i = 0; i < 3; ++i) {
        const unsigned so = WS_WDT + ((6 * (j >> 2) + 3 * cw + i) * 40 + 5 * dj) * 1024;
        touch4(sink, sw, lane * 16, so); touch(sink, sw, lane * 16, so + 4096);
      }
      wait_vm<15>();
    };
    if (normal) {
      if (cw < nch) {
        f32x4 ka[2][2];
        kgen<2>(pw, dt, lane, L_KG + cw * KG_SZ, L_MISC + 68 + cw * 4, ka, [&]() {
          lds_wait(L_MISC + 16 + cw * 4);
#pragma unroll
          for (int i = 0; i < 4; ++i) ka[i >> 1][i & 1] = *(LAS f32x4*)lds(L_KG + cw * KG_SZ + 16 * 544 + i * 1024 + lane * 16);
        });
        STAMP(12);
#pragma unroll
        for (int rt = 0; rt < 2; ++rt)
          kraw[rt] = v4u{pk(ka[rt][0][0], ka[rt][0][1]), pk(ka[rt][0][2], ka[rt][0][3]), pk(ka[rt][1][0], ka[rt][1][1]), pk(ka[rt][1][2], ka[rt][1][3])};
        STAMP(2);
        gather_k();
      }
      wait_lg();
      STAMP(1);
      if (cw < 2) {
        g1_loader<20, 2>(rot, STAGE, [&](int s, unsigned so) { g1_issue<6>(sx, sg, su, gl, cw, s, so, 192 * ti, ch); });
        warm_wd();
      } else {
        BAR(0);
        colsum(rw, WS_CSP + (ch * 10 + ti) * 512, 0, lane);
        if (CSW6) colsum(rw, WS_CSP + (ch * 10 + ti) * 512, 1, lane);
        if (LNB == 0 && ln2) ln8_store(rw, tb, lane, lo);
        BAR(1);
        if (LNB == 1 && ln2) ln8_store(rw, tb, lane, lo);
        if (LNB == 1) BAR(2);
        wait_vm<0>();
        if (LNB == 0) BAR(2);
#pragma unroll 1
        for (int k = 3; k < 8; ++k) BAR(k);
        CsIn ci;
        cs_load(rw, ch, lane, ci);
        BAR(8);
        cs_put(ci, L_CS, lane);
        wait_lg();
      }
      BAR(9);
      if (cw < nch) conv_image(pre, L_H + cw * H_SZ, L_K3 + cw * 320, lane);
    } else if (cw < 2) {
      g1_issue_rem(sx, sg, su, gl, cw, 5 * (xcd & 1), 0, ch, ch2);
      wait_vm<0>();
      g1_loader<24, 1>(5 * (xcd & 1), 2 * STAGE, [&](int s, unsigned so) { g1_issue_rem(sx, sg, su, gl, cw, s, so, ch, ch2); });
      warm_wd(); BAR(9);
    } else {
#pragma unroll 1
      for (int s = 0; s < 8; ++s) bar();
      CsIn ci[2];
      cs_load(rw, ch, lane, ci[0]); cs_load(rw, ch2, lane, ci[1]);
      bar();
      cs_put(ci[0], L_CS, lane); cs_put(ci[1], L_CS + 512, lane);
      wait_lg();
      bar();
    }
    asm volatile("s_waitcnt vmcnt(0)" : "+v"(sink)::"memory");
    STAMP(4);
    if (cw < nch) {
      lds_wait(L_MISC + 64);
      conv(rw, cw, d0 + cw, lane, pre, L_MISC + 48 + cw * 4);
      STAMP(6);
      lds_set(L_MISC + 100 + cw * 4);
      wait_vm<0>();
      if (lane == 0) put_flag(rw, WS_F2 + (d0 + cw) * 8, tag_of());
    }
    STAMP(7);
    finish_hi();
  } else {
    const v2u tag = tag7;
    if (lane == 0) *(LAS v4u*)lds(L_MISC) = v4u{tag.x, tag.y, 0, 0};
    wait_lg();
    STAMP(1);
    BAR(0);
    if (normal) {
      if (!CSW6) colsum(rw, WS_CSP + (ch * 10 + ti) * 512, 1, lane);
      if (LNB == 0) ln8_store(rw, tb, lane, lo);
    }
    BAR(1);
    if (LNB == 1 && normal) ln8_store(rw, tb, lane, lo);
    BAR(2);
    if (LNB == 1) BAR(3);
    wait_vm<0>();
    if (lane == 0) put_flag(rw, WS_F0 + b * 8, tag);
    if (LNB == 0) BAR(3);
    BAR(4);
    BAR(5);
    Poll4 p0;
    poll4_issue(rw, WS_F0, 256, lane, p0);
    BAR(6);
    poll4_finish(rw, WS_F0, 256, tag, lane, p0);
    STAMP(2);
    if (b == 0 && lane == 0) put_flag(rw, WS_EPOCH, tag);
    const int row0 = normal ? 192 * ti : 1920;
    v2u st[3];
#pragma unroll
    for (int k = 0; k < 3; ++k) { const int r = row0 + lane + 64 * k; st[k] = __builtin_amdgcn_raw_buffer_load_b64(rw, (r >> 3) * 128 + (r & 7) * 8, WS_STATS, SC1); }
    cfence();
    BAR(7); BAR(8);
#pragma unroll
    for (int k = 0; k < 3; ++k) {
      const float m = __uint_as_float(st[k].x), den = __uint_as_float(st[k].y);
      *(LAS f32x4*)lds(L_ST + (lane + 64 * k) * 16) = f32x4{-m, vmul(den, den), vmul(-1.44269504f, __builtin_amdgcn_rcpf(den)), den};
    }
    v4u ur[16];
    auto ldu = [&](int p) { ur[p] = __builtin_amdgcn_raw_buffer_load_b128(rw, (lane >> 2) * 10240 + (lane & 3) * 16, WS_U8 + p * 163840 + d0 * 16, SC1); };
    if ((lane & 3) < nch) {
#pragma unroll
      for (int p = 0; p < NU1; ++p) ldu(p);
    }
    wait_lg();
    BAR(9);
    if ((lane & 3) < nch) {
#pragma unroll
      for (int p = NU1; p < 16; ++p) ldu(p);
    }
    if (lane < 32) {
#pragma unroll
      for (int k = 0; k < 3; ++k) *(LAS v4u*)lds(L_U + k * U_SZ + lane * 16) = v4u{0, 0, 0, 0};
    }
    if ((lane & 3) < nch) {
#pragma unroll
      for (int p = 0; p < NU1; ++p) *(LAS v4u*)lds(L_U + (lane & 3) * U_SZ + 512 + p * 256 + (lane >> 2) * 16) = ur[p];
    }
    if ((lane & 3) < nch) {
#pragma unroll
      for (int p = NU1; p < 16; ++p) *(LAS v4u*)lds(L_U + (lane & 3) * U_SZ + 512 + p * 256 + (lane >> 2) * 16) = ur[p];
    }
    wait_lg();
    lds_set(L_MISC + 64);
    STAMP(4);
    lds_wait4(L_MISC + 112);
#pragma unroll 1
    for (int w = 3; w >= 4 - STGB; --w) {
#pragma unroll
      for (int s = 0; s < NSTG; ++s) {
        dma5(sw, pbase(w) + s * 5120, lane * 16, WS_WDT + ((12 * w + kst(12 - NSTG + s)) * 40 + 5 * dj) * 1024);
        if (PACE) __builtin_amdgcn_s_sleep(PACE);
      }
    }
    STAMP(3);
    wait_vm<0>();
    lds_wait4(L_MISC + 80);
    STAMP(8);
    poll<2>(rw, WS_F1 + (t64 < 30 ? (t64 / 3) * 768 : 7680), t64 < 30 ? 96 : 64, tag, lane);
    lds_set(L_MISC + 32);
    STAMP(5); RSTAMP(34);
    lds_wait(L_MISC + 36);
    poll<2>(rw, WS_F2 + dj * 640, 80, tag, lane);
#pragma unroll 1
    for (int k = 0; k < nch; ++k) lds_wait(L_MISC + 100 + k * 4);
    STAMP(6);
    const unsigned va = (lane >> 3) * 4096 + (lane & 7) * 16, sa = WS_AT + 80 * dj * 4096 + t64 * 128;
    dma4<SC1>(sw, L_AT, va, va + 31744, va + 63488, va + 95232, sa);
    dma4<SC1>(sw, L_AT + 4096, va, va + 31744, va + 63488, va + 95232, sa + 131072);
    dma<SC1>(sw, L_AT + 8192, va, sa + 262144); dma<SC1>(sw, L_AT + 9216, va, sa + 294912);
    STAMP(7);
    finish_hi();
  }
  DUMP();
#endif
}

extern "C" void kernel_launch(void* const* d_in, const int* in_sizes, int n_in, void* d_out, int out_size, void* d_ws, size_t ws_size,
                              hipStream_t stream) {
  (void)hipFuncSetAttribute((const void*)fused_s4_ffn, hipFuncAttributeMaxDynamicSharedMemorySize, LDS_BYTES);
  fused_s4_ffn<<<256, 512, LDS_BYTES, stream>>>(
      (const uint16_t*)d_in[0], (const uint16_t*)d_in[1], (const uint16_t*)d_in[2], (const uint16_t*)d_in[3], (const uint16_t*)d_in[4],
      (const uint16_t*)d_in[5], (const uint16_t*)d_in[6], (const uint16_t*)d_in[7], (const uint16_t*)d_in[8], (const uint16_t*)d_in[10],
      (const uint16_t*)d_in[11], (const uint16_t*)d_in[12], (uint16_t*)d_out, d_ws);
}
```

```cpp
#include <hip/hip_runtime.h>
#include <stdint.h>

typedef short bf16x8 __attribute__((ext_vector_type(8)));
typedef short s16x4 __attribute__((ext_vector_type(4)));
typedef float f32x4 __attribute__((ext_vector_type(4)));
typedef float f32x2 __attribute__((ext_vector_type(2)));
typedef __bf16 bf16x2 __attribute__((ext_vector_type(2)));
typedef unsigned v4u __attribute__((ext_vector_type(4)));
typedef unsigned v2u __attribute__((ext_vector_type(2)));
typedef unsigned v8u __attribute__((ext_vector_type(8)));
typedef unsigned v16u __attribute__((ext_vector_type(16)));
typedef float f32x8 __attribute__((ext_vector_type(8)));
typedef float f32x16 __attribute__((ext_vector_type(16)));
typedef unsigned long long u64;
#define LAS __attribute__((address_space(3)))
#define DI __device__ __forceinline__
typedef __amdgpu_buffer_rsrc_t rsrc_t;

constexpr int L = 2048, D = 640, NS = 192, F = 1536;
constexpr int SC1 = 16, NT_ = 2;

constexpr unsigned WS_EPOCH = 0, WS_F0 = 1024, WS_F2 = 8192, WS_STATS = 16384, WS_F1 = 49152, WS_CSP = 65536, WS_U8 = 262144,
                   WS_WDT = 3u << 20, WS_H = 5u << 20, WS_AT = 12u << 20, WS_BYTES = 15u << 20;

constexpr unsigned STAGE = 40960, XB = 24576;
constexpr unsigned LX = 3 * STAGE;
constexpr unsigned L_LN = LX, LN_RS = 1296, L_AT = LX;
constexpr unsigned L_RAW = LX + 10240;
constexpr unsigned L_ST = L_RAW + 3 * 2048;
constexpr unsigned L_CS = L_ST + 3072;
constexpr unsigned L_MISC = L_CS + 1024;
constexpr unsigned L_U = L_MISC + 128, U_SZ = 4608;
constexpr unsigned L_K3 = L_U + 3 * U_SZ;
constexpr unsigned L_END = L_K3 + 960;
constexpr unsigned L_WDU = L_U, L_DUMMY = L_U + 8192;
constexpr unsigned L_KG = STAGE, KG_SZ = 16 * (544 + 288);
constexpr unsigned L_H = 2 * STAGE, H_CS = 2112, H_SZ = 8 * H_CS;
constexpr unsigned LDS_BYTES = 163840;
static_assert(L_END <= LDS_BYTES && L_H + 3 * H_SZ + 16 <= L_RAW, "lds");
constexpr unsigned M_K1 = L_MISC + 16;
constexpr unsigned M_H = L_MISC + 32;
constexpr unsigned M_G2 = L_MISC + 36;
constexpr unsigned M_D0 = L_MISC + 40;
constexpr unsigned M_EPI = L_MISC + 48;
constexpr unsigned M_U = L_MISC + 64;
constexpr unsigned M_KPUT = L_MISC + 68;
constexpr unsigned M_PREF = L_MISC + 80;
constexpr unsigned M_CONV = L_MISC + 100;
constexpr unsigned M_MAIN = L_MISC + 112;
constexpr int SPIN_MAX = 20000;
constexpr unsigned CODE_BYTES = 55808;

constexpr int g2_piece(int s, int t) { const int p = 4 * (s - 1) + t; return s >= 1 && p < 15 ? p : -1; }
constexpr int g2_wait(int step) {
  int n = 0, need = 0;
  for (int i = 0; i < 12; ++i) { ++n; if (i / 4 == step) need = n; }
  for (int s = 0; s < step; ++s)
    for (int t = 0; t < 4; ++t) {
      if (s + 3 < 12) { ++n; if (s + 3 == step) need = n; }
      const int p = g2_piece(s, t);
      if (p >= 0) { ++n; if (5 + p / 5 == step) need = n; }
    }
  return n - need;
}

#if __HIP_DEVICE_COMPILE__
extern __shared__ __attribute__((aligned(16))) char smem_[];
DI LAS char* lds(unsigned o) { return (LAS char*)smem_ + o; }
DI rsrc_t mkr(const void* p, unsigned bytes) { return __builtin_amdgcn_make_buffer_rsrc((void*)p, 0, bytes, 0x00020000); }
DI unsigned pk(float a, float b) { f32x2 f = {a, b}; return __builtin_bit_cast(unsigned, __builtin_convertvector(f, bf16x2)); }
DI unsigned pk2(f32x2 f) { return __builtin_bit_cast(unsigned, __builtin_convertvector(f, bf16x2)); }
DI float blo(unsigned v) { return __uint_as_float(v << 16); }
DI float bhi(unsigned v) { return __uint_as_float(v & 0xffff0000u); }
DI float rbf(float f) { return blo(pk(f, f)); }
DI void cfence() { asm volatile("" ::: "memory"); }
DI void bar() { cfence(); __builtin_amdgcn_s_barrier(); cfence(); }
template <int N> DI void wait_vm() { asm volatile("s_waitcnt vmcnt(%0)" ::"i"(N) : "memory"); }
DI void wait_lg() { asm volatile("s_waitcnt lgkmcnt(0)" ::: "memory"); }
DI bf16x8 trd(unsigned a0, unsigned a1) {
  s16x4 lo = __builtin_amdgcn_ds_read_tr16_b64_v4i16((LAS s16x4*)lds(a0));
  s16x4 hi = __builtin_amdgcn_ds_read_tr16_b64_v4i16((LAS s16x4*)lds(a1));
  return __builtin_shufflevector(lo, hi, 0, 1, 2, 3, 4, 5, 6, 7);
}
DI bf16x8 rd128(unsigned a) { return *(LAS bf16x8*)lds(a); }
DI f32x4 mfma(bf16x8 a, bf16x8 b, f32x4 c) { return __builtin_amdgcn_mfma_f32_16x16x32_bf16(a, b, c, 0, 0, 0); }
DI v4u srd(const void* p, unsigned bytes) { const u64 a = (u64)p; return v4u{(unsigned)a, (unsigned)(a >> 32) & 0xffffu, bytes, 0x00020000u}; }
template <int AUX> DI void dma(v4u r, unsigned ldsoff, unsigned voff, unsigned soff) {
  if (AUX == 0) asm volatile("s_mov_b32 m0, %0\n\ts_nop 0\n\tbuffer_load_dwordx4 %1, %2, %3 offen lds" ::"s"(ldsoff), "v"(voff), "s"(r), "s"(soff) : "memory");
  if (AUX == SC1) asm volatile("s_mov_b32 m0, %0\n\ts_nop 0\n\tbuffer_load_dwordx4 %1, %2, %3 offen sc1 lds" ::"s"(ldsoff), "v"(voff), "s"(r), "s"(soff) : "memory");
}
#define DMA4_(POL) asm volatile("s_mov_b32 m0, %0\n\ts_nop 0\n\tbuffer_load_dwordx4 %1, %5, %6 offen " POL "lds\n\t" \
    "buffer_load_dwordx4 %2, %5, %6 offen offset:1024 " POL "lds\n\tbuffer_load_dwordx4 %3, %5, %6 offen offset:2048 " POL "lds\n\t" \
    "buffer_load_dwordx4 %4, %5, %6 offen offset:3072 " POL "lds" ::"s"(ldsoff), "v"(v0), "v"(v1), "v"(v2), "v"(v3), "s"(r), "s"(soff) : "memory")
template <int AUX> DI void dma4(v4u r, unsigned ldsoff, unsigned v0, unsigned v1, unsigned v2, unsigned v3, unsigned soff) {
  if (AUX == 0) DMA4_("");
  if (AUX == NT_) DMA4_("nt ");
  if (AUX == SC1) DMA4_("sc1 ");
}
template <int AUX> DI void st128(v4u v, rsrc_t r, unsigned off) { __builtin_amdgcn_raw_buffer_store_b128(v, r, off, 0, AUX); }
DI void touch(v4u& sink, v4u r, unsigned vo, unsigned so) {
  asm volatile("buffer_load_dwordx4 %0, %1, %2, %3 offen sc1" : "+v"(sink) : "v"(vo), "s"(r), "s"(so) : "memory");
}
DI void touch4(v4u& sink, v4u r, unsigned vo, unsigned so) {
  asm volatile("buffer_load_dwordx4 %0, %1, %2, %3 offen sc1\n\tbuffer_load_dwordx4 %0, %1, %2, %3 offen offset:1024 sc1\n\t"
               "buffer_load_dwordx4 %0, %1, %2, %3 offen offset:2048 sc1\n\tbuffer_load_dwordx4 %0, %1, %2, %3 offen offset:3072 sc1"
               : "+v"(sink) : "v"(vo), "s"(r), "s"(so) : "memory");
}
DI void dma5(v4u r, unsigned ldsoff, unsigned vo, unsigned so) { dma4<SC1>(r, ldsoff, vo, vo, vo, vo, so); dma<SC1>(r, ldsoff + 4096, vo, so + 4096); }
DI unsigned vmov(unsigned x) { unsigned r; asm("v_mov_b32 %0, %1" : "=v"(r) : "v"(x)); return r; }
DI float vmul(float a, float b) { float r; asm("v_mul_f32 %0, %1, %2" : "=v"(r) : "v"(a), "v"(b)); return r; }
DI float rowsum16(float v) {
  v += __builtin_bit_cast(float, __builtin_amdgcn_update_dpp(0, __builtin_bit_cast(int, v), 0x128, 0xf, 0xf, false));
  v += __builtin_bit_cast(float, __builtin_amdgcn_update_dpp(0, __builtin_bit_cast(int, v), 0x124, 0xf, 0xf, false));
  v += __builtin_bit_cast(float, __builtin_amdgcn_update_dpp(0, __builtin_bit_cast(int, v), 0x122, 0xf, 0xf, false));
  v += __builtin_bit_cast(float, __builtin_amdgcn_update_dpp(0, __builtin_bit_cast(int, v), 0x121, 0xf, 0xf, false));
  return v;
}
DI f32x2 cmul(f32x2 a, f32x2 b) {
  f32x2 r;
  asm("v_pk_mul_f32 %0, %1, %2 op_sel_hi:[1,0]\n\tv_pk_fma_f32 %0, %1, %2, %0 op_sel:[1,1,0] op_sel_hi:[0,1,1] neg_lo:[0,1,0]" : "=&v"(r) : "v"(a), "v"(b));
  return r;
}
DI f32x2 cpow(float lr, float tr, float e) {
  float m = __builtin_amdgcn_exp2f(lr * e), th = __builtin_amdgcn_fractf(tr * e);
  return f32x2{m * __builtin_amdgcn_cosf(th), m * __builtin_amdgcn_sinf(th)};
}
DI v2u tag_of(void) { return *(LAS v2u*)lds(L_MISC); }
DI v2u epoch_tag(rsrc_t rw) {
  const v2u e = __builtin_amdgcn_raw_buffer_load_b64(rw, WS_EPOCH, 0, SC1);
  const u64 t = (((u64)e.y << 32) | e.x) + 0x9E3779B97F4A7C15ull;
  return v2u{(unsigned)t, (unsigned)(t >> 32)};
}
DI void put_flag(rsrc_t rw, unsigned off, v2u tag) { __builtin_amdgcn_raw_buffer_store_b64(tag, rw, off, 0, SC1); }
template <int R> DI void poll(rsrc_t rw, unsigned off, int n, v2u tag, int lane) {
#pragma unroll 1
  for (int spin = 0; spin < SPIN_MAX; ++spin) {
    bool ok = true;
#pragma unroll
    for (int r = 0; r < R; ++r) {
      int i = lane + 64 * r; i = i < n ? i : n - 1;
      v2u f = __builtin_amdgcn_raw_buffer_load_b64(rw, off + i * 8, 0, SC1);
      ok = ok && f.x == tag.x && f.y == tag.y;
    }
    if (__all(ok)) break;
    cfence();
  }
  cfence();
}
struct Poll4 { v2u f[4]; };
DI void poll4_issue(rsrc_t rw, unsigned off, int n, int lane, Poll4& p) {
#pragma unroll
  for (int r = 0; r < 4; ++r) { int i = lane + 64 * r; i = i < n ? i : n - 1; p.f[r] = __builtin_amdgcn_raw_buffer_load_b64(rw, off + i * 8, 0, SC1); }
  cfence();
}
DI void poll4_finish(rsrc_t rw, unsigned off, int n, v2u tag, int lane, const Poll4& p) {
  bool ok = true;
#pragma unroll
  for (int r = 0; r < 4; ++r) ok = ok && p.f[r].x == tag.x && p.f[r].y == tag.y;
  if (!__all(ok)) poll<4>(rw, off, n, tag, lane);
  cfence();
}
DI void lds_set(unsigned o) { cfence(); *(volatile LAS unsigned*)lds(o) = 1; }
DI void lds_wait(unsigned o) {
#pragma unroll 1
  for (int spin = 0; spin < 16 * SPIN_MAX && *(volatile LAS unsigned*)lds(o) == 0; ++spin) __builtin_amdgcn_s_sleep(1);
  cfence();
}
DI void lds_wait4(unsigned o) {
#pragma unroll 1
  for (int spin = 0; spin < 16 * SPIN_MAX; ++spin) {
    const v4u f = *(volatile LAS v4u*)lds(o);
    if (f[0] & f[1] & f[2] & f[3]) break;
    __builtin_amdgcn_s_sleep(1);
  }
  cfence();
}

#define G1_MAIN_ASM \
  "v_mov_b32 v240, %[a0]\n\t" \
  "v_mov_b32 v241, %[a1]\n\t" \
  "v_mov_b32 v242, %[a2]\n\t" \
  "v_mov_b32 v243, %[a3]\n\t" \
  "v_add_u32 v244, 81920, %[a0]\n\t" \
  "v_add_u32 v245, 81920, %[a1]\n\t" \
  "v_add_u32 v246, 81920, %[a2]\n\t" \
  "v_add_u32 v247, 81920, %[a3]\n\t" \
  "v_add_u32 v248, 40960, %[a0]\n\t" \
  "v_add_u32 v249, 40960, %[a1]\n\t" \
  "v_add_u32 v250, 40960, %[a2]\n\t" \
  "v_add_u32 v251, 40960, %[a3]\n\t" \
  "s_mov_b32 %[cnt], 3\n\t" \
  "s_barrier\n\t" \
  "ds_read_b64_tr_b16 v[160:161], v240 offset:0\n\t" \
  "ds_read_b64_tr_b16 v[162:163], v240 offset:512\n\t" \
  "ds_read_b64_tr_b16 v[164:165], v241 offset:0\n\t" \
  "ds_read_b64_tr_b16 v[166:167], v241 offset:512\n\t" \
  "ds_read_b64_tr_b16 v[168:169], v240 offset:8192\n\t" \
  "ds_read_b64_tr_b16 v[170:171], v240 offset:8704\n\t" \
  "ds_read_b64_tr_b16 v[172:173], v241 offset:8192\n\t" \
  "ds_read_b64_tr_b16 v[174:175], v241 offset:8704\n\t" \
  "ds_read_b128 v[176:179], v242 offset:0\n\t" \
  "ds_read_b128 v[180:183], v242 offset:2048\n\t" \
  "ds_read_b128 v[184:187], v242 offset:4096\n\t" \
  "ds_read_b128 v[188:191], v242 offset:6144\n\t" \
  "ds_read_b128 v[192:195], v242 offset:8192\n\t" \
  "ds_read_b128 v[196:199], v242 offset:10240\n\t" \
  "s_waitcnt lgkmcnt(0)\n\t" \
  ".Lg1_%=:\n\t" \
  "v_mfma_f32_16x16x32_bf16 v[64:67], v[160:163], v[176:179], v[64:67]\n\t" \
  "ds_read_b64_tr_b16 v[200:201], v240 offset:4096\n\t" \
  "v_mfma_f32_16x16x32_bf16 v[68:71], v[164:167], v[176:179], v[68:71]\n\t" \
  "ds_read_b64_tr_b16 v[202:203], v240 offset:4608\n\t" \
  "v_mfma_f32_16x16x32_bf16 v[72:75], v[168:171], v[176:179], v[72:75]\n\t" \
  "ds_read_b64_tr_b16 v[204:205], v241 offset:4096\n\t" \
  "v_mfma_f32_16x16x32_bf16 v[76:79], v[172:175], v[176:179], v[76:79]\n\t" \
  "ds_read_b64_tr_b16 v[206:207], v241 offset:4608\n\t" \
  "v_mfma_f32_16x16x32_bf16 v[80:83], v[160:163], v[180:183], v[80:83]\n\t" \
  "ds_read_b64_tr_b16 v[208:209], v240 offset:12288\n\t" \
  "v_mfma_f32_16x16x32_bf16 v[84:87], v[164:167], v[180:183], v[84:87]\n\t" \
  "ds_read_b64_tr_b16 v[210:211], v240 offset:12800\n\t" \
  "v_mfma_f32_16x16x32_bf16 v[88:91], v[168:171], v[180:183], v[88:91]\n\t" \
  "ds_read_b64_tr_b16 v[212:213], v241 offset:12288\n\t" \
  "v_mfma_f32_16x16x32_bf16 v[92:95], v[172:175], v[180:183], v[92:95]\n\t" \
  "ds_read_b64_tr_b16 v[214:215], v241 offset:12800\n\t" \
  "v_mfma_f32_16x16x32_bf16 v[96:99], v[160:163], v[184:187], v[96:99]\n\t" \
  "ds_read_b128 v[216:219], v243 offset:0\n\t" \
  "v_mfma_f32_16x16x32_bf16 v[100:103], v[164:167], v[184:187], v[100:103]\n\t" \
  "ds_read_b128 v[220:223], v243 offset:2048\n\t" \
  "v_mfma_f32_16x16x32_bf16 v[104:107], v[168:171], v[184:187], v[104:107]\n\t" \
  "ds_read_b128 v[224:227], v243 offset:4096\n\t" \
  "v_mfma_f32_16x16x32_bf16 v[108:111], v[172:175], v[184:187], v[108:111]\n\t" \
  "ds_read_b128 v[228:231], v243 offset:6144\n\t" \
  "v_mfma_f32_16x16x32_bf16 v[112:115], v[160:163], v[188:191], v[112:115]\n\t" \
  "ds_read_b128 v[232:235], v243 offset:8192\n\t" \
  "v_mfma_f32_16x16x32_bf16 v[116:119], v[164:167], v[188:191], v[116:119]\n\t" \
  "ds_read_b128 v[236:239], v243 offset:10240\n\t" \
  "v_mfma_f32_16x16x32_bf16 v[120:123], v[168:171], v[188:191], v[120:123]\n\t" \
  "v_mfma_f32_16x16x32_bf16 v[124:127], v[172:175], v[188:191], v[124:127]\n\t" \
  "v_mfma_f32_16x16x32_bf16 v[128:131], v[160:163], v[192:195], v[128:131]\n\t" \
  "v_mfma_f32_16x16x32_bf16 v[132:135], v[164:167], v[192:195], v[132:135]\n\t" \
  "v_mfma_f32_16x16x32_bf16 v[136:139], v[168:171], v[192:195], v[136:139]\n\t" \
  "v_mfma_f32_16x16x32_bf16 v[140:143], v[172:175], v[192:195], v[140:143]\n\t" \
  "v_mfma_f32_16x16x32_bf16 v[144:147], v[160:163], v[196:199], v[144:147]\n\t" \
  "v_mfma_f32_16x16x32_bf16 v[148:151], v[164:167], v[196:199], v[148:151]\n\t" \
  "v_mfma_f32_16x16x32_bf16 v[152:155], v[168:171], v[196:199], v[152:155]\n\t" \
  "v_mfma_f32_16x16x32_bf16 v[156:159], v[172:175], v[196:199], v[156:159]\n\t" \
  "s_waitcnt vmcnt(0) lgkmcnt(0)\n\t" \
  "s_barrier\n\t" \
  "v_mfma_f32_16x16x32_bf16 v[64:67], v[200:203], v[216:219], v[64:67]\n\t" \
  "ds_read_b64_tr_b16 v[160:161], v244 offset:0\n\t" \
  "v_mfma_f32_16x16x32_bf16 v[68:71], v[204:207], v[216:219], v[68:71]\n\t" \
  "ds_read_b64_tr_b16 v[162:163], v244 offset:512\n\t" \
  "v_mfma_f32_16x16x32_bf16 v[72:75], v[208:211], v[216:219], v[72:75]\n\t" \
  "ds_read_b64_tr_b16 v[164:165], v245 offset:0\n\t" \
  "v_mfma_f32_16x16x32_bf16 v[76:79], v[212:215], v[216:219], v[76:79]\n\t" \
  "ds_read_b64_tr_b16 v[166:167], v245 offset:512\n\t" \
  "v_mfma_f32_16x16x32_bf16 v[80:83], v[200:203], v[220:223], v[80:83]\n\t" \
  "ds_read_b64_tr_b16 v[168:169], v244 offset:8192\n\t" \
  "v_mfma_f32_16x16x32_bf16 v[84:87], v[204:207], v[220:223], v[84:87]\n\t" \
  "ds_read_b64_tr_b16 v[170:171], v244 offset:8704\n\t" \
  "v_mfma_f32_16x16x32_bf16 v[88:91], v[208:211], v[220:223], v[88:91]\n\t" \
  "ds_read_b64_tr_b16 v[172:173], v245 offset:8192\n\t" \
  "v_mfma_f32_16x16x32_bf16 v[92:95], v[212:215], v[220:223], v[92:95]\n\t" \
  "ds_read_b64_tr_b16 v[174:175], v245 offset:8704\n\t" \
  "v_mfma_f32_16x16x32_bf16 v[96:99], v[200:203], v[224:227], v[96:99]\n\t" \
  "ds_read_b128 v[176:179], v246 offset:0\n\t" \
  "v_mfma_f32_16x16x32_bf16 v[100:103], v[204:207], v[224:227], v[100:103]\n\t" \
  "ds_read_b128 v[180:183], v246 offset:2048\n\t" \
  "v_mfma_f32_16x16x32_bf16 v[104:107], v[208:211], v[224:227], v[104:107]\n\t" \
  "ds_read_b128 v[184:187], v246 offset:4096\n\t" \
  "v_mfma_f32_16x16x32_bf16 v[108:111], v[212:215], v[224:227], v[108:111]\n\t" \
  "ds_read_b128 v[188:191], v246 offset:6144\n\t" \
  "v_mfma_f32_16x16x32_bf16 v[112:115], v[200:203], v[228:231], v[112:115]\n\t" \
  "ds_read_b128 v[192:195], v246 offset:8192\n\t" \
  "v_mfma_f32_16x16x32_bf16 v[116:119], v[204:207], v[228:231], v[116:119]\n\t" \
  "ds_read_b128 v[196:199], v246 offset:10240\n\t" \
  "v_mfma_f32_16x16x32_bf16 v[120:123], v[208:211], v[228:231], v[120:123]\n\t" \
  "v_mfma_f32_16x16x32_bf16 v[124:127], v[212:215], v[228:231], v[124:127]\n\t" \
  "v_mfma_f32_16x16x32_bf16 v[128:131], v[200:203], v[232:235], v[128:131]\n\t" \
  "v_mfma_f32_16x16x32_bf16 v[132:135], v[204:207], v[232:235], v[132:135]\n\t" \
  "v_mfma_f32_16x16x32_bf16 v[136:139], v[208:211], v[232:235], v[136:139]\n\t" \
  "v_mfma_f32_16x16x32_bf16 v[140:143], v[212:215], v[232:235], v[140:143]\n\t" \
  "v_mfma_f32_16x16x32_bf16 v[144:147], v[200:203], v[236:239], v[144:147]\n\t" \
  "v_mfma_f32_16x16x32_bf16 v[148:151], v[204:207], v[236:239], v[148:151]\n\t" \
  "v_mfma_f32_16x16x32_bf16 v[152:155], v[208:211], v[236:239], v[152:155]\n\t" \
  "v_mfma_f32_16x16x32_bf16 v[156:159], v[212:215], v[236:239], v[156:159]\n\t" \
  "s_waitcnt lgkmcnt(0)\n\t" \
  "v_mfma_f32_16x16x32_bf16 v[64:67], v[160:163], v[176:179], v[64:67]\n\t" \
  "ds_read_b64_tr_b16 v[200:201], v244 offset:4096\n\t" \
  "v_mfma_f32_16x16x32_bf16 v[68:71], v[164:167], v[176:179], v[68:71]\n\t" \
  "ds_read_b64_tr_b16 v[202:203], v244 offset:4608\n\t" \
  "v_mfma_f32_16x16x32_bf16 v[72:75], v[168:171], v[176:179], v[72:75]\n\t" \
  "ds_read_b64_tr_b16 v[204:205], v245 offset:4096\n\t" \
  "v_mfma_f32_16x16x32_bf16 v[76:79], v[172:175], v[176:179], v[76:79]\n\t" \
  "ds_read_b64_tr_b16 v[206:207], v245 offset:4608\n\t" \
  "v_mfma_f32_16x16x32_bf16 v[80:83], v[160:163], v[180:183], v[80:83]\n\t" \
  "ds_read_b64_tr_b16 v[208:209], v244 offset:12288\n\t" \
  "v_mfma_f32_16x16x32_bf16 v[84:87], v[164:167], v[180:183], v[84:87]\n\t" \
  "ds_read_b64_tr_b16 v[210:211], v244 offset:12800\n\t" \
  "v_mfma_f32_16x16x32_bf16 v[88:91], v[168:171], v[180:183], v[88:91]\n\t" \
  "ds_read_b64_tr_b16 v[212:213], v245 offset:12288\n\t" \
  "v_mfma_f32_16x16x32_bf16 v[92:95], v[172:175], v[180:183], v[92:95]\n\t" \
  "ds_read_b64_tr_b16 v[214:215], v245 offset:12800\n\t" \
  "v_mfma_f32_16x16x32_bf16 v[96:99], v[160:163], v[184:187], v[96:99]\n\t" \
  "ds_read_b128 v[216:219], v247 offset:0\n\t" \
  "v_mfma_f32_16x16x32_bf16 v[100:103], v[164:167], v[184:187], v[100:103]\n\t" \
  "ds_read_b128 v[220:223], v247 offset:2048\n\t" \
  "v_mfma_f32_16x16x32_bf16 v[104:107], v[168:171], v[184:187], v[104:107]\n\t" \
  "ds_read_b128 v[224:227], v247 offset:4096\n\t" \
  "v_mfma_f32_16x16x32_bf16 v[108:111], v[172:175], v[184:187], v[108:111]\n\t" \
  "ds_read_b128 v[228:231], v247 offset:6144\n\t" \
  "v_mfma_f32_16x16x32_bf16 v[112:115], v[160:163], v[188:191], v[112:115]\n\t" \
  "ds_read_b128 v[232:235], v247 offset:8192\n\t" \
  "v_mfma_f32_16x16x32_bf16 v[116:119], v[164:167], v[188:191], v[116:119]\n\t" \
  "ds_read_b128 v[236:239], v247 offset:10240\n\t" \
  "v_mfma_f32_16x16x32_bf16 v[120:123], v[168:171], v[188:191], v[120:123]\n\t" \
  "v_mfma_f32_16x16x32_bf16 v[124:127], v[172:175], v[188:191], v[124:127]\n\t" \
  "v_mfma_f32_16x16x32_bf16 v[128:131], v[160:163], v[192:195], v[128:131]\n\t" \
  "v_mfma_f32_16x16x32_bf16 v[132:135], v[164:167], v[192:195], v[132:135]\n\t" \
  "v_mfma_f32_16x16x32_bf16 v[136:139], v[168:171], v[192:195], v[136:139]\n\t" \
  "v_mfma_f32_16x16x32_bf16 v[140:143], v[172:175], v[192:195], v[140:143]\n\t" \
  "v_mfma_f32_16x16x32_bf16 v[144:147], v[160:163], v[196:199], v[144:147]\n\t" \
  "v_mfma_f32_16x16x32_bf16 v[148:151], v[164:167], v[196:199], v[148:151]\n\t" \
  "v_mfma_f32_16x16x32_bf16 v[152:155], v[168:171], v[196:199], v[152:155]\n\t" \
  "v_mfma_f32_16x16x32_bf16 v[156:159], v[172:175], v[196:199], v[156:159]\n\t" \
  "s_waitcnt vmcnt(0) lgkmcnt(0)\n\t" \
  "s_barrier\n\t" \
  "v_mfma_f32_16x16x32_bf16 v[64:67], v[200:203], v[216:219], v[64:67]\n\t" \
  "ds_read_b64_tr_b16 v[160:161], v248 offset:0\n\t" \
  "v_mfma_f32_16x16x32_bf16 v[68:71], v[204:207], v[216:219], v[68:71]\n\t" \
  "ds_read_b64_tr_b16 v[162:163], v248 offset:512\n\t" \
  "v_mfma_f32_16x16x32_bf16 v[72:75], v[208:211], v[216:219], v[72:75]\n\t" \
  "ds_read_b64_tr_b16 v[164:165], v249 offset:0\n\t" \
  "v_mfma_f32_16x16x32_bf16 v[76:79], v[212:215], v[216:219], v[76:79]\n\t" \
  "ds_read_b64_tr_b16 v[166:167], v249 offset:512\n\t" \
  "v_mfma_f32_16x16x32_bf16 v[80:83], v[200:203], v[220:223], v[80:83]\n\t" \
  "ds_read_b64_tr_b16 v[168:169], v248 offset:8192\n\t" \
  "v_mfma_f32_16x16x32_bf16 v[84:87], v[204:207], v[220:223], v[84:87]\n\t" \
  "ds_read_b64_tr_b16 v[170:171], v248 offset:8704\n\t" \
  "v_mfma_f32_16x16x32_bf16 v[88:91], v[208:211], v[220:223], v[88:91]\n\t" \
  "ds_read_b64_tr_b16 v[172:173], v249 offset:8192\n\t" \
  "v_mfma_f32_16x16x32_bf16 v[92:95], v[212:215], v[220:223], v[92:95]\n\t" \
  "ds_read_b64_tr_b16 v[174:175], v249 offset:8704\n\t" \
  "v_mfma_f32_16x16x32_bf16 v[96:99], v[200:203], v[224:227], v[96:99]\n\t" \
  "ds_read_b128 v[176:179], v250 offset:0\n\t" \
  "v_mfma_f32_16x16x32_bf16 v[100:103], v[204:207], v[224:227], v[100:103]\n\t" \
  "ds_read_b128 v[180:183], v250 offset:2048\n\t" \
  "v_mfma_f32_16x16x32_bf16 v[104:107], v[208:211], v[224:227], v[104:107]\n\t" \
  "ds_read_b128 v[184:187], v250 offset:4096\n\t" \
  "v_mfma_f32_16x16x32_bf16 v[108:111], v[212:215], v[224:227], v[108:111]\n\t" \
  "ds_read_b128 v[188:191], v250 offset:6144\n\t" \
  "v_mfma_f32_16x16x32_bf16 v[112:115], v[200:203], v[228:231], v[112:115]\n\t" \
  "ds_read_b128 v[192:195], v250 offset:8192\n\t" \
  "v_mfma_f32_16x16x32_bf16 v[116:119], v[204:207], v[228:231], v[116:119]\n\t" \
  "ds_read_b128 v[196:199], v250 offset:10240\n\t" \
  "v_mfma_f32_16x16x32_bf16 v[120:123], v[208:211], v[228:231], v[120:123]\n\t" \
  "v_mfma_f32_16x16x32_bf16 v[124:127], v[212:215], v[228:231], v[124:127]\n\t" \
  "v_mfma_f32_16x16x32_bf16 v[128:131], v[200:203], v[232:235], v[128:131]\n\t" \
  "v_mfma_f32_16x16x32_bf16 v[132:135], v[204:207], v[232:235], v[132:135]\n\t" \
  "v_mfma_f32_16x16x32_bf16 v[136:139], v[208:211], v[232:235], v[136:139]\n\t" \
  "v_mfma_f32_16x16x32_bf16 v[140:143], v[212:215], v[232:235], v[140:143]\n\t" \
  "v_mfma_f32_16x16x32_bf16 v[144:147], v[200:203], v[236:239], v[144:147]\n\t" \
  "v_mfma_f32_16x16x32_bf16 v[148:151], v[204:207], v[236:239], v[148:151]\n\t" \
  "v_mfma_f32_16x16x32_bf16 v[152:155], v[208:211], v[236:239], v[152:155]\n\t" \
  "v_mfma_f32_16x16x32_bf16 v[156:159], v[212:215], v[236:239], v[156:159]\n\t" \
  "s_waitcnt lgkmcnt(0)\n\t" \
  "v_mfma_f32_16x16x32_bf16 v[64:67], v[160:163], v[176:179], v[64:67]\n\t" \
  "ds_read_b64_tr_b16 v[200:201], v248 offset:4096\n\t" \
  "v_mfma_f32_16x16x32_bf16 v[68:71], v[164:167], v[176:179], v[68:71]\n\t" \
  "ds_read_b64_tr_b16 v[202:203], v248 offset:4608\n\t" \
  "v_mfma_f32_16x16x32_bf16 v[72:75], v[168:171], v[176:179], v[72:75]\n\t" \
  "ds_read_b64_tr_b16 v[204:205], v249 offset:4096\n\t" \
  "v_mfma_f32_16x16x32_bf16 v[76:79], v[172:175], v[176:179], v[76:79]\n\t" \
  "ds_read_b64_tr_b16 v[206:207], v249 offset:4608\n\t" \
  "v_mfma_f32_16x16x32_bf16 v[80:83], v[160:163], v[180:183], v[80:83]\n\t" \
  "ds_read_b64_tr_b16 v[208:209], v248 offset:12288\n\t" \
  "v_mfma_f32_16x16x32_bf16 v[84:87], v[164:167], v[180:183], v[84:87]\n\t" \
  "ds_read_b64_tr_b16 v[210:211], v248 offset:12800\n\t" \
  "v_mfma_f32_16x16x32_bf16 v[88:91], v[168:171], v[180:183], v[88:91]\n\t" \
  "ds_read_b64_tr_b16 v[212:213], v249 offset:12288\n\t" \
  "v_mfma_f32_16x16x32_bf16 v[92:95], v[172:175], v[180:183], v[92:95]\n\t" \
  "ds_read_b64_tr_b16 v[214:215], v249 offset:12800\n\t" \
  "v_mfma_f32_16x16x32_bf16 v[96:99], v[160:163], v[184:187], v[96:99]\n\t" \
  "ds_read_b128 v[216:219], v251 offset:0\n\t" \
  "v_mfma_f32_16x16x32_bf16 v[100:103], v[164:167], v[184:187], v[100:103]\n\t" \
  "ds_read_b128 v[220:223], v251 offset:2048\n\t" \
  "v_mfma_f32_16x16x32_bf16 v[104:107], v[168:171], v[184:187], v[104:107]\n\t" \
  "ds_read_b128 v[224:227], v251 offset:4096\n\t" \
  "v_mfma_f32_16x16x32_bf16 v[108:111], v[172:175], v[184:187], v[108:111]\n\t" \
  "ds_read_b128 v[228:231], v251 offset:6144\n\t" \
  "v_mfma_f32_16x16x32_bf16 v[112:115], v[160:163], v[188:191], v[112:115]\n\t" \
  "ds_read_b128 v[232:235], v251 offset:8192\n\t" \
  "v_mfma_f32_16x16x32_bf16 v[116:119], v[164:167], v[188:191], v[116:119]\n\t" \
  "ds_read_b128 v[236:239], v251 offset:10240\n\t" \
  "v_mfma_f32_16x16x32_bf16 v[120:123], v[168:171], v[188:191], v[120:123]\n\t" \
  "v_mfma_f32_16x16x32_bf16 v[124:127], v[172:175], v[188:191], v[124:127]\n\t" \
  "v_mfma_f32_16x16x32_bf16 v[128:131], v[160:163], v[192:195], v[128:131]\n\t" \
  "v_mfma_f32_16x16x32_bf16 v[132:135], v[164:167], v[192:195], v[132:135]\n\t" \
  "v_mfma_f32_16x16x32_bf16 v[136:139], v[168:171], v[192:195], v[136:139]\n\t" \
  "v_mfma_f32_16x16x32_bf16 v[140:143], v[172:175], v[192:195], v[140:143]\n\t" \
  "v_mfma_f32_16x16x32_bf16 v[144:147], v[160:163], v[196:199], v[144:147]\n\t" \
  "v_mfma_f32_16x16x32_bf16 v[148:151], v[164:167], v[196:199], v[148:151]\n\t" \
  "v_mfma_f32_16x16x32_bf16 v[152:155], v[168:171], v[196:199], v[152:155]\n\t" \
  "v_mfma_f32_16x16x32_bf16 v[156:159], v[172:175], v[196:199], v[156:159]\n\t" \
  "s_waitcnt vmcnt(0) lgkmcnt(0)\n\t" \
  "s_barrier\n\t" \
  "v_mfma_f32_16x16x32_bf16 v[64:67], v[200:203], v[216:219], v[64:67]\n\t" \
  "ds_read_b64_tr_b16 v[160:161], v240 offset:0\n\t" \
  "v_mfma_f32_16x16x32_bf16 v[68:71], v[204:207], v[216:219], v[68:71]\n\t" \
  "ds_read_b64_tr_b16 v[162:163], v240 offset:512\n\t" \
  "v_mfma_f32_16x16x32_bf16 v[72:75], v[208:211], v[216:219], v[72:75]\n\t" \
  "ds_read_b64_tr_b16 v[164:165], v241 offset:0\n\t" \
  "v_mfma_f32_16x16x32_bf16 v[76:79], v[212:215], v[216:219], v[76:79]\n\t" \
  "ds_read_b64_tr_b16 v[166:167], v241 offset:512\n\t" \
  "v_mfma_f32_16x16x32_bf16 v[80:83], v[200:203], v[220:223], v[80:83]\n\t" \
  "ds_read_b64_tr_b16 v[168:169], v240 offset:8192\n\t" \
  "v_mfma_f32_16x16x32_bf16 v[84:87], v[204:207], v[220:223], v[84:87]\n\t" \
  "ds_read_b64_tr_b16 v[170:171], v240 offset:8704\n\t" \
  "v_mfma_f32_16x16x32_bf16 v[88:91], v[208:211], v[220:223], v[88:91]\n\t" \
  "ds_read_b64_tr_b16 v[172:173], v241 offset:8192\n\t" \
  "v_mfma_f32_16x16x32_bf16 v[92:95], v[212:215], v[220:223], v[92:95]\n\t" \
  "ds_read_b64_tr_b16 v[174:175], v241 offset:8704\n\t" \
  "v_mfma_f32_16x16x32_bf16 v[96:99], v[200:203], v[224:227], v[96:99]\n\t" \
  "ds_read_b128 v[176:179], v242 offset:0\n\t" \
  "v_mfma_f32_16x16x32_bf16 v[100:103], v[204:207], v[224:227], v[100:103]\n\t" \
  "ds_read_b128 v[180:183], v242 offset:2048\n\t" \
  "v_mfma_f32_16x16x32_bf16 v[104:107], v[208:211], v[224:227], v[104:107]\n\t" \
  "ds_read_b128 v[184:187], v242 offset:4096\n\t" \
  "v_mfma_f32_16x16x32_bf16 v[108:111], v[212:215], v[224:227], v[108:111]\n\t" \
  "ds_read_b128 v[188:191], v242 offset:6144\n\t" \
  "v_mfma_f32_16x16x32_bf16 v[112:115], v[200:203], v[228:231], v[112:115]\n\t" \
  "ds_read_b128 v[192:195], v242 offset:8192\n\t" \
  "v_mfma_f32_16x16x32_bf16 v[116:119], v[204:207], v[228:231], v[116:119]\n\t" \
  "ds_read_b128 v[196:199], v242 offset:10240\n\t" \
  "v_mfma_f32_16x16x32_bf16 v[120:123], v[208:211], v[228:231], v[120:123]\n\t" \
  "v_mfma_f32_16x16x32_bf16 v[124:127], v[212:215], v[228:231], v[124:127]\n\t" \
  "v_mfma_f32_16x16x32_bf16 v[128:131], v[200:203], v[232:235], v[128:131]\n\t" \
  "v_mfma_f32_16x16x32_bf16 v[132:135], v[204:207], v[232:235], v[132:135]\n\t" \
  "v_mfma_f32_16x16x32_bf16 v[136:139], v[208:211], v[232:235], v[136:139]\n\t" \
  "v_mfma_f32_16x16x32_bf16 v[140:143], v[212:215], v[232:235], v[140:143]\n\t" \
  "v_mfma_f32_16x16x32_bf16 v[144:147], v[200:203], v[236:239], v[144:147]\n\t" \
  "v_mfma_f32_16x16x32_bf16 v[148:151], v[204:207], v[236:239], v[148:151]\n\t" \
  "v_mfma_f32_16x16x32_bf16 v[152:155], v[208:211], v[236:239], v[152:155]\n\t" \
  "v_mfma_f32_16x16x32_bf16 v[156:159], v[212:215], v[236:239], v[156:159]\n\t" \
  "s_sub_u32 %[cnt], %[cnt], 1\n\t" \
  "s_cmp_lg_u32 %[cnt], 0\n\t" \
  "s_waitcnt lgkmcnt(0)\n\t" \
  "s_cbranch_scc1 .Lg1_%=\n\t" \
  "v_mfma_f32_16x16x32_bf16 v[64:67], v[160:163], v[176:179], v[64:67]\n\t" \
  "ds_read_b64_tr_b16 v[200:201], v240 offset:4096\n\t" \
  "v_mfma_f32_16x16x32_bf16 v[68:71], v[164:167], v[176:179], v[68:71]\n\t" \
  "ds_read_b64_tr_b16 v[202:203], v240 offset:4608\n\t" \
  "v_mfma_f32_16x16x32_bf16 v[72:75], v[168:171], v[176:179], v[72:75]\n\t" \
  "ds_read_b64_tr_b16 v[204:205], v241 offset:4096\n\t" \
  "v_mfma_f32_16x16x32_bf16 v[76:79], v[172:175], v[176:179], v[76:79]\n\t" \
  "ds_read_b64_tr_b16 v[206:207], v241 offset:4608\n\t" \
  "v_mfma_f32_16x16x32_bf16 v[80:83], v[160:163], v[180:183], v[80:83]\n\t" \
  "ds_read_b64_tr_b16 v[208:209], v240 offset:12288\n\t" \
  "v_mfma_f32_16x16x32_bf16 v[84:87], v[164:167], v[180:183], v[84:87]\n\t" \
  "ds_read_b64_tr_b16 v[210:211], v240 offset:12800\n\t" \
  "v_mfma_f32_16x16x32_bf16 v[88:91], v[168:171], v[180:183], v[88:91]\n\t" \
  "ds_read_b64_tr_b16 v[212:213], v241 offset:12288\n\t" \
  "v_mfma_f32_16x16x32_bf16 v[92:95], v[172:175], v[180:183], v[92:95]\n\t" \
  "ds_read_b64_tr_b16 v[214:215], v241 offset:12800\n\t" \
  "v_mfma_f32_16x16x32_bf16 v[96:99], v[160:163], v[184:187], v[96:99]\n\t" \
  "ds_read_b128 v[216:219], v243 offset:0\n\t" \
  "v_mfma_f32_16x16x32_bf16 v[100:103], v[164:167], v[184:187], v[100:103]\n\t" \
  "ds_read_b128 v[220:223], v243 offset:2048\n\t" \
  "v_mfma_f32_16x16x32_bf16 v[104:107], v[168:171], v[184:187], v[104:107]\n\t" \
  "ds_read_b128 v[224:227], v243 offset:4096\n\t" \
  "v_mfma_f32_16x16x32_bf16 v[108:111], v[172:175], v[184:187], v[108:111]\n\t" \
  "ds_read_b128 v[228:231], v243 offset:6144\n\t" \
  "v_mfma_f32_16x16x32_bf16 v[112:115], v[160:163], v[188:191], v[112:115]\n\t" \
  "ds_read_b128 v[232:235], v243 offset:8192\n\t" \
  "v_mfma_f32_16x16x32_bf16 v[116:119], v[164:167], v[188:191], v[116:119]\n\t" \
  "ds_read_b128 v[236:239], v243 offset:10240\n\t" \
  "v_mfma_f32_16x16x32_bf16 v[120:123], v[168:171], v[188:191], v[120:123]\n\t" \
  "v_mfma_f32_16x16x32_bf16 v[124:127], v[172:175], v[188:191], v[124:127]\n\t" \
  "v_mfma_f32_16x16x32_bf16 v[128:131], v[160:163], v[192:195], v[128:131]\n\t" \
  "v_mfma_f32_16x16x32_bf16 v[132:135], v[164:167], v[192:195], v[132:135]\n\t" \
  "v_mfma_f32_16x16x32_bf16 v[136:139], v[168:171], v[192:195], v[136:139]\n\t" \
  "v_mfma_f32_16x16x32_bf16 v[140:143], v[172:175], v[192:195], v[140:143]\n\t" \
  "v_mfma_f32_16x16x32_bf16 v[144:147], v[160:163], v[196:199], v[144:147]\n\t" \
  "v_mfma_f32_16x16x32_bf16 v[148:151], v[164:167], v[196:199], v[148:151]\n\t" \
  "v_mfma_f32_16x16x32_bf16 v[152:155], v[168:171], v[196:199], v[152:155]\n\t" \
  "v_mfma_f32_16x16x32_bf16 v[156:159], v[172:175], v[196:199], v[156:159]\n\t" \
  "s_waitcnt lgkmcnt(0)\n\t" \
  "v_mfma_f32_16x16x32_bf16 v[64:67], v[200:203], v[216:219], v[64:67]\n\t" \
  "ds_read_b128 v[160:163], %[cs] offset:0\n\t" \
  "v_mfma_f32_16x16x32_bf16 v[68:71], v[204:207], v[216:219], v[68:71]\n\t" \
  "ds_read_b128 v[164:167], %[cs] offset:64\n\t" \
  "v_mfma_f32_16x16x32_bf16 v[72:75], v[208:211], v[216:219], v[72:75]\n\t" \
  "ds_read_b128 v[168:171], %[cs] offset:256\n\t" \
  "v_mfma_f32_16x16x32_bf16 v[76:79], v[212:215], v[216:219], v[76:79]\n\t" \
  "ds_read_b128 v[172:175], %[cs] offset:320\n\t" \
  "v_mfma_f32_16x16x32_bf16 v[80:83], v[200:203], v[220:223], v[80:83]\n\t" \
  "ds_read_b128 v[176:179], %[st] offset:0\n\t" \
  "v_mfma_f32_16x16x32_bf16 v[84:87], v[204:207], v[220:223], v[84:87]\n\t" \
  "ds_read_b128 v[180:183], %[st] offset:256\n\t" \
  "v_mfma_f32_16x16x32_bf16 v[88:91], v[208:211], v[220:223], v[88:91]\n\t" \
  "ds_read_b128 v[184:187], %[st] offset:512\n\t" \
  "v_mfma_f32_16x16x32_bf16 v[92:95], v[212:215], v[220:223], v[92:95]\n\t" \
  "ds_read_b128 v[188:191], %[st] offset:768\n\t" \
  "v_mfma_f32_16x16x32_bf16 v[96:99], v[200:203], v[224:227], v[96:99]\n\t" \
  "ds_read_b128 v[192:195], %[st] offset:1024\n\t" \
  "v_mfma_f32_16x16x32_bf16 v[100:103], v[204:207], v[224:227], v[100:103]\n\t" \
  "ds_read_b128 v[196:199], %[st] offset:1280\n\t" \
  "v_mfma_f32_16x16x32_bf16 v[104:107], v[208:211], v[224:227], v[104:107]\n\t" \
  "v_mfma_f32_16x16x32_bf16 v[108:111], v[212:215], v[224:227], v[108:111]\n\t" \
  "v_mfma_f32_16x16x32_bf16 v[112:115], v[200:203], v[228:231], v[112:115]\n\t" \
  "v_mfma_f32_16x16x32_bf16 v[116:119], v[204:207], v[228:231], v[116:119]\n\t" \
  "v_mfma_f32_16x16x32_bf16 v[120:123], v[208:211], v[228:231], v[120:123]\n\t" \
  "v_mfma_f32_16x16x32_bf16 v[124:127], v[212:215], v[228:231], v[124:127]\n\t" \
  "v_mfma_f32_16x16x32_bf16 v[128:131], v[200:203], v[232:235], v[128:131]\n\t" \
  "v_mfma_f32_16x16x32_bf16 v[132:135], v[204:207], v[232:235], v[132:135]\n\t" \
  "v_mfma_f32_16x16x32_bf16 v[136:139], v[208:211], v[232:235], v[136:139]\n\t" \
  "v_mfma_f32_16x16x32_bf16 v[140:143], v[212:215], v[232:235], v[140:143]\n\t" \
  "v_mfma_f32_16x16x32_bf16 v[144:147], v[200:203], v[236:239], v[144:147]\n\t" \
  "v_mfma_f32_16x16x32_bf16 v[148:151], v[204:207], v[236:239], v[148:151]\n\t" \
  "v_mfma_f32_16x16x32_bf16 v[152:155], v[208:211], v[236:239], v[152:155]\n\t" \
  "v_mfma_f32_16x16x32_bf16 v[156:159], v[212:215], v[236:239], v[156:159]\n\t" \
  "s_nop 7\n\t" \
  "s_waitcnt lgkmcnt(0)\n\t" \
  ""
#define G1_MAIN_CLOB "v200","v201","v202","v203","v204","v205","v206","v207","v208","v209","v210","v211","v212","v213","v214","v215","v216","v217","v218","v219","v220","v221","v222","v223","v224","v225","v226","v227","v228","v229","v230","v231","v232","v233","v234","v235","v236","v237","v238","v239","v240","v241","v242","v243","v244","v245","v246","v247","v248","v249","v250","v251"
#define CONV_FIRST_ASM \
  "ds_read_b128 v[96:99], %[aa] offset:0\n\t" \
  "ds_read_b128 v[100:103], %[aa] offset:512\n\t" \
  "ds_read_b128 v[104:107], %[aa] offset:1024\n\t" \
  "ds_read_b128 v[108:111], %[aa] offset:1536\n\t" \
  "ds_read_b128 v[140:143], %[ab] offset:4032\n\t" \
  "ds_read_b128 v[136:139], %[ab] offset:3520\n\t" \
  "ds_read_b128 v[132:135], %[ab] offset:3008\n\t" \
  "ds_read_b128 v[128:131], %[ab] offset:2496\n\t" \
  "ds_read_b128 v[124:127], %[ab] offset:1984\n\t" \
  "ds_read_b128 v[120:123], %[ab] offset:1472\n\t" \
  "ds_read_b128 v[116:119], %[ab] offset:960\n\t" \
  "ds_read_b128 v[112:115], %[ab] offset:448\n\t" \
  ""
#define CONV_MAIN_ASM \
  "v_mfma_f32_16x16x32_bf16 v[92:95], v[96:99], v[140:143], v[92:95]\n\t" \
  "ds_read_b128 v[144:147], %[aa] offset:64\n\t" \
  "v_mfma_f32_16x16x32_bf16 v[88:91], v[96:99], v[136:139], v[88:91]\n\t" \
  "ds_read_b128 v[148:151], %[aa] offset:576\n\t" \
  "v_mfma_f32_16x16x32_bf16 v[84:87], v[96:99], v[132:135], v[84:87]\n\t" \
  "ds_read_b128 v[152:155], %[aa] offset:1088\n\t" \
  "v_mfma_f32_16x16x32_bf16 v[80:83], v[96:99], v[128:131], v[80:83]\n\t" \
  "ds_read_b128 v[156:159], %[aa] offset:1600\n\t" \
  "v_mfma_f32_16x16x32_bf16 v[76:79], v[96:99], v[124:127], v[76:79]\n\t" \
  "ds_read_b128 v[188:191], %[ab] offset:3968\n\t" \
  "v_mfma_f32_16x16x32_bf16 v[72:75], v[96:99], v[120:123], v[72:75]\n\t" \
  "ds_read_b128 v[184:187], %[ab] offset:3456\n\t" \
  "v_mfma_f32_16x16x32_bf16 v[68:71], v[96:99], v[116:119], v[68:71]\n\t" \
  "ds_read_b128 v[180:183], %[ab] offset:2944\n\t" \
  "v_mfma_f32_16x16x32_bf16 v[64:67], v[96:99], v[112:115], v[64:67]\n\t" \
  "ds_read_b128 v[176:179], %[ab] offset:2432\n\t" \
  "v_mfma_f32_16x16x32_bf16 v[92:95], v[100:103], v[136:139], v[92:95]\n\t" \
  "ds_read_b128 v[172:175], %[ab] offset:1920\n\t" \
  "v_mfma_f32_16x16x32_bf16 v[88:91], v[100:103], v[132:135], v[88:91]\n\t" \
  "ds_read_b128 v[168:171], %[ab] offset:1408\n\t" \
  "v_mfma_f32_16x16x32_bf16 v[84:87], v[100:103], v[128:131], v[84:87]\n\t" \
  "ds_read_b128 v[164:167], %[ab] offset:896\n\t" \
  "v_mfma_f32_16x16x32_bf16 v[80:83], v[100:103], v[124:127], v[80:83]\n\t" \
  "ds_read_b128 v[160:163], %[ab] offset:384\n\t" \
  "v_mfma_f32_16x16x32_bf16 v[76:79], v[100:103], v[120:123], v[76:79]\n\t" \
  "v_mfma_f32_16x16x32_bf16 v[72:75], v[100:103], v[116:119], v[72:75]\n\t" \
  "v_mfma_f32_16x16x32_bf16 v[68:71], v[100:103], v[112:115], v[68:71]\n\t" \
  "v_mfma_f32_16x16x32_bf16 v[92:95], v[104:107], v[132:135], v[92:95]\n\t" \
  "v_mfma_f32_16x16x32_bf16 v[88:91], v[104:107], v[128:131], v[88:91]\n\t" \
  "v_mfma_f32_16x16x32_bf16 v[84:87], v[104:107], v[124:127], v[84:87]\n\t" \
  "v_mfma_f32_16x16x32_bf16 v[80:83], v[104:107], v[120:123], v[80:83]\n\t" \
  "v_mfma_f32_16x16x32_bf16 v[76:79], v[104:107], v[116:119], v[76:79]\n\t" \
  "v_mfma_f32_16x16x32_bf16 v[72:75], v[104:107], v[112:115], v[72:75]\n\t" \
  "v_mfma_f32_16x16x32_bf16 v[92:95], v[108:111], v[128:131], v[92:95]\n\t" \
  "v_mfma_f32_16x16x32_bf16 v[88:91], v[108:111], v[124:127], v[88:91]\n\t" \
  "v_mfma_f32_16x16x32_bf16 v[84:87], v[108:111], v[120:123], v[84:87]\n\t" \
  "v_mfma_f32_16x16x32_bf16 v[80:83], v[108:111], v[116:119], v[80:83]\n\t" \
  "v_mfma_f32_16x16x32_bf16 v[76:79], v[108:111], v[112:115], v[76:79]\n\t" \
  "s_waitcnt lgkmcnt(0)\n\t" \
  "v_mfma_f32_16x16x32_bf16 v[92:95], v[144:147], v[188:191], v[92:95]\n\t" \
  "ds_read_b128 v[96:99], %[aa] offset:128\n\t" \
  "v_mfma_f32_16x16x32_bf16 v[88:91], v[144:147], v[184:187], v[88:91]\n\t" \
  "ds_read_b128 v[100:103], %[aa] offset:640\n\t" \
  "v_mfma_f32_16x16x32_bf16 v[84:87], v[144:147], v[180:183], v[84:87]\n\t" \
  "ds_read_b128 v[104:107], %[aa] offset:1152\n\t" \
  "v_mfma_f32_16x16x32_bf16 v[80:83], v[144:147], v[176:179], v[80:83]\n\t" \
  "ds_read_b128 v[108:111], %[aa] offset:1664\n\t" \
  "v_mfma_f32_16x16x32_bf16 v[76:79], v[144:147], v[172:175], v[76:79]\n\t" \
  "ds_read_b128 v[140:143], %[ab] offset:3904\n\t" \
  "v_mfma_f32_16x16x32_bf16 v[72:75], v[144:147], v[168:171], v[72:75]\n\t" \
  "ds_read_b128 v[136:139], %[ab] offset:3392\n\t" \
  "v_mfma_f32_16x16x32_bf16 v[68:71], v[144:147], v[164:167], v[68:71]\n\t" \
  "ds_read_b128 v[132:135], %[ab] offset:2880\n\t" \
  "v_mfma_f32_16x16x32_bf16 v[64:67], v[144:147], v[160:163], v[64:67]\n\t" \
  "ds_read_b128 v[128:131], %[ab] offset:2368\n\t" \
  "v_mfma_f32_16x16x32_bf16 v[92:95], v[148:151], v[184:187], v[92:95]\n\t" \
  "ds_read_b128 v[124:127], %[ab] offset:1856\n\t" \
  "v_mfma_f32_16x16x32_bf16 v[88:91], v[148:151], v[180:183], v[88:91]\n\t" \
  "ds_read_b128 v[120:123], %[ab] offset:1344\n\t" \
  "v_mfma_f32_16x16x32_bf16 v[84:87], v[148:151], v[176:179], v[84:87]\n\t" \
  "ds_read_b128 v[116:119], %[ab] offset:832\n\t" \
  "v_mfma_f32_16x16x32_bf16 v[80:83], v[148:151], v[172:175], v[80:83]\n\t" \
  "ds_read_b128 v[112:115], %[ab] offset:320\n\t" \
  "v_mfma_f32_16x16x32_bf16 v[76:79], v[148:151], v[168:171], v[76:79]\n\t" \
  "v_mfma_f32_16x16x32_bf16 v[72:75], v[148:151], v[164:167], v[72:75]\n\t" \
  "v_mfma_f32_16x16x32_bf16 v[68:71], v[148:151], v[160:163], v[68:71]\n\t" \
  "v_mfma_f32_16x16x32_bf16 v[92:95], v[152:155], v[180:183], v[92:95]\n\t" \
  "v_mfma_f32_16x16x32_bf16 v[88:91], v[152:155], v[176:179], v[88:91]\n\t" \
  "v_mfma_f32_16x16x32_bf16 v[84:87], v[152:155], v[172:175], v[84:87]\n\t" \
  "v_mfma_f32_16x16x32_bf16 v[80:83], v[152:155], v[168:171], v[80:83]\n\t" \
  "v_mfma_f32_16x16x32_bf16 v[76:79], v[152:155], v[164:167], v[76:79]\n\t" \
  "v_mfma_f32_16x16x32_bf16 v[72:75], v[152:155], v[160:163], v[72:75]\n\t" \
  "v_mfma_f32_16x16x32_bf16 v[92:95], v[156:159], v[176:179], v[92:95]\n\t" \
  "v_mfma_f32_16x16x32_bf16 v[88:91], v[156:159], v[172:175], v[88:91]\n\t" \
  "v_mfma_f32_16x16x32_bf16 v[84:87], v[156:159], v[168:171], v[84:87]\n\t" \
  "v_mfma_f32_16x16x32_bf16 v[80:83], v[156:159], v[164:167], v[80:83]\n\t" \
  "v_mfma_f32_16x16x32_bf16 v[76:79], v[156:159], v[160:163], v[76:79]\n\t" \
  "s_waitcnt lgkmcnt(0)\n\t" \
  "v_mfma_f32_16x16x32_bf16 v[92:95], v[96:99], v[140:143], v[92:95]\n\t" \
  "ds_read_b128 v[144:147], %[aa] offset:192\n\t" \
  "v_mfma_f32_16x16x32_bf16 v[88:91], v[96:99], v[136:139], v[88:91]\n\t" \
  "ds_read_b128 v[148:151], %[aa] offset:704\n\t" \
  "v_mfma_f32_16x16x32_bf16 v[84:87], v[96:99], v[132:135], v[84:87]\n\t" \
  "ds_read_b128 v[152:155], %[aa] offset:1216\n\t" \
  "v_mfma_f32_16x16x32_bf16 v[80:83], v[96:99], v[128:131], v[80:83]\n\t" \
  "ds_read_b128 v[156:159], %[aa] offset:1728\n\t" \
  "v_mfma_f32_16x16x32_bf16 v[76:79], v[96:99], v[124:127], v[76:79]\n\t" \
  "ds_read_b128 v[188:191], %[ab] offset:3840\n\t" \
  "v_mfma_f32_16x16x32_bf16 v[72:75], v[96:99], v[120:123], v[72:75]\n\t" \
  "ds_read_b128 v[184:187], %[ab] offset:3328\n\t" \
  "v_mfma_f32_16x16x32_bf16 v[68:71], v[96:99], v[116:119], v[68:71]\n\t" \
  "ds_read_b128 v[180:183], %[ab] offset:2816\n\t" \
  "v_mfma_f32_16x16x32_bf16 v[64:67], v[96:99], v[112:115], v[64:67]\n\t" \
  "ds_read_b128 v[176:179], %[ab] offset:2304\n\t" \
  "v_mfma_f32_16x16x32_bf16 v[92:95], v[100:103], v[136:139], v[92:95]\n\t" \
  "ds_read_b128 v[172:175], %[ab] offset:1792\n\t" \
  "v_mfma_f32_16x16x32_bf16 v[88:91], v[100:103], v[132:135], v[88:91]\n\t" \
  "ds_read_b128 v[168:171], %[ab] offset:1280\n\t" \
  "v_mfma_f32_16x16x32_bf16 v[84:87], v[100:103], v[128:131], v[84:87]\n\t" \
  "ds_read_b128 v[164:167], %[ab] offset:768\n\t" \
  "v_mfma_f32_16x16x32_bf16 v[80:83], v[100:103], v[124:127], v[80:83]\n\t" \
  "ds_read_b128 v[160:163], %[ab] offset:256\n\t" \
  "v_mfma_f32_16x16x32_bf16 v[76:79], v[100:103], v[120:123], v[76:79]\n\t" \
  "v_mfma_f32_16x16x32_bf16 v[72:75], v[100:103], v[116:119], v[72:75]\n\t" \
  "v_mfma_f32_16x16x32_bf16 v[68:71], v[100:103], v[112:115], v[68:71]\n\t" \
  "v_mfma_f32_16x16x32_bf16 v[92:95], v[104:107], v[132:135], v[92:95]\n\t" \
  "v_mfma_f32_16x16x32_bf16 v[88:91], v[104:107], v[128:131], v[88:91]\n\t" \
  "v_mfma_f32_16x16x32_bf16 v[84:87], v[104:107], v[124:127], v[84:87]\n\t" \
  "v_mfma_f32_16x16x32_bf16 v[80:83], v[104:107], v[120:123], v[80:83]\n\t" \
  "v_mfma_f32_16x16x32_bf16 v[76:79], v[104:107], v[116:119], v[76:79]\n\t" \
  "v_mfma_f32_16x16x32_bf16 v[72:75], v[104:107], v[112:115], v[72:75]\n\t" \
  "v_mfma_f32_16x16x32_bf16 v[92:95], v[108:111], v[128:131], v[92:95]\n\t" \
  "v_mfma_f32_16x16x32_bf16 v[88:91], v[108:111], v[124:127], v[88:91]\n\t" \
  "v_mfma_f32_16x16x32_bf16 v[84:87], v[108:111], v[120:123], v[84:87]\n\t" \
  "v_mfma_f32_16x16x32_bf16 v[80:83], v[108:111], v[116:119], v[80:83]\n\t" \
  "v_mfma_f32_16x16x32_bf16 v[76:79], v[108:111], v[112:115], v[76:79]\n\t" \
  "s_waitcnt lgkmcnt(0)\n\t" \
  "v_mfma_f32_16x16x32_bf16 v[92:95], v[144:147], v[188:191], v[92:95]\n\t" \
  "ds_read_b128 v[96:99], %[aa] offset:256\n\t" \
  "v_mfma_f32_16x16x32_bf16 v[88:91], v[144:147], v[184:187], v[88:91]\n\t" \
  "ds_read_b128 v[100:103], %[aa] offset:768\n\t" \
  "v_mfma_f32_16x16x32_bf16 v[84:87], v[144:147], v[180:183], v[84:87]\n\t" \
  "ds_read_b128 v[104:107], %[aa] offset:1280\n\t" \
  "v_mfma_f32_16x16x32_bf16 v[80:83], v[144:147], v[176:179], v[80:83]\n\t" \
  "ds_read_b128 v[108:111], %[aa] offset:1792\n\t" \
  "v_mfma_f32_16x16x32_bf16 v[76:79], v[144:147], v[172:175], v[76:79]\n\t" \
  "ds_read_b128 v[140:143], %[ab] offset:3776\n\t" \
  "v_mfma_f32_16x16x32_bf16 v[72:75], v[144:147], v[168:171], v[72:75]\n\t" \
  "ds_read_b128 v[136:139], %[ab] offset:3264\n\t" \
  "v_mfma_f32_16x16x32_bf16 v[68:71], v[144:147], v[164:167], v[68:71]\n\t" \
  "ds_read_b128 v[132:135], %[ab] offset:2752\n\t" \
  "v_mfma_f32_16x16x32_bf16 v[64:67], v[144:147], v[160:163], v[64:67]\n\t" \
  "ds_read_b128 v[128:131], %[ab] offset:2240\n\t" \
  "v_mfma_f32_16x16x32_bf16 v[92:95], v[148:151], v[184:187], v[92:95]\n\t" \
  "ds_read_b128 v[124:127], %[ab] offset:1728\n\t" \
  "v_mfma_f32_16x16x32_bf16 v[88:91], v[148:151], v[180:183], v[88:91]\n\t" \
  "ds_read_b128 v[120:123], %[ab] offset:1216\n\t" \
  "v_mfma_f32_16x16x32_bf16 v[84:87], v[148:151], v[176:179], v[84:87]\n\t" \
  "ds_read_b128 v[116:119], %[ab] offset:704\n\t" \
  "v_mfma_f32_16x16x32_bf16 v[80:83], v[148:151], v[172:175], v[80:83]\n\t" \
  "ds_read_b128 v[112:115], %[ab] offset:192\n\t" \
  "v_mfma_f32_16x16x32_bf16 v[76:79], v[148:151], v[168:171], v[76:79]\n\t" \
  "v_mfma_f32_16x16x32_bf16 v[72:75], v[148:151], v[164:167], v[72:75]\n\t" \
  "v_mfma_f32_16x16x32_bf16 v[68:71], v[148:151], v[160:163], v[68:71]\n\t" \
  "v_mfma_f32_16x16x32_bf16 v[92:95], v[152:155], v[180:183], v[92:95]\n\t" \
  "v_mfma_f32_16x16x32_bf16 v[88:91], v[152:155], v[176:179], v[88:91]\n\t" \
  "v_mfma_f32_16x16x32_bf16 v[84:87], v[152:155], v[172:175], v[84:87]\n\t" \
  "v_mfma_f32_16x16x32_bf16 v[80:83], v[152:155], v[168:171], v[80:83]\n\t" \
  "v_mfma_f32_16x16x32_bf16 v[76:79], v[152:155], v[164:167], v[76:79]\n\t" \
  "v_mfma_f32_16x16x32_bf16 v[72:75], v[152:155], v[160:163], v[72:75]\n\t" \
  "v_mfma_f32_16x16x32_bf16 v[92:95], v[156:159], v[176:179], v[92:95]\n\t" \
  "v_mfma_f32_16x16x32_bf16 v[88:91], v[156:159], v[172:175], v[88:91]\n\t" \
  "v_mfma_f32_16x16x32_bf16 v[84:87], v[156:159], v[168:171], v[84:87]\n\t" \
  "v_mfma_f32_16x16x32_bf16 v[80:83], v[156:159], v[164:167], v[80:83]\n\t" \
  "v_mfma_f32_16x16x32_bf16 v[76:79], v[156:159], v[160:163], v[76:79]\n\t" \
  "s_waitcnt lgkmcnt(0)\n\t" \
  "v_mfma_f32_16x16x32_bf16 v[92:95], v[96:99], v[140:143], v[92:95]\n\t" \
  "ds_read_b128 v[144:147], %[aa] offset:320\n\t" \
  "v_mfma_f32_16x16x32_bf16 v[88:91], v[96:99], v[136:139], v[88:91]\n\t" \
  "ds_read_b128 v[148:151], %[aa] offset:832\n\t" \
  "v_mfma_f32_16x16x32_bf16 v[84:87], v[96:99], v[132:135], v[84:87]\n\t" \
  "ds_read_b128 v[152:155], %[aa] offset:1344\n\t" \
  "v_mfma_f32_16x16x32_bf16 v[80:83], v[96:99], v[128:131], v[80:83]\n\t" \
  "ds_read_b128 v[156:159], %[aa] offset:1856\n\t" \
  "v_mfma_f32_16x16x32_bf16 v[76:79], v[96:99], v[124:127], v[76:79]\n\t" \
  "ds_read_b128 v[188:191], %[ab] offset:3712\n\t" \
  "v_mfma_f32_16x16x32_bf16 v[72:75], v[96:99], v[120:123], v[72:75]\n\t" \
  "ds_read_b128 v[184:187], %[ab] offset:3200\n\t" \
  "v_mfma_f32_16x16x32_bf16 v[68:71], v[96:99], v[116:119], v[68:71]\n\t" \
  "ds_read_b128 v[180:183], %[ab] offset:2688\n\t" \
  "v_mfma_f32_16x16x32_bf16 v[64:67], v[96:99], v[112:115], v[64:67]\n\t" \
  "ds_read_b128 v[176:179], %[ab] offset:2176\n\t" \
  "v_mfma_f32_16x16x32_bf16 v[92:95], v[100:103], v[136:139], v[92:95]\n\t" \
  "ds_read_b128 v[172:175], %[ab] offset:1664\n\t" \
  "v_mfma_f32_16x16x32_bf16 v[88:91], v[100:103], v[132:135], v[88:91]\n\t" \
  "ds_read_b128 v[168:171], %[ab] offset:1152\n\t" \
  "v_mfma_f32_16x16x32_bf16 v[84:87], v[100:103], v[128:131], v[84:87]\n\t" \
  "ds_read_b128 v[164:167], %[ab] offset:640\n\t" \
  "v_mfma_f32_16x16x32_bf16 v[80:83], v[100:103], v[124:127], v[80:83]\n\t" \
  "ds_read_b128 v[160:163], %[ab] offset:128\n\t" \
  "v_mfma_f32_16x16x32_bf16 v[76:79], v[100:103], v[120:123], v[76:79]\n\t" \
  "v_mfma_f32_16x16x32_bf16 v[72:75], v[100:103], v[116:119], v[72:75]\n\t" \
  "v_mfma_f32_16x16x32_bf16 v[68:71], v[100:103], v[112:115], v[68:71]\n\t" \
  "v_mfma_f32_16x16x32_bf16 v[92:95], v[104:107], v[132:135], v[92:95]\n\t" \
  "v_mfma_f32_16x16x32_bf16 v[88:91], v[104:107], v[128:131], v[88:91]\n\t" \
  "v_mfma_f32_16x16x32_bf16 v[84:87], v[104:107], v[124:127], v[84:87]\n\t" \
  "v_mfma_f32_16x16x32_bf16 v[80:83], v[104:107], v[120:123], v[80:83]\n\t" \
  "v_mfma_f32_16x16x32_bf16 v[76:79], v[104:107], v[116:119], v[76:79]\n\t" \
  "v_mfma_f32_16x16x32_bf16 v[72:75], v[104:107], v[112:115], v[72:75]\n\t" \
  "v_mfma_f32_16x16x32_bf16 v[92:95], v[108:111], v[128:131], v[92:95]\n\t" \
  "v_mfma_f32_16x16x32_bf16 v[88:91], v[108:111], v[124:127], v[88:91]\n\t" \
  "v_mfma_f32_16x16x32_bf16 v[84:87], v[108:111], v[120:123], v[84:87]\n\t" \
  "v_mfma_f32_16x16x32_bf16 v[80:83], v[108:111], v[116:119], v[80:83]\n\t" \
  "v_mfma_f32_16x16x32_bf16 v[76:79], v[108:111], v[112:115], v[76:79]\n\t" \
  "s_waitcnt lgkmcnt(0)\n\t" \
  "v_mfma_f32_16x16x32_bf16 v[92:95], v[144:147], v[188:191], v[92:95]\n\t" \
  "ds_read_b128 v[96:99], %[aa] offset:384\n\t" \
  "v_mfma_f32_16x16x32_bf16 v[88:91], v[144:147], v[184:187], v[88:91]\n\t" \
  "ds_read_b128 v[100:103], %[aa] offset:896\n\t" \
  "v_mfma_f32_16x16x32_bf16 v[84:87], v[144:147], v[180:183], v[84:87]\n\t" \
  "ds_read_b128 v[104:107], %[aa] offset:1408\n\t" \
  "v_mfma_f32_16x16x32_bf16 v[80:83], v[144:147], v[176:179], v[80:83]\n\t" \
  "ds_read_b128 v[108:111], %[aa] offset:1920\n\t" \
  "v_mfma_f32_16x16x32_bf16 v[76:79], v[144:147], v[172:175], v[76:79]\n\t" \
  "ds_read_b128 v[140:143], %[ab] offset:3648\n\t" \
  "v_mfma_f32_16x16x32_bf16 v[72:75], v[144:147], v[168:171], v[72:75]\n\t" \
  "ds_read_b128 v[136:139], %[ab] offset:3136\n\t" \
  "v_mfma_f32_16x16x32_bf16 v[68:71], v[144:147], v[164:167], v[68:71]\n\t" \
  "ds_read_b128 v[132:135], %[ab] offset:2624\n\t" \
  "v_mfma_f32_16x16x32_bf16 v[64:67], v[144:147], v[160:163], v[64:67]\n\t" \
  "ds_read_b128 v[128:131], %[ab] offset:2112\n\t" \
  "v_mfma_f32_16x16x32_bf16 v[92:95], v[148:151], v[184:187], v[92:95]\n\t" \
  "ds_read_b128 v[124:127], %[ab] offset:1600\n\t" \
  "v_mfma_f32_16x16x32_bf16 v[88:91], v[148:151], v[180:183], v[88:91]\n\t" \
  "ds_read_b128 v[120:123], %[ab] offset:1088\n\t" \
  "v_mfma_f32_16x16x32_bf16 v[84:87], v[148:151], v[176:179], v[84:87]\n\t" \
  "ds_read_b128 v[116:119], %[ab] offset:576\n\t" \
  "v_mfma_f32_16x16x32_bf16 v[80:83], v[148:151], v[172:175], v[80:83]\n\t" \
  "ds_read_b128 v[112:115], %[ab] offset:64\n\t" \
  "v_mfma_f32_16x16x32_bf16 v[76:79], v[148:151], v[168:171], v[76:79]\n\t" \
  "v_mfma_f32_16x16x32_bf16 v[72:75], v[148:151], v[164:167], v[72:75]\n\t" \
  "v_mfma_f32_16x16x32_bf16 v[68:71], v[148:151], v[160:163], v[68:71]\n\t" \
  "v_mfma_f32_16x16x32_bf16 v[92:95], v[152:155], v[180:183], v[92:95]\n\t" \
  "v_mfma_f32_16x16x32_bf16 v[88:91], v[152:155], v[176:179], v[88:91]\n\t" \
  "v_mfma_f32_16x16x32_bf16 v[84:87], v[152:155], v[172:175], v[84:87]\n\t" \
  "v_mfma_f32_16x16x32_bf16 v[80:83], v[152:155], v[168:171], v[80:83]\n\t" \
  "v_mfma_f32_16x16x32_bf16 v[76:79], v[152:155], v[164:167], v[76:79]\n\t" \
  "v_mfma_f32_16x16x32_bf16 v[72:75], v[152:155], v[160:163], v[72:75]\n\t" \
  "v_mfma_f32_16x16x32_bf16 v[92:95], v[156:159], v[176:179], v[92:95]\n\t" \
  "v_mfma_f32_16x16x32_bf16 v[88:91], v[156:159], v[172:175], v[88:91]\n\t" \
  "v_mfma_f32_16x16x32_bf16 v[84:87], v[156:159], v[168:171], v[84:87]\n\t" \
  "v_mfma_f32_16x16x32_bf16 v[80:83], v[156:159], v[164:167], v[80:83]\n\t" \
  "v_mfma_f32_16x16x32_bf16 v[76:79], v[156:159], v[160:163], v[76:79]\n\t" \
  "s_waitcnt lgkmcnt(0)\n\t" \
  "v_mfma_f32_16x16x32_bf16 v[92:95], v[96:99], v[140:143], v[92:95]\n\t" \
  "ds_read_b128 v[144:147], %[aa] offset:448\n\t" \
  "v_mfma_f32_16x16x32_bf16 v[88:91], v[96:99], v[136:139], v[88:91]\n\t" \
  "ds_read_b128 v[148:151], %[aa] offset:960\n\t" \
  "v_mfma_f32_16x16x32_bf16 v[84:87], v[96:99], v[132:135], v[84:87]\n\t" \
  "ds_read_b128 v[152:155], %[aa] offset:1472\n\t" \
  "v_mfma_f32_16x16x32_bf16 v[80:83], v[96:99], v[128:131], v[80:83]\n\t" \
  "ds_read_b128 v[156:159], %[aa] offset:1984\n\t" \
  "v_mfma_f32_16x16x32_bf16 v[76:79], v[96:99], v[124:127], v[76:79]\n\t" \
  "ds_read_b128 v[188:191], %[ab] offset:3584\n\t" \
  "v_mfma_f32_16x16x32_bf16 v[72:75], v[96:99], v[120:123], v[72:75]\n\t" \
  "ds_read_b128 v[184:187], %[ab] offset:3072\n\t" \
  "v_mfma_f32_16x16x32_bf16 v[68:71], v[96:99], v[116:119], v[68:71]\n\t" \
  "ds_read_b128 v[180:183], %[ab] offset:2560\n\t" \
  "v_mfma_f32_16x16x32_bf16 v[64:67], v[96:99], v[112:115], v[64:67]\n\t" \
  "ds_read_b128 v[176:179], %[ab] offset:2048\n\t" \
  "v_mfma_f32_16x16x32_bf16 v[92:95], v[100:103], v[136:139], v[92:95]\n\t" \
  "ds_read_b128 v[172:175], %[ab] offset:1536\n\t" \
  "v_mfma_f32_16x16x32_bf16 v[88:91], v[100:103], v[132:135], v[88:91]\n\t" \
  "ds_read_b128 v[168:171], %[ab] offset:1024\n\t" \
  "v_mfma_f32_16x16x32_bf16 v[84:87], v[100:103], v[128:131], v[84:87]\n\t" \
  "ds_read_b128 v[164:167], %[ab] offset:512\n\t" \
  "v_mfma_f32_16x16x32_bf16 v[80:83], v[100:103], v[124:127], v[80:83]\n\t" \
  "ds_read_b128 v[160:163], %[ab] offset:0\n\t" \
  "v_mfma_f32_16x16x32_bf16 v[76:79], v[100:103], v[120:123], v[76:79]\n\t" \
  "v_mfma_f32_16x16x32_bf16 v[72:75], v[100:103], v[116:119], v[72:75]\n\t" \
  "v_mfma_f32_16x16x32_bf16 v[68:71], v[100:103], v[112:115], v[68:71]\n\t" \
  "v_mfma_f32_16x16x32_bf16 v[92:95], v[104:107], v[132:135], v[92:95]\n\t" \
  "v_mfma_f32_16x16x32_bf16 v[88:91], v[104:107], v[128:131], v[88:91]\n\t" \
  "v_mfma_f32_16x16x32_bf16 v[84:87], v[104:107], v[124:127], v[84:87]\n\t" \
  "v_mfma_f32_16x16x32_bf16 v[80:83], v[104:107], v[120:123], v[80:83]\n\t" \
  "v_mfma_f32_16x16x32_bf16 v[76:79], v[104:107], v[116:119], v[76:79]\n\t" \
  "v_mfma_f32_16x16x32_bf16 v[72:75], v[104:107], v[112:115], v[72:75]\n\t" \
  "v_mfma_f32_16x16x32_bf16 v[92:95], v[108:111], v[128:131], v[92:95]\n\t" \
  "v_mfma_f32_16x16x32_bf16 v[88:91], v[108:111], v[124:127], v[88:91]\n\t" \
  "v_mfma_f32_16x16x32_bf16 v[84:87], v[108:111], v[120:123], v[84:87]\n\t" \
  "v_mfma_f32_16x16x32_bf16 v[80:83], v[108:111], v[116:119], v[80:83]\n\t" \
  "v_mfma_f32_16x16x32_bf16 v[76:79], v[108:111], v[112:115], v[76:79]\n\t" \
  "s_waitcnt lgkmcnt(0)\n\t" \
  "v_mfma_f32_16x16x32_bf16 v[92:95], v[144:147], v[188:191], v[92:95]\n\t" \
  "v_mfma_f32_16x16x32_bf16 v[88:91], v[144:147], v[184:187], v[88:91]\n\t" \
  "v_mfma_f32_16x16x32_bf16 v[84:87], v[144:147], v[180:183], v[84:87]\n\t" \
  "v_mfma_f32_16x16x32_bf16 v[80:83], v[144:147], v[176:179], v[80:83]\n\t" \
  "v_mfma_f32_16x16x32_bf16 v[76:79], v[144:147], v[172:175], v[76:79]\n\t" \
  "v_mfma_f32_16x16x32_bf16 v[72:75], v[144:147], v[168:171], v[72:75]\n\t" \
  "v_mfma_f32_16x16x32_bf16 v[68:71], v[144:147], v[164:167], v[68:71]\n\t" \
  "v_mfma_f32_16x16x32_bf16 v[64:67], v[144:147], v[160:163], v[64:67]\n\t" \
  "v_mfma_f32_16x16x32_bf16 v[92:95], v[148:151], v[184:187], v[92:95]\n\t" \
  "v_mfma_f32_16x16x32_bf16 v[88:91], v[148:151], v[180:183], v[88:91]\n\t" \
  "v_mfma_f32_16x16x32_bf16 v[84:87], v[148:151], v[176:179], v[84:87]\n\t" \
  "v_mfma_f32_16x16x32_bf16 v[80:83], v[148:151], v[172:175], v[80:83]\n\t" \
  "v_mfma_f32_16x16x32_bf16 v[76:79], v[148:151], v[168:171], v[76:79]\n\t" \
  "v_mfma_f32_16x16x32_bf16 v[72:75], v[148:151], v[164:167], v[72:75]\n\t" \
  "v_mfma_f32_16x16x32_bf16 v[68:71], v[148:151], v[160:163], v[68:71]\n\t" \
  "v_mfma_f32_16x16x32_bf16 v[92:95], v[152:155], v[180:183], v[92:95]\n\t" \
  "v_mfma_f32_16x16x32_bf16 v[88:91], v[152:155], v[176:179], v[88:91]\n\t" \
  "v_mfma_f32_16x16x32_bf16 v[84:87], v[152:155], v[172:175], v[84:87]\n\t" \
  "v_mfma_f32_16x16x32_bf16 v[80:83], v[152:155], v[168:171], v[80:83]\n\t" \
  "v_mfma_f32_16x16x32_bf16 v[76:79], v[152:155], v[164:167], v[76:79]\n\t" \
  "v_mfma_f32_16x16x32_bf16 v[72:75], v[152:155], v[160:163], v[72:75]\n\t" \
  "v_mfma_f32_16x16x32_bf16 v[92:95], v[156:159], v[176:179], v[92:95]\n\t" \
  "v_mfma_f32_16x16x32_bf16 v[88:91], v[156:159], v[172:175], v[88:91]\n\t" \
  "v_mfma_f32_16x16x32_bf16 v[84:87], v[156:159], v[168:171], v[84:87]\n\t" \
  "v_mfma_f32_16x16x32_bf16 v[80:83], v[156:159], v[164:167], v[80:83]\n\t" \
  "v_mfma_f32_16x16x32_bf16 v[76:79], v[156:159], v[160:163], v[76:79]\n\t" \
  "s_nop 7\n\t" \
  ""
#define CONV_MAIN_CLOB "v144","v145","v146","v147","v148","v149","v150","v151","v152","v153","v154","v155","v156","v157","v158","v159","v160","v161","v162","v163","v164","v165","v166","v167","v168","v169","v170","v171","v172","v173","v174","v175","v176","v177","v178","v179","v180","v181","v182","v183","v184","v185","v186","v187","v188","v189","v190","v191"

struct G1Ld { unsigned x[4], w[4]; };
DI G1Ld g1_lanes(int lane) {
  const int r8 = lane >> 3, sl = lane & 7;
  G1Ld v;
#pragma unroll
  for (int p = 0; p < 4; ++p) {
    v.x[p] = (r8 + 8 * p) * 1280 + ((sl ^ (r8 >> 1) ^ (4 * (p & 1))) << 4) - 1024 * p;
    v.w[p] = (r8 + 8 * p) * 3072 + ((sl ^ (r8 & 2) ^ (4 * (p & 1))) << 4) - 1024 * p;
  }
  return v;
}
template <int NT, int PART = 3> DI void g1_issue(v4u rx, v4u rg, v4u ru, const G1Ld& v, int lw, int s, unsigned lb, int row0, int ch) {
  const unsigned xso = (row0 + lw * 16 * NT) * 1280 + s * 128, wso = ch * 128 + (lw * 32 + s * 64) * 3072;
  if (PART & 1) {
    dma4<0>(rg, lb + XB + lw * 4096, v.w[0], v.w[1], v.w[2], v.w[3], wso);
    dma4<0>(ru, lb + XB + 8192 + lw * 4096, v.w[0], v.w[1], v.w[2], v.w[3], wso);
  }
#pragma unroll
  for (int p = 0; p < ((PART & 2) ? NT / 2 : 0); ++p) dma4<0>(rx, lb + (lw * 2 * NT + 4 * p) * 1024, v.x[0], v.x[1], v.x[2], v.x[3], xso + p * 32 * 1280);
}
DI unsigned nxt(unsigned so) { return so == 0 ? 2 * STAGE : so - STAGE; }
DI unsigned rem_up(unsigned so) { return so == 0 ? LX : so == STAGE ? LX + 8192 : L_U; }
DI void g1_issue_rem(v4u rx, v4u rg, v4u ru, const G1Ld& v, int lw, int s, unsigned lb, int ch, int ch2) {
  g1_issue<4>(rx, rg, ru, v, lw, s, lb, 1920, ch);
  const unsigned wso = ch2 * 128 + (lw * 32 + s * 64) * 3072;
  dma4<0>(rg, lb + 16384 + lw * 4096, v.w[0], v.w[1], v.w[2], v.w[3], wso);
  dma4<0>(ru, rem_up(lb) + lw * 4096, v.w[0], v.w[1], v.w[2], v.w[3], wso);
}
template <int NP, int S0, class Issue> DI void g1_loader(int rot, unsigned so, Issue issue) {
#pragma unroll 1
  for (int s = S0; s < 10; ++s) {
    if (s >= S0 + 2) wait_vm<NP>();
    if (S0 == 1 ? s != 2 : s >= 2) bar();
    issue(s + rot >= 10 ? s + rot - 10 : s + rot, so);
    so = nxt(so);
  }
  wait_vm<NP>(); bar();
}

DI void colsum(rsrc_t rw, unsigned off, int mat, int lane) {
  const int c = lane & 15, g = lane >> 4, q = (lane >> 2) & 3, p = lane & 3;
  const bf16x8 ones = __builtin_bit_cast(bf16x8, v4u{vmov(0x3f803f80u), vmov(0x3f803f80u), vmov(0x3f803f80u), vmov(0x3f803f80u)});
  bf16x8 f[4][2];
#pragma unroll
  for (int a = 0; a < 4; ++a) {
    const unsigned ad = XB + mat * 8192 + (8 * g + q) * 128 + (((2 * a + (p >> 1)) ^ (q & 2) ^ ((g & 1) << 2)) << 4) + 8 * (p & 1);
    f[a][0] = trd(ad, ad + 512); f[a][1] = trd(ad + 4096, ad + 4608);
  }
  __builtin_amdgcn_sched_barrier(0);
  f32x4 r[4];
#pragma unroll
  for (int a = 0; a < 4; ++a) r[a] = mfma(f[a][0], ones, f32x4{0.f, 0.f, 0.f, 0.f});
#pragma unroll
  for (int a = 0; a < 4; ++a) r[a] = mfma(f[a][1], ones, r[a]);
  f32x4 v;
#pragma unroll
  for (int e = 0; e < 4; ++e) v[e] = (c & 2) ? ((c & 1) ? r[3][e] : r[2][e]) : ((c & 1) ? r[1][e] : r[0][e]);
  if (c < 4) st128<SC1>(__builtin_bit_cast(v4u, v), rw, off + mat * 256 + (16 * c + 4 * g) * 4);
}

struct CsIn { v2u p[10]; };
DI void cs_load(rsrc_t rw, int ch, int lane, CsIn& c) {
#pragma unroll
  for (int k = 0; k < 10; ++k) c.p[k] = __builtin_amdgcn_raw_buffer_load_b64(rw, lane * 8 + k * 512, WS_CSP + ch * 5120, SC1);
  cfence();
}
DI void cs_put(const CsIn& c, unsigned off, int lane) {
  float c0 = 0.f, c1 = 0.f;
#pragma unroll
  for (int k = 0; k < 10; ++k) { c0 += __uint_as_float(c.p[k].x); asm volatile("" : "+v"(c0)); c1 += __uint_as_float(c.p[k].y); }
  *(LAS f32x2*)lds(off + lane * 8) = f32x2{c0, c1};
}

struct G1Rd { unsigned aA[2], aB[2]; };
template <int NT> DI void g1_setup(int wid, int lane, G1Rd& r, f32x4 (&acc)[4][NT]) {
  const int wa = wid & 1, wt = wid >> 1;
  const int c = lane & 15, g = lane >> 4, q = (lane >> 2) & 3, p = lane & 3;
  unsigned (&aA)[2] = r.aA; unsigned (&aB)[2] = r.aB;
#pragma unroll
  for (int a = 0; a < 2; ++a) aA[a] = XB + (8 * g + q) * 128 + (((4 * wa + 2 * a + (p >> 1)) ^ (q & 2) ^ ((g & 1) << 2)) << 4) + 8 * (p & 1);
#pragma unroll
  for (int ks = 0; ks < 2; ++ks) aB[ks] = (16 * NT * wt + c) * 128 + (((4 * ks + g) ^ (c >> 1)) << 4);
#pragma unroll
  for (int a = 0; a < 4; ++a)
#pragma unroll
    for (int t = 0; t < NT; ++t) acc[a][t] = f32x4{0.f, 0.f, 0.f, 0.f};
  asm volatile("" : "+v"(aA[0]), "+v"(aA[1]), "+v"(aB[0]), "+v"(aB[1]));
}
template <int NT> struct G1Ep { f32x4 cs[4], st[NT]; };
template <int NT> DI void g1_ep_read(int wid, int lane, unsigned st_off, unsigned cs_off, G1Ep<NT>& e) {
  const int wa = wid & 1, wt = wid >> 1, c = lane & 15, g = lane >> 4;
#pragma unroll
  for (int a = 0; a < 4; ++a) e.cs[a] = *(LAS f32x4*)lds(cs_off + (a >> 1) * 256 + (32 * wa + 16 * (a & 1) + 4 * g) * 4);
#pragma unroll
  for (int t = 0; t < NT; ++t) e.st[t] = *(LAS f32x4*)lds(st_off + (16 * NT * wt + 16 * t + c) * 16);
}
template <class Tail> DI void g1_main_rem(const G1Rd& r, unsigned db, f32x4 (&acc4)[4][4], f32x4 (&acc2)[4][2], Tail tail) {
  const unsigned (&aA)[2] = r.aA; const unsigned (&aB)[2] = r.aB;
  bf16x8 fa[2][8], fb[2][6];
  auto rd = [&](int buf, int ks, unsigned o) {
    const unsigned ou = rem_up(o) - XB;
#pragma unroll
    for (int a = 0; a < 4; ++a) { unsigned ad = aA[a & 1] + o + (a >> 1) * 8192 + ks * 4096; fa[buf][a] = trd(ad, ad + 512); }
#pragma unroll
    for (int t = 0; t < 4; ++t) fb[buf][t] = rd128(aB[ks] + o + t * 2048);
#pragma unroll
    for (int a = 0; a < 4; ++a) { unsigned ad = aA[a & 1] + (a < 2 ? o - 8192 : ou) + ks * 4096; fa[buf][4 + a] = trd(ad, ad + 512); }
#pragma unroll
    for (int t = 0; t < 2; ++t) fb[buf][4 + t] = rd128(aB[ks] + o + db + t * 2048);
  };
  auto mm = [&](int buf) {
#pragma unroll
    for (int t = 0; t < 4; ++t)
#pragma unroll
      for (int a = 0; a < 4; ++a) acc4[a][t] = mfma(fa[buf][a], fb[buf][t], acc4[a][t]);
#pragma unroll
    for (int t = 0; t < 2; ++t)
#pragma unroll
      for (int a = 0; a < 4; ++a) acc2[a][t] = mfma(fa[buf][4 + a], fb[buf][4 + t], acc2[a][t]);
  };
  auto ilv = [&]() {
#pragma unroll
    for (int i = 0; i < 22; ++i) { __builtin_amdgcn_sched_group_barrier(0x008, 1, 0); __builtin_amdgcn_sched_group_barrier(0x100, 1, 0); }
    __builtin_amdgcn_sched_group_barrier(0x008, 2, 0);
    __builtin_amdgcn_sched_barrier(0);
  };
  unsigned so = 0;
  bar();
  rd(0, 0, so);
#pragma unroll 1
  for (int s = 0; s < 9; ++s) {
    rd(1, 1, so); mm(0); ilv();
    so = nxt(so);
    wait_lg();
    bar();
    __builtin_amdgcn_sched_barrier(0);
    rd(0, 0, so); mm(1); ilv();
  }
  rd(1, 1, so); mm(0); ilv();
  tail(); mm(1); ilv();
}
struct G1Acc { f32x16 a[6]; unsigned csa, sta; };
DI void g1_setup6(int wid, int lane, G1Acc& z) {
#pragma unroll
  for (int t = 0; t < 6; ++t) z.a[t] = f32x16{};
  z.csa = L_CS + (32 * (wid & 1) + 4 * (lane >> 4)) * 4; z.sta = L_ST + (96 * (wid >> 1) + (lane & 15)) * 16;
  asm volatile("" : "+{v[64:79]}"(z.a[0]), "+{v[80:95]}"(z.a[1]), "+{v[96:111]}"(z.a[2]), "+{v[112:127]}"(z.a[3]), "+{v[128:143]}"(z.a[4]),
               "+{v[144:159]}"(z.a[5]), "+v"(z.csa), "+v"(z.sta));
}
DI void g1_main6(const G1Rd& r, G1Acc& z, f32x4 (&acc)[4][6], G1Ep<6>& ep) {
  f32x16 (&a)[6] = z.a; f32x16 cs, s0; f32x8 s1; int cnt;
  const unsigned csa = z.csa, sta = z.sta;
  asm volatile(G1_MAIN_ASM
               : "+{v[64:79]}"(a[0]), "+{v[80:95]}"(a[1]), "+{v[96:111]}"(a[2]), "+{v[112:127]}"(a[3]), "+{v[128:143]}"(a[4]), "+{v[144:159]}"(a[5]),
                 "=&{v[160:175]}"(cs), "=&{v[176:191]}"(s0), "=&{v[192:199]}"(s1), [cnt] "=&s"(cnt)
               : [a0] "v"(r.aA[0]), [a1] "v"(r.aA[1]), [a2] "v"(r.aB[0]), [a3] "v"(r.aB[1]), [cs] "v"(csa), [st] "v"(sta)
               : "memory", "scc", G1_MAIN_CLOB);
#pragma unroll
  for (int t = 0; t < 6; ++t)
#pragma unroll
    for (int i = 0; i < 16; ++i) acc[i >> 2][t][i & 3] = a[t][i];
#pragma unroll
  for (int i = 0; i < 16; ++i) { ep.cs[i >> 2][i & 3] = cs[i]; ep.st[i >> 2][i & 3] = s0[i]; }
#pragma unroll
  for (int i = 0; i < 8; ++i) ep.st[4 + (i >> 2)][i & 3] = s1[i];
}
template <int NT, bool LAST> DI void g1_epilogue(rsrc_t rw, int wid, int lane, int t16_0, int ch, const G1Ep<NT>& ep, const f32x4 (&acc)[4][NT]) {
  const int wa = wid & 1, wt = wid >> 1;
  const f32x4 (&cs)[4] = ep.cs; const f32x4 (&st)[NT] = ep.st;
#pragma unroll
  for (int t = 0; t < NT; ++t) {
    const float nm = st[t][0], qq = st[t][1], rl = st[t][2];
    v4u o;
#pragma unroll
    for (int a = 0; a < 2; ++a) {
      f32x4 tg = cs[a] * nm + acc[a][t], tu = cs[2 + a] * nm + acc[2 + a][t];
      f32x4 ar = tg * rl, e;
#pragma unroll
      for (int r = 0; r < 4; ++r) e[r] = __builtin_amdgcn_exp2f(ar[r]);
      f32x4 dn = e * qq + qq, pr = tg * tu;
#pragma unroll
      for (int r = 0; r < 4; ++r) dn[r] = __builtin_amdgcn_rcpf(dn[r]);
      pr *= dn;
      if (LAST && t == NT - 1 && a == 1) { asm volatile("" : "+v"(pr)); lds_set(M_EPI + wid * 4); }
      o[2 * a] = pk(pr[0], pr[1]); o[2 * a + 1] = pk(pr[2], pr[3]);
    }
    st128<SC1>(o, rw, lane * 16 + WS_H + ((t16_0 + NT * wt + t) * 48 + 2 * ch + wa) * 1024);
  }
}

template <int NM, class Init> DI void kgen(const unsigned (&w)[6], float dt, int lane, unsigned tb, unsigned done, f32x4 (&acc)[2][2], Init init) {
  constexpr int RS = NM * 256 + 32, KS = NM * 4;
  float lr[NM], tr[NM]; f32x2 W[NM];
#pragma unroll
  for (int m = 0; m < NM; ++m) {
    float v[6];
#pragma unroll
    for (int i = 0; i < 6; ++i) v[i] = m == 0 ? blo(w[i]) : bhi(w[i]);
    lr[m] = dt * v[0] * 1.44269504f; tr[m] = dt * v[1] * 0.15915494f;
    const float br = dt * v[2], bi = dt * v[3];
    W[m] = f32x2{v[4] * br - v[5] * bi, -(v[4] * bi + v[5] * br)};
  }
  unsigned wa = tb + lane * 4 * NM, fa = tb + (lane & 15) * RS + (lane >> 4) * 16;
  asm volatile("" : "+v"(wa), "+v"(fa));
  unsigned wq[4] = {wa, wa + 4 * RS, wa + 8 * RS, wa + 12 * RS};
  if (NM == 1) asm volatile("" : "+v"(wq[1]), "+v"(wq[2]), "+v"(wq[3]));
  auto put = [&](int row, const f32x2* z) {
    if (NM == 2) *(LAS v2u*)lds(wa + row * RS) = v2u{pk2(z[0]), pk2(z[NM - 1])};
    else *(LAS unsigned*)lds(wq[row >> 2] + (row & 3) * RS) = pk2(z[0]);
  };
  f32x2 c[8][NM], hi[NM], t[NM], c8[NM];
#pragma unroll
  for (int m = 0; m < NM; ++m) {
    c[0][m] = f32x2{1.f, 0.f}; c[1][m] = cpow(lr[m], tr[m], 8.f);
    c[2][m] = cmul(c[1][m], c[1][m]); c[3][m] = cmul(c[2][m], c[1][m]); c[4][m] = cmul(c[2][m], c[2][m]);
    c[5][m] = cmul(c[4][m], c[1][m]); c[6][m] = cmul(c[4][m], c[2][m]); c[7][m] = cmul(c[4][m], c[3][m]);
    c8[m] = cmul(c[4][m], c[4][m]); hi[m] = c8[m];
  }
  bf16x8 fb[2][KS];
#pragma unroll
  for (int j = 0; j < 8; ++j) put(j, c[j]);
#pragma unroll
  for (int a = 1; a < 4; ++a) {
    if (a == 2) {
#pragma unroll
      for (int s = 0; s < KS; ++s) fb[0][s] = rd128(fa + s * 64);
    }
    put((8 * a) & 15, hi);
#pragma unroll
    for (int j = 1; j < 8; ++j) {
#pragma unroll
      for (int m = 0; m < NM; ++m) t[m] = cmul(hi[m], c[j][m]);
      put((8 * a + j) & 15, t);
    }
    if (a < 3) {
#pragma unroll
      for (int m = 0; m < NM; ++m) hi[m] = cmul(hi[m], c8[m]);
    }
  }
#pragma unroll
  for (int s = 0; s < KS; ++s) fb[1][s] = rd128(fa + s * 64);

  f32x2 z1[NM], zb[NM], r0[NM], r4[NM];
#pragma unroll
  for (int m = 0; m < NM; ++m) { z1[m] = cpow(lr[m], -tr[m], 1.f); zb[m] = cpow(lr[m], -tr[m], 256.f); t[m] = W[m]; }
#pragma unroll
  for (int b = 0; b < 8; ++b) {
    put(b, t);
    if ((b & 1) == 0) {
      f32x2 u[NM];
#pragma unroll
      for (int m = 0; m < NM; ++m) { u[m] = cmul(t[m], zb[m]); if (b == 0) r0[m] = u[m]; if (b == 4) r4[m] = u[m]; }
      put(8 + b / 2, u);
    }
    if (b < 7) {
#pragma unroll
      for (int m = 0; m < NM; ++m) t[m] = cmul(t[m], z1[m]);
    }
  }
  if (NM == 2) lds_set(done);
#pragma unroll
  for (int i = 0; i < 2; ++i) {
#pragma unroll
    for (int m = 0; m < NM; ++m) { r0[m] = cmul(r0[m], zb[m]); r4[m] = cmul(r4[m], zb[m]); }
    put(12 + 2 * i, r0); put(13 + 2 * i, r4);
  }
  init();
  bf16x8 fr[2][KS];
#pragma unroll
  for (int s = 0; s < KS; ++s) fr[0][s] = rd128(fa + s * 64);
#pragma unroll
  for (int i = 0; i < 5; ++i) {
#pragma unroll
    for (int m = 0; m < NM; ++m) r0[m] = cmul(r0[m], zb[m]);
    put(i, r0);
  }
#pragma unroll
  for (int s = 0; s < KS; ++s) fr[1][s] = rd128(fa + s * 64);
  __builtin_amdgcn_sched_barrier(0);
#pragma unroll
  for (int rt = 0; rt < 2; ++rt)
#pragma unroll
    for (int s = 0; s < KS; ++s) { acc[rt][0] = mfma(fr[rt][s], fb[0][s], acc[rt][0]); acc[rt][1] = mfma(fr[rt][s], fb[1][s], acc[rt][1]); }
}
struct LnOut { s16x4 tr[2][10]; v2u st; };
DI void ln8(rsrc_t rx, int tb, int lane, unsigned tile, LnOut& out) {
  const int r4 = lane >> 4, i = lane & 15;
  v4u raw[2][5];
#pragma unroll
  for (int h = 0; h < 2; ++h)
#pragma unroll
    for (int k = 0; k < 5; ++k) raw[h][k] = __builtin_amdgcn_raw_buffer_load_b128(rx, r4 * 1280 + i * 16 + k * 256, (tb * 8 + h * 4) * 1280, 0);
  const unsigned ta = tile + ((lane >> 2) & 3) * LN_RS + (16 * r4 + 4 * (lane & 3)) * 2;
#pragma unroll
  for (int h = 0; h < 2; ++h) {
    float s1 = 0.f, s2 = 0.f, s3 = 0.f, s4 = 0.f;
    const bf16x2 one2 = __builtin_bit_cast(bf16x2, 0x3f803f80u);
#pragma unroll
    for (int k = 0; k < 10; ++k) {
      const unsigned w0 = raw[h][k >> 1][2 * (k & 1)], w1 = raw[h][k >> 1][2 * (k & 1) + 1];
      const bf16x2 p0 = __builtin_bit_cast(bf16x2, w0), p1 = __builtin_bit_cast(bf16x2, w1);
      s1 = __builtin_amdgcn_fdot2_f32_bf16(p0, one2, s1, false); s2 = __builtin_amdgcn_fdot2_f32_bf16(p0, p0, s2, false);
      s3 = __builtin_amdgcn_fdot2_f32_bf16(p1, one2, s3, false); s4 = __builtin_amdgcn_fdot2_f32_bf16(p1, p1, s4, false);
    }
    const float mean = rowsum16(s1 + s3) * (1.f / D);
    const float var = rbf(fmaxf(rowsum16(s2 + s4) * (1.f / D) - mean * mean, 0.f));
    const float m = rbf(mean), den = rbf(sqrtf(rbf(var + 1e-5f)));
    const float ri = __builtin_amdgcn_rcpf(den), nb = -m * ri;
    if (h == 0 || (i & 1)) out.st = v2u{__float_as_uint(m), __float_as_uint(den)};
#pragma unroll
    for (int k = 0; k < 5; ++k) {
      v4u o;
#pragma unroll
      for (int e = 0; e < 4; ++e) { const unsigned w = raw[h][k][e]; o[e] = pk2(f32x2{blo(w), bhi(w)} * ri + nb); }
      *(LAS v4u*)lds(tile + r4 * LN_RS + i * 16 + k * 256) = o;
    }
    cfence();
#pragma unroll
    for (int it = 0; it < 10; ++it) out.tr[h][it] = __builtin_amdgcn_ds_read_tr16_b64_v4i16((LAS s16x4*)lds(ta + it * 128));
  }
}
DI void ln8_store(rsrc_t rw, int tb, int lane, const LnOut& in) {
  if ((lane & 15) < 2) __builtin_amdgcn_raw_buffer_store_b64(in.st, rw, ((lane & 1) * 4 + (lane >> 4)) * 8, WS_STATS + tb * 128, SC1);
#pragma unroll
  for (int it = 0; it < 10; ++it) {
    bf16x8 o = __builtin_shufflevector(in.tr[0][it], in.tr[1][it], 0, 1, 2, 3, 4, 5, 6, 7);
    st128<SC1>(__builtin_bit_cast(v4u, o), rw, lane * 16 + it * 1024 + WS_U8 + tb * 10240);
  }
}

DI unsigned prm(unsigned hi, unsigned lo, unsigned sel) { return __builtin_amdgcn_perm(hi, lo, sel); }
DI unsigned colo(int col) { return (col & 15) * 16 + ((col >> 4) & 1) * 8; }
template <int S> DI v4u kchunk(unsigned raw, int lane) {
  const int i = lane & 31, up = lane >> 5;
  if (S == 0) {
    const v2u a0 = *(LAS v2u*)lds(raw + colo(i) + 512 * up);
    const v2u a1 = *(LAS v2u*)lds(raw + (up ? (i == 31 ? 768 : 512 + colo(i + 1)) : 256 + colo(i)));
    const unsigned s0 = up ? 0x03020504u : 0x05040706u, s1 = up ? 0x01000100u : 0x05040706u, s2 = up ? 0x03020504u : 0x05040706u,
                   s3 = up ? 0x01000100u : 0x01000302u;
    return v4u{prm(up ? a1.x : a1.y, a0.y, s0), prm(a1.x, a0.y, s1), prm(a0.y, a0.x, s2), prm(0, a0.x, s3)};
  } else {
    const unsigned b0 = *(LAS unsigned*)lds(raw + 768 + colo(i) + 4 * up);
    const unsigned b1 = *(LAS unsigned*)lds(raw + (lane == 63 ? 1024 : 768 + colo(i + 1) + 4 * ((lane + 1) >> 5)));
    return v4u{prm(0, b1, 0x01000100), prm(0, b0, 0x03020302), prm(0, b0, 0x01000302), prm(0, b0, 0x01000100)};
  }
}
struct HWin { v8u w, e, o0, o1; };
DI void hwin(v4u x, v4u y, HWin& h) {
#pragma unroll
  for (int k = 0; k < 4; ++k) { h.w[k] = vmov(x[k]); h.w[4 + k] = y[k]; }
  asm volatile("" : "+v"(h.w));
#pragma unroll
  for (int k = 0; k < 6; ++k) {
    h.e[k] = vmov(h.w[k + 1]);
    h.o0[k] = __builtin_amdgcn_alignbit(h.w[k + 1], h.w[k], 16);
    asm("v_alignbit_b32 %0, %1, %2, 16" : "=v"(h.o1[k]) : "v"(h.w[k + 2]), "v"(h.w[k + 1]));
  }
  asm volatile("" : "+v"(h.e), "+v"(h.o0), "+v"(h.o1));
}
DI void hwrite(const HWin& h, unsigned a) {
  *(LAS v4u*)lds(a + 7 * H_CS) = __builtin_shufflevector(h.w, h.w, 0, 1, 2, 3);
  *(LAS v4u*)lds(a + 3 * H_CS) = __builtin_shufflevector(h.w, h.w, 2, 3, 4, 5);
  *(LAS v4u*)lds(a + 5 * H_CS) = __builtin_shufflevector(h.e, h.e, 0, 1, 2, 3);
  *(LAS v4u*)lds(a + 1 * H_CS) = __builtin_shufflevector(h.e, h.e, 2, 3, 4, 5);
  *(LAS v4u*)lds(a + 6 * H_CS) = __builtin_shufflevector(h.o0, h.o0, 0, 1, 2, 3);
  *(LAS v4u*)lds(a + 2 * H_CS) = __builtin_shufflevector(h.o0, h.o0, 2, 3, 4, 5);
  *(LAS v4u*)lds(a + 4 * H_CS) = __builtin_shufflevector(h.o1, h.o1, 0, 1, 2, 3);
  *(LAS v4u*)lds(a + 0 * H_CS) = __builtin_shufflevector(h.o1, h.o1, 2, 3, 4, 5);
}
struct ConvPre { v4u x0, x1; unsigned kk, k3; bf16x8 kt[4], b9, b7; };
DI void conv_masks(int lane, ConvPre& p) {
#pragma unroll
  for (int jj = 0; jj < 8; ++jj) {
    const int dl = 8 * (lane >> 4) + jj - 8 - (lane & 15);
    p.b9[jj] = (dl >= -4 && dl <= 4) ? 0x3f80 : 0; p.b7[jj] = (dl >= -3 && dl <= 3) ? 0x3f80 : 0;
  }
  asm volatile("" : "+v"(p.b9), "+v"(p.b7));
}
DI void conv_pre(unsigned raw, int lane, ConvPre& p) {
  p.x0 = kchunk<0>(raw, lane); p.x1 = kchunk<1>(raw, lane);
  p.kk = *(LAS uint16_t*)lds(raw + 1024);
  const int n0 = 128 - 2 * lane, n1 = n0 - 1;
  const unsigned c0 = *(LAS uint16_t*)lds(raw + 1024 + (n0 == 128 ? 256 : colo(n0) + 2 * (n0 >> 5)));
  const unsigned c1 = *(LAS uint16_t*)lds(raw + 1024 + colo(n1) + 2 * (n1 >> 5));
  p.k3 = c0 | (c1 << 16);
  asm volatile("" : "+v"(p.x0), "+v"(p.x1), "+v"(p.kk), "+v"(p.k3));
}
DI void conv_image(ConvPre& p, unsigned hb, unsigned k3, int lane) {
  HWin h[3];
  v4u y0, y1, cl;
#pragma unroll
  for (int k = 0; k < 4; ++k) {
    y0[k] = __builtin_amdgcn_update_dpp(0u, p.x0[k], 0x138, 0xf, 0xf, false);
    y1[k] = __builtin_amdgcn_update_dpp((unsigned)__builtin_amdgcn_readlane(p.x0[k], 63), p.x1[k], 0x138, 0xf, 0xf, false);
    cl[k] = __builtin_amdgcn_readlane(p.x1[k], 63);
  }
  hwin(p.x0, y0, h[0]);
  hwin(p.x1, y1, h[1]);
  const unsigned kk = p.kk, k2 = kk | (kk << 16), m0 = lane == 0 ? ~0u : 0u, m1 = lane == 1 ? ~0u : 0u;
  hwin(v4u{0, (kk << 16) & m0, k2 & m0, k2 & m0},
       v4u{cl[0] & m0, (cl[1] & m0) | ((kk << 16) & m1), (cl[2] & m0) | (k2 & m1), (cl[3] & m0) | (k2 & m1)}, h[2]);
  unsigned ka = k3 + (254 - 2 * (lane & 15) + 8 * (lane >> 4)) * 2 - 448, a0 = hb + (1 + lane) * 16, a2 = hb + (lane < 3 ? 129 + lane : lane == 3 ? 0 : 132) * 16;
  unsigned kw = k3 + lane * 4;
  asm volatile("" : "+v"(ka), "+v"(a0), "+v"(a2), "+v"(kw)::"memory");
  *(LAS unsigned*)lds(kw) = p.k3;
  if (lane < 16) *(LAS unsigned*)lds(kw + 256) = 0;
#pragma unroll
  for (int f = 0; f < 4; ++f) {
    v4u t;
#pragma unroll
    for (int e = 0; e < 4; ++e) t[e] = *(LAS unsigned*)lds(ka + (3 - f) * 64 + e * 4);
    p.kt[f] = __builtin_bit_cast(bf16x8, t);
  }
  hwrite(h[0], a0);
  hwrite(h[1], a0 + 1024);
  if (lane < 5) hwrite(h[2], a2);
}

DI void conv(rsrc_t rw, int cw, int d, int lane, const ConvPre& p, unsigned go) {
  const unsigned hb = L_H + cw * H_SZ, ub = L_U + cw * U_SZ;
  const int c = lane & 15, g = lane >> 4;
  unsigned aa = hb + (c & 7) * H_CS + (3 + (c >> 3) - g) * 16, ab = ub + 32 + 32 * c + 16 * g;
  f32x4 acc[8] = {};
  v16u fa, fb0, fb1;
  asm volatile(CONV_FIRST_ASM : "=&{v[96:111]}"(fa), "=&{v[112:127]}"(fb0), "=&{v[128:143]}"(fb1) : [aa] "v"(aa), [ab] "v"(ab) : "memory");
  {
    const bf16x8 fx = rd128(aa + 2048);
    bf16x8 ue[4];
#pragma unroll
    for (int f = 0; f < 4; ++f) ue[f] = rd128(ab + 432 + f * 512);
    lds_wait(go);
    asm volatile("s_waitcnt lgkmcnt(0)" : "+{v[96:111]}"(fa), "+{v[112:127]}"(fb0), "+{v[128:143]}"(fb1)::"memory");
    __builtin_amdgcn_s_setprio(2);
#pragma unroll
    for (int f = 0; f < 4; ++f) {
      const bf16x8 uo = __builtin_bit_cast(bf16x8, v4u{fb0[4 * f], fb0[4 * f + 1], fb0[4 * f + 2], fb0[4 * f + 3]});
      acc[4 + f] = mfma(fx, uo, acc[4 + f]);
      const f32x4 ve = mfma(ue[f], p.b9, f32x4{0.f, 0.f, 0.f, 0.f}), vo = mfma(uo, p.b7, f32x4{0.f, 0.f, 0.f, 0.f});
      const bf16x8 vf = __builtin_bit_cast(bf16x8, v4u{pk(ve[0], vo[0]), pk(ve[1], vo[1]), pk(ve[2], vo[2]), pk(ve[3], vo[3])});
#pragma unroll
      for (int n = 4 + f; n < 8; ++n) acc[n] = mfma(vf, p.kt[n - f - 4], acc[n]);
    }
  }
  f32x16 c0, c1;
#pragma unroll
  for (int i = 0; i < 16; ++i) { c0[i] = acc[i >> 2][i & 3]; c1[i] = acc[4 + (i >> 2)][i & 3]; }
  asm volatile("s_nop 7\n\t" CONV_MAIN_ASM
               : "+{v[64:79]}"(c0), "+{v[80:95]}"(c1), "+{v[96:111]}"(fa), "+{v[112:127]}"(fb0), "+{v[128:143]}"(fb1)
               : [aa] "v"(aa), [ab] "v"(ab) : "memory", CONV_MAIN_CLOB);
#pragma unroll
  for (int i = 0; i < 16; ++i) { acc[i >> 2][i & 3] = c0[i]; acc[4 + (i >> 2)][i & 3] = c1[i]; }
#pragma unroll
  for (int n = 0; n < 8; n += 2) {
    const auto s0 = __builtin_amdgcn_permlane16_swap(pk(acc[n][0], acc[n][1]), pk(acc[n + 1][0], acc[n + 1][1]), false, false);
    const auto s1 = __builtin_amdgcn_permlane16_swap(pk(acc[n][2], acc[n][3]), pk(acc[n + 1][2], acc[n + 1][3]), false, false);
    st128<SC1>(v4u{s0[0], s1[0], s0[1], s1[1]}, rw, WS_AT + d * 4096 + 32 * c + 16 * (g >> 1) + 512 * (n + (g & 1)));
  }
}
template <int D0, int ND> DI void finish(rsrc_t ro, int w, int lane, unsigned oo, const f32x4 (&own)[ND], const v2u (&xr)[ND]) {
  f32x4 p[3][ND]; v2u a[ND];
#pragma unroll
  for (int k = 1; k < 4; ++k)
#pragma unroll
    for (int i = 0; i < ND; ++i) p[k - 1][i] = *(LAS f32x4*)lds(((w + k) & 3) * 20480 + lane * 16 + (3 - k) * 5120 + (D0 + i) * 1024);
  const unsigned at = L_AT + (4 * (lane >> 4) + ((lane >> 2) & 3)) * 128 + (16 * w + 4 * (lane & 3)) * 2;
#pragma unroll
  for (int i = 0; i < ND; ++i) a[i] = __builtin_bit_cast(v2u, __builtin_amdgcn_ds_read_tr16_b64_v4i16((LAS s16x4*)lds(at + (D0 + i) * 2048)));
#pragma unroll
  for (int i = 0; i < ND; ++i) {
    const f32x4 r = (own[i] + f32x4{blo(xr[i].x), bhi(xr[i].x), blo(xr[i].y), bhi(xr[i].y)}) + (p[0][i] + p[1][i]) +
                    (p[2][i] + f32x4{blo(a[i].x), bhi(a[i].x), blo(a[i].y), bhi(a[i].y)});
    __builtin_amdgcn_raw_buffer_store_b64(v2u{pk(r[0], r[1]), pk(r[2], r[3])}, ro, oo + (D0 + i) * 32, 0, 0);
  }
}
#endif

__global__ __launch_bounds__(512, 2) void fused_s4_ffn(
    const uint16_t* __restrict__ x, const uint16_t* __restrict__ Are, const uint16_t* __restrict__ Aim, const uint16_t* __restrict__ Bre,
    const uint16_t* __restrict__ Bim, const uint16_t* __restrict__ Cre, const uint16_t* __restrict__ Cim, const uint16_t* __restrict__ logdt,
    const uint16_t* __restrict__ dskip, const uint16_t* __restrict__ wg, const uint16_t* __restrict__ wu, const uint16_t* __restrict__ wd,
    uint16_t* __restrict__ out, void* ws) {
#if __HIP_DEVICE_COMPILE__
  const int wid = __builtin_amdgcn_readfirstlane(threadIdx.x >> 6), lane = threadIdx.x & 63;
  const int b = blockIdx.x, xcd = b & 7, j = b >> 3;
  const bool normal = j < 30;
  const int rl = j % 5, ti = 5 * (xcd & 1) + rl;
  const int mm_ = 2 * (xcd >> 1) + (xcd & 1), par = j - 30;
  auto sel = [](bool c, int a, int b) { asm("" : "+s"(a), "+s"(b)); return c ? a : b; };
  const int ch = sel(normal, 6 * (xcd >> 1) + j / 5, 3 * mm_ + 2 * par);
  const int ch2 = 3 * mm_ + 1;
  const int nch = normal ? (j < 20 ? 3 : 2) : 0;
  const int d0 = 80 * xcd + sel(j < 20, 3 * j, 2 * j + 20);
  const int jj = (j + (xcd & 2)) & 31, dj = 4 * (xcd & 1) + (jj & 3);
  const int t64 = sel(normal, (30 * (xcd >> 1) + j) >> 2, 30 + (xcd >> 2));
  const int rot = ti;
  auto pbase = [](int w) -> unsigned { return w * 20480; };
  const rsrc_t rw = mkr(ws, WS_BYTES), rx = mkr(x, L * D * 2);
  auto finish_hi = [&]() {
    const int w = wid - 4;
    const unsigned oo = ((4 * t64 + w) * 16 + (lane & 15)) * 1280 + (80 * dj + 4 * (lane >> 4)) * 2;
    v2u xr[2];
#pragma unroll
    for (int i = 0; i < 2; ++i) xr[i] = __builtin_amdgcn_raw_buffer_load_b64(rx, oo + (3 + i) * 32, 0, 0);
    wait_vm<0>();
    bar();
    const f32x4 own[2] = {*(LAS f32x4*)lds(pbase(w) + lane * 16 + 15360), *(LAS f32x4*)lds(pbase(w) + lane * 16 + 16384)};
    finish<3, 2>(mkr(out, L * D * 2), w, lane, oo, own, xr);
  };
  if (wid == 6) {
    u64 pc; asm volatile("s_getpc_b64 %0" : "=s"(pc));
    dma<0>(srd((const void*)pc, CODE_BYTES), L_DUMMY, lane * 16, j * 1024);
    dma<0>(srd((const void*)pc, CODE_BYTES), L_DUMMY, lane * 16, 32768 + j * 1024);
  }
  if (wid == 7) { if (lane < 7) *(LAS v4u*)lds(M_K1 + lane * 16) = v4u{0, 0, 0, 0}; wait_lg(); }
  asm volatile("" ::"s"(x), "s"(Are), "s"(Aim), "s"(Bre), "s"(Bim), "s"(Cre), "s"(Cim), "s"(logdt), "s"(dskip), "s"(wg), "s"(wu), "s"(wd), "s"(out), "s"(ws));
  unsigned pw[6], ldw_ = 0, dsw_ = 0;
  const int kd = d0 + (wid & 3);
  if ((wid & 3) < nch) {
    const uint16_t* const par6[6] = {Are, Aim, Bre, Bim, Cre, Cim};
    const unsigned vo = wid >= 4 ? lane * 4 : 256 + (lane >> 1) * 4;
#pragma unroll
    for (int i = 0; i < 6; ++i) pw[i] = __builtin_amdgcn_raw_buffer_load_b32(mkr(par6[i], D * NS * 2), vo, kd * NS * 2, NT_);
    ldw_ = ((const __attribute__((address_space(4))) unsigned*)logdt)[kd >> 1];
    dsw_ = ((const __attribute__((address_space(4))) unsigned*)dskip)[kd >> 1];
  }
  ConvPre pre;
  if (wid >= 4) conv_masks(lane, pre);
  G1Ld gl = g1_lanes(lane);
  asm volatile("" : "+v"(gl.x[0]), "+v"(gl.x[1]), "+v"(gl.x[2]), "+v"(gl.x[3]), "+v"(gl.w[0]), "+v"(gl.w[1]), "+v"(gl.w[2]), "+v"(gl.w[3]));
  bar();
  const float dt = rbf(expf((kd & 1) ? bhi(ldw_) : blo(ldw_)));
  const v4u sg = srd(wg, D * F * 2), su = srd(wu, D * F * 2), sx = srd(x, L * D * 2), sw = srd(ws, WS_BYTES);
  if (wid < 4) {
    if (wid < nch) {
      f32x4 ka[2][2];
#pragma unroll
      for (int i = 0; i < 6; ++i) pw[i] = (lane & 1) ? pw[i] >> 16 : pw[i];
      kgen<1>(pw, dt, lane, L_KG + wid * KG_SZ + 16 * 544, 0, ka, [&]() {
#pragma unroll
        for (int i = 0; i < 4; ++i) ka[i >> 1][i & 1] = f32x4{0.f, 0.f, 0.f, 0.f};
        if (lane == 0) ka[0][0][0] = (kd & 1) ? bhi(dsw_) : blo(dsw_);
      });
#pragma unroll
      for (int i = 0; i < 4; ++i) *(LAS f32x4*)lds(L_KG + wid * KG_SZ + 16 * 544 + i * 1024 + lane * 16) = ka[i >> 1][i & 1];
      wait_lg();
      lds_set(M_K1 + wid * 4);
    } else if (wid == 3 && normal) {
      __builtin_amdgcn_s_sleep(14);
      const v4u sd = srd(wd, F * D * 2);
      const unsigned vd = (lane >> 3) * 1280 + (lane & 7) * 16;
#pragma unroll
      for (int p = 0; p < 2; ++p) dma4<NT_>(sd, L_WDU + p * 4096, vd, vd + 9216, vd + 18432, vd + 27648, (ch * 64 + p * 32) * 1280 + ti * 128);
#pragma unroll
      for (int h = 0; h < 2; ++h) g1_issue<6, 1>(sx, sg, su, gl, h, rot, 0, 192 * ti, ch);
      wait_vm<16>();
      const unsigned a0 = L_WDU + (4 * (lane >> 4) + ((lane >> 2) & 3)) * 128 + 8 * (lane & 3);
#pragma unroll
      for (int p = 0; p < 8; ++p) {
        const unsigned a = a0 + (p >> 2) * 4096 + (p & 3) * 32;
        st128<SC1>(__builtin_bit_cast(v4u, trd(a, a + 2048)), rw, lane * 16 + WS_WDT + ((2 * ch + (p >> 2)) * 40 + 4 * ti + (p & 3)) * 1024);
      }
    }
    G1Rd rdr;
    if (normal) {
      f32x4 acc[4][6];
      G1Acc z;
      g1_setup<6>(wid, lane, rdr, acc);
      g1_setup6(wid, lane, z);
      if (wid < 2) {
        lds_wait(M_KPUT); lds_wait(M_KPUT + 4);
        if (nch == 3) lds_wait(M_KPUT + 8);
        g1_issue<6, 2>(sx, sg, su, gl, wid, rot, 0, 192 * ti, ch);
        lds_set(M_D0 + wid * 4);
        wait_vm<0>();
      } else if (wid == 2) {
        lds_wait(M_D0); lds_wait(M_D0 + 4);
#pragma unroll 1
        for (int h = 0; h < 2; ++h) g1_issue<6>(sx, sg, su, gl, h, rot == 9 ? 0 : rot + 1, 2 * STAGE, 192 * ti, ch);
      } else {
        wait_vm<8>();
      }
      G1Ep<6> ep;
      g1_main6(rdr, z, acc, ep);
      lds_set(M_MAIN + wid * 4);
      g1_epilogue<6, true>(rw, wid, lane, 12 * ti, ch, ep, acc);
    } else {
      f32x4 acc4[4][4], acc2[4][2];
      G1Ep<4> ep4; G1Ep<2> ep2;
      g1_setup<2>(wid, lane, rdr, acc2);
      g1_setup<4>(wid, lane, rdr, acc4);
      g1_main_rem(rdr, (64 * par - 32 * (wid >> 1)) * 128, acc4, acc2, [&]() {
        g1_ep_read<4>(wid, lane, L_ST, L_CS, ep4);
        g1_ep_read<2>(wid, lane, L_ST + par * 1024, L_CS + 512, ep2);
      });
      lds_set(M_MAIN + wid * 4);
      g1_epilogue<4, false>(rw, wid, lane, 120, ch, ep4, acc4);
      g1_epilogue<2, true>(rw, wid, lane, 120 + 4 * par, ch2, ep2, acc2);
    }
    const v2u tag = tag_of();
    const unsigned wso = WS_WDT + (12 * wid * 40 + 5 * dj) * 1024, hso = WS_H + (4 * t64 * 48 + 12 * wid) * 1024;
    const unsigned vo0 = lane * 16, vo1 = lane * 16 + 4096, pw = pbase(wid) + lane * 16;
    v4u wr[5][5], hr[3][4];
#pragma unroll
    for (int i = 0; i < 25; ++i) asm volatile("" : "=v"(wr[i / 5][i % 5]));
#pragma unroll
    for (int i = 0; i < 12; ++i) asm volatile("" : "=v"(hr[i / 4][i % 4]));
    auto ld1 = [&](v4u& dst, unsigned vo, unsigned so, int imm) {
      if (imm == 0) asm volatile("buffer_load_dwordx4 %0, %1, %2, %3 offen sc1" : "+v"(dst) : "v"(vo), "s"(sw), "s"(so) : "memory");
      if (imm == 1) asm volatile("buffer_load_dwordx4 %0, %1, %2, %3 offen offset:1024 sc1" : "+v"(dst) : "v"(vo), "s"(sw), "s"(so) : "memory");
      if (imm == 2) asm volatile("buffer_load_dwordx4 %0, %1, %2, %3 offen offset:2048 sc1" : "+v"(dst) : "v"(vo), "s"(sw), "s"(so) : "memory");
      if (imm == 3) asm volatile("buffer_load_dwordx4 %0, %1, %2, %3 offen offset:3072 sc1" : "+v"(dst) : "v"(vo), "s"(sw), "s"(so) : "memory");
    };
    auto ldw = [&](int s, int dd) { ld1(wr[s % 5][dd], dd < 4 ? vo0 : vo1, wso + s * 40960, dd & 3); };
    unsigned ht[4];
#pragma unroll
    for (int t = 0; t < 4; ++t) ht[t] = vo0 + ((wid + 1 + t) & 3) * 49152;
    auto ldh = [&](int s, int t) { ld1(hr[s % 3][t], ht[t], hso + s * 1024, 0); };
    wait_vm<0>();
    if (lane == 0) put_flag(rw, WS_F1 + ((normal ? ti * 24 + ch : 240 + 2 * mm_ + par) * 4 + wid) * 8, tag);
#pragma unroll
    for (int i = 0; i < 25; ++i) ldw(i / 5, i % 5);
    if (wid == 0) {
      lds_wait4(M_MAIN);
#pragma unroll
      for (int s = 0; s < 4; ++s) dma5(sw, s * 5120, vo0, wso + (8 + s) * 40960);
    }
    lds_set(M_PREF + wid * 4);
    __builtin_amdgcn_s_waitcnt(0x0F70);
    lds_wait(M_H);
#pragma unroll
    for (int i = 0; i < 12; ++i) ldh(i / 4, i % 4);
    f32x4 acc[4][5];
    const int c = lane & 15, g = lane >> 4;
    const unsigned oo = ((4 * t64 + wid) * 16 + c) * 1280 + (80 * dj + 4 * g) * 2;
    v2u xr[3];
    auto mm1 = [&](int s, int t, int dd) {
      if (s == 0) asm volatile("v_mfma_f32_16x16x32_bf16 %0, %1, %2, 0" : "=&v"(acc[t][dd]) : "v"(wr[0][dd]), "v"(hr[0][t]));
      else asm volatile("v_mfma_f32_16x16x32_bf16 %0, %1, %2, %0" : "+v"(acc[t][dd]) : "v"(wr[s % 5][dd]), "v"(hr[s % 3][t]));
    };
#pragma unroll
    for (int s = 0; s < 10; ++s) {
      if (s == 8) wait_lg();
      asm volatile("s_waitcnt vmcnt(%0)" ::"i"(g2_wait(s)) : "memory");
#pragma unroll
      for (int t = 0; t < 4; ++t) {
#pragma unroll
        for (int dd = 0; dd < 5; ++dd) {
          mm1(s, t, dd);
          if (t == 3 && s >= 3 && s < 7)
            asm volatile("ds_read_b128 %0, %1 offset:%2" : "+v"(wr[s % 5][dd]) : "v"(pw), "i"((s - 3) * 5120 + dd * 1024) : "memory");
        }
        if (s + 3 < 12) ldh(s + 3, t);
        if (g2_piece(s, t) >= 0) ldw(5 + g2_piece(s, t) / 5, g2_piece(s, t) % 5);
      }
      if (s == 4 && wid == 0) lds_set(M_G2);
    }
#pragma unroll
    for (int dd = 0; dd < 3; ++dd) asm volatile("buffer_load_dwordx2 %0, %1, %2, 0 offen offset:%3" : "=&v"(xr[dd]) : "v"(oo), "s"(sx), "i"(dd * 32) : "memory");
#pragma unroll
    for (int t = 0; t < 4; ++t) {
      asm volatile("s_waitcnt vmcnt(%0)" ::"i"(6 - t) : "memory");
#pragma unroll
      for (int i = 0; i < 10; ++i) {
        mm1(10 + i / 5, t, i % 5);
        if (t > 0 && i >= 3 && i < 8)
          asm volatile("ds_write_b128 %0, %1 offset:%2" ::"v"(pw), "v"(acc[t - 1][i - 3]), "i"((t - 1) * 5120 + (i - 3) * 1024) : "memory");
      }
    }
    asm volatile("s_nop 7" : "+v"(acc[3][0]), "+v"(acc[3][1]), "+v"(acc[3][2]), "+v"(acc[3][3]), "+v"(acc[3][4]));
    *(LAS f32x4*)lds(pw + 15360) = acc[3][3];
    *(LAS f32x4*)lds(pw + 16384) = acc[3][4];
    asm volatile("s_waitcnt vmcnt(0) lgkmcnt(0)" : "+v"(xr[0]), "+v"(xr[1]), "+v"(xr[2])::"memory");
    bar();
    const f32x4 own[3] = {acc[3][0], acc[3][1], acc[3][2]};
    finish<0, 3>(mkr(out, L * D * 2), wid, lane, oo, own, xr);
    return;
  }
  const bool ln2 = wid == 6 && (j == 20 || j == 21);
  const int tb = ln2 ? 240 + 2 * xcd + j - 20 : 120 * (xcd & 1) + 30 * (xcd >> 1) + j;
  LnOut lo;
  v2u tag7 = {0, 0};
  if (wid == 7) tag7 = epoch_tag(rw);
  if (ln2 || (wid == 7 && normal)) ln8(rx, tb, lane, ln2 ? L_KG + 2 * KG_SZ : L_LN, lo);
  if (wid < 7) {
    const int cw = wid - 4;
    v4u sink = {0, 0, 0, 0};
    auto warm_wd = [&]() {
#pragma unroll
      for (int i = 0; i < 3; ++i) {
        const unsigned so = WS_WDT + ((6 * (jj >> 2) + 3 * cw + i) * 40 + 5 * dj) * 1024;
        touch4(sink, sw, lane * 16, so); touch(sink, sw, lane * 16, so + 4096);
      }
      wait_vm<15>();
    };
    if (normal) {
      if (cw < nch) {
        f32x4 ka[2][2];
        __builtin_amdgcn_s_setprio(1);
        kgen<2>(pw, dt, lane, L_KG + cw * KG_SZ, M_KPUT + cw * 4, ka, [&]() {
          lds_wait(M_K1 + cw * 4);
#pragma unroll
          for (int i = 0; i < 4; ++i) ka[i >> 1][i & 1] = *(LAS f32x4*)lds(L_KG + cw * KG_SZ + 16 * 544 + i * 1024 + lane * 16);
        });
        __builtin_amdgcn_s_setprio(0);
#pragma unroll
        for (int rt = 0; rt < 2; ++rt)
          *(LAS v4u*)lds(L_RAW + cw * 2048 + rt * 1024 + lane * 16) =
              v4u{pk(ka[rt][0][0], ka[rt][0][1]), pk(ka[rt][0][2], ka[rt][0][3]), pk(ka[rt][1][0], ka[rt][1][1]), pk(ka[rt][1][2], ka[rt][1][3])};
        conv_pre(L_RAW + cw * 2048, lane, pre);
      }
      wait_lg();
      if (cw < 2) {
        g1_loader<20, 2>(rot, STAGE, [&](int s, unsigned so) { g1_issue<6>(sx, sg, su, gl, cw, s, so, 192 * ti, ch); });
        warm_wd();
      } else {
        bar();
        colsum(rw, WS_CSP + (ch * 10 + ti) * 512, 0, lane);
        colsum(rw, WS_CSP + (ch * 10 + ti) * 512, 1, lane);
        if (ln2) ln8_store(rw, tb, lane, lo);
        bar();
        wait_vm<0>();
#pragma unroll 1
        for (int k = 2; k < 8; ++k) bar();
        CsIn ci;
        cs_load(rw, ch, lane, ci);
        bar();
        cs_put(ci, L_CS, lane);
        wait_lg();
      }
      bar();
      if (cw < nch) conv_image(pre, L_H + cw * H_SZ, L_K3 + cw * 320, lane);
    } else if (cw < 2) {
      const int rrot = 5 * (xcd & 1);
      g1_issue_rem(sx, sg, su, gl, cw, rrot, 0, ch, ch2);
      wait_vm<0>();
      g1_loader<24, 1>(rrot, 2 * STAGE, [&](int s, unsigned so) { g1_issue_rem(sx, sg, su, gl, cw, s, so, ch, ch2); });
      warm_wd(); bar();
    } else {
#pragma unroll 1
      for (int s = 0; s < 8; ++s) bar();
      CsIn ci[2];
      cs_load(rw, ch, lane, ci[0]); cs_load(rw, ch2, lane, ci[1]);
      bar();
      cs_put(ci[0], L_CS, lane); cs_put(ci[1], L_CS + 512, lane);
      wait_lg();
      bar();
    }
    asm volatile("s_waitcnt vmcnt(0)" : "+v"(sink)::"memory");
    __builtin_amdgcn_s_setprio(1);
    if (cw < nch) {
      lds_wait(M_U);
      conv(rw, cw, d0 + cw, lane, pre, M_EPI + cw * 4);
      lds_set(M_CONV + cw * 4);
      wait_vm<0>();
      if (lane == 0) put_flag(rw, WS_F2 + (d0 + cw) * 8, tag_of());
    }
    finish_hi();
  } else {
    const v2u tag = tag7;
    if (lane == 0) *(LAS v4u*)lds(L_MISC) = v4u{tag.x, tag.y, 0, 0};
    wait_lg();
    bar();
    if (normal) ln8_store(rw, tb, lane, lo);
    bar(); bar();
    wait_vm<0>();
    if (lane == 0) put_flag(rw, WS_F0 + b * 8, tag);
    bar(); bar(); bar();
    Poll4 p0;
    poll4_issue(rw, WS_F0, 256, lane, p0);
    bar();
    poll4_finish(rw, WS_F0, 256, tag, lane, p0);
    if (b == 0 && lane == 0) put_flag(rw, WS_EPOCH, tag);
    const int row0 = normal ? 192 * ti : 1920;
    v2u st[3];
#pragma unroll
    for (int k = 0; k < 3; ++k) { const int r = row0 + lane + 64 * k; st[k] = __builtin_amdgcn_raw_buffer_load_b64(rw, (r >> 3) * 128 + (r & 7) * 8, WS_STATS, SC1); }
    cfence();
    bar(); bar();
#pragma unroll
    for (int k = 0; k < 3; ++k) {
      const float m = __uint_as_float(st[k].x), den = __uint_as_float(st[k].y);
      *(LAS f32x4*)lds(L_ST + (lane + 64 * k) * 16) = f32x4{-m, vmul(den, den), vmul(-1.44269504f, __builtin_amdgcn_rcpf(den)), den};
    }
    wait_lg();
    bar();
    v4u ur[16];
    if ((lane & 3) < nch) {
#pragma unroll
      for (int p = 0; p < 16; ++p) ur[p] = __builtin_amdgcn_raw_buffer_load_b128(rw, (lane >> 2) * 10240 + (lane & 3) * 16, WS_U8 + p * 163840 + d0 * 16, SC1);
    }
    if (lane < 32) {
#pragma unroll
      for (int k = 0; k < 3; ++k) *(LAS v4u*)lds(L_U + k * U_SZ + lane * 16) = v4u{0, 0, 0, 0};
    }
    if ((lane & 3) < nch) {
#pragma unroll
      for (int p = 0; p < 16; ++p) *(LAS v4u*)lds(L_U + (lane & 3) * U_SZ + 512 + p * 256 + (lane >> 2) * 16) = ur[p];
    }
    wait_lg();
    lds_set(M_U);
    lds_wait4(M_MAIN);
#pragma unroll 1
    for (int w = 3; w >= 1; --w) {
#pragma unroll
      for (int s = 0; s < 4; ++s) dma5(sw, pbase(w) + s * 5120, lane * 16, WS_WDT + ((12 * w + 8 + s) * 40 + 5 * dj) * 1024);
    }
    wait_vm<0>();
    lds_wait4(M_PREF);
    poll<2>(rw, WS_F1 + (t64 < 30 ? (t64 / 3) * 768 : 7680), t64 < 30 ? 96 : 64, tag, lane);
    lds_set(M_H);
    lds_wait(M_G2);
    poll<2>(rw, WS_F2 + dj * 640, 80, tag, lane);
#pragma unroll 1
    for (int k = 0; k < nch; ++k) lds_wait(M_CONV + k * 4);
    const unsigned va = (lane >> 3) * 4096 + (lane & 7) * 16, sa = WS_AT + 80 * dj * 4096 + t64 * 128;
    dma4<SC1>(sw, L_AT, va, va + 31744, va + 63488, va + 95232, sa);
    dma4<SC1>(sw, L_AT + 4096, va, va + 31744, va + 63488, va + 95232, sa + 131072);
    dma<SC1>(sw, L_AT + 8192, va, sa + 262144); dma<SC1>(sw, L_AT + 9216, va, sa + 294912);
    finish_hi();
  }
#endif
}

extern "C" void kernel_launch(void* const* d_in, const int* in_sizes, int n_in, void* d_out, int out_size, void* d_ws, size_t ws_size,
                              hipStream_t stream) {
  (void)hipFuncSetAttribute((const void*)fused_s4_ffn, hipFuncAttributeMaxDynamicSharedMemorySize, LDS_BYTES);
  fused_s4_ffn<<<256, 512, LDS_BYTES, stream>>>(
      (const uint16_t*)d_in[0], (const uint16_t*)d_in[1], (const uint16_t*)d_in[2], (const uint16_t*)d_in[3], (const uint16_t*)d_in[4],
      (const uint16_t*)d_in[5], (const uint16_t*)d_in[6], (const uint16_t*)d_in[7], (const uint16_t*)d_in[8], (const uint16_t*)d_in[10],
      (const uint16_t*)d_in[11], (const uint16_t*)d_in[12], (uint16_t*)d_out, d_ws);
}
```
